```python
import torch

def _softmax(x, axis=-1):
    return torch.softmax(x, dim=axis)
def _gelu(x):
    return torch.nn.functional.gelu(x, approximate="tanh")
def _silu(x):
    return torch.nn.functional.silu(x)
def _sigmoid(x):
    return torch.sigmoid(x)
def _layernorm(x, axis=-1, eps=1e-5):
    m = x.mean(dim=axis, keepdim=True)
    v = x.var(dim=axis, keepdim=True, unbiased=False)
    return (x - m) / torch.sqrt(v + eps)
def _scatter_add(tgt, idx, src, axis=0):
    return tgt.clone().index_add_(axis, idx, src)
def _segment_sum(src, seg, n):
    out = torch.zeros((n,) + src.shape[1:], dtype=src.dtype)
    return out.index_add_(0, seg, src)
def _pad(x, pw):
    flat = tuple(v for pair in reversed(pw) for v in pair)
    return torch.nn.functional.pad(x, flat)

kernel_name = 'concat_heads__gqa_block_cached__mha_block_cached__near_ridge'

def setup_inputs():

    _w = (lambda a: (lambda t: t.to(torch.bfloat16) if t.is_floating_point() else t)(torch.as_tensor(a)))
    def _A_setup():
        B, Sq, D = 8, 176, 448
        Hq, Hkv, Sk, Dh = 16, 8, 2048, 256
        assert Hq % Hkv == 0, (Hq, Hkv)
        return {
            "x": _w((lambda *s: torch.randn(*s, dtype=torch.bfloat16) * 0.8)(B, Sq, D)),
            "k_cache": _w((lambda *s: torch.randn(*s, dtype=torch.bfloat16) * 0.8)(B, Hkv, Sk, Dh)),
            "v_cache": _w((lambda *s: torch.randn(*s, dtype=torch.bfloat16) * 0.05)(B, Hkv, Sk, Dh)),
            "wq": _w((lambda *s: torch.randn(*s, dtype=torch.bfloat16) * 0.8)(D, Hq * Dh) * (D ** -0.5)),
            "wo": _w((lambda *s: torch.randn(*s, dtype=torch.bfloat16) * 0.05)(Hq * Dh, D) * ((Hq * Dh) ** -0.5)),
        }
    _A_in = _A_setup()

    def _B_setup():
        B, Sq, D = 8, 176, 448
        H, Sk, Dh = 16, 2048, 256
        return {
            "x": _w((lambda *s: torch.randn(*s, dtype=torch.bfloat16) * 0.8)(B, Sq, D)),
            "k_cache": _w((lambda *s: torch.randn(*s, dtype=torch.bfloat16) * 0.8)(B, H, Sk, Dh)),
            "v_cache": _w((lambda *s: torch.randn(*s, dtype=torch.bfloat16) * 0.05)(B, H, Sk, Dh)),
            "wq": _w((lambda *s: torch.randn(*s, dtype=torch.bfloat16) * 0.8)(D, H * Dh) * (D ** -0.5)),
            "wo": _w((lambda *s: torch.randn(*s, dtype=torch.bfloat16) * 0.05)(H * Dh, D) * ((H * Dh) ** -0.5)),
        }
    _B_in = _B_setup()

    return {
        "A_x": _A_in["x"],
        "A_k_cache": _A_in["k_cache"],
        "A_v_cache": _A_in["v_cache"],
        "A_wq": _A_in["wq"],
        "A_wo": _A_in["wo"],
        "B_x": _B_in["x"],
        "B_k_cache": _B_in["k_cache"],
        "B_v_cache": _B_in["v_cache"],
        "B_wq": _B_in["wq"],
        "B_wo": _B_in["wo"],
    }

def reference(A_x, A_k_cache, A_v_cache, A_wq, A_wo, B_x, B_k_cache, B_v_cache, B_wq, B_wo):
    def _A_ref(x, k_cache, v_cache, wq, wo):
        B, Sq, D = x.shape
        Hq, Dh = 16, 256
        Hkv = k_cache.shape[1]
        rep = Hq // Hkv
        q = (lambda x, *d: x.permute(*d))((x @ wq).reshape(B, Sq, Hq, Dh), 0, 2, 1, 3)
        k = (lambda x, n, axis: torch.repeat_interleave(x, n, dim=axis))(k_cache, rep, axis=1)
        v = (lambda x, n, axis: torch.repeat_interleave(x, n, dim=axis))(v_cache, rep, axis=1)
        scores = torch.einsum("bhqd,bhkd->bhqk", q, k) * (Dh ** -0.5)
        p = _softmax(scores, axis=-1)
        o = torch.einsum("bhqk,bhkd->bhqd", p, v)
        return (lambda x, *d: x.permute(*d))(o, 0, 2, 1, 3).reshape(B, Sq, Hq * Dh) @ wo

    def _B_ref(x, k_cache, v_cache, wq, wo):
        B, Sq, D = x.shape
        H, Dh = 16, 256
        q = (lambda x, *d: x.permute(*d))((x @ wq).reshape(B, Sq, H, Dh), 0, 2, 1, 3)
        scores = torch.einsum("bhqd,bhkd->bhqk", q, k_cache) * (Dh ** -0.5)
        p = _softmax(scores, axis=-1)
        o = torch.einsum("bhqk,bhkd->bhqd", p, v_cache)
        return (lambda x, *d: x.permute(*d))(o, 0, 2, 1, 3).reshape(B, Sq, H * Dh) @ wo

    a = _A_ref(A_x, A_k_cache, A_v_cache, A_wq, A_wo)
    b = _B_ref(B_x, B_k_cache, B_v_cache, B_wq, B_wo)
    return torch.concatenate([a, b], axis=-1)
```

```cpp
#include <hip/hip_runtime.h>
#include <hip/hip_bf16.h>
#include <stdint.h>

static constexpr int NBATCH = 8, SQ = 176, DM = 448, NH = 16, DH = 256, SK = 2048;
static constexpr unsigned PART_BYTES = 11u * 7u * 2u * 1024u;

struct Args {
  const char* x[2]; const char* k[2]; const char* v[2]; const char* wq[2]; const char* wo[2];
  char* ws;
};

__global__ __launch_bounds__(256, 1) void attn_fused_kernel(Args a) {
  __shared__ __attribute__((aligned(16))) char lds[163840];
  const unsigned id = blockIdx.x, xcd = id & 7u, slot = id >> 3, prob = slot & 1u, i = slot >> 1;
  const unsigned b = 4u * (xcd & 1u) + (i >> 2), h = 4u * (xcd >> 1) + (i & 3u);
  const unsigned hkv = prob ? 16u : 8u, kvh = prob ? h : (h >> 1);
  const size_t kvoff = (size_t)(b * hkv + kvh) * (size_t)(SK * DH * 2);
  const __amdgpu_buffer_rsrc_t rk = __builtin_amdgcn_make_buffer_rsrc((void*)(a.k[prob] + kvoff), 0, SK * DH * 2, 0x00020000);
  const __amdgpu_buffer_rsrc_t rv = __builtin_amdgcn_make_buffer_rsrc((void*)(a.v[prob] + kvoff), 0, SK * DH * 2, 0x00020000);
  const __amdgpu_buffer_rsrc_t rx = __builtin_amdgcn_make_buffer_rsrc((void*)(a.x[prob] + (size_t)b * SQ * DM * 2), 0, SQ * DM * 2, 0x00020000);
  const __amdgpu_buffer_rsrc_t rwq = __builtin_amdgcn_make_buffer_rsrc((void*)(a.wq[prob] + h * DH * 2), 0, DM * NH * DH * 2 - h * DH * 2, 0x00020000);
  const __amdgpu_buffer_rsrc_t rwo = __builtin_amdgcn_make_buffer_rsrc((void*)(a.wo[prob] + (size_t)h * DH * DM * 2), 0, DH * DM * 2, 0x00020000);
  const unsigned unit = (prob * NBATCH + b) * NH + h;
  const __amdgpu_buffer_rsrc_t rp = __builtin_amdgcn_make_buffer_rsrc((void*)(a.ws + (size_t)unit * PART_BYTES), 0, PART_BYTES, 0x00020000);
  const unsigned wid = __builtin_amdgcn_readfirstlane(threadIdx.x >> 6);
  const unsigned ldsb = (unsigned)(size_t)lds;
  char* dbg = a.ws + (size_t)256 * PART_BYTES + (size_t)id * 128;
#define TSTART ((((b * hkv + kvh) * 5u + prob * 3u) * 7u) & 63u)
  const unsigned toff = (TSTART) * 16384u;
  const unsigned pfo = ((blockIdx.x >> 3) % 7u) * 16384u + wid * 4096u;
  const unsigned pfw = b * 28672u + wid * 8192u;
  asm volatile(
"LC0_%=:\n"
"s_memtime s[60:61]\n"
"s_memrealtime s[62:63]\n"
"s_cmp_eq_u32 %6, 3\n"
"s_cbranch_scc1 LW3_%=\n"
"s_lshl_b32 s40, %6, 12\n"
"s_add_u32 s40, s40, %7\n"
"s_lshl_b32 s48, %6, 13\n"
"s_add_u32 s48, s48, %7\n"
"s_mov_b32 s50, 0x3db8aa3b\n"
"s_mov_b32 s51, 0x3db8aa3b\n"
"v_mbcnt_lo_u32_b32 v240, -1, 0\n"
"v_mbcnt_hi_u32_b32 v240, -1, v240\n"
"v_and_b32 v241, 15, v240\n"
"v_lshrrev_b32 v242, 4, v240\n"
"v_bfe_u32 v243, v240, 2, 2\n"
"v_and_b32 v244, 3, v240\n"
"v_lshrrev_b32 v245, 5, v240\n"
"v_and_b32 v246, 31, v240\n"
"s_mul_i32 s46, %6, 48\n"
"v_lshrrev_b32 v247, 3, v240\n"
"v_and_b32 v248, 7, v240\n"
"v_xor_b32 v248, v248, v247\n"
"v_add_u32 v247, s46, v247\n"
"v_mul_u32_u24 v247, 896, v247\n"
"v_lshl_add_u32 v23, v248, 4, v247\n"
"v_add_u32 v24, 6144, v23\n"
"v_add_u32 v25, 12288, v23\n"
"v_add_u32 v26, 18432, v23\n"
"s_mul_i32 s49, %6, 12288\n"
"s_add_u32 s49, s49, 16384\n"
"s_add_u32 s49, s49, %7\n"
"s_mov_b32 s46, 0\n"
"s_add_u32 m0, s49, 0\n"
"s_nop 0\n"
"buffer_load_dwordx4 v23, %2, s46 offen offset:0 lds\n"
"buffer_load_dwordx4 v24, %2, s46 offen offset:1024 lds\n"
"buffer_load_dwordx4 v25, %2, s46 offen offset:2048 lds\n"
"buffer_load_dwordx4 v26, %2, s46 offen offset:3072 lds\n"
"s_mov_b32 s46, 28672\n"
"s_add_u32 m0, s49, 4096\n"
"s_nop 0\n"
"buffer_load_dwordx4 v23, %2, s46 offen offset:0 lds\n"
"buffer_load_dwordx4 v24, %2, s46 offen offset:1024 lds\n"
"s_lshl_b32 s46, %6, 3\n"
"v_add_u32 v247, s46, v245\n"
"v_add_u32 v247, 0, v247\n"
"v_bfe_u32 v248, v247, 2, 2\n"
"v_lshlrev_b32 v248, 3, v248\n"
"v_lshrrev_b32 v249, 4, v247\n"
"v_lshl_add_u32 v248, v249, 2, v248\n"
"v_and_b32 v249, 3, v247\n"
"v_add_u32 v248, v248, v249\n"
"v_lshlrev_b32 v248, 13, v248\n"
"v_and_b32 v247, 7, v247\n"
"v_lshlrev_b32 v247, 1, v247\n"
"v_xor_b32 v247, v247, v246\n"
"v_lshl_add_u32 v248, v247, 4, v248\n"
"v_subrev_u32 v8, 0, v248\n"
"v_add_u32 v247, s46, v245\n"
"v_add_u32 v247, 2, v247\n"
"v_bfe_u32 v248, v247, 2, 2\n"
"v_lshlrev_b32 v248, 3, v248\n"
"v_lshrrev_b32 v249, 4, v247\n"
"v_lshl_add_u32 v248, v249, 2, v248\n"
"v_and_b32 v249, 3, v247\n"
"v_add_u32 v248, v248, v249\n"
"v_lshlrev_b32 v248, 13, v248\n"
"v_and_b32 v247, 7, v247\n"
"v_lshlrev_b32 v247, 1, v247\n"
"v_xor_b32 v247, v247, v246\n"
"v_lshl_add_u32 v248, v247, 4, v248\n"
"v_subrev_u32 v9, 1024, v248\n"
"v_add_u32 v247, s46, v245\n"
"v_add_u32 v247, 4, v247\n"
"v_bfe_u32 v248, v247, 2, 2\n"
"v_lshlrev_b32 v248, 3, v248\n"
"v_lshrrev_b32 v249, 4, v247\n"
"v_lshl_add_u32 v248, v249, 2, v248\n"
"v_and_b32 v249, 3, v247\n"
"v_add_u32 v248, v248, v249\n"
"v_lshlrev_b32 v248, 13, v248\n"
"v_and_b32 v247, 7, v247\n"
"v_lshlrev_b32 v247, 1, v247\n"
"v_xor_b32 v247, v247, v246\n"
"v_lshl_add_u32 v248, v247, 4, v248\n"
"v_subrev_u32 v10, 2048, v248\n"
"v_add_u32 v247, s46, v245\n"
"v_add_u32 v247, 6, v247\n"
"v_bfe_u32 v248, v247, 2, 2\n"
"v_lshlrev_b32 v248, 3, v248\n"
"v_lshrrev_b32 v249, 4, v247\n"
"v_lshl_add_u32 v248, v249, 2, v248\n"
"v_and_b32 v249, 3, v247\n"
"v_add_u32 v248, v248, v249\n"
"v_lshlrev_b32 v248, 13, v248\n"
"v_and_b32 v247, 7, v247\n"
"v_lshlrev_b32 v247, 1, v247\n"
"v_xor_b32 v247, v247, v246\n"
"v_lshl_add_u32 v248, v247, 4, v248\n"
"v_subrev_u32 v11, 3072, v248\n"
"s_mov_b32 s46, 0\n"
"s_add_u32 m0, s40, 65536\n"
"s_nop 0\n"
"buffer_load_dwordx4 v8, %3, s46 offen offset:0 lds\n"
"buffer_load_dwordx4 v9, %3, s46 offen offset:1024 lds\n"
"buffer_load_dwordx4 v10, %3, s46 offen offset:2048 lds\n"
"buffer_load_dwordx4 v11, %3, s46 offen offset:3072 lds\n"
"s_mov_b32 s46, 128\n"
"s_add_u32 m0, s49, 6144\n"
"s_nop 0\n"
"buffer_load_dwordx4 v23, %2, s46 offen offset:0 lds\n"
"buffer_load_dwordx4 v24, %2, s46 offen offset:1024 lds\n"
"buffer_load_dwordx4 v25, %2, s46 offen offset:2048 lds\n"
"buffer_load_dwordx4 v26, %2, s46 offen offset:3072 lds\n"
"s_mov_b32 s46, 28800\n"
"s_add_u32 m0, s49, 10240\n"
"s_nop 0\n"
"buffer_load_dwordx4 v23, %2, s46 offen offset:0 lds\n"
"buffer_load_dwordx4 v24, %2, s46 offen offset:1024 lds\n"
"s_mov_b32 s46, 262144\n"
"s_add_u32 m0, s40, 81920\n"
"s_nop 0\n"
"buffer_load_dwordx4 v8, %3, s46 offen offset:0 lds\n"
"buffer_load_dwordx4 v9, %3, s46 offen offset:1024 lds\n"
"buffer_load_dwordx4 v10, %3, s46 offen offset:2048 lds\n"
"buffer_load_dwordx4 v11, %3, s46 offen offset:3072 lds\n"
"v_lshl_add_u32 v249, v242, 2, v243\n"
"v_and_b32 v248, 7, v249\n"
"v_lshrrev_b32 v247, 1, v244\n"
"v_add_u32 v247, 0, v247\n"
"v_xor_b32 v247, v247, v248\n"
"v_lshlrev_b32 v247, 5, v247\n"
"v_lshl_add_u32 v247, v249, 9, v247\n"
"v_and_b32 v0, 1, v244\n"
"v_lshl_add_u32 v247, v0, 4, v247\n"
"v_add_u32 v247, %7, v247\n"
"v_add_u32 v0, 0x10000, v247\n"
"v_add_u32 v4, 0x20000, v247\n"
"v_lshrrev_b32 v247, 1, v244\n"
"v_add_u32 v247, 2, v247\n"
"v_xor_b32 v247, v247, v248\n"
"v_lshlrev_b32 v247, 5, v247\n"
"v_lshl_add_u32 v247, v249, 9, v247\n"
"v_and_b32 v1, 1, v244\n"
"v_lshl_add_u32 v247, v1, 4, v247\n"
"v_add_u32 v247, %7, v247\n"
"v_add_u32 v1, 0x10000, v247\n"
"v_add_u32 v5, 0x20000, v247\n"
"v_lshrrev_b32 v247, 1, v244\n"
"v_add_u32 v247, 4, v247\n"
"v_xor_b32 v247, v247, v248\n"
"v_lshlrev_b32 v247, 5, v247\n"
"v_lshl_add_u32 v247, v249, 9, v247\n"
"v_and_b32 v2, 1, v244\n"
"v_lshl_add_u32 v247, v2, 4, v247\n"
"v_add_u32 v247, %7, v247\n"
"v_add_u32 v2, 0x10000, v247\n"
"v_add_u32 v6, 0x20000, v247\n"
"v_lshrrev_b32 v247, 1, v244\n"
"v_add_u32 v247, 6, v247\n"
"v_xor_b32 v247, v247, v248\n"
"v_lshlrev_b32 v247, 5, v247\n"
"v_lshl_add_u32 v247, v249, 9, v247\n"
"v_and_b32 v3, 1, v244\n"
"v_lshl_add_u32 v247, v3, 4, v247\n"
"v_add_u32 v247, %7, v247\n"
"v_add_u32 v3, 0x10000, v247\n"
"v_add_u32 v7, 0x20000, v247\n"
"v_and_b32 v248, 7, v241\n"
"v_add_u32 v247, 0, v242\n"
"v_xor_b32 v247, v247, v248\n"
"v_lshlrev_b32 v247, 4, v247\n"
"v_lshl_add_u32 v247, v241, 7, v247\n"
"v_add_u32 v21, s49, v247\n"
"v_add_u32 v247, 4, v242\n"
"v_xor_b32 v247, v247, v248\n"
"v_lshlrev_b32 v247, 4, v247\n"
"v_lshl_add_u32 v247, v241, 7, v247\n"
"v_add_u32 v22, s49, v247\n"
"s_mov_b32 s46, 524288\n"
"s_add_u32 m0, s40, 98304\n"
"s_nop 0\n"
"buffer_load_dwordx4 v8, %3, s46 offen offset:0 lds\n"
"buffer_load_dwordx4 v9, %3, s46 offen offset:1024 lds\n"
"buffer_load_dwordx4 v10, %3, s46 offen offset:2048 lds\n"
"buffer_load_dwordx4 v11, %3, s46 offen offset:3072 lds\n"
"s_lshl_b32 s46, %6, 3\n"
"s_lshl_b32 s47, %6, 12\n"
"v_lshlrev_b32 v249, 9, v245\n"
"v_add_u32 v249, s47, v249\n"
"v_add_u32 v247, s46, v245\n"
"v_add_u32 v247, 0, v247\n"
"v_and_b32 v247, 15, v247\n"
"v_xor_b32 v247, v247, v246\n"
"v_lshl_add_u32 v12, v247, 4, v249\n"
"v_add_u32 v247, s46, v245\n"
"v_add_u32 v247, 2, v247\n"
"v_and_b32 v247, 15, v247\n"
"v_xor_b32 v247, v247, v246\n"
"v_lshl_add_u32 v13, v247, 4, v249\n"
"v_add_u32 v247, s46, v245\n"
"v_add_u32 v247, 4, v247\n"
"v_and_b32 v247, 15, v247\n"
"v_xor_b32 v247, v247, v246\n"
"v_lshl_add_u32 v14, v247, 4, v249\n"
"v_add_u32 v247, s46, v245\n"
"v_add_u32 v247, 6, v247\n"
"v_and_b32 v247, 15, v247\n"
"v_xor_b32 v247, v247, v246\n"
"v_lshl_add_u32 v15, v247, 4, v249\n"
"v_add_u32 v247, 0, v245\n"
"v_and_b32 v247, 7, v247\n"
"v_lshlrev_b32 v247, 1, v247\n"
"v_xor_b32 v247, v247, v246\n"
"v_lshl_add_u32 v16, v247, 4, v249\n"
"v_add_u32 v247, 2, v245\n"
"v_and_b32 v247, 7, v247\n"
"v_lshlrev_b32 v247, 1, v247\n"
"v_xor_b32 v247, v247, v246\n"
"v_lshl_add_u32 v17, v247, 4, v249\n"
"v_add_u32 v247, 4, v245\n"
"v_and_b32 v247, 7, v247\n"
"v_lshlrev_b32 v247, 1, v247\n"
"v_xor_b32 v247, v247, v246\n"
"v_lshl_add_u32 v18, v247, 4, v249\n"
"v_add_u32 v247, 6, v245\n"
"v_and_b32 v247, 7, v247\n"
"v_lshlrev_b32 v247, 1, v247\n"
"v_xor_b32 v247, v247, v246\n"
"v_lshl_add_u32 v19, v247, 4, v249\n"
"s_mov_b32 s46, 786432\n"
"s_add_u32 m0, s40, 114688\n"
"s_nop 0\n"
"buffer_load_dwordx4 v8, %3, s46 offen offset:0 lds\n"
"buffer_load_dwordx4 v9, %3, s46 offen offset:1024 lds\n"
"buffer_load_dwordx4 v10, %3, s46 offen offset:2048 lds\n"
"buffer_load_dwordx4 v11, %3, s46 offen offset:3072 lds\n"
"s_getpc_b64 s[88:89]\n"
"LPCA3_%=:\n"
"s_sub_u32 s88, s88, LPCA3_%=-LC0_%=\n"
"s_subb_u32 s89, s89, 0\n"
"s_and_b32 s89, s89, 0xffff\n"
"s_mov_b32 s90, LEND_%=-LC0_%=+2560\n"
"s_mov_b32 s91, 0x20000\n"
"v_lshlrev_b32 v27, 6, v240\n"
"v_add_u32 v27, %11, v27\n"
"v_lshlrev_b32 v29, 7, v240\n"
"v_add_u32 v29, %12, v29\n"
"s_waitcnt vmcnt(22)\n"
"ds_read_b128 a[0:3], v21 offset:0\n"
"ds_read_b128 a[4:7], v21 offset:2048\n"
"ds_read_b128 a[8:11], v21 offset:4096\n"
"ds_read_b128 a[12:15], v22 offset:0\n"
"ds_read_b128 a[16:19], v22 offset:2048\n"
"ds_read_b128 a[20:23], v22 offset:4096\n"
"s_waitcnt vmcnt(18) lgkmcnt(0)\n"
"s_barrier\n"
"s_waitcnt lgkmcnt(0)\n"
"ds_read_b64_tr_b16 v[32:33], v0 offset:0\n"
"ds_read_b64_tr_b16 v[34:35], v0 offset:8192\n"
"ds_read_b64_tr_b16 v[36:37], v0 offset:8\n"
"ds_read_b64_tr_b16 v[38:39], v0 offset:8200\n"
"ds_read_b64_tr_b16 v[40:41], v1 offset:0\n"
"ds_read_b64_tr_b16 v[42:43], v1 offset:8192\n"
"ds_read_b64_tr_b16 v[44:45], v1 offset:8\n"
"ds_read_b64_tr_b16 v[46:47], v1 offset:8200\n"
"s_waitcnt lgkmcnt(6)\n"
"v_mfma_f32_16x16x32_bf16 v[64:67], v[32:35], a[0:3], 0\n"
"ds_read_b64_tr_b16 v[48:49], v2 offset:0\n"
"ds_read_b64_tr_b16 v[50:51], v2 offset:8192\n"
"v_mfma_f32_16x16x32_bf16 v[68:71], v[32:35], a[4:7], 0\n"
"s_mov_b32 s46, 256\n"
"s_add_u32 m0, s49, 0\n"
"v_mfma_f32_16x16x32_bf16 v[72:75], v[32:35], a[8:11], 0\n"
"buffer_load_dwordx4 v23, %2, s46 offen offset:0 lds\n"
"ds_read_b64_tr_b16 v[52:53], v2 offset:8\n"
"s_waitcnt lgkmcnt(7)\n"
"v_mfma_f32_16x16x32_bf16 v[76:79], v[36:39], a[0:3], 0\n"
"ds_read_b64_tr_b16 v[54:55], v2 offset:8200\n"
"v_mfma_f32_16x16x32_bf16 v[80:83], v[36:39], a[4:7], 0\n"
"buffer_load_dwordx4 v24, %2, s46 offen offset:1024 lds\n"
"ds_read_b64_tr_b16 v[56:57], v3 offset:0\n"
"v_mfma_f32_16x16x32_bf16 v[84:87], v[36:39], a[8:11], 0\n"
"ds_read_b64_tr_b16 v[58:59], v3 offset:8192\n"
"s_waitcnt lgkmcnt(5)\n"
"v_mfma_f32_16x16x32_bf16 v[88:91], v[40:43], a[0:3], 0\n"
"v_mfma_f32_16x16x32_bf16 v[92:95], v[40:43], a[4:7], 0\n"
"buffer_load_dwordx4 v25, %2, s46 offen offset:2048 lds\n"
"ds_read_b64_tr_b16 v[60:61], v3 offset:8\n"
"v_mfma_f32_16x16x32_bf16 v[96:99], v[40:43], a[8:11], 0\n"
"ds_read_b64_tr_b16 v[62:63], v3 offset:8200\n"
"v_mfma_f32_16x16x32_bf16 v[100:103], v[44:47], a[0:3], 0\n"
"v_mfma_f32_16x16x32_bf16 v[104:107], v[44:47], a[4:7], 0\n"
"buffer_load_dwordx4 v26, %2, s46 offen offset:3072 lds\n"
"ds_read_b64_tr_b16 v[32:33], v0 offset:256\n"
"v_mfma_f32_16x16x32_bf16 v[108:111], v[44:47], a[8:11], 0\n"
"ds_read_b64_tr_b16 v[34:35], v0 offset:8448\n"
"v_accvgpr_write_b32 a168, 0\n"
"s_waitcnt lgkmcnt(5)\n"
"v_mfma_f32_16x16x32_bf16 v[112:115], v[48:51], a[0:3], 0\n"
"s_mov_b32 s46, 28928\n"
"s_add_u32 m0, s49, 4096\n"
"v_mfma_f32_16x16x32_bf16 v[116:119], v[48:51], a[4:7], 0\n"
"buffer_load_dwordx4 v23, %2, s46 offen offset:0 lds\n"
"ds_read_b64_tr_b16 v[36:37], v0 offset:264\n"
"v_mfma_f32_16x16x32_bf16 v[120:123], v[48:51], a[8:11], 0\n"
"ds_read_b64_tr_b16 v[38:39], v0 offset:8456\n"
"v_accvgpr_write_b32 a169, 0\n"
"v_mfma_f32_16x16x32_bf16 v[124:127], v[52:55], a[0:3], 0\n"
"v_accvgpr_write_b32 a170, 0\n"
"v_mfma_f32_16x16x32_bf16 v[224:227], v[52:55], a[4:7], 0\n"
"buffer_load_dwordx4 v24, %2, s46 offen offset:1024 lds\n"
"ds_read_b64_tr_b16 v[40:41], v1 offset:256\n"
"v_mfma_f32_16x16x32_bf16 v[228:231], v[52:55], a[8:11], 0\n"
"ds_read_b64_tr_b16 v[42:43], v1 offset:8448\n"
"s_waitcnt lgkmcnt(4)\n"
"v_mfma_f32_16x16x32_bf16 v[232:235], v[56:59], a[0:3], 0\n"
"s_mov_b32 s46, 1048576\n"
"s_add_u32 m0, s40, 131072\n"
"v_mfma_f32_16x16x32_bf16 v[236:239], v[56:59], a[4:7], 0\n"
"buffer_load_dwordx4 v8, %3, s46 offen offset:0 lds\n"
"ds_read_b64_tr_b16 v[44:45], v1 offset:264\n"
"v_mfma_f32_16x16x32_bf16 v[240:243], v[56:59], a[8:11], 0\n"
"ds_read_b64_tr_b16 v[46:47], v1 offset:8456\n"
"v_mfma_f32_16x16x32_bf16 v[244:247], v[60:63], a[0:3], 0\n"
"v_mfma_f32_16x16x32_bf16 v[248:251], v[60:63], a[4:7], 0\n"
"buffer_load_dwordx4 v9, %3, s46 offen offset:1024 lds\n"
"ds_read_b64_tr_b16 v[48:49], v2 offset:256\n"
"v_mfma_f32_16x16x32_bf16 v[252:255], v[60:63], a[8:11], 0\n"
"ds_read_b64_tr_b16 v[50:51], v2 offset:8448\n"
"v_mfma_f32_16x16x32_bf16 v[128:131], v[32:35], a[0:3], 0\n"
"v_mfma_f32_16x16x32_bf16 v[132:135], v[32:35], a[4:7], 0\n"
"buffer_load_dwordx4 v10, %3, s46 offen offset:2048 lds\n"
"ds_read_b64_tr_b16 v[52:53], v2 offset:264\n"
"v_mfma_f32_16x16x32_bf16 v[136:139], v[32:35], a[8:11], 0\n"
"ds_read_b64_tr_b16 v[54:55], v2 offset:8456\n"
"s_waitcnt lgkmcnt(6)\n"
"v_mfma_f32_16x16x32_bf16 v[140:143], v[36:39], a[0:3], 0\n"
"v_mfma_f32_16x16x32_bf16 v[144:147], v[36:39], a[4:7], 0\n"
"ds_read_b64_tr_b16 v[56:57], v3 offset:256\n"
"ds_read_b64_tr_b16 v[58:59], v3 offset:8448\n"
"v_mfma_f32_16x16x32_bf16 v[148:151], v[36:39], a[8:11], 0\n"
"ds_read_b64_tr_b16 v[60:61], v3 offset:264\n"
"ds_read_b64_tr_b16 v[62:63], v3 offset:8456\n"
"v_mfma_f32_16x16x32_bf16 v[152:155], v[40:43], a[0:3], 0\n"
"buffer_load_dwordx4 v11, %3, s46 offen offset:3072 lds\n"
"s_mov_b32 s46, 1310720\n"
"v_mfma_f32_16x16x32_bf16 v[156:159], v[40:43], a[4:7], 0\n"
"s_add_u32 m0, s40, 147456\n"
"v_mfma_f32_16x16x32_bf16 v[160:163], v[40:43], a[8:11], 0\n"
"buffer_load_dwordx4 v8, %3, s46 offen offset:0 lds\n"
"s_waitcnt lgkmcnt(4)\n"
"v_mfma_f32_16x16x32_bf16 v[164:167], v[44:47], a[0:3], 0\n"
"v_mfma_f32_16x16x32_bf16 v[168:171], v[44:47], a[4:7], 0\n"
"buffer_load_dwordx4 v9, %3, s46 offen offset:1024 lds\n"
"v_mfma_f32_16x16x32_bf16 v[172:175], v[44:47], a[8:11], 0\n"
"v_mfma_f32_16x16x32_bf16 v[176:179], v[48:51], a[0:3], 0\n"
"v_mfma_f32_16x16x32_bf16 v[180:183], v[48:51], a[4:7], 0\n"
"buffer_load_dwordx4 v10, %3, s46 offen offset:2048 lds\n"
"v_mfma_f32_16x16x32_bf16 v[184:187], v[48:51], a[8:11], 0\n"
"s_waitcnt vmcnt(21) lgkmcnt(0)\n"
"s_barrier\n"
"v_mfma_f32_16x16x32_bf16 v[188:191], v[52:55], a[0:3], 0\n"
"ds_read_b128 a[24:27], v21 offset:6144\n"
"ds_read_b128 a[28:31], v21 offset:8192\n"
"v_mfma_f32_16x16x32_bf16 v[192:195], v[52:55], a[4:7], 0\n"
"buffer_load_dwordx4 v11, %3, s46 offen offset:3072 lds\n"
"ds_read_b128 a[32:35], v21 offset:10240\n"
"v_mfma_f32_16x16x32_bf16 v[196:199], v[52:55], a[8:11], 0\n"
"ds_read_b128 a[36:39], v22 offset:6144\n"
"ds_read_b128 a[40:43], v22 offset:8192\n"
"v_mfma_f32_16x16x32_bf16 v[200:203], v[56:59], a[0:3], 0\n"
"ds_read_b128 a[44:47], v22 offset:10240\n"
"ds_read_b64_tr_b16 v[32:33], v0 offset:16384\n"
"v_mfma_f32_16x16x32_bf16 v[204:207], v[56:59], a[4:7], 0\n"
"ds_read_b64_tr_b16 v[34:35], v0 offset:24576\n"
"ds_read_b64_tr_b16 v[36:37], v0 offset:16392\n"
"v_mfma_f32_16x16x32_bf16 v[208:211], v[56:59], a[8:11], 0\n"
"ds_read_b64_tr_b16 v[38:39], v0 offset:24584\n"
"ds_read_b64_tr_b16 v[40:41], v1 offset:16384\n"
"v_mfma_f32_16x16x32_bf16 v[212:215], v[60:63], a[0:3], 0\n"
"ds_read_b64_tr_b16 v[42:43], v1 offset:24576\n"
"ds_read_b64_tr_b16 v[44:45], v1 offset:16392\n"
"v_mfma_f32_16x16x32_bf16 v[216:219], v[60:63], a[4:7], 0\n"
"ds_read_b64_tr_b16 v[46:47], v1 offset:24584\n"
"v_mfma_f32_16x16x32_bf16 v[220:223], v[60:63], a[8:11], 0\n"
"s_waitcnt lgkmcnt(6)\n"
"v_mfma_f32_16x16x32_bf16 v[64:67], v[32:35], a[12:15], v[64:67]\n"
"ds_read_b64_tr_b16 v[48:49], v2 offset:16384\n"
"ds_read_b64_tr_b16 v[50:51], v2 offset:24576\n"
"v_mfma_f32_16x16x32_bf16 v[68:71], v[32:35], a[16:19], v[68:71]\n"
"s_mov_b32 s46, 1572864\n"
"s_add_u32 m0, s40, 65536\n"
"v_mfma_f32_16x16x32_bf16 v[72:75], v[32:35], a[20:23], v[72:75]\n"
"buffer_load_dwordx4 v8, %3, s46 offen offset:0 lds\n"
"ds_read_b64_tr_b16 v[52:53], v2 offset:16392\n"
"s_waitcnt lgkmcnt(6)\n"
"v_mfma_f32_16x16x32_bf16 v[76:79], v[36:39], a[12:15], v[76:79]\n"
"ds_read_b64_tr_b16 v[54:55], v2 offset:24584\n"
"v_mfma_f32_16x16x32_bf16 v[80:83], v[36:39], a[16:19], v[80:83]\n"
"ds_read_b64_tr_b16 v[56:57], v3 offset:16384\n"
"ds_read_b64_tr_b16 v[58:59], v3 offset:24576\n"
"v_mfma_f32_16x16x32_bf16 v[84:87], v[36:39], a[20:23], v[84:87]\n"
"s_waitcnt lgkmcnt(6)\n"
"v_mfma_f32_16x16x32_bf16 v[88:91], v[40:43], a[12:15], v[88:91]\n"
"v_mfma_f32_16x16x32_bf16 v[92:95], v[40:43], a[16:19], v[92:95]\n"
"ds_read_b64_tr_b16 v[60:61], v3 offset:16392\n"
"ds_read_b64_tr_b16 v[62:63], v3 offset:24584\n"
"v_mfma_f32_16x16x32_bf16 v[96:99], v[40:43], a[20:23], v[96:99]\n"
"v_mfma_f32_16x16x32_bf16 v[100:103], v[44:47], a[12:15], v[100:103]\n"
"v_mfma_f32_16x16x32_bf16 v[104:107], v[44:47], a[16:19], v[104:107]\n"
"ds_read_b64_tr_b16 v[32:33], v0 offset:16640\n"
"ds_read_b64_tr_b16 v[34:35], v0 offset:24832\n"
"v_mfma_f32_16x16x32_bf16 v[108:111], v[44:47], a[20:23], v[108:111]\n"
"buffer_load_dwordx4 v9, %3, s46 offen offset:1024 lds\n"
"v_accvgpr_write_b32 a0, 0\n"
"s_waitcnt lgkmcnt(5)\n"
"v_mfma_f32_16x16x32_bf16 v[112:115], v[48:51], a[12:15], v[112:115]\n"
"v_accvgpr_write_b32 a1, 0\n"
"v_accvgpr_write_b32 a2, 0\n"
"v_mfma_f32_16x16x32_bf16 v[116:119], v[48:51], a[16:19], v[116:119]\n"
"ds_read_b64_tr_b16 v[36:37], v0 offset:16648\n"
"ds_read_b64_tr_b16 v[38:39], v0 offset:24840\n"
"v_mfma_f32_16x16x32_bf16 v[120:123], v[48:51], a[20:23], v[120:123]\n"
"v_accvgpr_write_b32 a3, 0\n"
"v_accvgpr_write_b32 a4, 0\n"
"v_mfma_f32_16x16x32_bf16 v[124:127], v[52:55], a[12:15], v[124:127]\n"
"v_accvgpr_write_b32 a5, 0\n"
"v_accvgpr_write_b32 a6, 0\n"
"v_mfma_f32_16x16x32_bf16 v[224:227], v[52:55], a[16:19], v[224:227]\n"
"ds_read_b64_tr_b16 v[40:41], v1 offset:16640\n"
"ds_read_b64_tr_b16 v[42:43], v1 offset:24832\n"
"v_mfma_f32_16x16x32_bf16 v[228:231], v[52:55], a[20:23], v[228:231]\n"
"v_accvgpr_write_b32 a7, 0\n"
"v_accvgpr_write_b32 a8, 0\n"
"s_waitcnt lgkmcnt(4)\n"
"v_mfma_f32_16x16x32_bf16 v[232:235], v[56:59], a[12:15], v[232:235]\n"
"v_accvgpr_write_b32 a9, 0\n"
"v_accvgpr_write_b32 a10, 0\n"
"v_mfma_f32_16x16x32_bf16 v[236:239], v[56:59], a[16:19], v[236:239]\n"
"ds_read_b64_tr_b16 v[44:45], v1 offset:16648\n"
"ds_read_b64_tr_b16 v[46:47], v1 offset:24840\n"
"v_mfma_f32_16x16x32_bf16 v[240:243], v[56:59], a[20:23], v[240:243]\n"
"v_accvgpr_write_b32 a11, 0\n"
"v_accvgpr_write_b32 a171, 0\n"
"v_mfma_f32_16x16x32_bf16 v[244:247], v[60:63], a[12:15], v[244:247]\n"
"v_accvgpr_write_b32 a172, 0\n"
"v_accvgpr_write_b32 a173, 0\n"
"v_mfma_f32_16x16x32_bf16 v[248:251], v[60:63], a[16:19], v[248:251]\n"
"buffer_load_dwordx4 v10, %3, s46 offen offset:2048 lds\n"
"ds_read_b64_tr_b16 v[48:49], v2 offset:16640\n"
"v_mfma_f32_16x16x32_bf16 v[252:255], v[60:63], a[20:23], v[252:255]\n"
"ds_read_b64_tr_b16 v[50:51], v2 offset:24832\n"
"v_mfma_f32_16x16x32_bf16 v[128:131], v[32:35], a[12:15], v[128:131]\n"
"v_mfma_f32_16x16x32_bf16 v[132:135], v[32:35], a[16:19], v[132:135]\n"
"ds_read_b64_tr_b16 v[52:53], v2 offset:16648\n"
"ds_read_b64_tr_b16 v[54:55], v2 offset:24840\n"
"v_mfma_f32_16x16x32_bf16 v[136:139], v[32:35], a[20:23], v[136:139]\n"
"s_waitcnt lgkmcnt(4)\n"
"v_mfma_f32_16x16x32_bf16 v[140:143], v[36:39], a[12:15], v[140:143]\n"
"v_mfma_f32_16x16x32_bf16 v[144:147], v[36:39], a[16:19], v[144:147]\n"
"ds_read_b64_tr_b16 v[56:57], v3 offset:16640\n"
"ds_read_b64_tr_b16 v[58:59], v3 offset:24832\n"
"v_mfma_f32_16x16x32_bf16 v[148:151], v[36:39], a[20:23], v[148:151]\n"
"ds_read_b64_tr_b16 v[60:61], v3 offset:16648\n"
"ds_read_b64_tr_b16 v[62:63], v3 offset:24840\n"
"v_mfma_f32_16x16x32_bf16 v[152:155], v[40:43], a[12:15], v[152:155]\n"
"v_mfma_f32_16x16x32_bf16 v[156:159], v[40:43], a[16:19], v[156:159]\n"
"v_mfma_f32_16x16x32_bf16 v[160:163], v[40:43], a[20:23], v[160:163]\n"
"v_mfma_f32_16x16x32_bf16 v[164:167], v[44:47], a[12:15], v[164:167]\n"
"buffer_load_dwordx4 v11, %3, s46 offen offset:3072 lds\n"
"v_mfma_f32_16x16x32_bf16 v[168:171], v[44:47], a[16:19], v[168:171]\n"
"v_mfma_f32_16x16x32_bf16 v[172:175], v[44:47], a[20:23], v[172:175]\n"
"s_waitcnt lgkmcnt(4)\n"
"v_mfma_f32_16x16x32_bf16 v[176:179], v[48:51], a[12:15], v[176:179]\n"
"v_mfma_f32_16x16x32_bf16 v[180:183], v[48:51], a[16:19], v[180:183]\n"
"v_mfma_f32_16x16x32_bf16 v[184:187], v[48:51], a[20:23], v[184:187]\n"
"s_waitcnt vmcnt(22) lgkmcnt(0)\n"
"s_barrier\n"
"v_mfma_f32_16x16x32_bf16 v[188:191], v[52:55], a[12:15], v[188:191]\n"
"ds_read_b64_tr_b16 v[32:33], v0 offset:32768\n"
"ds_read_b64_tr_b16 v[34:35], v0 offset:40960\n"
"v_mfma_f32_16x16x32_bf16 v[192:195], v[52:55], a[16:19], v[192:195]\n"
"ds_read_b64_tr_b16 v[36:37], v0 offset:32776\n"
"ds_read_b64_tr_b16 v[38:39], v0 offset:40968\n"
"v_mfma_f32_16x16x32_bf16 v[196:199], v[52:55], a[20:23], v[196:199]\n"
"ds_read_b64_tr_b16 v[40:41], v1 offset:32768\n"
"ds_read_b64_tr_b16 v[42:43], v1 offset:40960\n"
"v_mfma_f32_16x16x32_bf16 v[200:203], v[56:59], a[12:15], v[200:203]\n"
"ds_read_b64_tr_b16 v[44:45], v1 offset:32776\n"
"ds_read_b64_tr_b16 v[46:47], v1 offset:40968\n"
"v_mfma_f32_16x16x32_bf16 v[204:207], v[56:59], a[16:19], v[204:207]\n"
"v_mfma_f32_16x16x32_bf16 v[208:211], v[56:59], a[20:23], v[208:211]\n"
"v_mfma_f32_16x16x32_bf16 v[212:215], v[60:63], a[12:15], v[212:215]\n"
"v_mfma_f32_16x16x32_bf16 v[216:219], v[60:63], a[16:19], v[216:219]\n"
"v_mfma_f32_16x16x32_bf16 v[220:223], v[60:63], a[20:23], v[220:223]\n"
"s_waitcnt lgkmcnt(6)\n"
"v_mfma_f32_16x16x32_bf16 v[64:67], v[32:35], a[24:27], v[64:67]\n"
"ds_read_b64_tr_b16 v[48:49], v2 offset:32768\n"
"ds_read_b64_tr_b16 v[50:51], v2 offset:40960\n"
"v_mfma_f32_16x16x32_bf16 v[68:71], v[32:35], a[28:31], v[68:71]\n"
"s_mov_b32 s46, 384\n"
"s_add_u32 m0, s49, 6144\n"
"v_mfma_f32_16x16x32_bf16 v[72:75], v[32:35], a[32:35], v[72:75]\n"
"buffer_load_dwordx4 v23, %2, s46 offen offset:0 lds\n"
"ds_read_b64_tr_b16 v[52:53], v2 offset:32776\n"
"s_waitcnt lgkmcnt(3)\n"
"v_mfma_f32_16x16x32_bf16 v[76:79], v[36:39], a[24:27], v[76:79]\n"
"ds_read_b64_tr_b16 v[54:55], v2 offset:40968\n"
"v_mfma_f32_16x16x32_bf16 v[80:83], v[36:39], a[28:31], v[80:83]\n"
"ds_read_b64_tr_b16 v[56:57], v3 offset:32768\n"
"ds_read_b64_tr_b16 v[58:59], v3 offset:40960\n"
"v_mfma_f32_16x16x32_bf16 v[84:87], v[36:39], a[32:35], v[84:87]\n"
"buffer_load_dwordx4 v24, %2, s46 offen offset:1024 lds\n"
"v_mfma_f32_16x16x32_bf16 v[88:91], v[40:43], a[24:27], v[88:91]\n"
"v_mfma_f32_16x16x32_bf16 v[92:95], v[40:43], a[28:31], v[92:95]\n"
"ds_read_b64_tr_b16 v[60:61], v3 offset:32776\n"
"ds_read_b64_tr_b16 v[62:63], v3 offset:40968\n"
"v_mfma_f32_16x16x32_bf16 v[96:99], v[40:43], a[32:35], v[96:99]\n"
"v_mfma_f32_16x16x32_bf16 v[100:103], v[44:47], a[24:27], v[100:103]\n"
"buffer_load_dwordx4 v25, %2, s46 offen offset:2048 lds\n"
"v_mfma_f32_16x16x32_bf16 v[104:107], v[44:47], a[28:31], v[104:107]\n"
"ds_read_b64_tr_b16 v[32:33], v0 offset:33024\n"
"ds_read_b64_tr_b16 v[34:35], v0 offset:41216\n"
"v_mfma_f32_16x16x32_bf16 v[108:111], v[44:47], a[32:35], v[108:111]\n"
"v_accvgpr_write_b32 a12, 0\n"
"v_accvgpr_write_b32 a13, 0\n"
"s_waitcnt lgkmcnt(4)\n"
"v_mfma_f32_16x16x32_bf16 v[112:115], v[48:51], a[24:27], v[112:115]\n"
"v_accvgpr_write_b32 a14, 0\n"
"v_accvgpr_write_b32 a15, 0\n"
"v_mfma_f32_16x16x32_bf16 v[116:119], v[48:51], a[28:31], v[116:119]\n"
"buffer_load_dwordx4 v26, %2, s46 offen offset:3072 lds\n"
"ds_read_b64_tr_b16 v[36:37], v0 offset:33032\n"
"v_mfma_f32_16x16x32_bf16 v[120:123], v[48:51], a[32:35], v[120:123]\n"
"ds_read_b64_tr_b16 v[38:39], v0 offset:41224\n"
"v_accvgpr_write_b32 a16, 0\n"
"v_mfma_f32_16x16x32_bf16 v[124:127], v[52:55], a[24:27], v[124:127]\n"
"v_accvgpr_write_b32 a17, 0\n"
"v_accvgpr_write_b32 a18, 0\n"
"v_mfma_f32_16x16x32_bf16 v[224:227], v[52:55], a[28:31], v[224:227]\n"
"ds_read_b64_tr_b16 v[40:41], v1 offset:33024\n"
"ds_read_b64_tr_b16 v[42:43], v1 offset:41216\n"
"v_mfma_f32_16x16x32_bf16 v[228:231], v[52:55], a[32:35], v[228:231]\n"
"s_mov_b32 s46, 29056\n"
"s_add_u32 m0, s49, 10240\n"
"v_mfma_f32_16x16x32_bf16 v[232:235], v[56:59], a[24:27], v[232:235]\n"
"buffer_load_dwordx4 v23, %2, s46 offen offset:0 lds\n"
"v_accvgpr_write_b32 a19, 0\n"
"v_mfma_f32_16x16x32_bf16 v[236:239], v[56:59], a[28:31], v[236:239]\n"
"ds_read_b64_tr_b16 v[44:45], v1 offset:33032\n"
"ds_read_b64_tr_b16 v[46:47], v1 offset:41224\n"
"v_mfma_f32_16x16x32_bf16 v[240:243], v[56:59], a[32:35], v[240:243]\n"
"v_accvgpr_write_b32 a20, 0\n"
"v_accvgpr_write_b32 a21, 0\n"
"s_waitcnt lgkmcnt(4)\n"
"v_mfma_f32_16x16x32_bf16 v[244:247], v[60:63], a[24:27], v[244:247]\n"
"v_accvgpr_write_b32 a22, 0\n"
"v_accvgpr_write_b32 a23, 0\n"
"v_mfma_f32_16x16x32_bf16 v[248:251], v[60:63], a[28:31], v[248:251]\n"
"buffer_load_dwordx4 v24, %2, s46 offen offset:1024 lds\n"
"ds_read_b64_tr_b16 v[48:49], v2 offset:33024\n"
"v_mfma_f32_16x16x32_bf16 v[252:255], v[60:63], a[32:35], v[252:255]\n"
"ds_read_b64_tr_b16 v[50:51], v2 offset:41216\n"
"v_accvgpr_write_b32 a174, 0\n"
"v_mfma_f32_16x16x32_bf16 v[128:131], v[32:35], a[24:27], v[128:131]\n"
"v_accvgpr_write_b32 a175, 0\n"
"v_accvgpr_write_b32 a176, 0\n"
"v_mfma_f32_16x16x32_bf16 v[132:135], v[32:35], a[28:31], v[132:135]\n"
"ds_read_b64_tr_b16 v[52:53], v2 offset:33032\n"
"ds_read_b64_tr_b16 v[54:55], v2 offset:41224\n"
"v_mfma_f32_16x16x32_bf16 v[136:139], v[32:35], a[32:35], v[136:139]\n"
"s_mov_b32 s46, 1835008\n"
"s_add_u32 m0, s40, 81920\n"
"v_mfma_f32_16x16x32_bf16 v[140:143], v[36:39], a[24:27], v[140:143]\n"
"buffer_load_dwordx4 v8, %3, s46 offen offset:0 lds\n"
"v_mfma_f32_16x16x32_bf16 v[144:147], v[36:39], a[28:31], v[144:147]\n"
"ds_read_b64_tr_b16 v[56:57], v3 offset:33024\n"
"ds_read_b64_tr_b16 v[58:59], v3 offset:41216\n"
"v_mfma_f32_16x16x32_bf16 v[148:151], v[36:39], a[32:35], v[148:151]\n"
"ds_read_b64_tr_b16 v[60:61], v3 offset:33032\n"
"ds_read_b64_tr_b16 v[62:63], v3 offset:41224\n"
"s_waitcnt lgkmcnt(6)\n"
"v_mfma_f32_16x16x32_bf16 v[152:155], v[40:43], a[24:27], v[152:155]\n"
"v_mfma_f32_16x16x32_bf16 v[156:159], v[40:43], a[28:31], v[156:159]\n"
"buffer_load_dwordx4 v9, %3, s46 offen offset:1024 lds\n"
"v_mfma_f32_16x16x32_bf16 v[160:163], v[40:43], a[32:35], v[160:163]\n"
"v_mfma_f32_16x16x32_bf16 v[164:167], v[44:47], a[24:27], v[164:167]\n"
"v_mfma_f32_16x16x32_bf16 v[168:171], v[44:47], a[28:31], v[168:171]\n"
"v_mfma_f32_16x16x32_bf16 v[172:175], v[44:47], a[32:35], v[172:175]\n"
"buffer_load_dwordx4 v10, %3, s46 offen offset:2048 lds\n"
"v_mfma_f32_16x16x32_bf16 v[176:179], v[48:51], a[24:27], v[176:179]\n"
"v_mfma_f32_16x16x32_bf16 v[180:183], v[48:51], a[28:31], v[180:183]\n"
"v_mfma_f32_16x16x32_bf16 v[184:187], v[48:51], a[32:35], v[184:187]\n"
"s_waitcnt vmcnt(21) lgkmcnt(0)\n"
"s_barrier\n"
"v_mfma_f32_16x16x32_bf16 v[188:191], v[52:55], a[24:27], v[188:191]\n"
"buffer_load_dwordx4 v11, %3, s46 offen offset:3072 lds\n"
"ds_read_b128 a[48:51], v21 offset:0\n"
"v_mfma_f32_16x16x32_bf16 v[192:195], v[52:55], a[28:31], v[192:195]\n"
"ds_read_b128 a[52:55], v21 offset:2048\n"
"ds_read_b128 a[56:59], v21 offset:4096\n"
"v_mfma_f32_16x16x32_bf16 v[196:199], v[52:55], a[32:35], v[196:199]\n"
"ds_read_b128 a[60:63], v22 offset:0\n"
"ds_read_b128 a[64:67], v22 offset:2048\n"
"v_mfma_f32_16x16x32_bf16 v[200:203], v[56:59], a[24:27], v[200:203]\n"
"ds_read_b128 a[68:71], v22 offset:4096\n"
"ds_read_b64_tr_b16 v[32:33], v0 offset:49152\n"
"v_mfma_f32_16x16x32_bf16 v[204:207], v[56:59], a[28:31], v[204:207]\n"
"ds_read_b64_tr_b16 v[34:35], v0 offset:57344\n"
"ds_read_b64_tr_b16 v[36:37], v0 offset:49160\n"
"v_mfma_f32_16x16x32_bf16 v[208:211], v[56:59], a[32:35], v[208:211]\n"
"ds_read_b64_tr_b16 v[38:39], v0 offset:57352\n"
"ds_read_b64_tr_b16 v[40:41], v1 offset:49152\n"
"v_mfma_f32_16x16x32_bf16 v[212:215], v[60:63], a[24:27], v[212:215]\n"
"ds_read_b64_tr_b16 v[42:43], v1 offset:57344\n"
"ds_read_b64_tr_b16 v[44:45], v1 offset:49160\n"
"v_mfma_f32_16x16x32_bf16 v[216:219], v[60:63], a[28:31], v[216:219]\n"
"ds_read_b64_tr_b16 v[46:47], v1 offset:57352\n"
"v_mfma_f32_16x16x32_bf16 v[220:223], v[60:63], a[32:35], v[220:223]\n"
"s_waitcnt lgkmcnt(6)\n"
"v_mfma_f32_16x16x32_bf16 v[64:67], v[32:35], a[36:39], v[64:67]\n"
"ds_read_b64_tr_b16 v[48:49], v2 offset:49152\n"
"ds_read_b64_tr_b16 v[50:51], v2 offset:57344\n"
"v_mfma_f32_16x16x32_bf16 v[68:71], v[32:35], a[40:43], v[68:71]\n"
"s_mov_b32 s46, 2097152\n"
"s_add_u32 m0, s40, 98304\n"
"v_mfma_f32_16x16x32_bf16 v[72:75], v[32:35], a[44:47], v[72:75]\n"
"buffer_load_dwordx4 v8, %3, s46 offen offset:0 lds\n"
"ds_read_b64_tr_b16 v[52:53], v2 offset:49160\n"
"s_waitcnt lgkmcnt(6)\n"
"v_mfma_f32_16x16x32_bf16 v[76:79], v[36:39], a[36:39], v[76:79]\n"
"ds_read_b64_tr_b16 v[54:55], v2 offset:57352\n"
"v_mfma_f32_16x16x32_bf16 v[80:83], v[36:39], a[40:43], v[80:83]\n"
"ds_read_b64_tr_b16 v[56:57], v3 offset:49152\n"
"ds_read_b64_tr_b16 v[58:59], v3 offset:57344\n"
"v_mfma_f32_16x16x32_bf16 v[84:87], v[36:39], a[44:47], v[84:87]\n"
"s_waitcnt lgkmcnt(6)\n"
"v_mfma_f32_16x16x32_bf16 v[88:91], v[40:43], a[36:39], v[88:91]\n"
"v_mfma_f32_16x16x32_bf16 v[92:95], v[40:43], a[40:43], v[92:95]\n"
"ds_read_b64_tr_b16 v[60:61], v3 offset:49160\n"
"ds_read_b64_tr_b16 v[62:63], v3 offset:57352\n"
"v_mfma_f32_16x16x32_bf16 v[96:99], v[40:43], a[44:47], v[96:99]\n"
"v_mfma_f32_16x16x32_bf16 v[100:103], v[44:47], a[36:39], v[100:103]\n"
"v_mfma_f32_16x16x32_bf16 v[104:107], v[44:47], a[40:43], v[104:107]\n"
"ds_read_b64_tr_b16 v[32:33], v0 offset:49408\n"
"ds_read_b64_tr_b16 v[34:35], v0 offset:57600\n"
"v_mfma_f32_16x16x32_bf16 v[108:111], v[44:47], a[44:47], v[108:111]\n"
"buffer_load_dwordx4 v9, %3, s46 offen offset:1024 lds\n"
"v_accvgpr_write_b32 a24, 0\n"
"s_waitcnt lgkmcnt(5)\n"
"v_mfma_f32_16x16x32_bf16 v[112:115], v[48:51], a[36:39], v[112:115]\n"
"v_accvgpr_write_b32 a25, 0\n"
"v_accvgpr_write_b32 a26, 0\n"
"v_mfma_f32_16x16x32_bf16 v[116:119], v[48:51], a[40:43], v[116:119]\n"
"ds_read_b64_tr_b16 v[36:37], v0 offset:49416\n"
"ds_read_b64_tr_b16 v[38:39], v0 offset:57608\n"
"v_mfma_f32_16x16x32_bf16 v[120:123], v[48:51], a[44:47], v[120:123]\n"
"v_accvgpr_write_b32 a27, 0\n"
"v_accvgpr_write_b32 a28, 0\n"
"v_mfma_f32_16x16x32_bf16 v[124:127], v[52:55], a[36:39], v[124:127]\n"
"v_accvgpr_write_b32 a29, 0\n"
"v_accvgpr_write_b32 a30, 0\n"
"v_mfma_f32_16x16x32_bf16 v[224:227], v[52:55], a[40:43], v[224:227]\n"
"ds_read_b64_tr_b16 v[40:41], v1 offset:49408\n"
"ds_read_b64_tr_b16 v[42:43], v1 offset:57600\n"
"v_mfma_f32_16x16x32_bf16 v[228:231], v[52:55], a[44:47], v[228:231]\n"
"v_accvgpr_write_b32 a31, 0\n"
"v_accvgpr_write_b32 a32, 0\n"
"s_waitcnt lgkmcnt(4)\n"
"v_mfma_f32_16x16x32_bf16 v[232:235], v[56:59], a[36:39], v[232:235]\n"
"v_accvgpr_write_b32 a33, 0\n"
"v_accvgpr_write_b32 a34, 0\n"
"v_mfma_f32_16x16x32_bf16 v[236:239], v[56:59], a[40:43], v[236:239]\n"
"ds_read_b64_tr_b16 v[44:45], v1 offset:49416\n"
"ds_read_b64_tr_b16 v[46:47], v1 offset:57608\n"
"v_mfma_f32_16x16x32_bf16 v[240:243], v[56:59], a[44:47], v[240:243]\n"
"v_accvgpr_write_b32 a35, 0\n"
"v_accvgpr_write_b32 a177, 0\n"
"v_mfma_f32_16x16x32_bf16 v[244:247], v[60:63], a[36:39], v[244:247]\n"
"v_accvgpr_write_b32 a178, 0\n"
"v_accvgpr_write_b32 a179, 0\n"
"v_mfma_f32_16x16x32_bf16 v[248:251], v[60:63], a[40:43], v[248:251]\n"
"buffer_load_dwordx4 v10, %3, s46 offen offset:2048 lds\n"
"ds_read_b64_tr_b16 v[48:49], v2 offset:49408\n"
"v_mfma_f32_16x16x32_bf16 v[252:255], v[60:63], a[44:47], v[252:255]\n"
"ds_read_b64_tr_b16 v[50:51], v2 offset:57600\n"
"v_mfma_f32_16x16x32_bf16 v[128:131], v[32:35], a[36:39], v[128:131]\n"
"v_mfma_f32_16x16x32_bf16 v[132:135], v[32:35], a[40:43], v[132:135]\n"
"ds_read_b64_tr_b16 v[52:53], v2 offset:49416\n"
"ds_read_b64_tr_b16 v[54:55], v2 offset:57608\n"
"v_mfma_f32_16x16x32_bf16 v[136:139], v[32:35], a[44:47], v[136:139]\n"
"s_waitcnt lgkmcnt(4)\n"
"v_mfma_f32_16x16x32_bf16 v[140:143], v[36:39], a[36:39], v[140:143]\n"
"v_mfma_f32_16x16x32_bf16 v[144:147], v[36:39], a[40:43], v[144:147]\n"
"ds_read_b64_tr_b16 v[56:57], v3 offset:49408\n"
"ds_read_b64_tr_b16 v[58:59], v3 offset:57600\n"
"v_mfma_f32_16x16x32_bf16 v[148:151], v[36:39], a[44:47], v[148:151]\n"
"ds_read_b64_tr_b16 v[60:61], v3 offset:49416\n"
"ds_read_b64_tr_b16 v[62:63], v3 offset:57608\n"
"v_mfma_f32_16x16x32_bf16 v[152:155], v[40:43], a[36:39], v[152:155]\n"
"v_mfma_f32_16x16x32_bf16 v[156:159], v[40:43], a[40:43], v[156:159]\n"
"v_mfma_f32_16x16x32_bf16 v[160:163], v[40:43], a[44:47], v[160:163]\n"
"v_mfma_f32_16x16x32_bf16 v[164:167], v[44:47], a[36:39], v[164:167]\n"
"buffer_load_dwordx4 v11, %3, s46 offen offset:3072 lds\n"
"v_mfma_f32_16x16x32_bf16 v[168:171], v[44:47], a[40:43], v[168:171]\n"
"v_mfma_f32_16x16x32_bf16 v[172:175], v[44:47], a[44:47], v[172:175]\n"
"s_waitcnt lgkmcnt(4)\n"
"v_mfma_f32_16x16x32_bf16 v[176:179], v[48:51], a[36:39], v[176:179]\n"
"v_mfma_f32_16x16x32_bf16 v[180:183], v[48:51], a[40:43], v[180:183]\n"
"v_mfma_f32_16x16x32_bf16 v[184:187], v[48:51], a[44:47], v[184:187]\n"
"s_waitcnt vmcnt(22) lgkmcnt(0)\n"
"s_barrier\n"
"v_mfma_f32_16x16x32_bf16 v[188:191], v[52:55], a[36:39], v[188:191]\n"
"ds_read_b64_tr_b16 v[32:33], v4 offset:0\n"
"ds_read_b64_tr_b16 v[34:35], v4 offset:8192\n"
"v_mfma_f32_16x16x32_bf16 v[192:195], v[52:55], a[40:43], v[192:195]\n"
"ds_read_b64_tr_b16 v[36:37], v4 offset:8\n"
"ds_read_b64_tr_b16 v[38:39], v4 offset:8200\n"
"v_mfma_f32_16x16x32_bf16 v[196:199], v[52:55], a[44:47], v[196:199]\n"
"ds_read_b64_tr_b16 v[40:41], v5 offset:0\n"
"ds_read_b64_tr_b16 v[42:43], v5 offset:8192\n"
"v_mfma_f32_16x16x32_bf16 v[200:203], v[56:59], a[36:39], v[200:203]\n"
"ds_read_b64_tr_b16 v[44:45], v5 offset:8\n"
"ds_read_b64_tr_b16 v[46:47], v5 offset:8200\n"
"v_mfma_f32_16x16x32_bf16 v[204:207], v[56:59], a[40:43], v[204:207]\n"
"v_mfma_f32_16x16x32_bf16 v[208:211], v[56:59], a[44:47], v[208:211]\n"
"v_mfma_f32_16x16x32_bf16 v[212:215], v[60:63], a[36:39], v[212:215]\n"
"v_mfma_f32_16x16x32_bf16 v[216:219], v[60:63], a[40:43], v[216:219]\n"
"v_mfma_f32_16x16x32_bf16 v[220:223], v[60:63], a[44:47], v[220:223]\n"
"s_waitcnt lgkmcnt(6)\n"
"v_mfma_f32_16x16x32_bf16 v[64:67], v[32:35], a[48:51], v[64:67]\n"
"ds_read_b64_tr_b16 v[48:49], v6 offset:0\n"
"ds_read_b64_tr_b16 v[50:51], v6 offset:8192\n"
"v_mfma_f32_16x16x32_bf16 v[68:71], v[32:35], a[52:55], v[68:71]\n"
"s_mov_b32 s46, 512\n"
"s_add_u32 m0, s49, 0\n"
"v_mfma_f32_16x16x32_bf16 v[72:75], v[32:35], a[56:59], v[72:75]\n"
"buffer_load_dwordx4 v23, %2, s46 offen offset:0 lds\n"
"ds_read_b64_tr_b16 v[52:53], v6 offset:8\n"
"s_waitcnt lgkmcnt(3)\n"
"v_mfma_f32_16x16x32_bf16 v[76:79], v[36:39], a[48:51], v[76:79]\n"
"ds_read_b64_tr_b16 v[54:55], v6 offset:8200\n"
"v_mfma_f32_16x16x32_bf16 v[80:83], v[36:39], a[52:55], v[80:83]\n"
"ds_read_b64_tr_b16 v[56:57], v7 offset:0\n"
"ds_read_b64_tr_b16 v[58:59], v7 offset:8192\n"
"v_mfma_f32_16x16x32_bf16 v[84:87], v[36:39], a[56:59], v[84:87]\n"
"buffer_load_dwordx4 v24, %2, s46 offen offset:1024 lds\n"
"v_mfma_f32_16x16x32_bf16 v[88:91], v[40:43], a[48:51], v[88:91]\n"
"v_mfma_f32_16x16x32_bf16 v[92:95], v[40:43], a[52:55], v[92:95]\n"
"ds_read_b64_tr_b16 v[60:61], v7 offset:8\n"
"ds_read_b64_tr_b16 v[62:63], v7 offset:8200\n"
"v_mfma_f32_16x16x32_bf16 v[96:99], v[40:43], a[56:59], v[96:99]\n"
"v_mfma_f32_16x16x32_bf16 v[100:103], v[44:47], a[48:51], v[100:103]\n"
"buffer_load_dwordx4 v25, %2, s46 offen offset:2048 lds\n"
"v_mfma_f32_16x16x32_bf16 v[104:107], v[44:47], a[52:55], v[104:107]\n"
"ds_read_b64_tr_b16 v[32:33], v4 offset:256\n"
"ds_read_b64_tr_b16 v[34:35], v4 offset:8448\n"
"v_mfma_f32_16x16x32_bf16 v[108:111], v[44:47], a[56:59], v[108:111]\n"
"v_accvgpr_write_b32 a36, 0\n"
"v_accvgpr_write_b32 a37, 0\n"
"s_waitcnt lgkmcnt(4)\n"
"v_mfma_f32_16x16x32_bf16 v[112:115], v[48:51], a[48:51], v[112:115]\n"
"v_accvgpr_write_b32 a38, 0\n"
"v_accvgpr_write_b32 a39, 0\n"
"v_mfma_f32_16x16x32_bf16 v[116:119], v[48:51], a[52:55], v[116:119]\n"
"buffer_load_dwordx4 v26, %2, s46 offen offset:3072 lds\n"
"ds_read_b64_tr_b16 v[36:37], v4 offset:264\n"
"v_mfma_f32_16x16x32_bf16 v[120:123], v[48:51], a[56:59], v[120:123]\n"
"ds_read_b64_tr_b16 v[38:39], v4 offset:8456\n"
"v_accvgpr_write_b32 a40, 0\n"
"v_mfma_f32_16x16x32_bf16 v[124:127], v[52:55], a[48:51], v[124:127]\n"
"v_accvgpr_write_b32 a41, 0\n"
"v_accvgpr_write_b32 a42, 0\n"
"v_mfma_f32_16x16x32_bf16 v[224:227], v[52:55], a[52:55], v[224:227]\n"
"ds_read_b64_tr_b16 v[40:41], v5 offset:256\n"
"ds_read_b64_tr_b16 v[42:43], v5 offset:8448\n"
"v_mfma_f32_16x16x32_bf16 v[228:231], v[52:55], a[56:59], v[228:231]\n"
"s_mov_b32 s46, 29184\n"
"s_add_u32 m0, s49, 4096\n"
"v_mfma_f32_16x16x32_bf16 v[232:235], v[56:59], a[48:51], v[232:235]\n"
"buffer_load_dwordx4 v23, %2, s46 offen offset:0 lds\n"
"v_accvgpr_write_b32 a43, 0\n"
"v_mfma_f32_16x16x32_bf16 v[236:239], v[56:59], a[52:55], v[236:239]\n"
"ds_read_b64_tr_b16 v[44:45], v5 offset:264\n"
"ds_read_b64_tr_b16 v[46:47], v5 offset:8456\n"
"v_mfma_f32_16x16x32_bf16 v[240:243], v[56:59], a[56:59], v[240:243]\n"
"v_accvgpr_write_b32 a44, 0\n"
"v_accvgpr_write_b32 a45, 0\n"
"s_waitcnt lgkmcnt(4)\n"
"v_mfma_f32_16x16x32_bf16 v[244:247], v[60:63], a[48:51], v[244:247]\n"
"v_accvgpr_write_b32 a46, 0\n"
"v_accvgpr_write_b32 a47, 0\n"
"v_mfma_f32_16x16x32_bf16 v[248:251], v[60:63], a[52:55], v[248:251]\n"
"buffer_load_dwordx4 v24, %2, s46 offen offset:1024 lds\n"
"ds_read_b64_tr_b16 v[48:49], v6 offset:256\n"
"v_mfma_f32_16x16x32_bf16 v[252:255], v[60:63], a[56:59], v[252:255]\n"
"ds_read_b64_tr_b16 v[50:51], v6 offset:8448\n"
"v_accvgpr_write_b32 a180, 0\n"
"v_mfma_f32_16x16x32_bf16 v[128:131], v[32:35], a[48:51], v[128:131]\n"
"v_accvgpr_write_b32 a181, 0\n"
"v_accvgpr_write_b32 a182, 0\n"
"v_mfma_f32_16x16x32_bf16 v[132:135], v[32:35], a[52:55], v[132:135]\n"
"ds_read_b64_tr_b16 v[52:53], v6 offset:264\n"
"ds_read_b64_tr_b16 v[54:55], v6 offset:8456\n"
"v_mfma_f32_16x16x32_bf16 v[136:139], v[32:35], a[56:59], v[136:139]\n"
"s_mov_b32 s46, 2359296\n"
"s_add_u32 m0, s40, 114688\n"
"v_mfma_f32_16x16x32_bf16 v[140:143], v[36:39], a[48:51], v[140:143]\n"
"buffer_load_dwordx4 v8, %3, s46 offen offset:0 lds\n"
"v_mfma_f32_16x16x32_bf16 v[144:147], v[36:39], a[52:55], v[144:147]\n"
"ds_read_b64_tr_b16 v[56:57], v7 offset:256\n"
"ds_read_b64_tr_b16 v[58:59], v7 offset:8448\n"
"v_mfma_f32_16x16x32_bf16 v[148:151], v[36:39], a[56:59], v[148:151]\n"
"ds_read_b64_tr_b16 v[60:61], v7 offset:264\n"
"ds_read_b64_tr_b16 v[62:63], v7 offset:8456\n"
"s_waitcnt lgkmcnt(6)\n"
"v_mfma_f32_16x16x32_bf16 v[152:155], v[40:43], a[48:51], v[152:155]\n"
"v_mfma_f32_16x16x32_bf16 v[156:159], v[40:43], a[52:55], v[156:159]\n"
"buffer_load_dwordx4 v9, %3, s46 offen offset:1024 lds\n"
"v_mfma_f32_16x16x32_bf16 v[160:163], v[40:43], a[56:59], v[160:163]\n"
"v_mfma_f32_16x16x32_bf16 v[164:167], v[44:47], a[48:51], v[164:167]\n"
"v_mfma_f32_16x16x32_bf16 v[168:171], v[44:47], a[52:55], v[168:171]\n"
"v_mfma_f32_16x16x32_bf16 v[172:175], v[44:47], a[56:59], v[172:175]\n"
"buffer_load_dwordx4 v10, %3, s46 offen offset:2048 lds\n"
"v_mfma_f32_16x16x32_bf16 v[176:179], v[48:51], a[48:51], v[176:179]\n"
"v_mfma_f32_16x16x32_bf16 v[180:183], v[48:51], a[52:55], v[180:183]\n"
"v_mfma_f32_16x16x32_bf16 v[184:187], v[48:51], a[56:59], v[184:187]\n"
"s_waitcnt vmcnt(17) lgkmcnt(0)\n"
"s_barrier\n"
"v_mfma_f32_16x16x32_bf16 v[188:191], v[52:55], a[48:51], v[188:191]\n"
"buffer_load_dwordx4 v11, %3, s46 offen offset:3072 lds\n"
"ds_read_b128 a[72:75], v21 offset:6144\n"
"v_mfma_f32_16x16x32_bf16 v[192:195], v[52:55], a[52:55], v[192:195]\n"
"ds_read_b128 a[76:79], v21 offset:8192\n"
"ds_read_b128 a[80:83], v21 offset:10240\n"
"v_mfma_f32_16x16x32_bf16 v[196:199], v[52:55], a[56:59], v[196:199]\n"
"ds_read_b128 a[84:87], v22 offset:6144\n"
"ds_read_b128 a[88:91], v22 offset:8192\n"
"v_mfma_f32_16x16x32_bf16 v[200:203], v[56:59], a[48:51], v[200:203]\n"
"ds_read_b128 a[92:95], v22 offset:10240\n"
"ds_read_b64_tr_b16 v[32:33], v4 offset:16384\n"
"v_mfma_f32_16x16x32_bf16 v[204:207], v[56:59], a[52:55], v[204:207]\n"
"ds_read_b64_tr_b16 v[34:35], v4 offset:24576\n"
"ds_read_b64_tr_b16 v[36:37], v4 offset:16392\n"
"v_mfma_f32_16x16x32_bf16 v[208:211], v[56:59], a[56:59], v[208:211]\n"
"ds_read_b64_tr_b16 v[38:39], v4 offset:24584\n"
"ds_read_b64_tr_b16 v[40:41], v5 offset:16384\n"
"v_mfma_f32_16x16x32_bf16 v[212:215], v[60:63], a[48:51], v[212:215]\n"
"ds_read_b64_tr_b16 v[42:43], v5 offset:24576\n"
"ds_read_b64_tr_b16 v[44:45], v5 offset:16392\n"
"v_mfma_f32_16x16x32_bf16 v[216:219], v[60:63], a[52:55], v[216:219]\n"
"ds_read_b64_tr_b16 v[46:47], v5 offset:24584\n"
"v_mfma_f32_16x16x32_bf16 v[220:223], v[60:63], a[56:59], v[220:223]\n"
"s_waitcnt lgkmcnt(6)\n"
"v_mfma_f32_16x16x32_bf16 v[64:67], v[32:35], a[60:63], v[64:67]\n"
"ds_read_b64_tr_b16 v[48:49], v6 offset:16384\n"
"ds_read_b64_tr_b16 v[50:51], v6 offset:24576\n"
"v_mfma_f32_16x16x32_bf16 v[68:71], v[32:35], a[64:67], v[68:71]\n"
"s_mov_b32 s46, 2621440\n"
"s_add_u32 m0, s40, 131072\n"
"v_mfma_f32_16x16x32_bf16 v[72:75], v[32:35], a[68:71], v[72:75]\n"
"buffer_load_dwordx4 v8, %3, s46 offen offset:0 lds\n"
"ds_read_b64_tr_b16 v[52:53], v6 offset:16392\n"
"s_waitcnt lgkmcnt(6)\n"
"v_mfma_f32_16x16x32_bf16 v[76:79], v[36:39], a[60:63], v[76:79]\n"
"ds_read_b64_tr_b16 v[54:55], v6 offset:24584\n"
"v_mfma_f32_16x16x32_bf16 v[80:83], v[36:39], a[64:67], v[80:83]\n"
"ds_read_b64_tr_b16 v[56:57], v7 offset:16384\n"
"ds_read_b64_tr_b16 v[58:59], v7 offset:24576\n"
"v_mfma_f32_16x16x32_bf16 v[84:87], v[36:39], a[68:71], v[84:87]\n"
"s_waitcnt lgkmcnt(6)\n"
"v_mfma_f32_16x16x32_bf16 v[88:91], v[40:43], a[60:63], v[88:91]\n"
"v_mfma_f32_16x16x32_bf16 v[92:95], v[40:43], a[64:67], v[92:95]\n"
"ds_read_b64_tr_b16 v[60:61], v7 offset:16392\n"
"ds_read_b64_tr_b16 v[62:63], v7 offset:24584\n"
"v_mfma_f32_16x16x32_bf16 v[96:99], v[40:43], a[68:71], v[96:99]\n"
"v_mfma_f32_16x16x32_bf16 v[100:103], v[44:47], a[60:63], v[100:103]\n"
"v_mfma_f32_16x16x32_bf16 v[104:107], v[44:47], a[64:67], v[104:107]\n"
"ds_read_b64_tr_b16 v[32:33], v4 offset:16640\n"
"ds_read_b64_tr_b16 v[34:35], v4 offset:24832\n"
"v_mfma_f32_16x16x32_bf16 v[108:111], v[44:47], a[68:71], v[108:111]\n"
"buffer_load_dwordx4 v9, %3, s46 offen offset:1024 lds\n"
"v_accvgpr_write_b32 a48, 0\n"
"s_waitcnt lgkmcnt(5)\n"
"v_mfma_f32_16x16x32_bf16 v[112:115], v[48:51], a[60:63], v[112:115]\n"
"v_accvgpr_write_b32 a49, 0\n"
"v_accvgpr_write_b32 a50, 0\n"
"v_mfma_f32_16x16x32_bf16 v[116:119], v[48:51], a[64:67], v[116:119]\n"
"ds_read_b64_tr_b16 v[36:37], v4 offset:16648\n"
"ds_read_b64_tr_b16 v[38:39], v4 offset:24840\n"
"v_mfma_f32_16x16x32_bf16 v[120:123], v[48:51], a[68:71], v[120:123]\n"
"v_accvgpr_write_b32 a51, 0\n"
"v_accvgpr_write_b32 a52, 0\n"
"v_mfma_f32_16x16x32_bf16 v[124:127], v[52:55], a[60:63], v[124:127]\n"
"v_accvgpr_write_b32 a53, 0\n"
"v_accvgpr_write_b32 a54, 0\n"
"v_mfma_f32_16x16x32_bf16 v[224:227], v[52:55], a[64:67], v[224:227]\n"
"ds_read_b64_tr_b16 v[40:41], v5 offset:16640\n"
"ds_read_b64_tr_b16 v[42:43], v5 offset:24832\n"
"v_mfma_f32_16x16x32_bf16 v[228:231], v[52:55], a[68:71], v[228:231]\n"
"v_accvgpr_write_b32 a55, 0\n"
"v_accvgpr_write_b32 a56, 0\n"
"s_waitcnt lgkmcnt(4)\n"
"v_mfma_f32_16x16x32_bf16 v[232:235], v[56:59], a[60:63], v[232:235]\n"
"v_accvgpr_write_b32 a57, 0\n"
"v_accvgpr_write_b32 a58, 0\n"
"v_mfma_f32_16x16x32_bf16 v[236:239], v[56:59], a[64:67], v[236:239]\n"
"ds_read_b64_tr_b16 v[44:45], v5 offset:16648\n"
"ds_read_b64_tr_b16 v[46:47], v5 offset:24840\n"
"v_mfma_f32_16x16x32_bf16 v[240:243], v[56:59], a[68:71], v[240:243]\n"
"v_accvgpr_write_b32 a59, 0\n"
"v_accvgpr_write_b32 a183, 0\n"
"v_mfma_f32_16x16x32_bf16 v[244:247], v[60:63], a[60:63], v[244:247]\n"
"v_accvgpr_write_b32 a184, 0\n"
"v_accvgpr_write_b32 a185, 0\n"
"v_mfma_f32_16x16x32_bf16 v[248:251], v[60:63], a[64:67], v[248:251]\n"
"buffer_load_dwordx4 v10, %3, s46 offen offset:2048 lds\n"
"ds_read_b64_tr_b16 v[48:49], v6 offset:16640\n"
"v_mfma_f32_16x16x32_bf16 v[252:255], v[60:63], a[68:71], v[252:255]\n"
"ds_read_b64_tr_b16 v[50:51], v6 offset:24832\n"
"v_mfma_f32_16x16x32_bf16 v[128:131], v[32:35], a[60:63], v[128:131]\n"
"v_mfma_f32_16x16x32_bf16 v[132:135], v[32:35], a[64:67], v[132:135]\n"
"ds_read_b64_tr_b16 v[52:53], v6 offset:16648\n"
"ds_read_b64_tr_b16 v[54:55], v6 offset:24840\n"
"v_mfma_f32_16x16x32_bf16 v[136:139], v[32:35], a[68:71], v[136:139]\n"
"s_waitcnt lgkmcnt(4)\n"
"v_mfma_f32_16x16x32_bf16 v[140:143], v[36:39], a[60:63], v[140:143]\n"
"v_mfma_f32_16x16x32_bf16 v[144:147], v[36:39], a[64:67], v[144:147]\n"
"ds_read_b64_tr_b16 v[56:57], v7 offset:16640\n"
"ds_read_b64_tr_b16 v[58:59], v7 offset:24832\n"
"v_mfma_f32_16x16x32_bf16 v[148:151], v[36:39], a[68:71], v[148:151]\n"
"ds_read_b64_tr_b16 v[60:61], v7 offset:16648\n"
"ds_read_b64_tr_b16 v[62:63], v7 offset:24840\n"
"v_mfma_f32_16x16x32_bf16 v[152:155], v[40:43], a[60:63], v[152:155]\n"
"v_mfma_f32_16x16x32_bf16 v[156:159], v[40:43], a[64:67], v[156:159]\n"
"v_mfma_f32_16x16x32_bf16 v[160:163], v[40:43], a[68:71], v[160:163]\n"
"v_mfma_f32_16x16x32_bf16 v[164:167], v[44:47], a[60:63], v[164:167]\n"
"buffer_load_dwordx4 v11, %3, s46 offen offset:3072 lds\n"
"v_mfma_f32_16x16x32_bf16 v[168:171], v[44:47], a[64:67], v[168:171]\n"
"v_mfma_f32_16x16x32_bf16 v[172:175], v[44:47], a[68:71], v[172:175]\n"
"s_waitcnt lgkmcnt(4)\n"
"v_mfma_f32_16x16x32_bf16 v[176:179], v[48:51], a[60:63], v[176:179]\n"
"v_mfma_f32_16x16x32_bf16 v[180:183], v[48:51], a[64:67], v[180:183]\n"
"v_mfma_f32_16x16x32_bf16 v[184:187], v[48:51], a[68:71], v[184:187]\n"
"s_waitcnt vmcnt(28) lgkmcnt(0)\n"
"s_barrier\n"
"v_mfma_f32_16x16x32_bf16 v[188:191], v[52:55], a[60:63], v[188:191]\n"
"ds_read_b64_tr_b16 v[32:33], v0 offset:0\n"
"ds_read_b64_tr_b16 v[34:35], v0 offset:8192\n"
"v_mfma_f32_16x16x32_bf16 v[192:195], v[52:55], a[64:67], v[192:195]\n"
"ds_read_b64_tr_b16 v[36:37], v0 offset:8\n"
"ds_read_b64_tr_b16 v[38:39], v0 offset:8200\n"
"v_mfma_f32_16x16x32_bf16 v[196:199], v[52:55], a[68:71], v[196:199]\n"
"ds_read_b64_tr_b16 v[40:41], v1 offset:0\n"
"ds_read_b64_tr_b16 v[42:43], v1 offset:8192\n"
"v_mfma_f32_16x16x32_bf16 v[200:203], v[56:59], a[60:63], v[200:203]\n"
"ds_read_b64_tr_b16 v[44:45], v1 offset:8\n"
"ds_read_b64_tr_b16 v[46:47], v1 offset:8200\n"
"v_mfma_f32_16x16x32_bf16 v[204:207], v[56:59], a[64:67], v[204:207]\n"
"v_mfma_f32_16x16x32_bf16 v[208:211], v[56:59], a[68:71], v[208:211]\n"
"v_mfma_f32_16x16x32_bf16 v[212:215], v[60:63], a[60:63], v[212:215]\n"
"v_mfma_f32_16x16x32_bf16 v[216:219], v[60:63], a[64:67], v[216:219]\n"
"v_mfma_f32_16x16x32_bf16 v[220:223], v[60:63], a[68:71], v[220:223]\n"
"s_waitcnt lgkmcnt(6)\n"
"v_mfma_f32_16x16x32_bf16 v[64:67], v[32:35], a[72:75], v[64:67]\n"
"ds_read_b64_tr_b16 v[48:49], v2 offset:0\n"
"ds_read_b64_tr_b16 v[50:51], v2 offset:8192\n"
"v_mfma_f32_16x16x32_bf16 v[68:71], v[32:35], a[76:79], v[68:71]\n"
"s_mov_b32 s46, 640\n"
"s_add_u32 m0, s49, 6144\n"
"v_mfma_f32_16x16x32_bf16 v[72:75], v[32:35], a[80:83], v[72:75]\n"
"buffer_load_dwordx4 v23, %2, s46 offen offset:0 lds\n"
"ds_read_b64_tr_b16 v[52:53], v2 offset:8\n"
"s_waitcnt lgkmcnt(3)\n"
"v_mfma_f32_16x16x32_bf16 v[76:79], v[36:39], a[72:75], v[76:79]\n"
"ds_read_b64_tr_b16 v[54:55], v2 offset:8200\n"
"v_mfma_f32_16x16x32_bf16 v[80:83], v[36:39], a[76:79], v[80:83]\n"
"ds_read_b64_tr_b16 v[56:57], v3 offset:0\n"
"ds_read_b64_tr_b16 v[58:59], v3 offset:8192\n"
"v_mfma_f32_16x16x32_bf16 v[84:87], v[36:39], a[80:83], v[84:87]\n"
"buffer_load_dwordx4 v24, %2, s46 offen offset:1024 lds\n"
"v_mfma_f32_16x16x32_bf16 v[88:91], v[40:43], a[72:75], v[88:91]\n"
"v_mfma_f32_16x16x32_bf16 v[92:95], v[40:43], a[76:79], v[92:95]\n"
"ds_read_b64_tr_b16 v[60:61], v3 offset:8\n"
"ds_read_b64_tr_b16 v[62:63], v3 offset:8200\n"
"v_mfma_f32_16x16x32_bf16 v[96:99], v[40:43], a[80:83], v[96:99]\n"
"v_mfma_f32_16x16x32_bf16 v[100:103], v[44:47], a[72:75], v[100:103]\n"
"buffer_load_dwordx4 v25, %2, s46 offen offset:2048 lds\n"
"v_mfma_f32_16x16x32_bf16 v[104:107], v[44:47], a[76:79], v[104:107]\n"
"ds_read_b64_tr_b16 v[32:33], v0 offset:256\n"
"ds_read_b64_tr_b16 v[34:35], v0 offset:8448\n"
"v_mfma_f32_16x16x32_bf16 v[108:111], v[44:47], a[80:83], v[108:111]\n"
"v_accvgpr_write_b32 a60, 0\n"
"v_accvgpr_write_b32 a61, 0\n"
"s_waitcnt lgkmcnt(4)\n"
"v_mfma_f32_16x16x32_bf16 v[112:115], v[48:51], a[72:75], v[112:115]\n"
"v_accvgpr_write_b32 a62, 0\n"
"v_accvgpr_write_b32 a63, 0\n"
"v_mfma_f32_16x16x32_bf16 v[116:119], v[48:51], a[76:79], v[116:119]\n"
"buffer_load_dwordx4 v26, %2, s46 offen offset:3072 lds\n"
"ds_read_b64_tr_b16 v[36:37], v0 offset:264\n"
"v_mfma_f32_16x16x32_bf16 v[120:123], v[48:51], a[80:83], v[120:123]\n"
"ds_read_b64_tr_b16 v[38:39], v0 offset:8456\n"
"v_accvgpr_write_b32 a64, 0\n"
"v_mfma_f32_16x16x32_bf16 v[124:127], v[52:55], a[72:75], v[124:127]\n"
"v_accvgpr_write_b32 a65, 0\n"
"v_accvgpr_write_b32 a66, 0\n"
"v_mfma_f32_16x16x32_bf16 v[224:227], v[52:55], a[76:79], v[224:227]\n"
"ds_read_b64_tr_b16 v[40:41], v1 offset:256\n"
"ds_read_b64_tr_b16 v[42:43], v1 offset:8448\n"
"v_mfma_f32_16x16x32_bf16 v[228:231], v[52:55], a[80:83], v[228:231]\n"
"s_mov_b32 s46, 29312\n"
"s_add_u32 m0, s49, 10240\n"
"v_mfma_f32_16x16x32_bf16 v[232:235], v[56:59], a[72:75], v[232:235]\n"
"buffer_load_dwordx4 v23, %2, s46 offen offset:0 lds\n"
"v_accvgpr_write_b32 a67, 0\n"
"v_mfma_f32_16x16x32_bf16 v[236:239], v[56:59], a[76:79], v[236:239]\n"
"ds_read_b64_tr_b16 v[44:45], v1 offset:264\n"
"ds_read_b64_tr_b16 v[46:47], v1 offset:8456\n"
"v_mfma_f32_16x16x32_bf16 v[240:243], v[56:59], a[80:83], v[240:243]\n"
"v_accvgpr_write_b32 a68, 0\n"
"v_accvgpr_write_b32 a69, 0\n"
"s_waitcnt lgkmcnt(4)\n"
"v_mfma_f32_16x16x32_bf16 v[244:247], v[60:63], a[72:75], v[244:247]\n"
"v_accvgpr_write_b32 a70, 0\n"
"v_accvgpr_write_b32 a71, 0\n"
"v_mfma_f32_16x16x32_bf16 v[248:251], v[60:63], a[76:79], v[248:251]\n"
"buffer_load_dwordx4 v24, %2, s46 offen offset:1024 lds\n"
"ds_read_b64_tr_b16 v[48:49], v2 offset:256\n"
"v_mfma_f32_16x16x32_bf16 v[252:255], v[60:63], a[80:83], v[252:255]\n"
"ds_read_b64_tr_b16 v[50:51], v2 offset:8448\n"
"v_accvgpr_write_b32 a186, 0\n"
"v_mfma_f32_16x16x32_bf16 v[128:131], v[32:35], a[72:75], v[128:131]\n"
"v_accvgpr_write_b32 a187, 0\n"
"v_accvgpr_write_b32 a188, 0\n"
"v_mfma_f32_16x16x32_bf16 v[132:135], v[32:35], a[76:79], v[132:135]\n"
"ds_read_b64_tr_b16 v[52:53], v2 offset:264\n"
"ds_read_b64_tr_b16 v[54:55], v2 offset:8456\n"
"v_mfma_f32_16x16x32_bf16 v[136:139], v[32:35], a[80:83], v[136:139]\n"
"s_mov_b32 s46, 2883584\n"
"s_add_u32 m0, s40, 147456\n"
"v_mfma_f32_16x16x32_bf16 v[140:143], v[36:39], a[72:75], v[140:143]\n"
"buffer_load_dwordx4 v8, %3, s46 offen offset:0 lds\n"
"v_mfma_f32_16x16x32_bf16 v[144:147], v[36:39], a[76:79], v[144:147]\n"
"ds_read_b64_tr_b16 v[56:57], v3 offset:256\n"
"ds_read_b64_tr_b16 v[58:59], v3 offset:8448\n"
"v_mfma_f32_16x16x32_bf16 v[148:151], v[36:39], a[80:83], v[148:151]\n"
"ds_read_b64_tr_b16 v[60:61], v3 offset:264\n"
"ds_read_b64_tr_b16 v[62:63], v3 offset:8456\n"
"s_waitcnt lgkmcnt(6)\n"
"v_mfma_f32_16x16x32_bf16 v[152:155], v[40:43], a[72:75], v[152:155]\n"
"v_mfma_f32_16x16x32_bf16 v[156:159], v[40:43], a[76:79], v[156:159]\n"
"buffer_load_dwordx4 v9, %3, s46 offen offset:1024 lds\n"
"v_mfma_f32_16x16x32_bf16 v[160:163], v[40:43], a[80:83], v[160:163]\n"
"v_mfma_f32_16x16x32_bf16 v[164:167], v[44:47], a[72:75], v[164:167]\n"
"v_mfma_f32_16x16x32_bf16 v[168:171], v[44:47], a[76:79], v[168:171]\n"
"v_mfma_f32_16x16x32_bf16 v[172:175], v[44:47], a[80:83], v[172:175]\n"
"buffer_load_dwordx4 v10, %3, s46 offen offset:2048 lds\n"
"v_mfma_f32_16x16x32_bf16 v[176:179], v[48:51], a[72:75], v[176:179]\n"
"v_mfma_f32_16x16x32_bf16 v[180:183], v[48:51], a[76:79], v[180:183]\n"
"v_mfma_f32_16x16x32_bf16 v[184:187], v[48:51], a[80:83], v[184:187]\n"
"s_waitcnt vmcnt(17) lgkmcnt(0)\n"
"s_barrier\n"
"v_mfma_f32_16x16x32_bf16 v[188:191], v[52:55], a[72:75], v[188:191]\n"
"buffer_load_dwordx4 v11, %3, s46 offen offset:3072 lds\n"
"ds_read_b128 a[96:99], v21 offset:0\n"
"v_mfma_f32_16x16x32_bf16 v[192:195], v[52:55], a[76:79], v[192:195]\n"
"ds_read_b128 a[100:103], v21 offset:2048\n"
"ds_read_b128 a[104:107], v21 offset:4096\n"
"v_mfma_f32_16x16x32_bf16 v[196:199], v[52:55], a[80:83], v[196:199]\n"
"ds_read_b128 a[108:111], v22 offset:0\n"
"ds_read_b128 a[112:115], v22 offset:2048\n"
"v_mfma_f32_16x16x32_bf16 v[200:203], v[56:59], a[72:75], v[200:203]\n"
"ds_read_b128 a[116:119], v22 offset:4096\n"
"ds_read_b64_tr_b16 v[32:33], v0 offset:16384\n"
"v_mfma_f32_16x16x32_bf16 v[204:207], v[56:59], a[76:79], v[204:207]\n"
"ds_read_b64_tr_b16 v[34:35], v0 offset:24576\n"
"ds_read_b64_tr_b16 v[36:37], v0 offset:16392\n"
"v_mfma_f32_16x16x32_bf16 v[208:211], v[56:59], a[80:83], v[208:211]\n"
"ds_read_b64_tr_b16 v[38:39], v0 offset:24584\n"
"ds_read_b64_tr_b16 v[40:41], v1 offset:16384\n"
"v_mfma_f32_16x16x32_bf16 v[212:215], v[60:63], a[72:75], v[212:215]\n"
"ds_read_b64_tr_b16 v[42:43], v1 offset:24576\n"
"ds_read_b64_tr_b16 v[44:45], v1 offset:16392\n"
"v_mfma_f32_16x16x32_bf16 v[216:219], v[60:63], a[76:79], v[216:219]\n"
"ds_read_b64_tr_b16 v[46:47], v1 offset:24584\n"
"v_mfma_f32_16x16x32_bf16 v[220:223], v[60:63], a[80:83], v[220:223]\n"
"s_waitcnt lgkmcnt(6)\n"
"v_mfma_f32_16x16x32_bf16 v[64:67], v[32:35], a[84:87], v[64:67]\n"
"ds_read_b64_tr_b16 v[48:49], v2 offset:16384\n"
"ds_read_b64_tr_b16 v[50:51], v2 offset:24576\n"
"v_mfma_f32_16x16x32_bf16 v[68:71], v[32:35], a[88:91], v[68:71]\n"
"s_mov_b32 s46, 3145728\n"
"s_add_u32 m0, s40, 65536\n"
"v_mfma_f32_16x16x32_bf16 v[72:75], v[32:35], a[92:95], v[72:75]\n"
"buffer_load_dwordx4 v8, %3, s46 offen offset:0 lds\n"
"ds_read_b64_tr_b16 v[52:53], v2 offset:16392\n"
"s_waitcnt lgkmcnt(6)\n"
"v_mfma_f32_16x16x32_bf16 v[76:79], v[36:39], a[84:87], v[76:79]\n"
"ds_read_b64_tr_b16 v[54:55], v2 offset:24584\n"
"v_mfma_f32_16x16x32_bf16 v[80:83], v[36:39], a[88:91], v[80:83]\n"
"ds_read_b64_tr_b16 v[56:57], v3 offset:16384\n"
"ds_read_b64_tr_b16 v[58:59], v3 offset:24576\n"
"v_mfma_f32_16x16x32_bf16 v[84:87], v[36:39], a[92:95], v[84:87]\n"
"s_waitcnt lgkmcnt(6)\n"
"v_mfma_f32_16x16x32_bf16 v[88:91], v[40:43], a[84:87], v[88:91]\n"
"buffer_load_dwordx4 v9, %3, s46 offen offset:1024 lds\n"
"v_mfma_f32_16x16x32_bf16 v[92:95], v[40:43], a[88:91], v[92:95]\n"
"ds_read_b64_tr_b16 v[60:61], v3 offset:16392\n"
"ds_read_b64_tr_b16 v[62:63], v3 offset:24584\n"
"v_mfma_f32_16x16x32_bf16 v[96:99], v[40:43], a[92:95], v[96:99]\n"
"v_mfma_f32_16x16x32_bf16 v[100:103], v[44:47], a[84:87], v[100:103]\n"
"v_mfma_f32_16x16x32_bf16 v[104:107], v[44:47], a[88:91], v[104:107]\n"
"ds_read_b64_tr_b16 v[32:33], v0 offset:16640\n"
"ds_read_b64_tr_b16 v[34:35], v0 offset:24832\n"
"v_mfma_f32_16x16x32_bf16 v[108:111], v[44:47], a[92:95], v[108:111]\n"
"buffer_load_dwordx4 v10, %3, s46 offen offset:2048 lds\n"
"v_accvgpr_write_b32 a72, 0\n"
"s_waitcnt lgkmcnt(4)\n"
"v_mfma_f32_16x16x32_bf16 v[112:115], v[48:51], a[84:87], v[112:115]\n"
"v_accvgpr_write_b32 a73, 0\n"
"v_accvgpr_write_b32 a74, 0\n"
"v_mfma_f32_16x16x32_bf16 v[116:119], v[48:51], a[88:91], v[116:119]\n"
"ds_read_b64_tr_b16 v[36:37], v0 offset:16648\n"
"ds_read_b64_tr_b16 v[38:39], v0 offset:24840\n"
"v_mfma_f32_16x16x32_bf16 v[120:123], v[48:51], a[92:95], v[120:123]\n"
"v_accvgpr_write_b32 a75, 0\n"
"v_accvgpr_write_b32 a76, 0\n"
"v_mfma_f32_16x16x32_bf16 v[124:127], v[52:55], a[84:87], v[124:127]\n"
"v_accvgpr_write_b32 a77, 0\n"
"v_accvgpr_write_b32 a78, 0\n"
"v_mfma_f32_16x16x32_bf16 v[224:227], v[52:55], a[88:91], v[224:227]\n"
"ds_read_b64_tr_b16 v[40:41], v1 offset:16640\n"
"ds_read_b64_tr_b16 v[42:43], v1 offset:24832\n"
"v_mfma_f32_16x16x32_bf16 v[228:231], v[52:55], a[92:95], v[228:231]\n"
"buffer_load_dwordx4 v11, %3, s46 offen offset:3072 lds\n"
"v_accvgpr_write_b32 a79, 0\n"
"v_mfma_f32_16x16x32_bf16 v[232:235], v[56:59], a[84:87], v[232:235]\n"
"v_accvgpr_write_b32 a80, 0\n"
"v_accvgpr_write_b32 a81, 0\n"
"v_mfma_f32_16x16x32_bf16 v[236:239], v[56:59], a[88:91], v[236:239]\n"
"ds_read_b64_tr_b16 v[44:45], v1 offset:16648\n"
"ds_read_b64_tr_b16 v[46:47], v1 offset:24840\n"
"v_mfma_f32_16x16x32_bf16 v[240:243], v[56:59], a[92:95], v[240:243]\n"
"v_accvgpr_write_b32 a82, 0\n"
"v_accvgpr_write_b32 a83, 0\n"
"s_waitcnt lgkmcnt(4)\n"
"v_mfma_f32_16x16x32_bf16 v[244:247], v[60:63], a[84:87], v[244:247]\n"
"s_add_u32 s47, %9, 0\n"
"s_and_b32 s47, s47, 0xfffff\n"
"v_mfma_f32_16x16x32_bf16 v[248:251], v[60:63], a[88:91], v[248:251]\n"
"s_add_u32 m0, s40, 0\n"
"ds_read_b64_tr_b16 v[48:49], v2 offset:16640\n"
"v_mfma_f32_16x16x32_bf16 v[252:255], v[60:63], a[92:95], v[252:255]\n"
"buffer_load_dwordx4 v12, %0, s47 offen offset:0 lds\n"
"ds_read_b64_tr_b16 v[50:51], v2 offset:24832\n"
"v_mfma_f32_16x16x32_bf16 v[128:131], v[32:35], a[84:87], v[128:131]\n"
"v_accvgpr_write_b32 a189, 0\n"
"v_accvgpr_write_b32 a190, 0\n"
"v_mfma_f32_16x16x32_bf16 v[132:135], v[32:35], a[88:91], v[132:135]\n"
"ds_read_b64_tr_b16 v[52:53], v2 offset:16648\n"
"ds_read_b64_tr_b16 v[54:55], v2 offset:24840\n"
"v_mfma_f32_16x16x32_bf16 v[136:139], v[32:35], a[92:95], v[136:139]\n"
"v_accvgpr_write_b32 a191, 0\n"
"v_mfma_f32_16x16x32_bf16 v[140:143], v[36:39], a[84:87], v[140:143]\n"
"buffer_load_dwordx4 v13, %0, s47 offen offset:1024 lds\n"
"v_mfma_f32_16x16x32_bf16 v[144:147], v[36:39], a[88:91], v[144:147]\n"
"ds_read_b64_tr_b16 v[56:57], v3 offset:16640\n"
"ds_read_b64_tr_b16 v[58:59], v3 offset:24832\n"
"v_mfma_f32_16x16x32_bf16 v[148:151], v[36:39], a[92:95], v[148:151]\n"
"ds_read_b64_tr_b16 v[60:61], v3 offset:16648\n"
"ds_read_b64_tr_b16 v[62:63], v3 offset:24840\n"
"s_waitcnt lgkmcnt(6)\n"
"v_mfma_f32_16x16x32_bf16 v[152:155], v[40:43], a[84:87], v[152:155]\n"
"v_mfma_f32_16x16x32_bf16 v[156:159], v[40:43], a[88:91], v[156:159]\n"
"v_mfma_f32_16x16x32_bf16 v[160:163], v[40:43], a[92:95], v[160:163]\n"
"v_mfma_f32_16x16x32_bf16 v[164:167], v[44:47], a[84:87], v[164:167]\n"
"buffer_load_dwordx4 v14, %0, s47 offen offset:2048 lds\n"
"v_mfma_f32_16x16x32_bf16 v[168:171], v[44:47], a[88:91], v[168:171]\n"
"v_mfma_f32_16x16x32_bf16 v[172:175], v[44:47], a[92:95], v[172:175]\n"
"v_mfma_f32_16x16x32_bf16 v[176:179], v[48:51], a[84:87], v[176:179]\n"
"v_mfma_f32_16x16x32_bf16 v[180:183], v[48:51], a[88:91], v[180:183]\n"
"v_mfma_f32_16x16x32_bf16 v[184:187], v[48:51], a[92:95], v[184:187]\n"
"s_waitcnt vmcnt(31) lgkmcnt(0)\n"
"s_barrier\n"
"v_mfma_f32_16x16x32_bf16 v[188:191], v[52:55], a[84:87], v[188:191]\n"
"buffer_load_dwordx4 v15, %0, s47 offen offset:3072 lds\n"
"ds_read_b64_tr_b16 v[32:33], v0 offset:32768\n"
"v_mfma_f32_16x16x32_bf16 v[192:195], v[52:55], a[88:91], v[192:195]\n"
"ds_read_b64_tr_b16 v[34:35], v0 offset:40960\n"
"ds_read_b64_tr_b16 v[36:37], v0 offset:32776\n"
"v_mfma_f32_16x16x32_bf16 v[196:199], v[52:55], a[92:95], v[196:199]\n"
"ds_read_b64_tr_b16 v[38:39], v0 offset:40968\n"
"ds_read_b64_tr_b16 v[40:41], v1 offset:32768\n"
"v_mfma_f32_16x16x32_bf16 v[200:203], v[56:59], a[84:87], v[200:203]\n"
"ds_read_b64_tr_b16 v[42:43], v1 offset:40960\n"
"ds_read_b64_tr_b16 v[44:45], v1 offset:32776\n"
"v_mfma_f32_16x16x32_bf16 v[204:207], v[56:59], a[88:91], v[204:207]\n"
"ds_read_b64_tr_b16 v[46:47], v1 offset:40968\n"
"v_mfma_f32_16x16x32_bf16 v[208:211], v[56:59], a[92:95], v[208:211]\n"
"v_mfma_f32_16x16x32_bf16 v[212:215], v[60:63], a[84:87], v[212:215]\n"
"v_mfma_f32_16x16x32_bf16 v[216:219], v[60:63], a[88:91], v[216:219]\n"
"v_mfma_f32_16x16x32_bf16 v[220:223], v[60:63], a[92:95], v[220:223]\n"
"s_waitcnt lgkmcnt(6)\n"
"v_mfma_f32_16x16x32_bf16 v[64:67], v[32:35], a[96:99], v[64:67]\n"
"ds_read_b64_tr_b16 v[48:49], v2 offset:32768\n"
"ds_read_b64_tr_b16 v[50:51], v2 offset:40960\n"
"v_mfma_f32_16x16x32_bf16 v[68:71], v[32:35], a[100:103], v[68:71]\n"
"s_mov_b32 s46, 768\n"
"s_add_u32 m0, s49, 0\n"
"v_mfma_f32_16x16x32_bf16 v[72:75], v[32:35], a[104:107], v[72:75]\n"
"buffer_load_dwordx4 v23, %2, s46 offen offset:0 lds\n"
"ds_read_b64_tr_b16 v[52:53], v2 offset:32776\n"
"s_waitcnt lgkmcnt(4)\n"
"v_mfma_f32_16x16x32_bf16 v[76:79], v[36:39], a[96:99], v[76:79]\n"
"ds_read_b64_tr_b16 v[54:55], v2 offset:40968\n"
"v_mfma_f32_16x16x32_bf16 v[80:83], v[36:39], a[100:103], v[80:83]\n"
"buffer_load_dwordx4 v24, %2, s46 offen offset:1024 lds\n"
"ds_read_b64_tr_b16 v[56:57], v3 offset:32768\n"
"v_mfma_f32_16x16x32_bf16 v[84:87], v[36:39], a[104:107], v[84:87]\n"
"ds_read_b64_tr_b16 v[58:59], v3 offset:40960\n"
"v_mfma_f32_16x16x32_bf16 v[88:91], v[40:43], a[96:99], v[88:91]\n"
"v_mfma_f32_16x16x32_bf16 v[92:95], v[40:43], a[100:103], v[92:95]\n"
"ds_read_b64_tr_b16 v[60:61], v3 offset:32776\n"
"ds_read_b64_tr_b16 v[62:63], v3 offset:40968\n"
"v_mfma_f32_16x16x32_bf16 v[96:99], v[40:43], a[104:107], v[96:99]\n"
"buffer_load_dwordx4 v25, %2, s46 offen offset:2048 lds\n"
"s_waitcnt lgkmcnt(6)\n"
"v_mfma_f32_16x16x32_bf16 v[100:103], v[44:47], a[96:99], v[100:103]\n"
"v_mfma_f32_16x16x32_bf16 v[104:107], v[44:47], a[100:103], v[104:107]\n"
"ds_read_b64_tr_b16 v[32:33], v0 offset:33024\n"
"ds_read_b64_tr_b16 v[34:35], v0 offset:41216\n"
"v_mfma_f32_16x16x32_bf16 v[108:111], v[44:47], a[104:107], v[108:111]\n"
"buffer_load_dwordx4 v26, %2, s46 offen offset:3072 lds\n"
"v_accvgpr_write_b32 a84, 0\n"
"v_mfma_f32_16x16x32_bf16 v[112:115], v[48:51], a[96:99], v[112:115]\n"
"v_accvgpr_write_b32 a85, 0\n"
"v_accvgpr_write_b32 a86, 0\n"
"v_mfma_f32_16x16x32_bf16 v[116:119], v[48:51], a[100:103], v[116:119]\n"
"ds_read_b64_tr_b16 v[36:37], v0 offset:33032\n"
"ds_read_b64_tr_b16 v[38:39], v0 offset:41224\n"
"v_mfma_f32_16x16x32_bf16 v[120:123], v[48:51], a[104:107], v[120:123]\n"
"s_mov_b32 s46, 29440\n"
"s_add_u32 m0, s49, 4096\n"
"s_waitcnt lgkmcnt(4)\n"
"v_mfma_f32_16x16x32_bf16 v[124:127], v[52:55], a[96:99], v[124:127]\n"
"buffer_load_dwordx4 v23, %2, s46 offen offset:0 lds\n"
"v_accvgpr_write_b32 a87, 0\n"
"v_mfma_f32_16x16x32_bf16 v[224:227], v[52:55], a[100:103], v[224:227]\n"
"ds_read_b64_tr_b16 v[40:41], v1 offset:33024\n"
"ds_read_b64_tr_b16 v[42:43], v1 offset:41216\n"
"v_mfma_f32_16x16x32_bf16 v[228:231], v[52:55], a[104:107], v[228:231]\n"
"v_accvgpr_write_b32 a88, 0\n"
"v_accvgpr_write_b32 a89, 0\n"
"v_mfma_f32_16x16x32_bf16 v[232:235], v[56:59], a[96:99], v[232:235]\n"
"buffer_load_dwordx4 v24, %2, s46 offen offset:1024 lds\n"
"v_accvgpr_write_b32 a90, 0\n"
"v_mfma_f32_16x16x32_bf16 v[236:239], v[56:59], a[100:103], v[236:239]\n"
"ds_read_b64_tr_b16 v[44:45], v1 offset:33032\n"
"ds_read_b64_tr_b16 v[46:47], v1 offset:41224\n"
"v_mfma_f32_16x16x32_bf16 v[240:243], v[56:59], a[104:107], v[240:243]\n"
"v_accvgpr_write_b32 a91, 0\n"
"v_accvgpr_write_b32 a92, 0\n"
"v_mfma_f32_16x16x32_bf16 v[244:247], v[60:63], a[96:99], v[244:247]\n"
"s_mov_b32 s46, 3407872\n"
"s_add_u32 m0, s40, 81920\n"
"v_mfma_f32_16x16x32_bf16 v[248:251], v[60:63], a[100:103], v[248:251]\n"
"buffer_load_dwordx4 v8, %3, s46 offen offset:0 lds\n"
"ds_read_b64_tr_b16 v[48:49], v2 offset:33024\n"
"v_mfma_f32_16x16x32_bf16 v[252:255], v[60:63], a[104:107], v[252:255]\n"
"ds_read_b64_tr_b16 v[50:51], v2 offset:41216\n"
"v_accvgpr_write_b32 a93, 0\n"
"s_waitcnt lgkmcnt(4)\n"
"v_mfma_f32_16x16x32_bf16 v[128:131], v[32:35], a[96:99], v[128:131]\n"
"v_accvgpr_write_b32 a94, 0\n"
"v_accvgpr_write_b32 a95, 0\n"
"v_mfma_f32_16x16x32_bf16 v[132:135], v[32:35], a[100:103], v[132:135]\n"
"ds_read_b64_tr_b16 v[52:53], v2 offset:33032\n"
"ds_read_b64_tr_b16 v[54:55], v2 offset:41224\n"
"v_mfma_f32_16x16x32_bf16 v[136:139], v[32:35], a[104:107], v[136:139]\n"
"buffer_load_dwordx4 v9, %3, s46 offen offset:1024 lds\n"
"v_accvgpr_write_b32 a192, 0\n"
"v_mfma_f32_16x16x32_bf16 v[140:143], v[36:39], a[96:99], v[140:143]\n"
"v_accvgpr_write_b32 a193, 0\n"
"v_accvgpr_write_b32 a194, 0\n"
"v_mfma_f32_16x16x32_bf16 v[144:147], v[36:39], a[100:103], v[144:147]\n"
"ds_read_b64_tr_b16 v[56:57], v3 offset:33024\n"
"ds_read_b64_tr_b16 v[58:59], v3 offset:41216\n"
"v_mfma_f32_16x16x32_bf16 v[148:151], v[36:39], a[104:107], v[148:151]\n"
"ds_read_b64_tr_b16 v[60:61], v3 offset:33032\n"
"ds_read_b64_tr_b16 v[62:63], v3 offset:41224\n"
"v_mfma_f32_16x16x32_bf16 v[152:155], v[40:43], a[96:99], v[152:155]\n"
"buffer_load_dwordx4 v10, %3, s46 offen offset:2048 lds\n"
"v_mfma_f32_16x16x32_bf16 v[156:159], v[40:43], a[100:103], v[156:159]\n"
"v_mfma_f32_16x16x32_bf16 v[160:163], v[40:43], a[104:107], v[160:163]\n"
"s_waitcnt lgkmcnt(4)\n"
"v_mfma_f32_16x16x32_bf16 v[164:167], v[44:47], a[96:99], v[164:167]\n"
"buffer_load_dwordx4 v11, %3, s46 offen offset:3072 lds\n"
"v_mfma_f32_16x16x32_bf16 v[168:171], v[44:47], a[100:103], v[168:171]\n"
"v_mfma_f32_16x16x32_bf16 v[172:175], v[44:47], a[104:107], v[172:175]\n"
"v_mfma_f32_16x16x32_bf16 v[176:179], v[48:51], a[96:99], v[176:179]\n"
"buffer_load_dword a200, v27, s[88:91], 0 offen offset:0\n"
"v_mfma_f32_16x16x32_bf16 v[180:183], v[48:51], a[100:103], v[180:183]\n"
"v_mfma_f32_16x16x32_bf16 v[184:187], v[48:51], a[104:107], v[184:187]\n"
"s_waitcnt vmcnt(23) lgkmcnt(0)\n"
"s_barrier\n"
"v_mfma_f32_16x16x32_bf16 v[188:191], v[52:55], a[96:99], v[188:191]\n"
"ds_read_b128 a[120:123], v21 offset:6144\n"
"ds_read_b128 a[124:127], v21 offset:8192\n"
"v_mfma_f32_16x16x32_bf16 v[192:195], v[52:55], a[100:103], v[192:195]\n"
"buffer_load_dword a200, v29, %4, 0 offen offset:0\n"
"ds_read_b128 a[128:131], v21 offset:10240\n"
"v_mfma_f32_16x16x32_bf16 v[196:199], v[52:55], a[104:107], v[196:199]\n"
"ds_read_b128 a[132:135], v22 offset:6144\n"
"ds_read_b128 a[136:139], v22 offset:8192\n"
"v_mfma_f32_16x16x32_bf16 v[200:203], v[56:59], a[96:99], v[200:203]\n"
"ds_read_b128 a[140:143], v22 offset:10240\n"
"ds_read_b64_tr_b16 v[32:33], v0 offset:49152\n"
"v_mfma_f32_16x16x32_bf16 v[204:207], v[56:59], a[100:103], v[204:207]\n"
"ds_read_b64_tr_b16 v[34:35], v0 offset:57344\n"
"ds_read_b64_tr_b16 v[36:37], v0 offset:49160\n"
"v_mfma_f32_16x16x32_bf16 v[208:211], v[56:59], a[104:107], v[208:211]\n"
"ds_read_b64_tr_b16 v[38:39], v0 offset:57352\n"
"ds_read_b64_tr_b16 v[40:41], v1 offset:49152\n"
"v_mfma_f32_16x16x32_bf16 v[212:215], v[60:63], a[96:99], v[212:215]\n"
"ds_read_b64_tr_b16 v[42:43], v1 offset:57344\n"
"ds_read_b64_tr_b16 v[44:45], v1 offset:49160\n"
"v_mfma_f32_16x16x32_bf16 v[216:219], v[60:63], a[100:103], v[216:219]\n"
"ds_read_b64_tr_b16 v[46:47], v1 offset:57352\n"
"v_mfma_f32_16x16x32_bf16 v[220:223], v[60:63], a[104:107], v[220:223]\n"
"s_waitcnt lgkmcnt(6)\n"
"v_mfma_f32_16x16x32_bf16 v[64:67], v[32:35], a[108:111], v[64:67]\n"
"ds_read_b64_tr_b16 v[48:49], v2 offset:49152\n"
"ds_read_b64_tr_b16 v[50:51], v2 offset:57344\n"
"v_mfma_f32_16x16x32_bf16 v[68:71], v[32:35], a[112:115], v[68:71]\n"
"s_add_u32 s47, %9, 0\n"
"s_and_b32 s47, s47, 0xfffff\n"
"v_mfma_f32_16x16x32_bf16 v[72:75], v[32:35], a[116:119], v[72:75]\n"
"s_add_u32 m0, s40, 98304\n"
"ds_read_b64_tr_b16 v[52:53], v2 offset:49160\n"
"s_waitcnt lgkmcnt(6)\n"
"v_mfma_f32_16x16x32_bf16 v[76:79], v[36:39], a[108:111], v[76:79]\n"
"buffer_load_dwordx4 v16, %1, s47 offen offset:0 lds\n"
"ds_read_b64_tr_b16 v[54:55], v2 offset:57352\n"
"v_mfma_f32_16x16x32_bf16 v[80:83], v[36:39], a[112:115], v[80:83]\n"
"ds_read_b64_tr_b16 v[56:57], v3 offset:49152\n"
"ds_read_b64_tr_b16 v[58:59], v3 offset:57344\n"
"v_mfma_f32_16x16x32_bf16 v[84:87], v[36:39], a[116:119], v[84:87]\n"
"s_waitcnt lgkmcnt(5)\n"
"v_mfma_f32_16x16x32_bf16 v[88:91], v[40:43], a[108:111], v[88:91]\n"
"v_mfma_f32_16x16x32_bf16 v[92:95], v[40:43], a[112:115], v[92:95]\n"
"ds_read_b64_tr_b16 v[60:61], v3 offset:49160\n"
"ds_read_b64_tr_b16 v[62:63], v3 offset:57352\n"
"v_mfma_f32_16x16x32_bf16 v[96:99], v[40:43], a[116:119], v[96:99]\n"
"v_mfma_f32_16x16x32_bf16 v[100:103], v[44:47], a[108:111], v[100:103]\n"
"v_mfma_f32_16x16x32_bf16 v[104:107], v[44:47], a[112:115], v[104:107]\n"
"ds_read_b64_tr_b16 v[32:33], v0 offset:49408\n"
"ds_read_b64_tr_b16 v[34:35], v0 offset:57600\n"
"v_mfma_f32_16x16x32_bf16 v[108:111], v[44:47], a[116:119], v[108:111]\n"
"buffer_load_dwordx4 v17, %1, s47 offen offset:1024 lds\n"
"v_accvgpr_write_b32 a96, 0\n"
"s_waitcnt lgkmcnt(5)\n"
"v_mfma_f32_16x16x32_bf16 v[112:115], v[48:51], a[108:111], v[112:115]\n"
"v_accvgpr_write_b32 a97, 0\n"
"v_accvgpr_write_b32 a98, 0\n"
"v_mfma_f32_16x16x32_bf16 v[116:119], v[48:51], a[112:115], v[116:119]\n"
"ds_read_b64_tr_b16 v[36:37], v0 offset:49416\n"
"ds_read_b64_tr_b16 v[38:39], v0 offset:57608\n"
"v_mfma_f32_16x16x32_bf16 v[120:123], v[48:51], a[116:119], v[120:123]\n"
"v_accvgpr_write_b32 a99, 0\n"
"v_accvgpr_write_b32 a100, 0\n"
"v_mfma_f32_16x16x32_bf16 v[124:127], v[52:55], a[108:111], v[124:127]\n"
"v_accvgpr_write_b32 a101, 0\n"
"v_accvgpr_write_b32 a102, 0\n"
"v_mfma_f32_16x16x32_bf16 v[224:227], v[52:55], a[112:115], v[224:227]\n"
"ds_read_b64_tr_b16 v[40:41], v1 offset:49408\n"
"ds_read_b64_tr_b16 v[42:43], v1 offset:57600\n"
"v_mfma_f32_16x16x32_bf16 v[228:231], v[52:55], a[116:119], v[228:231]\n"
"v_accvgpr_write_b32 a103, 0\n"
"v_accvgpr_write_b32 a104, 0\n"
"s_waitcnt lgkmcnt(4)\n"
"v_mfma_f32_16x16x32_bf16 v[232:235], v[56:59], a[108:111], v[232:235]\n"
"v_accvgpr_write_b32 a105, 0\n"
"v_accvgpr_write_b32 a106, 0\n"
"v_mfma_f32_16x16x32_bf16 v[236:239], v[56:59], a[112:115], v[236:239]\n"
"ds_read_b64_tr_b16 v[44:45], v1 offset:49416\n"
"ds_read_b64_tr_b16 v[46:47], v1 offset:57608\n"
"v_mfma_f32_16x16x32_bf16 v[240:243], v[56:59], a[116:119], v[240:243]\n"
"v_accvgpr_write_b32 a107, 0\n"
"v_accvgpr_write_b32 a195, 0\n"
"v_mfma_f32_16x16x32_bf16 v[244:247], v[60:63], a[108:111], v[244:247]\n"
"v_accvgpr_write_b32 a196, 0\n"
"v_accvgpr_write_b32 a197, 0\n"
"v_mfma_f32_16x16x32_bf16 v[248:251], v[60:63], a[112:115], v[248:251]\n"
"buffer_load_dwordx4 v18, %1, s47 offen offset:2048 lds\n"
"ds_read_b64_tr_b16 v[48:49], v2 offset:49408\n"
"v_mfma_f32_16x16x32_bf16 v[252:255], v[60:63], a[116:119], v[252:255]\n"
"ds_read_b64_tr_b16 v[50:51], v2 offset:57600\n"
"v_mfma_f32_16x16x32_bf16 v[128:131], v[32:35], a[108:111], v[128:131]\n"
"v_mfma_f32_16x16x32_bf16 v[132:135], v[32:35], a[112:115], v[132:135]\n"
"ds_read_b64_tr_b16 v[52:53], v2 offset:49416\n"
"ds_read_b64_tr_b16 v[54:55], v2 offset:57608\n"
"v_mfma_f32_16x16x32_bf16 v[136:139], v[32:35], a[116:119], v[136:139]\n"
"s_waitcnt lgkmcnt(4)\n"
"v_mfma_f32_16x16x32_bf16 v[140:143], v[36:39], a[108:111], v[140:143]\n"
"v_mfma_f32_16x16x32_bf16 v[144:147], v[36:39], a[112:115], v[144:147]\n"
"ds_read_b64_tr_b16 v[56:57], v3 offset:49408\n"
"ds_read_b64_tr_b16 v[58:59], v3 offset:57600\n"
"v_mfma_f32_16x16x32_bf16 v[148:151], v[36:39], a[116:119], v[148:151]\n"
"ds_read_b64_tr_b16 v[60:61], v3 offset:49416\n"
"ds_read_b64_tr_b16 v[62:63], v3 offset:57608\n"
"v_mfma_f32_16x16x32_bf16 v[152:155], v[40:43], a[108:111], v[152:155]\n"
"v_mfma_f32_16x16x32_bf16 v[156:159], v[40:43], a[112:115], v[156:159]\n"
"v_mfma_f32_16x16x32_bf16 v[160:163], v[40:43], a[116:119], v[160:163]\n"
"v_mfma_f32_16x16x32_bf16 v[164:167], v[44:47], a[108:111], v[164:167]\n"
"buffer_load_dwordx4 v19, %1, s47 offen offset:3072 lds\n"
"v_mfma_f32_16x16x32_bf16 v[168:171], v[44:47], a[112:115], v[168:171]\n"
"v_mfma_f32_16x16x32_bf16 v[172:175], v[44:47], a[116:119], v[172:175]\n"
"s_waitcnt lgkmcnt(4)\n"
"v_mfma_f32_16x16x32_bf16 v[176:179], v[48:51], a[108:111], v[176:179]\n"
"v_mfma_f32_16x16x32_bf16 v[180:183], v[48:51], a[112:115], v[180:183]\n"
"v_mfma_f32_16x16x32_bf16 v[184:187], v[48:51], a[116:119], v[184:187]\n"
"s_waitcnt vmcnt(34) lgkmcnt(0)\n"
"s_barrier\n"
"v_mfma_f32_16x16x32_bf16 v[188:191], v[52:55], a[108:111], v[188:191]\n"
"ds_read_b64_tr_b16 v[32:33], v4 offset:0\n"
"ds_read_b64_tr_b16 v[34:35], v4 offset:8192\n"
"v_mfma_f32_16x16x32_bf16 v[192:195], v[52:55], a[112:115], v[192:195]\n"
"ds_read_b64_tr_b16 v[36:37], v4 offset:8\n"
"ds_read_b64_tr_b16 v[38:39], v4 offset:8200\n"
"v_mfma_f32_16x16x32_bf16 v[196:199], v[52:55], a[116:119], v[196:199]\n"
"ds_read_b64_tr_b16 v[40:41], v5 offset:0\n"
"ds_read_b64_tr_b16 v[42:43], v5 offset:8192\n"
"v_mfma_f32_16x16x32_bf16 v[200:203], v[56:59], a[108:111], v[200:203]\n"
"ds_read_b64_tr_b16 v[44:45], v5 offset:8\n"
"ds_read_b64_tr_b16 v[46:47], v5 offset:8200\n"
"v_mfma_f32_16x16x32_bf16 v[204:207], v[56:59], a[112:115], v[204:207]\n"
"v_mfma_f32_16x16x32_bf16 v[208:211], v[56:59], a[116:119], v[208:211]\n"
"v_mfma_f32_16x16x32_bf16 v[212:215], v[60:63], a[108:111], v[212:215]\n"
"v_mfma_f32_16x16x32_bf16 v[216:219], v[60:63], a[112:115], v[216:219]\n"
"v_mfma_f32_16x16x32_bf16 v[220:223], v[60:63], a[116:119], v[220:223]\n"
"s_waitcnt lgkmcnt(6)\n"
"v_mfma_f32_16x16x32_bf16 v[64:67], v[32:35], a[120:123], v[64:67]\n"
"ds_read_b64_tr_b16 v[48:49], v6 offset:0\n"
"ds_read_b64_tr_b16 v[50:51], v6 offset:8192\n"
"v_mfma_f32_16x16x32_bf16 v[68:71], v[32:35], a[124:127], v[68:71]\n"
"s_add_u32 s47, %9, 16384\n"
"s_and_b32 s47, s47, 0xfffff\n"
"v_mfma_f32_16x16x32_bf16 v[72:75], v[32:35], a[128:131], v[72:75]\n"
"s_add_u32 m0, s40, 114688\n"
"ds_read_b64_tr_b16 v[52:53], v6 offset:8\n"
"s_waitcnt lgkmcnt(3)\n"
"v_mfma_f32_16x16x32_bf16 v[76:79], v[36:39], a[120:123], v[76:79]\n"
"buffer_load_dwordx4 v16, %1, s47 offen offset:0 lds\n"
"ds_read_b64_tr_b16 v[54:55], v6 offset:8200\n"
"v_mfma_f32_16x16x32_bf16 v[80:83], v[36:39], a[124:127], v[80:83]\n"
"ds_read_b64_tr_b16 v[56:57], v7 offset:0\n"
"ds_read_b64_tr_b16 v[58:59], v7 offset:8192\n"
"v_mfma_f32_16x16x32_bf16 v[84:87], v[36:39], a[128:131], v[84:87]\n"
"v_mfma_f32_16x16x32_bf16 v[88:91], v[40:43], a[120:123], v[88:91]\n"
"v_mfma_f32_16x16x32_bf16 v[92:95], v[40:43], a[124:127], v[92:95]\n"
"ds_read_b64_tr_b16 v[60:61], v7 offset:8\n"
"ds_read_b64_tr_b16 v[62:63], v7 offset:8200\n"
"v_mfma_f32_16x16x32_bf16 v[96:99], v[40:43], a[128:131], v[96:99]\n"
"v_mfma_f32_16x16x32_bf16 v[100:103], v[44:47], a[120:123], v[100:103]\n"
"v_mfma_f32_16x16x32_bf16 v[104:107], v[44:47], a[124:127], v[104:107]\n"
"ds_read_b64_tr_b16 v[32:33], v4 offset:256\n"
"ds_read_b64_tr_b16 v[34:35], v4 offset:8448\n"
"v_mfma_f32_16x16x32_bf16 v[108:111], v[44:47], a[128:131], v[108:111]\n"
"buffer_load_dwordx4 v17, %1, s47 offen offset:1024 lds\n"
"v_accvgpr_write_b32 a108, 0\n"
"s_waitcnt lgkmcnt(6)\n"
"v_mfma_f32_16x16x32_bf16 v[112:115], v[48:51], a[120:123], v[112:115]\n"
"v_accvgpr_write_b32 a109, 0\n"
"v_accvgpr_write_b32 a110, 0\n"
"v_mfma_f32_16x16x32_bf16 v[116:119], v[48:51], a[124:127], v[116:119]\n"
"ds_read_b64_tr_b16 v[36:37], v4 offset:264\n"
"ds_read_b64_tr_b16 v[38:39], v4 offset:8456\n"
"v_mfma_f32_16x16x32_bf16 v[120:123], v[48:51], a[128:131], v[120:123]\n"
"v_accvgpr_write_b32 a111, 0\n"
"v_accvgpr_write_b32 a112, 0\n"
"v_mfma_f32_16x16x32_bf16 v[124:127], v[52:55], a[120:123], v[124:127]\n"
"v_accvgpr_write_b32 a113, 0\n"
"v_accvgpr_write_b32 a114, 0\n"
"v_mfma_f32_16x16x32_bf16 v[224:227], v[52:55], a[124:127], v[224:227]\n"
"ds_read_b64_tr_b16 v[40:41], v5 offset:256\n"
"ds_read_b64_tr_b16 v[42:43], v5 offset:8448\n"
"v_mfma_f32_16x16x32_bf16 v[228:231], v[52:55], a[128:131], v[228:231]\n"
"v_accvgpr_write_b32 a115, 0\n"
"v_accvgpr_write_b32 a116, 0\n"
"s_waitcnt lgkmcnt(4)\n"
"v_mfma_f32_16x16x32_bf16 v[232:235], v[56:59], a[120:123], v[232:235]\n"
"v_accvgpr_write_b32 a117, 0\n"
"v_accvgpr_write_b32 a118, 0\n"
"v_mfma_f32_16x16x32_bf16 v[236:239], v[56:59], a[124:127], v[236:239]\n"
"ds_read_b64_tr_b16 v[44:45], v5 offset:264\n"
"ds_read_b64_tr_b16 v[46:47], v5 offset:8456\n"
"v_mfma_f32_16x16x32_bf16 v[240:243], v[56:59], a[128:131], v[240:243]\n"
"v_accvgpr_write_b32 a119, 0\n"
"v_accvgpr_write_b32 a198, 0\n"
"v_mfma_f32_16x16x32_bf16 v[244:247], v[60:63], a[120:123], v[244:247]\n"
"v_accvgpr_write_b32 a199, 0\n"
"v_accvgpr_write_b32 a200, 0\n"
"v_mfma_f32_16x16x32_bf16 v[248:251], v[60:63], a[124:127], v[248:251]\n"
"buffer_load_dwordx4 v18, %1, s47 offen offset:2048 lds\n"
"ds_read_b64_tr_b16 v[48:49], v6 offset:256\n"
"v_mfma_f32_16x16x32_bf16 v[252:255], v[60:63], a[128:131], v[252:255]\n"
"ds_read_b64_tr_b16 v[50:51], v6 offset:8448\n"
"v_mfma_f32_16x16x32_bf16 v[128:131], v[32:35], a[120:123], v[128:131]\n"
"v_mfma_f32_16x16x32_bf16 v[132:135], v[32:35], a[124:127], v[132:135]\n"
"ds_read_b64_tr_b16 v[52:53], v6 offset:264\n"
"ds_read_b64_tr_b16 v[54:55], v6 offset:8456\n"
"v_mfma_f32_16x16x32_bf16 v[136:139], v[32:35], a[128:131], v[136:139]\n"
"s_waitcnt lgkmcnt(4)\n"
"v_mfma_f32_16x16x32_bf16 v[140:143], v[36:39], a[120:123], v[140:143]\n"
"v_mfma_f32_16x16x32_bf16 v[144:147], v[36:39], a[124:127], v[144:147]\n"
"ds_read_b64_tr_b16 v[56:57], v7 offset:256\n"
"ds_read_b64_tr_b16 v[58:59], v7 offset:8448\n"
"v_mfma_f32_16x16x32_bf16 v[148:151], v[36:39], a[128:131], v[148:151]\n"
"ds_read_b64_tr_b16 v[60:61], v7 offset:264\n"
"ds_read_b64_tr_b16 v[62:63], v7 offset:8456\n"
"v_mfma_f32_16x16x32_bf16 v[152:155], v[40:43], a[120:123], v[152:155]\n"
"v_mfma_f32_16x16x32_bf16 v[156:159], v[40:43], a[124:127], v[156:159]\n"
"v_mfma_f32_16x16x32_bf16 v[160:163], v[40:43], a[128:131], v[160:163]\n"
"v_mfma_f32_16x16x32_bf16 v[164:167], v[44:47], a[120:123], v[164:167]\n"
"buffer_load_dwordx4 v19, %1, s47 offen offset:3072 lds\n"
"v_mfma_f32_16x16x32_bf16 v[168:171], v[44:47], a[124:127], v[168:171]\n"
"v_mfma_f32_16x16x32_bf16 v[172:175], v[44:47], a[128:131], v[172:175]\n"
"s_waitcnt lgkmcnt(4)\n"
"v_mfma_f32_16x16x32_bf16 v[176:179], v[48:51], a[120:123], v[176:179]\n"
"v_mfma_f32_16x16x32_bf16 v[180:183], v[48:51], a[124:127], v[180:183]\n"
"v_mfma_f32_16x16x32_bf16 v[184:187], v[48:51], a[128:131], v[184:187]\n"
"s_waitcnt vmcnt(14) lgkmcnt(0)\n"
"s_barrier\n"
"v_mfma_f32_16x16x32_bf16 v[188:191], v[52:55], a[120:123], v[188:191]\n"
"ds_read_b128 a[144:147], v21 offset:0\n"
"ds_read_b128 a[148:151], v21 offset:2048\n"
"v_mfma_f32_16x16x32_bf16 v[192:195], v[52:55], a[124:127], v[192:195]\n"
"ds_read_b128 a[152:155], v21 offset:4096\n"
"ds_read_b128 a[156:159], v22 offset:0\n"
"v_mfma_f32_16x16x32_bf16 v[196:199], v[52:55], a[128:131], v[196:199]\n"
"ds_read_b128 a[160:163], v22 offset:2048\n"
"ds_read_b128 a[164:167], v22 offset:4096\n"
"v_mfma_f32_16x16x32_bf16 v[200:203], v[56:59], a[120:123], v[200:203]\n"
"ds_read_b64_tr_b16 v[32:33], v4 offset:16384\n"
"ds_read_b64_tr_b16 v[34:35], v4 offset:24576\n"
"v_mfma_f32_16x16x32_bf16 v[204:207], v[56:59], a[124:127], v[204:207]\n"
"ds_read_b64_tr_b16 v[36:37], v4 offset:16392\n"
"ds_read_b64_tr_b16 v[38:39], v4 offset:24584\n"
"v_mfma_f32_16x16x32_bf16 v[208:211], v[56:59], a[128:131], v[208:211]\n"
"ds_read_b64_tr_b16 v[40:41], v5 offset:16384\n"
"ds_read_b64_tr_b16 v[42:43], v5 offset:24576\n"
"v_mfma_f32_16x16x32_bf16 v[212:215], v[60:63], a[120:123], v[212:215]\n"
"ds_read_b64_tr_b16 v[44:45], v5 offset:16392\n"
"ds_read_b64_tr_b16 v[46:47], v5 offset:24584\n"
"v_mfma_f32_16x16x32_bf16 v[216:219], v[60:63], a[124:127], v[216:219]\n"
"v_mfma_f32_16x16x32_bf16 v[220:223], v[60:63], a[128:131], v[220:223]\n"
"s_waitcnt lgkmcnt(6)\n"
"v_mfma_f32_16x16x32_bf16 v[64:67], v[32:35], a[132:135], v[64:67]\n"
"ds_read_b64_tr_b16 v[48:49], v6 offset:16384\n"
"ds_read_b64_tr_b16 v[50:51], v6 offset:24576\n"
"v_mfma_f32_16x16x32_bf16 v[68:71], v[32:35], a[136:139], v[68:71]\n"
"ds_read_b64_tr_b16 v[52:53], v6 offset:16392\n"
"ds_read_b64_tr_b16 v[54:55], v6 offset:24584\n"
"v_mfma_f32_16x16x32_bf16 v[72:75], v[32:35], a[140:143], v[72:75]\n"
"s_waitcnt lgkmcnt(8)\n"
"v_mfma_f32_16x16x32_bf16 v[76:79], v[36:39], a[132:135], v[76:79]\n"
"v_mfma_f32_16x16x32_bf16 v[80:83], v[36:39], a[136:139], v[80:83]\n"
"ds_read_b64_tr_b16 v[56:57], v7 offset:16384\n"
"ds_read_b64_tr_b16 v[58:59], v7 offset:24576\n"
"v_mfma_f32_16x16x32_bf16 v[84:87], v[36:39], a[140:143], v[84:87]\n"
"s_waitcnt lgkmcnt(6)\n"
"v_mfma_f32_16x16x32_bf16 v[88:91], v[40:43], a[132:135], v[88:91]\n"
"v_mfma_f32_16x16x32_bf16 v[92:95], v[40:43], a[136:139], v[92:95]\n"
"ds_read_b64_tr_b16 v[60:61], v7 offset:16392\n"
"ds_read_b64_tr_b16 v[62:63], v7 offset:24584\n"
"v_mfma_f32_16x16x32_bf16 v[96:99], v[40:43], a[140:143], v[96:99]\n"
"v_mfma_f32_16x16x32_bf16 v[100:103], v[44:47], a[132:135], v[100:103]\n"
"v_mfma_f32_16x16x32_bf16 v[104:107], v[44:47], a[136:139], v[104:107]\n"
"ds_read_b64_tr_b16 v[32:33], v4 offset:16640\n"
"ds_read_b64_tr_b16 v[34:35], v4 offset:24832\n"
"v_mfma_f32_16x16x32_bf16 v[108:111], v[44:47], a[140:143], v[108:111]\n"
"v_accvgpr_write_b32 a120, 0\n"
"v_accvgpr_write_b32 a121, 0\n"
"s_waitcnt lgkmcnt(5)\n"
"v_mfma_f32_16x16x32_bf16 v[112:115], v[48:51], a[132:135], v[112:115]\n"
"v_accvgpr_write_b32 a122, 0\n"
"v_accvgpr_write_b32 a123, 0\n"
"v_mfma_f32_16x16x32_bf16 v[116:119], v[48:51], a[136:139], v[116:119]\n"
"ds_read_b64_tr_b16 v[36:37], v4 offset:16648\n"
"ds_read_b64_tr_b16 v[38:39], v4 offset:24840\n"
"v_mfma_f32_16x16x32_bf16 v[120:123], v[48:51], a[140:143], v[120:123]\n"
"v_accvgpr_write_b32 a124, 0\n"
"v_accvgpr_write_b32 a125, 0\n"
"v_mfma_f32_16x16x32_bf16 v[124:127], v[52:55], a[132:135], v[124:127]\n"
"v_accvgpr_write_b32 a126, 0\n"
"v_accvgpr_write_b32 a127, 0\n"
"v_mfma_f32_16x16x32_bf16 v[224:227], v[52:55], a[136:139], v[224:227]\n"
"ds_read_b64_tr_b16 v[40:41], v5 offset:16640\n"
"ds_read_b64_tr_b16 v[42:43], v5 offset:24832\n"
"v_mfma_f32_16x16x32_bf16 v[228:231], v[52:55], a[140:143], v[228:231]\n"
"v_accvgpr_write_b32 a128, 0\n"
"v_accvgpr_write_b32 a129, 0\n"
"s_waitcnt lgkmcnt(4)\n"
"v_mfma_f32_16x16x32_bf16 v[232:235], v[56:59], a[132:135], v[232:235]\n"
"v_accvgpr_write_b32 a130, 0\n"
"v_accvgpr_write_b32 a131, 0\n"
"v_mfma_f32_16x16x32_bf16 v[236:239], v[56:59], a[136:139], v[236:239]\n"
"ds_read_b64_tr_b16 v[44:45], v5 offset:16648\n"
"ds_read_b64_tr_b16 v[46:47], v5 offset:24840\n"
"v_mfma_f32_16x16x32_bf16 v[240:243], v[56:59], a[140:143], v[240:243]\n"
"v_accvgpr_write_b32 a201, 0\n"
"v_accvgpr_write_b32 a202, 0\n"
"v_mfma_f32_16x16x32_bf16 v[244:247], v[60:63], a[132:135], v[244:247]\n"
"v_accvgpr_write_b32 a203, 0\n"
"v_mfma_f32_16x16x32_bf16 v[248:251], v[60:63], a[136:139], v[248:251]\n"
"ds_read_b64_tr_b16 v[48:49], v6 offset:16640\n"
"ds_read_b64_tr_b16 v[50:51], v6 offset:24832\n"
"v_mfma_f32_16x16x32_bf16 v[252:255], v[60:63], a[140:143], v[252:255]\n"
"v_mfma_f32_16x16x32_bf16 v[128:131], v[32:35], a[132:135], v[128:131]\n"
"v_mfma_f32_16x16x32_bf16 v[132:135], v[32:35], a[136:139], v[132:135]\n"
"ds_read_b64_tr_b16 v[52:53], v6 offset:16648\n"
"ds_read_b64_tr_b16 v[54:55], v6 offset:24840\n"
"v_mfma_f32_16x16x32_bf16 v[136:139], v[32:35], a[140:143], v[136:139]\n"
"s_waitcnt lgkmcnt(5)\n"
"v_mfma_f32_16x16x32_bf16 v[140:143], v[36:39], a[132:135], v[140:143]\n"
"v_mfma_f32_16x16x32_bf16 v[144:147], v[36:39], a[136:139], v[144:147]\n"
"ds_read_b64_tr_b16 v[56:57], v7 offset:16640\n"
"ds_read_b64_tr_b16 v[58:59], v7 offset:24832\n"
"v_mfma_f32_16x16x32_bf16 v[148:151], v[36:39], a[140:143], v[148:151]\n"
"ds_read_b64_tr_b16 v[60:61], v7 offset:16648\n"
"ds_read_b64_tr_b16 v[62:63], v7 offset:24840\n"
"v_mfma_f32_16x16x32_bf16 v[152:155], v[40:43], a[132:135], v[152:155]\n"
"v_mfma_f32_16x16x32_bf16 v[156:159], v[40:43], a[136:139], v[156:159]\n"
"v_mfma_f32_16x16x32_bf16 v[160:163], v[40:43], a[140:143], v[160:163]\n"
"s_waitcnt lgkmcnt(6)\n"
"v_mfma_f32_16x16x32_bf16 v[164:167], v[44:47], a[132:135], v[164:167]\n"
"v_mfma_f32_16x16x32_bf16 v[168:171], v[44:47], a[136:139], v[168:171]\n"
"v_mfma_f32_16x16x32_bf16 v[172:175], v[44:47], a[140:143], v[172:175]\n"
"v_mfma_f32_16x16x32_bf16 v[176:179], v[48:51], a[132:135], v[176:179]\n"
"v_mfma_f32_16x16x32_bf16 v[180:183], v[48:51], a[136:139], v[180:183]\n"
"v_mfma_f32_16x16x32_bf16 v[184:187], v[48:51], a[140:143], v[184:187]\n"
"s_waitcnt vmcnt(24) lgkmcnt(0)\n"
"s_barrier\n"
"v_mfma_f32_16x16x32_bf16 v[188:191], v[52:55], a[132:135], v[188:191]\n"
"ds_read_b64_tr_b16 v[32:33], v0 offset:0\n"
"ds_read_b64_tr_b16 v[34:35], v0 offset:8192\n"
"v_mfma_f32_16x16x32_bf16 v[192:195], v[52:55], a[136:139], v[192:195]\n"
"ds_read_b64_tr_b16 v[36:37], v0 offset:8\n"
"ds_read_b64_tr_b16 v[38:39], v0 offset:8200\n"
"v_mfma_f32_16x16x32_bf16 v[196:199], v[52:55], a[140:143], v[196:199]\n"
"ds_read_b64_tr_b16 v[40:41], v1 offset:0\n"
"ds_read_b64_tr_b16 v[42:43], v1 offset:8192\n"
"v_mfma_f32_16x16x32_bf16 v[200:203], v[56:59], a[132:135], v[200:203]\n"
"ds_read_b64_tr_b16 v[44:45], v1 offset:8\n"
"ds_read_b64_tr_b16 v[46:47], v1 offset:8200\n"
"v_mfma_f32_16x16x32_bf16 v[204:207], v[56:59], a[136:139], v[204:207]\n"
"v_mfma_f32_16x16x32_bf16 v[208:211], v[56:59], a[140:143], v[208:211]\n"
"v_mfma_f32_16x16x32_bf16 v[212:215], v[60:63], a[132:135], v[212:215]\n"
"v_mfma_f32_16x16x32_bf16 v[216:219], v[60:63], a[136:139], v[216:219]\n"
"v_mfma_f32_16x16x32_bf16 v[220:223], v[60:63], a[140:143], v[220:223]\n"
"s_waitcnt lgkmcnt(6)\n"
"v_mfma_f32_16x16x32_bf16 v[64:67], v[32:35], a[144:147], v[64:67]\n"
"ds_read_b64_tr_b16 v[48:49], v2 offset:0\n"
"ds_read_b64_tr_b16 v[50:51], v2 offset:8192\n"
"v_mfma_f32_16x16x32_bf16 v[68:71], v[32:35], a[148:151], v[68:71]\n"
"s_add_u32 s47, %9, 16384\n"
"s_and_b32 s47, s47, 0xfffff\n"
"v_mfma_f32_16x16x32_bf16 v[72:75], v[32:35], a[152:155], v[72:75]\n"
"s_add_u32 m0, s40, 16384\n"
"ds_read_b64_tr_b16 v[52:53], v2 offset:8\n"
"s_waitcnt lgkmcnt(3)\n"
"v_mfma_f32_16x16x32_bf16 v[76:79], v[36:39], a[144:147], v[76:79]\n"
"buffer_load_dwordx4 v12, %0, s47 offen offset:0 lds\n"
"ds_read_b64_tr_b16 v[54:55], v2 offset:8200\n"
"v_mfma_f32_16x16x32_bf16 v[80:83], v[36:39], a[148:151], v[80:83]\n"
"ds_read_b64_tr_b16 v[56:57], v3 offset:0\n"
"ds_read_b64_tr_b16 v[58:59], v3 offset:8192\n"
"v_mfma_f32_16x16x32_bf16 v[84:87], v[36:39], a[152:155], v[84:87]\n"
"v_mfma_f32_16x16x32_bf16 v[88:91], v[40:43], a[144:147], v[88:91]\n"
"buffer_load_dwordx4 v13, %0, s47 offen offset:1024 lds\n"
"v_mfma_f32_16x16x32_bf16 v[92:95], v[40:43], a[148:151], v[92:95]\n"
"ds_read_b64_tr_b16 v[60:61], v3 offset:8\n"
"ds_read_b64_tr_b16 v[62:63], v3 offset:8200\n"
"v_mfma_f32_16x16x32_bf16 v[96:99], v[40:43], a[152:155], v[96:99]\n"
"v_mfma_f32_16x16x32_bf16 v[100:103], v[44:47], a[144:147], v[100:103]\n"
"v_mfma_f32_16x16x32_bf16 v[104:107], v[44:47], a[148:151], v[104:107]\n"
"ds_read_b64_tr_b16 v[32:33], v0 offset:256\n"
"ds_read_b64_tr_b16 v[34:35], v0 offset:8448\n"
"v_mfma_f32_16x16x32_bf16 v[108:111], v[44:47], a[152:155], v[108:111]\n"
"buffer_load_dwordx4 v14, %0, s47 offen offset:2048 lds\n"
"v_accvgpr_write_b32 a132, 0\n"
"s_waitcnt lgkmcnt(5)\n"
"v_mfma_f32_16x16x32_bf16 v[112:115], v[48:51], a[144:147], v[112:115]\n"
"v_accvgpr_write_b32 a133, 0\n"
"v_accvgpr_write_b32 a134, 0\n"
"v_mfma_f32_16x16x32_bf16 v[116:119], v[48:51], a[148:151], v[116:119]\n"
"ds_read_b64_tr_b16 v[36:37], v0 offset:264\n"
"ds_read_b64_tr_b16 v[38:39], v0 offset:8456\n"
"v_mfma_f32_16x16x32_bf16 v[120:123], v[48:51], a[152:155], v[120:123]\n"
"v_accvgpr_write_b32 a135, 0\n"
"v_accvgpr_write_b32 a136, 0\n"
"v_mfma_f32_16x16x32_bf16 v[124:127], v[52:55], a[144:147], v[124:127]\n"
"v_accvgpr_write_b32 a137, 0\n"
"v_accvgpr_write_b32 a138, 0\n"
"v_mfma_f32_16x16x32_bf16 v[224:227], v[52:55], a[148:151], v[224:227]\n"
"ds_read_b64_tr_b16 v[40:41], v1 offset:256\n"
"ds_read_b64_tr_b16 v[42:43], v1 offset:8448\n"
"v_mfma_f32_16x16x32_bf16 v[228:231], v[52:55], a[152:155], v[228:231]\n"
"buffer_load_dwordx4 v15, %0, s47 offen offset:3072 lds\n"
"v_accvgpr_write_b32 a139, 0\n"
"s_waitcnt lgkmcnt(4)\n"
"v_mfma_f32_16x16x32_bf16 v[232:235], v[56:59], a[144:147], v[232:235]\n"
"v_accvgpr_write_b32 a140, 0\n"
"v_accvgpr_write_b32 a141, 0\n"
"v_mfma_f32_16x16x32_bf16 v[236:239], v[56:59], a[148:151], v[236:239]\n"
"ds_read_b64_tr_b16 v[44:45], v1 offset:264\n"
"ds_read_b64_tr_b16 v[46:47], v1 offset:8456\n"
"v_mfma_f32_16x16x32_bf16 v[240:243], v[56:59], a[152:155], v[240:243]\n"
"v_accvgpr_write_b32 a142, 0\n"
"v_accvgpr_write_b32 a143, 0\n"
"v_mfma_f32_16x16x32_bf16 v[244:247], v[60:63], a[144:147], v[244:247]\n"
"s_add_u32 s47, %9, 32768\n"
"s_and_b32 s47, s47, 0xfffff\n"
"v_mfma_f32_16x16x32_bf16 v[248:251], v[60:63], a[148:151], v[248:251]\n"
"s_add_u32 m0, s40, 32768\n"
"ds_read_b64_tr_b16 v[48:49], v2 offset:256\n"
"v_mfma_f32_16x16x32_bf16 v[252:255], v[60:63], a[152:155], v[252:255]\n"
"buffer_load_dwordx4 v12, %0, s47 offen offset:0 lds\n"
"ds_read_b64_tr_b16 v[50:51], v2 offset:8448\n"
"v_mfma_f32_16x16x32_bf16 v[128:131], v[32:35], a[144:147], v[128:131]\n"
"v_mfma_f32_16x16x32_bf16 v[132:135], v[32:35], a[148:151], v[132:135]\n"
"ds_read_b64_tr_b16 v[52:53], v2 offset:264\n"
"ds_read_b64_tr_b16 v[54:55], v2 offset:8456\n"
"v_mfma_f32_16x16x32_bf16 v[136:139], v[32:35], a[152:155], v[136:139]\n"
"s_waitcnt lgkmcnt(4)\n"
"v_mfma_f32_16x16x32_bf16 v[140:143], v[36:39], a[144:147], v[140:143]\n"
"buffer_load_dwordx4 v13, %0, s47 offen offset:1024 lds\n"
"v_mfma_f32_16x16x32_bf16 v[144:147], v[36:39], a[148:151], v[144:147]\n"
"ds_read_b64_tr_b16 v[56:57], v3 offset:256\n"
"ds_read_b64_tr_b16 v[58:59], v3 offset:8448\n"
"v_mfma_f32_16x16x32_bf16 v[148:151], v[36:39], a[152:155], v[148:151]\n"
"ds_read_b64_tr_b16 v[60:61], v3 offset:264\n"
"ds_read_b64_tr_b16 v[62:63], v3 offset:8456\n"
"v_mfma_f32_16x16x32_bf16 v[152:155], v[40:43], a[144:147], v[152:155]\n"
"v_mfma_f32_16x16x32_bf16 v[156:159], v[40:43], a[148:151], v[156:159]\n"
"v_mfma_f32_16x16x32_bf16 v[160:163], v[40:43], a[152:155], v[160:163]\n"
"v_mfma_f32_16x16x32_bf16 v[164:167], v[44:47], a[144:147], v[164:167]\n"
"buffer_load_dwordx4 v14, %0, s47 offen offset:2048 lds\n"
"v_mfma_f32_16x16x32_bf16 v[168:171], v[44:47], a[148:151], v[168:171]\n"
"v_mfma_f32_16x16x32_bf16 v[172:175], v[44:47], a[152:155], v[172:175]\n"
"s_waitcnt lgkmcnt(4)\n"
"v_mfma_f32_16x16x32_bf16 v[176:179], v[48:51], a[144:147], v[176:179]\n"
"v_mfma_f32_16x16x32_bf16 v[180:183], v[48:51], a[148:151], v[180:183]\n"
"v_mfma_f32_16x16x32_bf16 v[184:187], v[48:51], a[152:155], v[184:187]\n"
"s_waitcnt vmcnt(17) lgkmcnt(0)\n"
"s_barrier\n"
"v_mfma_f32_16x16x32_bf16 v[188:191], v[52:55], a[144:147], v[188:191]\n"
"buffer_load_dwordx4 v15, %0, s47 offen offset:3072 lds\n"
"ds_read_b64_tr_b16 v[32:33], v0 offset:16384\n"
"v_mfma_f32_16x16x32_bf16 v[192:195], v[52:55], a[148:151], v[192:195]\n"
"ds_read_b64_tr_b16 v[34:35], v0 offset:24576\n"
"ds_read_b64_tr_b16 v[36:37], v0 offset:16392\n"
"v_mfma_f32_16x16x32_bf16 v[196:199], v[52:55], a[152:155], v[196:199]\n"
"ds_read_b64_tr_b16 v[38:39], v0 offset:24584\n"
"ds_read_b64_tr_b16 v[40:41], v1 offset:16384\n"
"v_mfma_f32_16x16x32_bf16 v[200:203], v[56:59], a[144:147], v[200:203]\n"
"ds_read_b64_tr_b16 v[42:43], v1 offset:24576\n"
"ds_read_b64_tr_b16 v[44:45], v1 offset:16392\n"
"v_mfma_f32_16x16x32_bf16 v[204:207], v[56:59], a[148:151], v[204:207]\n"
"ds_read_b64_tr_b16 v[46:47], v1 offset:24584\n"
"v_mfma_f32_16x16x32_bf16 v[208:211], v[56:59], a[152:155], v[208:211]\n"
"v_mfma_f32_16x16x32_bf16 v[212:215], v[60:63], a[144:147], v[212:215]\n"
"v_mfma_f32_16x16x32_bf16 v[216:219], v[60:63], a[148:151], v[216:219]\n"
"v_mfma_f32_16x16x32_bf16 v[220:223], v[60:63], a[152:155], v[220:223]\n"
"s_waitcnt lgkmcnt(6)\n"
"v_mfma_f32_16x16x32_bf16 v[64:67], v[32:35], a[156:159], v[64:67]\n"
"ds_read_b64_tr_b16 v[48:49], v2 offset:16384\n"
"ds_read_b64_tr_b16 v[50:51], v2 offset:24576\n"
"v_mfma_f32_16x16x32_bf16 v[68:71], v[32:35], a[160:163], v[68:71]\n"
"s_add_u32 s47, %9, 49152\n"
"s_and_b32 s47, s47, 0xfffff\n"
"v_mfma_f32_16x16x32_bf16 v[72:75], v[32:35], a[164:167], v[72:75]\n"
"s_add_u32 m0, s40, 49152\n"
"ds_read_b64_tr_b16 v[52:53], v2 offset:16392\n"
"s_waitcnt lgkmcnt(4)\n"
"v_mfma_f32_16x16x32_bf16 v[76:79], v[36:39], a[156:159], v[76:79]\n"
"buffer_load_dwordx4 v12, %0, s47 offen offset:0 lds\n"
"ds_read_b64_tr_b16 v[54:55], v2 offset:24584\n"
"v_mfma_f32_16x16x32_bf16 v[80:83], v[36:39], a[160:163], v[80:83]\n"
"ds_read_b64_tr_b16 v[56:57], v3 offset:16384\n"
"ds_read_b64_tr_b16 v[58:59], v3 offset:24576\n"
"v_mfma_f32_16x16x32_bf16 v[84:87], v[36:39], a[164:167], v[84:87]\n"
"v_mfma_f32_16x16x32_bf16 v[88:91], v[40:43], a[156:159], v[88:91]\n"
"buffer_load_dwordx4 v13, %0, s47 offen offset:1024 lds\n"
"v_mfma_f32_16x16x32_bf16 v[92:95], v[40:43], a[160:163], v[92:95]\n"
"ds_read_b64_tr_b16 v[60:61], v3 offset:16392\n"
"ds_read_b64_tr_b16 v[62:63], v3 offset:24584\n"
"v_mfma_f32_16x16x32_bf16 v[96:99], v[40:43], a[164:167], v[96:99]\n"
"s_waitcnt lgkmcnt(6)\n"
"v_mfma_f32_16x16x32_bf16 v[100:103], v[44:47], a[156:159], v[100:103]\n"
"v_mfma_f32_16x16x32_bf16 v[104:107], v[44:47], a[160:163], v[104:107]\n"
"ds_read_b64_tr_b16 v[32:33], v0 offset:16640\n"
"ds_read_b64_tr_b16 v[34:35], v0 offset:24832\n"
"v_mfma_f32_16x16x32_bf16 v[108:111], v[44:47], a[164:167], v[108:111]\n"
"buffer_load_dwordx4 v14, %0, s47 offen offset:2048 lds\n"
"v_accvgpr_write_b32 a144, 0\n"
"v_mfma_f32_16x16x32_bf16 v[112:115], v[48:51], a[156:159], v[112:115]\n"
"v_accvgpr_write_b32 a145, 0\n"
"v_accvgpr_write_b32 a146, 0\n"
"v_mfma_f32_16x16x32_bf16 v[116:119], v[48:51], a[160:163], v[116:119]\n"
"ds_read_b64_tr_b16 v[36:37], v0 offset:16648\n"
"ds_read_b64_tr_b16 v[38:39], v0 offset:24840\n"
"v_mfma_f32_16x16x32_bf16 v[120:123], v[48:51], a[164:167], v[120:123]\n"
"v_accvgpr_write_b32 a147, 0\n"
"v_accvgpr_write_b32 a148, 0\n"
"s_waitcnt lgkmcnt(4)\n"
"v_mfma_f32_16x16x32_bf16 v[124:127], v[52:55], a[156:159], v[124:127]\n"
"v_accvgpr_write_b32 a149, 0\n"
"v_accvgpr_write_b32 a150, 0\n"
"v_mfma_f32_16x16x32_bf16 v[224:227], v[52:55], a[160:163], v[224:227]\n"
"ds_read_b64_tr_b16 v[40:41], v1 offset:16640\n"
"ds_read_b64_tr_b16 v[42:43], v1 offset:24832\n"
"v_mfma_f32_16x16x32_bf16 v[228:231], v[52:55], a[164:167], v[228:231]\n"
"buffer_load_dwordx4 v15, %0, s47 offen offset:3072 lds\n"
"v_accvgpr_write_b32 a151, 0\n"
"v_mfma_f32_16x16x32_bf16 v[232:235], v[56:59], a[156:159], v[232:235]\n"
"v_accvgpr_write_b32 a152, 0\n"
"v_accvgpr_write_b32 a153, 0\n"
"v_mfma_f32_16x16x32_bf16 v[236:239], v[56:59], a[160:163], v[236:239]\n"
"ds_read_b64_tr_b16 v[44:45], v1 offset:16648\n"
"ds_read_b64_tr_b16 v[46:47], v1 offset:24840\n"
"v_mfma_f32_16x16x32_bf16 v[240:243], v[56:59], a[164:167], v[240:243]\n"
"v_accvgpr_write_b32 a154, 0\n"
"v_accvgpr_write_b32 a155, 0\n"
"v_mfma_f32_16x16x32_bf16 v[244:247], v[60:63], a[156:159], v[244:247]\n"
"s_add_u32 s47, %9, 32768\n"
"s_and_b32 s47, s47, 0xfffff\n"
"v_mfma_f32_16x16x32_bf16 v[248:251], v[60:63], a[160:163], v[248:251]\n"
"s_add_u32 m0, s40, 65536\n"
"ds_read_b64_tr_b16 v[48:49], v2 offset:16640\n"
"v_mfma_f32_16x16x32_bf16 v[252:255], v[60:63], a[164:167], v[252:255]\n"
"buffer_load_dwordx4 v16, %1, s47 offen offset:0 lds\n"
"ds_read_b64_tr_b16 v[50:51], v2 offset:24832\n"
"s_waitcnt lgkmcnt(4)\n"
"v_mfma_f32_16x16x32_bf16 v[128:131], v[32:35], a[156:159], v[128:131]\n"
"v_mfma_f32_16x16x32_bf16 v[132:135], v[32:35], a[160:163], v[132:135]\n"
"ds_read_b64_tr_b16 v[52:53], v2 offset:16648\n"
"ds_read_b64_tr_b16 v[54:55], v2 offset:24840\n"
"v_mfma_f32_16x16x32_bf16 v[136:139], v[32:35], a[164:167], v[136:139]\n"
"v_mfma_f32_16x16x32_bf16 v[140:143], v[36:39], a[156:159], v[140:143]\n"
"buffer_load_dwordx4 v17, %1, s47 offen offset:1024 lds\n"
"v_mfma_f32_16x16x32_bf16 v[144:147], v[36:39], a[160:163], v[144:147]\n"
"ds_read_b64_tr_b16 v[56:57], v3 offset:16640\n"
"ds_read_b64_tr_b16 v[58:59], v3 offset:24832\n"
"v_mfma_f32_16x16x32_bf16 v[148:151], v[36:39], a[164:167], v[148:151]\n"
"ds_read_b64_tr_b16 v[60:61], v3 offset:16648\n"
"ds_read_b64_tr_b16 v[62:63], v3 offset:24840\n"
"v_mfma_f32_16x16x32_bf16 v[152:155], v[40:43], a[156:159], v[152:155]\n"
"v_mfma_f32_16x16x32_bf16 v[156:159], v[40:43], a[160:163], v[156:159]\n"
"v_mfma_f32_16x16x32_bf16 v[160:163], v[40:43], a[164:167], v[160:163]\n"
"s_waitcnt lgkmcnt(6)\n"
"v_mfma_f32_16x16x32_bf16 v[164:167], v[44:47], a[156:159], v[164:167]\n"
"buffer_load_dwordx4 v18, %1, s47 offen offset:2048 lds\n"
"v_mfma_f32_16x16x32_bf16 v[168:171], v[44:47], a[160:163], v[168:171]\n"
"v_mfma_f32_16x16x32_bf16 v[172:175], v[44:47], a[164:167], v[172:175]\n"
"v_mfma_f32_16x16x32_bf16 v[176:179], v[48:51], a[156:159], v[176:179]\n"
"v_mfma_f32_16x16x32_bf16 v[180:183], v[48:51], a[160:163], v[180:183]\n"
"v_mfma_f32_16x16x32_bf16 v[184:187], v[48:51], a[164:167], v[184:187]\n"
"buffer_load_dwordx4 v19, %1, s47 offen offset:3072 lds\n"
"s_waitcnt lgkmcnt(2)\n"
"v_mfma_f32_16x16x32_bf16 v[188:191], v[52:55], a[156:159], v[188:191]\n"
"v_mfma_f32_16x16x32_bf16 v[192:195], v[52:55], a[160:163], v[192:195]\n"
"v_mfma_f32_16x16x32_bf16 v[196:199], v[52:55], a[164:167], v[196:199]\n"
"v_mfma_f32_16x16x32_bf16 v[200:203], v[56:59], a[156:159], v[200:203]\n"
"v_mfma_f32_16x16x32_bf16 v[204:207], v[56:59], a[160:163], v[204:207]\n"
"v_mfma_f32_16x16x32_bf16 v[208:211], v[56:59], a[164:167], v[208:211]\n"
"s_waitcnt lgkmcnt(0)\n"
"v_mfma_f32_16x16x32_bf16 v[212:215], v[60:63], a[156:159], v[212:215]\n"
"v_mfma_f32_16x16x32_bf16 v[216:219], v[60:63], a[160:163], v[216:219]\n"
"v_mfma_f32_16x16x32_bf16 v[220:223], v[60:63], a[164:167], v[220:223]\n"
"v_accvgpr_write_b32 a156, 0\n"
"v_accvgpr_write_b32 a157, 0\n"
"v_accvgpr_write_b32 a158, 0\n"
"v_accvgpr_write_b32 a159, 0\n"
"v_accvgpr_write_b32 a160, 0\n"
"v_accvgpr_write_b32 a161, 0\n"
"v_accvgpr_write_b32 a162, 0\n"
"v_accvgpr_write_b32 a163, 0\n"
"v_accvgpr_write_b32 a164, 0\n"
"v_accvgpr_write_b32 a165, 0\n"
"v_accvgpr_write_b32 a166, 0\n"
"v_accvgpr_write_b32 a167, 0\n"
"s_nop 4\n"
"v_pk_mul_f32 v[128:129], v[128:129], s[50:51]\n"
"v_pk_mul_f32 v[130:131], v[130:131], s[50:51]\n"
"v_pk_mul_f32 v[140:141], v[140:141], s[50:51]\n"
"v_pk_mul_f32 v[142:143], v[142:143], s[50:51]\n"
"v_cvt_pk_bf16_f32 v128, v128, v129\n"
"v_cvt_pk_bf16_f32 v130, v130, v131\n"
"v_cvt_pk_bf16_f32 v140, v140, v141\n"
"v_cvt_pk_bf16_f32 v142, v142, v143\n"
"v_accvgpr_write_b32 a204, v128\n"
"v_accvgpr_write_b32 a205, v130\n"
"v_accvgpr_write_b32 a206, v140\n"
"v_accvgpr_write_b32 a207, v142\n"
"v_pk_mul_f32 v[132:133], v[132:133], s[50:51]\n"
"v_pk_mul_f32 v[134:135], v[134:135], s[50:51]\n"
"v_pk_mul_f32 v[144:145], v[144:145], s[50:51]\n"
"v_pk_mul_f32 v[146:147], v[146:147], s[50:51]\n"
"v_cvt_pk_bf16_f32 v132, v132, v133\n"
"v_cvt_pk_bf16_f32 v134, v134, v135\n"
"v_cvt_pk_bf16_f32 v144, v144, v145\n"
"v_cvt_pk_bf16_f32 v146, v146, v147\n"
"v_accvgpr_write_b32 a220, v132\n"
"v_accvgpr_write_b32 a221, v134\n"
"v_accvgpr_write_b32 a222, v144\n"
"v_accvgpr_write_b32 a223, v146\n"
"v_pk_mul_f32 v[136:137], v[136:137], s[50:51]\n"
"v_pk_mul_f32 v[138:139], v[138:139], s[50:51]\n"
"v_pk_mul_f32 v[148:149], v[148:149], s[50:51]\n"
"v_pk_mul_f32 v[150:151], v[150:151], s[50:51]\n"
"v_cvt_pk_bf16_f32 v136, v136, v137\n"
"v_cvt_pk_bf16_f32 v138, v138, v139\n"
"v_cvt_pk_bf16_f32 v148, v148, v149\n"
"v_cvt_pk_bf16_f32 v150, v150, v151\n"
"v_accvgpr_write_b32 a236, v136\n"
"v_accvgpr_write_b32 a237, v138\n"
"v_accvgpr_write_b32 a238, v148\n"
"v_accvgpr_write_b32 a239, v150\n"
"v_pk_mul_f32 v[152:153], v[152:153], s[50:51]\n"
"v_pk_mul_f32 v[154:155], v[154:155], s[50:51]\n"
"v_pk_mul_f32 v[164:165], v[164:165], s[50:51]\n"
"v_pk_mul_f32 v[166:167], v[166:167], s[50:51]\n"
"v_cvt_pk_bf16_f32 v152, v152, v153\n"
"v_cvt_pk_bf16_f32 v154, v154, v155\n"
"v_cvt_pk_bf16_f32 v164, v164, v165\n"
"v_cvt_pk_bf16_f32 v166, v166, v167\n"
"v_accvgpr_write_b32 a208, v152\n"
"v_accvgpr_write_b32 a209, v154\n"
"v_accvgpr_write_b32 a210, v164\n"
"v_accvgpr_write_b32 a211, v166\n"
"v_pk_mul_f32 v[156:157], v[156:157], s[50:51]\n"
"v_pk_mul_f32 v[158:159], v[158:159], s[50:51]\n"
"v_pk_mul_f32 v[168:169], v[168:169], s[50:51]\n"
"v_pk_mul_f32 v[170:171], v[170:171], s[50:51]\n"
"v_cvt_pk_bf16_f32 v156, v156, v157\n"
"v_cvt_pk_bf16_f32 v158, v158, v159\n"
"v_cvt_pk_bf16_f32 v168, v168, v169\n"
"v_cvt_pk_bf16_f32 v170, v170, v171\n"
"v_accvgpr_write_b32 a224, v156\n"
"v_accvgpr_write_b32 a225, v158\n"
"v_accvgpr_write_b32 a226, v168\n"
"v_accvgpr_write_b32 a227, v170\n"
"v_pk_mul_f32 v[160:161], v[160:161], s[50:51]\n"
"v_pk_mul_f32 v[162:163], v[162:163], s[50:51]\n"
"v_pk_mul_f32 v[172:173], v[172:173], s[50:51]\n"
"v_pk_mul_f32 v[174:175], v[174:175], s[50:51]\n"
"v_cvt_pk_bf16_f32 v160, v160, v161\n"
"v_cvt_pk_bf16_f32 v162, v162, v163\n"
"v_cvt_pk_bf16_f32 v172, v172, v173\n"
"v_cvt_pk_bf16_f32 v174, v174, v175\n"
"v_accvgpr_write_b32 a240, v160\n"
"v_accvgpr_write_b32 a241, v162\n"
"v_accvgpr_write_b32 a242, v172\n"
"v_accvgpr_write_b32 a243, v174\n"
"v_pk_mul_f32 v[176:177], v[176:177], s[50:51]\n"
"v_pk_mul_f32 v[178:179], v[178:179], s[50:51]\n"
"v_pk_mul_f32 v[188:189], v[188:189], s[50:51]\n"
"v_pk_mul_f32 v[190:191], v[190:191], s[50:51]\n"
"v_cvt_pk_bf16_f32 v176, v176, v177\n"
"v_cvt_pk_bf16_f32 v178, v178, v179\n"
"v_cvt_pk_bf16_f32 v188, v188, v189\n"
"v_cvt_pk_bf16_f32 v190, v190, v191\n"
"v_accvgpr_write_b32 a212, v176\n"
"v_accvgpr_write_b32 a213, v178\n"
"v_accvgpr_write_b32 a214, v188\n"
"v_accvgpr_write_b32 a215, v190\n"
"v_pk_mul_f32 v[180:181], v[180:181], s[50:51]\n"
"v_pk_mul_f32 v[182:183], v[182:183], s[50:51]\n"
"v_pk_mul_f32 v[192:193], v[192:193], s[50:51]\n"
"v_pk_mul_f32 v[194:195], v[194:195], s[50:51]\n"
"v_cvt_pk_bf16_f32 v180, v180, v181\n"
"v_cvt_pk_bf16_f32 v182, v182, v183\n"
"v_cvt_pk_bf16_f32 v192, v192, v193\n"
"v_cvt_pk_bf16_f32 v194, v194, v195\n"
"v_accvgpr_write_b32 a228, v180\n"
"v_accvgpr_write_b32 a229, v182\n"
"v_accvgpr_write_b32 a230, v192\n"
"v_accvgpr_write_b32 a231, v194\n"
"v_pk_mul_f32 v[184:185], v[184:185], s[50:51]\n"
"v_pk_mul_f32 v[186:187], v[186:187], s[50:51]\n"
"v_pk_mul_f32 v[196:197], v[196:197], s[50:51]\n"
"v_pk_mul_f32 v[198:199], v[198:199], s[50:51]\n"
"v_cvt_pk_bf16_f32 v184, v184, v185\n"
"v_cvt_pk_bf16_f32 v186, v186, v187\n"
"v_cvt_pk_bf16_f32 v196, v196, v197\n"
"v_cvt_pk_bf16_f32 v198, v198, v199\n"
"v_accvgpr_write_b32 a244, v184\n"
"v_accvgpr_write_b32 a245, v186\n"
"v_accvgpr_write_b32 a246, v196\n"
"v_accvgpr_write_b32 a247, v198\n"
"v_pk_mul_f32 v[200:201], v[200:201], s[50:51]\n"
"v_pk_mul_f32 v[202:203], v[202:203], s[50:51]\n"
"v_pk_mul_f32 v[212:213], v[212:213], s[50:51]\n"
"v_pk_mul_f32 v[214:215], v[214:215], s[50:51]\n"
"v_cvt_pk_bf16_f32 v200, v200, v201\n"
"v_cvt_pk_bf16_f32 v202, v202, v203\n"
"v_cvt_pk_bf16_f32 v212, v212, v213\n"
"v_cvt_pk_bf16_f32 v214, v214, v215\n"
"v_accvgpr_write_b32 a216, v200\n"
"v_accvgpr_write_b32 a217, v202\n"
"v_accvgpr_write_b32 a218, v212\n"
"v_accvgpr_write_b32 a219, v214\n"
"v_pk_mul_f32 v[204:205], v[204:205], s[50:51]\n"
"v_pk_mul_f32 v[206:207], v[206:207], s[50:51]\n"
"v_pk_mul_f32 v[216:217], v[216:217], s[50:51]\n"
"v_pk_mul_f32 v[218:219], v[218:219], s[50:51]\n"
"v_cvt_pk_bf16_f32 v204, v204, v205\n"
"v_cvt_pk_bf16_f32 v206, v206, v207\n"
"v_cvt_pk_bf16_f32 v216, v216, v217\n"
"v_cvt_pk_bf16_f32 v218, v218, v219\n"
"v_accvgpr_write_b32 a232, v204\n"
"v_accvgpr_write_b32 a233, v206\n"
"v_accvgpr_write_b32 a234, v216\n"
"v_accvgpr_write_b32 a235, v218\n"
"v_pk_mul_f32 v[208:209], v[208:209], s[50:51]\n"
"v_pk_mul_f32 v[210:211], v[210:211], s[50:51]\n"
"v_pk_mul_f32 v[220:221], v[220:221], s[50:51]\n"
"v_pk_mul_f32 v[222:223], v[222:223], s[50:51]\n"
"v_cvt_pk_bf16_f32 v208, v208, v209\n"
"v_cvt_pk_bf16_f32 v210, v210, v211\n"
"v_cvt_pk_bf16_f32 v220, v220, v221\n"
"v_cvt_pk_bf16_f32 v222, v222, v223\n"
"v_accvgpr_write_b32 a248, v208\n"
"v_accvgpr_write_b32 a249, v210\n"
"v_accvgpr_write_b32 a250, v220\n"
"v_accvgpr_write_b32 a251, v222\n"
"v_pk_mul_f32 v[64:65], v[64:65], s[50:51]\n"
"v_pk_mul_f32 v[66:67], v[66:67], s[50:51]\n"
"v_pk_mul_f32 v[76:77], v[76:77], s[50:51]\n"
"v_pk_mul_f32 v[78:79], v[78:79], s[50:51]\n"
"v_cvt_pk_bf16_f32 v144, v64, v65\n"
"v_cvt_pk_bf16_f32 v145, v66, v67\n"
"v_cvt_pk_bf16_f32 v146, v76, v77\n"
"v_cvt_pk_bf16_f32 v147, v78, v79\n"
"v_pk_mul_f32 v[68:69], v[68:69], s[50:51]\n"
"v_pk_mul_f32 v[70:71], v[70:71], s[50:51]\n"
"v_pk_mul_f32 v[80:81], v[80:81], s[50:51]\n"
"v_pk_mul_f32 v[82:83], v[82:83], s[50:51]\n"
"v_cvt_pk_bf16_f32 v160, v68, v69\n"
"v_cvt_pk_bf16_f32 v161, v70, v71\n"
"v_cvt_pk_bf16_f32 v162, v80, v81\n"
"v_cvt_pk_bf16_f32 v163, v82, v83\n"
"v_pk_mul_f32 v[72:73], v[72:73], s[50:51]\n"
"v_pk_mul_f32 v[74:75], v[74:75], s[50:51]\n"
"v_pk_mul_f32 v[84:85], v[84:85], s[50:51]\n"
"v_pk_mul_f32 v[86:87], v[86:87], s[50:51]\n"
"v_cvt_pk_bf16_f32 v176, v72, v73\n"
"v_cvt_pk_bf16_f32 v177, v74, v75\n"
"v_cvt_pk_bf16_f32 v178, v84, v85\n"
"v_cvt_pk_bf16_f32 v179, v86, v87\n"
"v_pk_mul_f32 v[88:89], v[88:89], s[50:51]\n"
"v_pk_mul_f32 v[90:91], v[90:91], s[50:51]\n"
"v_pk_mul_f32 v[100:101], v[100:101], s[50:51]\n"
"v_pk_mul_f32 v[102:103], v[102:103], s[50:51]\n"
"v_cvt_pk_bf16_f32 v148, v88, v89\n"
"v_cvt_pk_bf16_f32 v149, v90, v91\n"
"v_cvt_pk_bf16_f32 v150, v100, v101\n"
"v_cvt_pk_bf16_f32 v151, v102, v103\n"
"v_pk_mul_f32 v[92:93], v[92:93], s[50:51]\n"
"v_pk_mul_f32 v[94:95], v[94:95], s[50:51]\n"
"v_pk_mul_f32 v[104:105], v[104:105], s[50:51]\n"
"v_pk_mul_f32 v[106:107], v[106:107], s[50:51]\n"
"v_cvt_pk_bf16_f32 v164, v92, v93\n"
"v_cvt_pk_bf16_f32 v165, v94, v95\n"
"v_cvt_pk_bf16_f32 v166, v104, v105\n"
"v_cvt_pk_bf16_f32 v167, v106, v107\n"
"v_pk_mul_f32 v[96:97], v[96:97], s[50:51]\n"
"v_pk_mul_f32 v[98:99], v[98:99], s[50:51]\n"
"v_pk_mul_f32 v[108:109], v[108:109], s[50:51]\n"
"v_pk_mul_f32 v[110:111], v[110:111], s[50:51]\n"
"v_cvt_pk_bf16_f32 v180, v96, v97\n"
"v_cvt_pk_bf16_f32 v181, v98, v99\n"
"v_cvt_pk_bf16_f32 v182, v108, v109\n"
"v_cvt_pk_bf16_f32 v183, v110, v111\n"
"v_pk_mul_f32 v[112:113], v[112:113], s[50:51]\n"
"v_pk_mul_f32 v[114:115], v[114:115], s[50:51]\n"
"v_pk_mul_f32 v[124:125], v[124:125], s[50:51]\n"
"v_pk_mul_f32 v[126:127], v[126:127], s[50:51]\n"
"v_cvt_pk_bf16_f32 v152, v112, v113\n"
"v_cvt_pk_bf16_f32 v153, v114, v115\n"
"v_cvt_pk_bf16_f32 v154, v124, v125\n"
"v_cvt_pk_bf16_f32 v155, v126, v127\n"
"v_pk_mul_f32 v[116:117], v[116:117], s[50:51]\n"
"v_pk_mul_f32 v[118:119], v[118:119], s[50:51]\n"
"v_pk_mul_f32 v[224:225], v[224:225], s[50:51]\n"
"v_pk_mul_f32 v[226:227], v[226:227], s[50:51]\n"
"v_cvt_pk_bf16_f32 v168, v116, v117\n"
"v_cvt_pk_bf16_f32 v169, v118, v119\n"
"v_cvt_pk_bf16_f32 v170, v224, v225\n"
"v_cvt_pk_bf16_f32 v171, v226, v227\n"
"v_pk_mul_f32 v[120:121], v[120:121], s[50:51]\n"
"v_pk_mul_f32 v[122:123], v[122:123], s[50:51]\n"
"v_pk_mul_f32 v[228:229], v[228:229], s[50:51]\n"
"v_pk_mul_f32 v[230:231], v[230:231], s[50:51]\n"
"v_cvt_pk_bf16_f32 v184, v120, v121\n"
"v_cvt_pk_bf16_f32 v185, v122, v123\n"
"v_cvt_pk_bf16_f32 v186, v228, v229\n"
"v_cvt_pk_bf16_f32 v187, v230, v231\n"
"v_pk_mul_f32 v[232:233], v[232:233], s[50:51]\n"
"v_pk_mul_f32 v[234:235], v[234:235], s[50:51]\n"
"v_pk_mul_f32 v[244:245], v[244:245], s[50:51]\n"
"v_pk_mul_f32 v[246:247], v[246:247], s[50:51]\n"
"v_cvt_pk_bf16_f32 v156, v232, v233\n"
"v_cvt_pk_bf16_f32 v157, v234, v235\n"
"v_cvt_pk_bf16_f32 v158, v244, v245\n"
"v_cvt_pk_bf16_f32 v159, v246, v247\n"
"v_pk_mul_f32 v[236:237], v[236:237], s[50:51]\n"
"v_pk_mul_f32 v[238:239], v[238:239], s[50:51]\n"
"v_pk_mul_f32 v[248:249], v[248:249], s[50:51]\n"
"v_pk_mul_f32 v[250:251], v[250:251], s[50:51]\n"
"v_cvt_pk_bf16_f32 v172, v236, v237\n"
"v_cvt_pk_bf16_f32 v173, v238, v239\n"
"v_cvt_pk_bf16_f32 v174, v248, v249\n"
"v_cvt_pk_bf16_f32 v175, v250, v251\n"
"v_pk_mul_f32 v[240:241], v[240:241], s[50:51]\n"
"v_pk_mul_f32 v[242:243], v[242:243], s[50:51]\n"
"v_pk_mul_f32 v[252:253], v[252:253], s[50:51]\n"
"v_pk_mul_f32 v[254:255], v[254:255], s[50:51]\n"
"v_cvt_pk_bf16_f32 v188, v240, v241\n"
"v_cvt_pk_bf16_f32 v189, v242, v243\n"
"v_cvt_pk_bf16_f32 v190, v252, v253\n"
"v_cvt_pk_bf16_f32 v191, v254, v255\n"
"v_mbcnt_lo_u32_b32 v240, -1, 0\n"
"v_mbcnt_hi_u32_b32 v240, -1, v240\n"
"v_and_b32 v241, 15, v240\n"
"v_lshrrev_b32 v242, 4, v240\n"
"v_bfe_u32 v243, v240, 2, 2\n"
"v_and_b32 v244, 3, v240\n"
"v_lshrrev_b32 v245, 5, v240\n"
"v_and_b32 v246, 31, v240\n"
"v_add_u32 v247, 0, v242\n"
"v_xor_b32 v247, v247, v241\n"
"v_lshlrev_b32 v247, 4, v247\n"
"v_lshl_add_u32 v247, v241, 9, v247\n"
"v_add_u32 v0, %7, v247\n"
"v_add_u32 v247, 4, v242\n"
"v_xor_b32 v247, v247, v241\n"
"v_lshlrev_b32 v247, 4, v247\n"
"v_lshl_add_u32 v247, v241, 9, v247\n"
"v_add_u32 v1, %7, v247\n"
"v_add_u32 v247, 8, v242\n"
"v_xor_b32 v247, v247, v241\n"
"v_lshlrev_b32 v247, 4, v247\n"
"v_lshl_add_u32 v247, v241, 9, v247\n"
"v_add_u32 v2, %7, v247\n"
"v_add_u32 v247, 12, v242\n"
"v_xor_b32 v247, v247, v241\n"
"v_lshlrev_b32 v247, 4, v247\n"
"v_lshl_add_u32 v247, v241, 9, v247\n"
"v_add_u32 v3, %7, v247\n"
"v_lshl_add_u32 v249, v242, 2, v243\n"
"v_and_b32 v248, 7, v249\n"
"v_lshlrev_b32 v249, 9, v249\n"
"v_lshl_add_u32 v249, v244, 3, v249\n"
"v_add_u32 v249, 0x10000, v249\n"
"v_add_u32 v249, %7, v249\n"
"v_xor_b32 v247, 0, v248\n"
"v_lshl_add_u32 v4, v247, 5, v249\n"
"v_xor_b32 v247, 1, v248\n"
"v_lshl_add_u32 v5, v247, 5, v249\n"
"v_xor_b32 v247, 2, v248\n"
"v_lshl_add_u32 v6, v247, 5, v249\n"
"v_xor_b32 v247, 3, v248\n"
"v_lshl_add_u32 v7, v247, 5, v249\n"
"v_xor_b32 v247, 4, v248\n"
"v_lshl_add_u32 v8, v247, 5, v249\n"
"v_xor_b32 v247, 5, v248\n"
"v_lshl_add_u32 v9, v247, 5, v249\n"
"v_xor_b32 v247, 6, v248\n"
"v_lshl_add_u32 v10, v247, 5, v249\n"
"v_xor_b32 v247, 7, v248\n"
"v_lshl_add_u32 v11, v247, 5, v249\n"
"v_mov_b32 v20, 0\n"
"v_mov_b32 v21, 0\n"
"v_mov_b32 v22, 0\n"
"v_mov_b32 v23, 0\n"
"v_mov_b32 v24, 0\n"
"v_mov_b32 v25, 0\n"
"v_mov_b32 v26, 0\n"
"v_mov_b32 v27, 0\n"
"v_mov_b32 v28, 0\n"
"v_mov_b32 v29, 0\n"
"v_mov_b32 v30, 0\n"
"v_mov_b32 v31, 0\n"
"v_mov_b32 v205, 0\n"
"v_mov_b32 v206, 0\n"
"v_mov_b32 v207, 0\n"
"s_lshl_b32 s46, %6, 6\n"
"v_lshrrev_b32 v247, 3, v240\n"
"v_add_u32 v247, s46, v247\n"
"v_mul_u32_u24 v247, 896, v247\n"
"v_bfe_u32 v248, v240, 4, 2\n"
"v_lshlrev_b32 v248, 1, v248\n"
"v_and_b32 v208, 7, v240\n"
"v_xor_b32 v248, v248, v208\n"
"v_lshl_add_u32 v208, v248, 4, v247\n"
"v_add_u32 v209, 6144, v208\n"
"v_add_u32 v210, 12288, v208\n"
"v_add_u32 v211, 18432, v208\n"
"s_mov_b32 s44, 0xff800000\n"
"s_waitcnt vmcnt(12)\n"
"s_barrier\n"
"ds_read_b128 v[80:83], v0 offset:0\n"
"ds_read_b128 v[84:87], v1 offset:0\n"
"ds_read_b128 v[88:91], v2 offset:0\n"
"ds_read_b128 v[92:95], v3 offset:0\n"
"s_waitcnt lgkmcnt(3)\n"
"v_mfma_f32_16x16x32_bf16 v[32:35], v[80:83], v[144:147], v[20:23]\n"
"ds_read_b128 v[96:99], v0 offset:256\n"
"v_mfma_f32_16x16x32_bf16 v[36:39], v[80:83], v[160:163], v[24:27]\n"
"v_mfma_f32_16x16x32_bf16 v[40:43], v[80:83], v[176:179], v[28:31]\n"
"ds_read_b128 v[100:103], v1 offset:256\n"
"s_waitcnt lgkmcnt(4)\n"
"v_mfma_f32_16x16x32_bf16 v[32:35], v[84:87], v[148:151], v[32:35]\n"
"v_mfma_f32_16x16x32_bf16 v[36:39], v[84:87], v[164:167], v[36:39]\n"
"v_mfma_f32_16x16x32_bf16 v[40:43], v[84:87], v[180:183], v[40:43]\n"
"ds_read_b128 v[104:107], v2 offset:256\n"
"s_waitcnt lgkmcnt(4)\n"
"v_mfma_f32_16x16x32_bf16 v[32:35], v[88:91], v[152:155], v[32:35]\n"
"v_mfma_f32_16x16x32_bf16 v[36:39], v[88:91], v[168:171], v[36:39]\n"
"v_mfma_f32_16x16x32_bf16 v[40:43], v[88:91], v[184:187], v[40:43]\n"
"ds_read_b128 v[108:111], v3 offset:256\n"
"s_waitcnt lgkmcnt(3)\n"
"v_mfma_f32_16x16x32_bf16 v[32:35], v[92:95], v[156:159], v[32:35]\n"
"v_mfma_f32_16x16x32_bf16 v[36:39], v[92:95], v[172:175], v[36:39]\n"
"v_mfma_f32_16x16x32_bf16 v[40:43], v[92:95], v[188:191], v[40:43]\n"
"ds_read_b128 v[112:115], v0 offset:8192\n"
"v_mfma_f32_16x16x32_bf16 v[32:35], v[96:99], a[204:207], v[32:35]\n"
"v_mfma_f32_16x16x32_bf16 v[36:39], v[96:99], a[220:223], v[36:39]\n"
"v_mfma_f32_16x16x32_bf16 v[40:43], v[96:99], a[236:239], v[40:43]\n"
"ds_read_b128 v[116:119], v1 offset:8192\n"
"s_waitcnt lgkmcnt(3)\n"
"v_mfma_f32_16x16x32_bf16 v[32:35], v[100:103], a[208:211], v[32:35]\n"
"v_mfma_f32_16x16x32_bf16 v[36:39], v[100:103], a[224:227], v[36:39]\n"
"v_mfma_f32_16x16x32_bf16 v[40:43], v[100:103], a[240:243], v[40:43]\n"
"ds_read_b128 v[120:123], v2 offset:8192\n"
"v_mfma_f32_16x16x32_bf16 v[32:35], v[104:107], a[212:215], v[32:35]\n"
"v_mfma_f32_16x16x32_bf16 v[36:39], v[104:107], a[228:231], v[36:39]\n"
"v_mfma_f32_16x16x32_bf16 v[40:43], v[104:107], a[244:247], v[40:43]\n"
"ds_read_b128 v[124:127], v3 offset:8192\n"
"s_waitcnt lgkmcnt(3)\n"
"v_mfma_f32_16x16x32_bf16 v[32:35], v[108:111], a[216:219], v[32:35]\n"
"v_mfma_f32_16x16x32_bf16 v[36:39], v[108:111], a[232:235], v[36:39]\n"
"v_mfma_f32_16x16x32_bf16 v[40:43], v[108:111], a[248:251], v[40:43]\n"
"ds_read_b128 v[128:131], v0 offset:8448\n"
"v_mfma_f32_16x16x32_bf16 v[44:47], v[112:115], v[144:147], v[20:23]\n"
"v_mfma_f32_16x16x32_bf16 v[48:51], v[112:115], v[160:163], v[24:27]\n"
"v_mfma_f32_16x16x32_bf16 v[52:55], v[112:115], v[176:179], v[28:31]\n"
"ds_read_b128 v[132:135], v1 offset:8448\n"
"s_waitcnt lgkmcnt(3)\n"
"v_mfma_f32_16x16x32_bf16 v[44:47], v[116:119], v[148:151], v[44:47]\n"
"v_mfma_f32_16x16x32_bf16 v[48:51], v[116:119], v[164:167], v[48:51]\n"
"v_mfma_f32_16x16x32_bf16 v[52:55], v[116:119], v[180:183], v[52:55]\n"
"ds_read_b128 v[136:139], v2 offset:8448\n"
"v_mfma_f32_16x16x32_bf16 v[44:47], v[120:123], v[152:155], v[44:47]\n"
"v_mfma_f32_16x16x32_bf16 v[48:51], v[120:123], v[168:171], v[48:51]\n"
"v_mfma_f32_16x16x32_bf16 v[52:55], v[120:123], v[184:187], v[52:55]\n"
"ds_read_b128 v[140:143], v3 offset:8448\n"
"s_waitcnt lgkmcnt(3)\n"
"v_mfma_f32_16x16x32_bf16 v[44:47], v[124:127], v[156:159], v[44:47]\n"
"v_mfma_f32_16x16x32_bf16 v[48:51], v[124:127], v[172:175], v[48:51]\n"
"v_mfma_f32_16x16x32_bf16 v[52:55], v[124:127], v[188:191], v[52:55]\n"
"v_mfma_f32_16x16x32_bf16 v[44:47], v[128:131], a[204:207], v[44:47]\n"
"v_mfma_f32_16x16x32_bf16 v[48:51], v[128:131], a[220:223], v[48:51]\n"
"v_mfma_f32_16x16x32_bf16 v[52:55], v[128:131], a[236:239], v[52:55]\n"
"s_waitcnt lgkmcnt(2)\n"
"v_mfma_f32_16x16x32_bf16 v[44:47], v[132:135], a[208:211], v[44:47]\n"
"v_mfma_f32_16x16x32_bf16 v[48:51], v[132:135], a[224:227], v[48:51]\n"
"v_mfma_f32_16x16x32_bf16 v[52:55], v[132:135], a[240:243], v[52:55]\n"
"s_waitcnt lgkmcnt(1)\n"
"v_mfma_f32_16x16x32_bf16 v[44:47], v[136:139], a[212:215], v[44:47]\n"
"v_mfma_f32_16x16x32_bf16 v[48:51], v[136:139], a[228:231], v[48:51]\n"
"v_mfma_f32_16x16x32_bf16 v[52:55], v[136:139], a[244:247], v[52:55]\n"
"s_waitcnt lgkmcnt(0)\n"
"v_mfma_f32_16x16x32_bf16 v[44:47], v[140:143], a[216:219], v[44:47]\n"
"v_mfma_f32_16x16x32_bf16 v[48:51], v[140:143], a[232:235], v[48:51]\n"
"v_mfma_f32_16x16x32_bf16 v[52:55], v[140:143], a[248:251], v[52:55]\n"
"s_nop 7\n"
"s_branch LSTUB0_%=\n"
"LRET0_%=:\n"
"v_exp_f32 v32, v32\n"
"v_exp_f32 v33, v33\n"
"v_exp_f32 v34, v34\n"
"v_exp_f32 v35, v35\n"
"v_exp_f32 v36, v36\n"
"v_exp_f32 v37, v37\n"
"v_exp_f32 v38, v38\n"
"v_exp_f32 v39, v39\n"
"v_exp_f32 v40, v40\n"
"v_exp_f32 v41, v41\n"
"v_exp_f32 v42, v42\n"
"v_exp_f32 v43, v43\n"
"ds_read_b128 v[80:83], v0 offset:16384\n"
"ds_read_b128 v[84:87], v1 offset:16384\n"
"ds_read_b128 v[88:91], v2 offset:16384\n"
"v_cvt_pk_bf16_f32 v56, v32, v33\n"
"v_cvt_pk_bf16_f32 v57, v34, v35\n"
"v_add_f32 v205, v205, v32\n"
"v_add_f32 v205, v205, v33\n"
"v_add_f32 v205, v205, v34\n"
"v_add_f32 v205, v205, v35\n"
"v_cvt_pk_bf16_f32 v60, v36, v37\n"
"v_cvt_pk_bf16_f32 v61, v38, v39\n"
"v_add_f32 v206, v206, v36\n"
"v_add_f32 v206, v206, v37\n"
"v_add_f32 v206, v206, v38\n"
"v_add_f32 v206, v206, v39\n"
"v_cvt_pk_bf16_f32 v64, v40, v41\n"
"v_cvt_pk_bf16_f32 v65, v42, v43\n"
"v_add_f32 v207, v207, v40\n"
"v_add_f32 v207, v207, v41\n"
"v_add_f32 v207, v207, v42\n"
"v_add_f32 v207, v207, v43\n"
"s_waitcnt lgkmcnt(0)\n"
"s_add_u32 s41, %9, 65536\n"
"s_and_b32 s41, s41, 0xfffff\n"
"s_add_u32 s42, %9, 49152\n"
"s_and_b32 s42, s42, 0xfffff\n"
"s_mov_b32 s43, 15\n"
"s_memtime s[52:53]\n"
"s_memrealtime s[58:59]\n"
"s_waitcnt lgkmcnt(0)\n"
"s_mov_b32 s54, s52\n"
"s_mov_b32 s55, 0\n"
"s_sub_u32 s56, s52, s60\n"
"s_sub_u32 s58, s58, s62\n"
"s_mul_i32 s58, s58, 71\n"
"s_lshr_b32 s58, s58, 2\n"
"s_cmp_gt_u32 s56, s58\n"
"s_cselect_b32 s57, 64, 0\n"
"s_cmp_eq_u32 %10, 1\n"
"s_cbranch_scc1 LLOOPB3_%=\n"
"LLOOPA3_%=:\n"
"s_waitcnt lgkmcnt(0)\n"
"v_mfma_f32_16x16x32_bf16 v[32:35], v[80:83], v[144:147], v[20:23]\n"
"ds_read_b128 v[92:95], v3 offset:16384\n"
"ds_read_b128 v[96:99], v0 offset:16640\n"
"v_mfma_f32_16x16x32_bf16 v[36:39], v[80:83], v[160:163], v[24:27]\n"
"v_exp_f32 v44, v44\n"
"v_mfma_f32_16x16x32_bf16 v[40:43], v[80:83], v[176:179], v[28:31]\n"
"ds_read_b128 v[100:103], v1 offset:16640\n"
"v_add_f32 v205, v205, v44\n"
"v_mfma_f32_16x16x32_bf16 v[32:35], v[84:87], v[148:151], v[32:35]\n"
"v_exp_f32 v45, v45\n"
"v_mfma_f32_16x16x32_bf16 v[36:39], v[84:87], v[164:167], v[36:39]\n"
"v_exp_f32 v46, v46\n"
"v_mfma_f32_16x16x32_bf16 v[40:43], v[84:87], v[180:183], v[40:43]\n"
"ds_read_b128 v[104:107], v2 offset:16640\n"
"v_cvt_pk_bf16_f32 v58, v44, v45\n"
"v_mfma_f32_16x16x32_bf16 v[32:35], v[88:91], v[152:155], v[32:35]\n"
"v_exp_f32 v47, v47\n"
"v_mfma_f32_16x16x32_bf16 v[36:39], v[88:91], v[168:171], v[36:39]\n"
"v_exp_f32 v48, v48\n"
"v_mfma_f32_16x16x32_bf16 v[40:43], v[88:91], v[184:187], v[40:43]\n"
"ds_read_b128 v[108:111], v3 offset:16640\n"
"v_cvt_pk_bf16_f32 v59, v46, v47\n"
"s_waitcnt lgkmcnt(2)\n"
"v_mfma_f32_16x16x32_bf16 v[32:35], v[92:95], v[156:159], v[32:35]\n"
"v_exp_f32 v49, v49\n"
"v_mfma_f32_16x16x32_bf16 v[36:39], v[92:95], v[172:175], v[36:39]\n"
"v_exp_f32 v50, v50\n"
"v_mfma_f32_16x16x32_bf16 v[40:43], v[92:95], v[188:191], v[40:43]\n"
"ds_read_b128 v[112:115], v0 offset:24576\n"
"v_add_f32 v205, v205, v45\n"
"v_mfma_f32_16x16x32_bf16 v[32:35], v[96:99], a[204:207], v[32:35]\n"
"v_exp_f32 v51, v51\n"
"v_mfma_f32_16x16x32_bf16 v[36:39], v[96:99], a[220:223], v[36:39]\n"
"v_add_f32 v205, v205, v46\n"
"v_add_f32 v205, v205, v47\n"
"v_mfma_f32_16x16x32_bf16 v[40:43], v[96:99], a[236:239], v[40:43]\n"
"ds_read_b128 v[116:119], v1 offset:24576\n"
"v_cvt_pk_bf16_f32 v62, v48, v49\n"
"v_mfma_f32_16x16x32_bf16 v[32:35], v[100:103], a[208:211], v[32:35]\n"
"v_exp_f32 v52, v52\n"
"v_mfma_f32_16x16x32_bf16 v[36:39], v[100:103], a[224:227], v[36:39]\n"
"v_exp_f32 v53, v53\n"
"v_mfma_f32_16x16x32_bf16 v[40:43], v[100:103], a[240:243], v[40:43]\n"
"ds_read_b128 v[120:123], v2 offset:24576\n"
"v_cvt_pk_bf16_f32 v63, v50, v51\n"
"s_waitcnt lgkmcnt(2)\n"
"v_mfma_f32_16x16x32_bf16 v[32:35], v[104:107], a[212:215], v[32:35]\n"
"v_exp_f32 v54, v54\n"
"v_mfma_f32_16x16x32_bf16 v[36:39], v[104:107], a[228:231], v[36:39]\n"
"v_exp_f32 v55, v55\n"
"v_mfma_f32_16x16x32_bf16 v[40:43], v[104:107], a[244:247], v[40:43]\n"
"v_add_f32 v206, v206, v48\n"
"v_add_f32 v206, v206, v49\n"
"v_mfma_f32_16x16x32_bf16 v[32:35], v[108:111], a[216:219], v[32:35]\n"
"v_add_f32 v206, v206, v50\n"
"v_add_f32 v206, v206, v51\n"
"v_mfma_f32_16x16x32_bf16 v[36:39], v[108:111], a[232:235], v[36:39]\n"
"v_cvt_pk_bf16_f32 v66, v52, v53\n"
"v_cvt_pk_bf16_f32 v67, v54, v55\n"
"v_mfma_f32_16x16x32_bf16 v[40:43], v[108:111], a[248:251], v[40:43]\n"
"v_add_f32 v207, v207, v52\n"
"v_add_f32 v207, v207, v53\n"
"v_mfma_f32_16x16x32_bf16 v[44:47], v[112:115], v[144:147], v[20:23]\n"
"v_add_f32 v207, v207, v54\n"
"v_add_f32 v207, v207, v55\n"
"v_mfma_f32_16x16x32_bf16 v[48:51], v[112:115], v[160:163], v[24:27]\n"
"ds_read_b128 v[124:127], v3 offset:24576\n"
"ds_read_b128 v[128:131], v0 offset:24832\n"
"v_mfma_f32_16x16x32_bf16 v[52:55], v[112:115], v[176:179], v[28:31]\n"
"ds_read_b128 v[132:135], v1 offset:24832\n"
"s_waitcnt lgkmcnt(3)\n"
"v_mfma_f32_16x16x32_bf16 v[44:47], v[116:119], v[148:151], v[44:47]\n"
"s_add_u32 m0, s40, 0\n"
"v_max3_f32 v196, v32, v33, v34\n"
"v_mfma_f32_16x16x32_bf16 v[48:51], v[116:119], v[164:167], v[48:51]\n"
"v_max3_f32 v196, v196, v35, v36\n"
"v_max3_f32 v196, v196, v37, v38\n"
"v_mfma_f32_16x16x32_bf16 v[52:55], v[116:119], v[180:183], v[52:55]\n"
"ds_read_b128 v[136:139], v2 offset:24832\n"
"v_max3_f32 v196, v196, v39, v40\n"
"v_mfma_f32_16x16x32_bf16 v[44:47], v[120:123], v[152:155], v[44:47]\n"
"v_max3_f32 v196, v196, v41, v42\n"
"v_max3_f32 v196, v196, v43, v43\n"
"v_mfma_f32_16x16x32_bf16 v[48:51], v[120:123], v[168:171], v[48:51]\n"
"ds_read_b64_tr_b16 v[80:81], v4 offset:32768\n"
"ds_read_b64_tr_b16 v[82:83], v4 offset:40960\n"
"v_mfma_f32_16x16x32_bf16 v[52:55], v[120:123], v[184:187], v[52:55]\n"
"ds_read_b128 v[140:143], v3 offset:24832\n"
"buffer_load_dwordx4 v12, %0, s41 offen offset:0 lds\n"
"s_waitcnt lgkmcnt(4)\n"
"v_mfma_f32_16x16x32_bf16 v[44:47], v[124:127], v[156:159], v[44:47]\n"
"ds_read_b64_tr_b16 v[84:85], v5 offset:32768\n"
"ds_read_b64_tr_b16 v[86:87], v5 offset:40960\n"
"v_mfma_f32_16x16x32_bf16 v[48:51], v[124:127], v[172:175], v[48:51]\n"
"v_mfma_f32_16x16x32_bf16 v[52:55], v[124:127], v[188:191], v[52:55]\n"
"buffer_load_dwordx4 v13, %0, s41 offen offset:1024 lds\n"
"v_mfma_f32_16x16x32_bf16 v[44:47], v[128:131], a[204:207], v[44:47]\n"
"ds_read_b64_tr_b16 v[88:89], v6 offset:32768\n"
"ds_read_b64_tr_b16 v[90:91], v6 offset:40960\n"
"v_mfma_f32_16x16x32_bf16 v[48:51], v[128:131], a[220:223], v[48:51]\n"
"v_mfma_f32_16x16x32_bf16 v[52:55], v[128:131], a[236:239], v[52:55]\n"
"v_mfma_f32_16x16x32_bf16 v[44:47], v[132:135], a[208:211], v[44:47]\n"
"buffer_load_dwordx4 v14, %0, s41 offen offset:2048 lds\n"
"ds_read_b64_tr_b16 v[92:93], v7 offset:32768\n"
"v_mfma_f32_16x16x32_bf16 v[48:51], v[132:135], a[224:227], v[48:51]\n"
"ds_read_b64_tr_b16 v[94:95], v7 offset:40960\n"
"v_mfma_f32_16x16x32_bf16 v[52:55], v[132:135], a[240:243], v[52:55]\n"
"s_waitcnt lgkmcnt(4)\n"
"v_mfma_f32_16x16x32_bf16 v[44:47], v[136:139], a[212:215], v[44:47]\n"
"ds_read_b64_tr_b16 v[96:97], v8 offset:32768\n"
"ds_read_b64_tr_b16 v[98:99], v8 offset:40960\n"
"v_mfma_f32_16x16x32_bf16 v[48:51], v[136:139], a[228:231], v[48:51]\n"
"buffer_load_dwordx4 v15, %0, s41 offen offset:3072 lds\n"
"v_mfma_f32_16x16x32_bf16 v[52:55], v[136:139], a[244:247], v[52:55]\n"
"v_mfma_f32_16x16x32_bf16 v[44:47], v[140:143], a[216:219], v[44:47]\n"
"ds_read_b64_tr_b16 v[100:101], v9 offset:32768\n"
"ds_read_b64_tr_b16 v[102:103], v9 offset:40960\n"
"v_mfma_f32_16x16x32_bf16 v[48:51], v[140:143], a[232:235], v[48:51]\n"
"v_mfma_f32_16x16x32_bf16 v[52:55], v[140:143], a[248:251], v[52:55]\n"
"s_add_u32 s41, s41, 16384\n"
"v_mfma_f32_16x16x32_bf16 a[0:3], v[80:83], v[56:59], a[0:3]\n"
"ds_read_b64_tr_b16 v[104:105], v10 offset:32768\n"
"ds_read_b64_tr_b16 v[106:107], v10 offset:40960\n"
"v_mfma_f32_16x16x32_bf16 a[4:7], v[80:83], v[60:63], a[4:7]\n"
"v_mfma_f32_16x16x32_bf16 a[8:11], v[80:83], v[64:67], a[8:11]\n"
"v_mfma_f32_16x16x32_bf16 a[12:15], v[84:87], v[56:59], a[12:15]\n"
"s_and_b32 s41, s41, 0xfffff\n"
"ds_read_b64_tr_b16 v[108:109], v11 offset:32768\n"
"v_mfma_f32_16x16x32_bf16 a[16:19], v[84:87], v[60:63], a[16:19]\n"
"ds_read_b64_tr_b16 v[110:111], v11 offset:40960\n"
"v_max3_f32 v196, v196, v44, v45\n"
"v_mfma_f32_16x16x32_bf16 a[20:23], v[84:87], v[64:67], a[20:23]\n"
"v_max3_f32 v196, v196, v46, v47\n"
"v_max3_f32 v196, v196, v48, v49\n"
"s_waitcnt lgkmcnt(4)\n"
"v_mfma_f32_16x16x32_bf16 a[24:27], v[88:91], v[56:59], a[24:27]\n"
"v_max3_f32 v196, v196, v50, v51\n"
"v_max3_f32 v196, v196, v52, v53\n"
"v_mfma_f32_16x16x32_bf16 a[28:31], v[88:91], v[60:63], a[28:31]\n"
"v_max3_f32 v196, v196, v54, v55\n"
"v_cmp_lt_f32 vcc, 0x41000000, v196\n"
"v_mfma_f32_16x16x32_bf16 a[32:35], v[88:91], v[64:67], a[32:35]\n"
"s_cbranch_vccnz LSTUB1_%=\n"
"LRET1_%=:\n"
"ds_read_b64_tr_b16 v[112:113], v4 offset:33024\n"
"v_mfma_f32_16x16x32_bf16 a[36:39], v[92:95], v[56:59], a[36:39]\n"
"ds_read_b64_tr_b16 v[114:115], v4 offset:41216\n"
"ds_read_b64_tr_b16 v[116:117], v5 offset:33024\n"
"v_mfma_f32_16x16x32_bf16 a[40:43], v[92:95], v[60:63], a[40:43]\n"
"ds_read_b64_tr_b16 v[118:119], v5 offset:41216\n"
"v_mfma_f32_16x16x32_bf16 a[44:47], v[92:95], v[64:67], a[44:47]\n"
"v_exp_f32 v32, v32\n"
"v_mfma_f32_16x16x32_bf16 a[48:51], v[96:99], v[56:59], a[48:51]\n"
"s_add_u32 m0, s40, 81920\n"
"ds_read_b64_tr_b16 v[120:121], v6 offset:33024\n"
"v_mfma_f32_16x16x32_bf16 a[52:55], v[96:99], v[60:63], a[52:55]\n"
"ds_read_b64_tr_b16 v[122:123], v6 offset:41216\n"
"v_add_f32 v205, v205, v32\n"
"v_mfma_f32_16x16x32_bf16 a[56:59], v[96:99], v[64:67], a[56:59]\n"
"v_exp_f32 v33, v33\n"
"v_mfma_f32_16x16x32_bf16 a[60:63], v[100:103], v[56:59], a[60:63]\n"
"ds_read_b64_tr_b16 v[124:125], v7 offset:33024\n"
"ds_read_b64_tr_b16 v[126:127], v7 offset:41216\n"
"v_mfma_f32_16x16x32_bf16 a[64:67], v[100:103], v[60:63], a[64:67]\n"
"v_exp_f32 v34, v34\n"
"v_mfma_f32_16x16x32_bf16 a[68:71], v[100:103], v[64:67], a[68:71]\n"
"buffer_load_dwordx4 v16, %1, s42 offen offset:0 lds\n"
"v_cvt_pk_bf16_f32 v68, v32, v33\n"
"s_waitcnt lgkmcnt(3)\n"
"v_mfma_f32_16x16x32_bf16 a[72:75], v[104:107], v[56:59], a[72:75]\n"
"ds_read_b64_tr_b16 v[128:129], v8 offset:33024\n"
"ds_read_b64_tr_b16 v[130:131], v8 offset:41216\n"
"v_mfma_f32_16x16x32_bf16 a[76:79], v[104:107], v[60:63], a[76:79]\n"
"v_exp_f32 v35, v35\n"
"v_mfma_f32_16x16x32_bf16 a[80:83], v[104:107], v[64:67], a[80:83]\n"
"v_exp_f32 v36, v36\n"
"v_mfma_f32_16x16x32_bf16 a[84:87], v[108:111], v[56:59], a[84:87]\n"
"ds_read_b64_tr_b16 v[132:133], v9 offset:33024\n"
"ds_read_b64_tr_b16 v[134:135], v9 offset:41216\n"
"v_mfma_f32_16x16x32_bf16 a[88:91], v[108:111], v[60:63], a[88:91]\n"
"buffer_load_dwordx4 v17, %1, s42 offen offset:1024 lds\n"
"v_cvt_pk_bf16_f32 v69, v34, v35\n"
"v_mfma_f32_16x16x32_bf16 a[92:95], v[108:111], v[64:67], a[92:95]\n"
"v_exp_f32 v37, v37\n"
"v_mfma_f32_16x16x32_bf16 a[96:99], v[112:115], v[56:59], a[96:99]\n"
"ds_read_b64_tr_b16 v[136:137], v10 offset:33024\n"
"ds_read_b64_tr_b16 v[138:139], v10 offset:41216\n"
"v_mfma_f32_16x16x32_bf16 a[100:103], v[112:115], v[60:63], a[100:103]\n"
"v_exp_f32 v38, v38\n"
"v_mfma_f32_16x16x32_bf16 a[104:107], v[112:115], v[64:67], a[104:107]\n"
"v_exp_f32 v39, v39\n"
"v_mfma_f32_16x16x32_bf16 a[108:111], v[116:119], v[56:59], a[108:111]\n"
"ds_read_b64_tr_b16 v[140:141], v11 offset:33024\n"
"ds_read_b64_tr_b16 v[142:143], v11 offset:41216\n"
"v_mfma_f32_16x16x32_bf16 a[112:115], v[116:119], v[60:63], a[112:115]\n"
"s_memtime s[52:53]\n"
"buffer_load_dwordx4 v18, %1, s42 offen offset:2048 lds\n"
"v_mfma_f32_16x16x32_bf16 a[116:119], v[116:119], v[64:67], a[116:119]\n"
"v_exp_f32 v40, v40\n"
"s_waitcnt lgkmcnt(3)\n"
"v_mfma_f32_16x16x32_bf16 a[120:123], v[120:123], v[56:59], a[120:123]\n"
"v_exp_f32 v41, v41\n"
"v_mfma_f32_16x16x32_bf16 a[124:127], v[120:123], v[60:63], a[124:127]\n"
"v_exp_f32 v42, v42\n"
"v_mfma_f32_16x16x32_bf16 a[128:131], v[120:123], v[64:67], a[128:131]\n"
"buffer_load_dwordx4 v19, %1, s42 offen offset:3072 lds\n"
"v_add_f32 v205, v205, v33\n"
"v_mfma_f32_16x16x32_bf16 a[132:135], v[124:127], v[56:59], a[132:135]\n"
"v_exp_f32 v43, v43\n"
"v_mfma_f32_16x16x32_bf16 a[136:139], v[124:127], v[60:63], a[136:139]\n"
"v_add_f32 v205, v205, v34\n"
"v_add_f32 v205, v205, v35\n"
"v_mfma_f32_16x16x32_bf16 a[140:143], v[124:127], v[64:67], a[140:143]\n"
"v_cvt_pk_bf16_f32 v72, v36, v37\n"
"v_cvt_pk_bf16_f32 v73, v38, v39\n"
"v_mfma_f32_16x16x32_bf16 a[144:147], v[128:131], v[56:59], a[144:147]\n"
"v_add_f32 v206, v206, v36\n"
"v_add_f32 v206, v206, v37\n"
"v_mfma_f32_16x16x32_bf16 a[148:151], v[128:131], v[60:63], a[148:151]\n"
"s_add_u32 s42, s42, 16384\n"
"v_add_f32 v206, v206, v38\n"
"v_mfma_f32_16x16x32_bf16 a[152:155], v[128:131], v[64:67], a[152:155]\n"
"s_waitcnt vmcnt(16) lgkmcnt(0)\n"
"s_barrier\n"
"v_mfma_f32_16x16x32_bf16 a[156:159], v[132:135], v[56:59], a[156:159]\n"
"ds_read_b128 v[80:83], v0 offset:32768\n"
"ds_read_b128 v[84:87], v1 offset:32768\n"
"v_mfma_f32_16x16x32_bf16 a[160:163], v[132:135], v[60:63], a[160:163]\n"
"ds_read_b128 v[88:91], v2 offset:32768\n"
"v_add_f32 v206, v206, v39\n"
"v_mfma_f32_16x16x32_bf16 a[164:167], v[132:135], v[64:67], a[164:167]\n"
"v_cvt_pk_bf16_f32 v76, v40, v41\n"
"v_cvt_pk_bf16_f32 v77, v42, v43\n"
"v_mfma_f32_16x16x32_bf16 a[168:171], v[136:139], v[56:59], a[168:171]\n"
"v_add_f32 v207, v207, v40\n"
"v_add_f32 v207, v207, v41\n"
"v_mfma_f32_16x16x32_bf16 a[172:175], v[136:139], v[60:63], a[172:175]\n"
"v_add_f32 v207, v207, v42\n"
"v_add_f32 v207, v207, v43\n"
"v_mfma_f32_16x16x32_bf16 a[176:179], v[136:139], v[64:67], a[176:179]\n"
"s_sub_u32 s56, s52, s54\n"
"s_mov_b32 s54, s52\n"
"v_mfma_f32_16x16x32_bf16 a[180:183], v[140:143], v[56:59], a[180:183]\n"
"s_sub_u32 s56, s56, s55\n"
"s_cmp_gt_u32 s56, 2100\n"
"v_mfma_f32_16x16x32_bf16 a[184:187], v[140:143], v[60:63], a[184:187]\n"
"s_cselect_b32 s56, 128, -16\n"
"s_add_i32 s55, s55, s56\n"
"v_mfma_f32_16x16x32_bf16 a[188:191], v[140:143], v[64:67], a[188:191]\n"
"s_max_i32 s55, s55, s57\n"
"s_min_i32 s55, s55, 512\n"
"s_lshr_b32 s56, s55, 6\n"
"s_cmp_lg_u32 s56, 0\n"
"s_cbranch_scc1 LY5_%=\n"
"LZ5_%=:\n"
"s_and_b32 s42, s42, 0xfffff\n"
"s_waitcnt lgkmcnt(0)\n"
"v_mfma_f32_16x16x32_bf16 v[32:35], v[80:83], v[144:147], v[20:23]\n"
"ds_read_b128 v[92:95], v3 offset:32768\n"
"ds_read_b128 v[96:99], v0 offset:33024\n"
"v_mfma_f32_16x16x32_bf16 v[36:39], v[80:83], v[160:163], v[24:27]\n"
"v_exp_f32 v44, v44\n"
"v_mfma_f32_16x16x32_bf16 v[40:43], v[80:83], v[176:179], v[28:31]\n"
"ds_read_b128 v[100:103], v1 offset:33024\n"
"v_add_f32 v205, v205, v44\n"
"v_mfma_f32_16x16x32_bf16 v[32:35], v[84:87], v[148:151], v[32:35]\n"
"v_exp_f32 v45, v45\n"
"v_mfma_f32_16x16x32_bf16 v[36:39], v[84:87], v[164:167], v[36:39]\n"
"v_exp_f32 v46, v46\n"
"v_mfma_f32_16x16x32_bf16 v[40:43], v[84:87], v[180:183], v[40:43]\n"
"ds_read_b128 v[104:107], v2 offset:33024\n"
"v_cvt_pk_bf16_f32 v70, v44, v45\n"
"v_mfma_f32_16x16x32_bf16 v[32:35], v[88:91], v[152:155], v[32:35]\n"
"v_exp_f32 v47, v47\n"
"v_mfma_f32_16x16x32_bf16 v[36:39], v[88:91], v[168:171], v[36:39]\n"
"v_exp_f32 v48, v48\n"
"v_mfma_f32_16x16x32_bf16 v[40:43], v[88:91], v[184:187], v[40:43]\n"
"ds_read_b128 v[108:111], v3 offset:33024\n"
"v_cvt_pk_bf16_f32 v71, v46, v47\n"
"s_waitcnt lgkmcnt(2)\n"
"v_mfma_f32_16x16x32_bf16 v[32:35], v[92:95], v[156:159], v[32:35]\n"
"v_exp_f32 v49, v49\n"
"v_mfma_f32_16x16x32_bf16 v[36:39], v[92:95], v[172:175], v[36:39]\n"
"v_exp_f32 v50, v50\n"
"v_mfma_f32_16x16x32_bf16 v[40:43], v[92:95], v[188:191], v[40:43]\n"
"ds_read_b128 v[112:115], v0 offset:40960\n"
"v_add_f32 v205, v205, v45\n"
"v_mfma_f32_16x16x32_bf16 v[32:35], v[96:99], a[204:207], v[32:35]\n"
"v_exp_f32 v51, v51\n"
"v_mfma_f32_16x16x32_bf16 v[36:39], v[96:99], a[220:223], v[36:39]\n"
"v_add_f32 v205, v205, v46\n"
"v_add_f32 v205, v205, v47\n"
"v_mfma_f32_16x16x32_bf16 v[40:43], v[96:99], a[236:239], v[40:43]\n"
"ds_read_b128 v[116:119], v1 offset:40960\n"
"v_cvt_pk_bf16_f32 v74, v48, v49\n"
"v_mfma_f32_16x16x32_bf16 v[32:35], v[100:103], a[208:211], v[32:35]\n"
"v_exp_f32 v52, v52\n"
"v_mfma_f32_16x16x32_bf16 v[36:39], v[100:103], a[224:227], v[36:39]\n"
"v_exp_f32 v53, v53\n"
"v_mfma_f32_16x16x32_bf16 v[40:43], v[100:103], a[240:243], v[40:43]\n"
"ds_read_b128 v[120:123], v2 offset:40960\n"
"v_cvt_pk_bf16_f32 v75, v50, v51\n"
"s_waitcnt lgkmcnt(2)\n"
"v_mfma_f32_16x16x32_bf16 v[32:35], v[104:107], a[212:215], v[32:35]\n"
"v_exp_f32 v54, v54\n"
"v_mfma_f32_16x16x32_bf16 v[36:39], v[104:107], a[228:231], v[36:39]\n"
"v_exp_f32 v55, v55\n"
"v_mfma_f32_16x16x32_bf16 v[40:43], v[104:107], a[244:247], v[40:43]\n"
"v_add_f32 v206, v206, v48\n"
"v_add_f32 v206, v206, v49\n"
"v_mfma_f32_16x16x32_bf16 v[32:35], v[108:111], a[216:219], v[32:35]\n"
"v_add_f32 v206, v206, v50\n"
"v_add_f32 v206, v206, v51\n"
"v_mfma_f32_16x16x32_bf16 v[36:39], v[108:111], a[232:235], v[36:39]\n"
"v_cvt_pk_bf16_f32 v78, v52, v53\n"
"v_cvt_pk_bf16_f32 v79, v54, v55\n"
"v_mfma_f32_16x16x32_bf16 v[40:43], v[108:111], a[248:251], v[40:43]\n"
"v_add_f32 v207, v207, v52\n"
"v_add_f32 v207, v207, v53\n"
"v_mfma_f32_16x16x32_bf16 v[44:47], v[112:115], v[144:147], v[20:23]\n"
"v_add_f32 v207, v207, v54\n"
"v_add_f32 v207, v207, v55\n"
"v_mfma_f32_16x16x32_bf16 v[48:51], v[112:115], v[160:163], v[24:27]\n"
"ds_read_b128 v[124:127], v3 offset:40960\n"
"ds_read_b128 v[128:131], v0 offset:41216\n"
"v_mfma_f32_16x16x32_bf16 v[52:55], v[112:115], v[176:179], v[28:31]\n"
"ds_read_b128 v[132:135], v1 offset:41216\n"
"s_waitcnt lgkmcnt(3)\n"
"v_mfma_f32_16x16x32_bf16 v[44:47], v[116:119], v[148:151], v[44:47]\n"
"s_add_u32 m0, s40, 16384\n"
"v_max3_f32 v196, v32, v33, v34\n"
"v_mfma_f32_16x16x32_bf16 v[48:51], v[116:119], v[164:167], v[48:51]\n"
"v_max3_f32 v196, v196, v35, v36\n"
"v_max3_f32 v196, v196, v37, v38\n"
"v_mfma_f32_16x16x32_bf16 v[52:55], v[116:119], v[180:183], v[52:55]\n"
"ds_read_b128 v[136:139], v2 offset:41216\n"
"v_max3_f32 v196, v196, v39, v40\n"
"v_mfma_f32_16x16x32_bf16 v[44:47], v[120:123], v[152:155], v[44:47]\n"
"v_max3_f32 v196, v196, v41, v42\n"
"v_max3_f32 v196, v196, v43, v43\n"
"v_mfma_f32_16x16x32_bf16 v[48:51], v[120:123], v[168:171], v[48:51]\n"
"ds_read_b64_tr_b16 v[80:81], v4 offset:49152\n"
"ds_read_b64_tr_b16 v[82:83], v4 offset:57344\n"
"v_mfma_f32_16x16x32_bf16 v[52:55], v[120:123], v[184:187], v[52:55]\n"
"ds_read_b128 v[140:143], v3 offset:41216\n"
"buffer_load_dwordx4 v12, %0, s41 offen offset:0 lds\n"
"s_waitcnt lgkmcnt(4)\n"
"v_mfma_f32_16x16x32_bf16 v[44:47], v[124:127], v[156:159], v[44:47]\n"
"ds_read_b64_tr_b16 v[84:85], v5 offset:49152\n"
"ds_read_b64_tr_b16 v[86:87], v5 offset:57344\n"
"v_mfma_f32_16x16x32_bf16 v[48:51], v[124:127], v[172:175], v[48:51]\n"
"v_mfma_f32_16x16x32_bf16 v[52:55], v[124:127], v[188:191], v[52:55]\n"
"buffer_load_dwordx4 v13, %0, s41 offen offset:1024 lds\n"
"v_mfma_f32_16x16x32_bf16 v[44:47], v[128:131], a[204:207], v[44:47]\n"
"ds_read_b64_tr_b16 v[88:89], v6 offset:49152\n"
"ds_read_b64_tr_b16 v[90:91], v6 offset:57344\n"
"v_mfma_f32_16x16x32_bf16 v[48:51], v[128:131], a[220:223], v[48:51]\n"
"v_mfma_f32_16x16x32_bf16 v[52:55], v[128:131], a[236:239], v[52:55]\n"
"v_mfma_f32_16x16x32_bf16 v[44:47], v[132:135], a[208:211], v[44:47]\n"
"buffer_load_dwordx4 v14, %0, s41 offen offset:2048 lds\n"
"ds_read_b64_tr_b16 v[92:93], v7 offset:49152\n"
"v_mfma_f32_16x16x32_bf16 v[48:51], v[132:135], a[224:227], v[48:51]\n"
"ds_read_b64_tr_b16 v[94:95], v7 offset:57344\n"
"v_mfma_f32_16x16x32_bf16 v[52:55], v[132:135], a[240:243], v[52:55]\n"
"s_waitcnt lgkmcnt(4)\n"
"v_mfma_f32_16x16x32_bf16 v[44:47], v[136:139], a[212:215], v[44:47]\n"
"ds_read_b64_tr_b16 v[96:97], v8 offset:49152\n"
"ds_read_b64_tr_b16 v[98:99], v8 offset:57344\n"
"v_mfma_f32_16x16x32_bf16 v[48:51], v[136:139], a[228:231], v[48:51]\n"
"buffer_load_dwordx4 v15, %0, s41 offen offset:3072 lds\n"
"v_mfma_f32_16x16x32_bf16 v[52:55], v[136:139], a[244:247], v[52:55]\n"
"v_mfma_f32_16x16x32_bf16 v[44:47], v[140:143], a[216:219], v[44:47]\n"
"ds_read_b64_tr_b16 v[100:101], v9 offset:49152\n"
"ds_read_b64_tr_b16 v[102:103], v9 offset:57344\n"
"v_mfma_f32_16x16x32_bf16 v[48:51], v[140:143], a[232:235], v[48:51]\n"
"v_mfma_f32_16x16x32_bf16 v[52:55], v[140:143], a[248:251], v[52:55]\n"
"s_add_u32 s41, s41, 16384\n"
"v_mfma_f32_16x16x32_bf16 a[0:3], v[80:83], v[68:71], a[0:3]\n"
"ds_read_b64_tr_b16 v[104:105], v10 offset:49152\n"
"ds_read_b64_tr_b16 v[106:107], v10 offset:57344\n"
"v_mfma_f32_16x16x32_bf16 a[4:7], v[80:83], v[72:75], a[4:7]\n"
"v_mfma_f32_16x16x32_bf16 a[8:11], v[80:83], v[76:79], a[8:11]\n"
"v_mfma_f32_16x16x32_bf16 a[12:15], v[84:87], v[68:71], a[12:15]\n"
"s_and_b32 s41, s41, 0xfffff\n"
"ds_read_b64_tr_b16 v[108:109], v11 offset:49152\n"
"v_mfma_f32_16x16x32_bf16 a[16:19], v[84:87], v[72:75], a[16:19]\n"
"ds_read_b64_tr_b16 v[110:111], v11 offset:57344\n"
"v_max3_f32 v196, v196, v44, v45\n"
"v_mfma_f32_16x16x32_bf16 a[20:23], v[84:87], v[76:79], a[20:23]\n"
"v_max3_f32 v196, v196, v46, v47\n"
"v_max3_f32 v196, v196, v48, v49\n"
"s_waitcnt lgkmcnt(4)\n"
"v_mfma_f32_16x16x32_bf16 a[24:27], v[88:91], v[68:71], a[24:27]\n"
"v_max3_f32 v196, v196, v50, v51\n"
"v_max3_f32 v196, v196, v52, v53\n"
"v_mfma_f32_16x16x32_bf16 a[28:31], v[88:91], v[72:75], a[28:31]\n"
"v_max3_f32 v196, v196, v54, v55\n"
"v_cmp_lt_f32 vcc, 0x41000000, v196\n"
"v_mfma_f32_16x16x32_bf16 a[32:35], v[88:91], v[76:79], a[32:35]\n"
"s_cbranch_vccnz LSTUB2_%=\n"
"LRET2_%=:\n"
"ds_read_b64_tr_b16 v[112:113], v4 offset:49408\n"
"v_mfma_f32_16x16x32_bf16 a[36:39], v[92:95], v[68:71], a[36:39]\n"
"ds_read_b64_tr_b16 v[114:115], v4 offset:57600\n"
"ds_read_b64_tr_b16 v[116:117], v5 offset:49408\n"
"v_mfma_f32_16x16x32_bf16 a[40:43], v[92:95], v[72:75], a[40:43]\n"
"ds_read_b64_tr_b16 v[118:119], v5 offset:57600\n"
"v_mfma_f32_16x16x32_bf16 a[44:47], v[92:95], v[76:79], a[44:47]\n"
"v_exp_f32 v32, v32\n"
"v_mfma_f32_16x16x32_bf16 a[48:51], v[96:99], v[68:71], a[48:51]\n"
"s_add_u32 m0, s40, 98304\n"
"ds_read_b64_tr_b16 v[120:121], v6 offset:49408\n"
"v_mfma_f32_16x16x32_bf16 a[52:55], v[96:99], v[72:75], a[52:55]\n"
"ds_read_b64_tr_b16 v[122:123], v6 offset:57600\n"
"v_add_f32 v205, v205, v32\n"
"v_mfma_f32_16x16x32_bf16 a[56:59], v[96:99], v[76:79], a[56:59]\n"
"v_exp_f32 v33, v33\n"
"v_mfma_f32_16x16x32_bf16 a[60:63], v[100:103], v[68:71], a[60:63]\n"
"ds_read_b64_tr_b16 v[124:125], v7 offset:49408\n"
"ds_read_b64_tr_b16 v[126:127], v7 offset:57600\n"
"v_mfma_f32_16x16x32_bf16 a[64:67], v[100:103], v[72:75], a[64:67]\n"
"v_exp_f32 v34, v34\n"
"v_mfma_f32_16x16x32_bf16 a[68:71], v[100:103], v[76:79], a[68:71]\n"
"buffer_load_dwordx4 v16, %1, s42 offen offset:0 lds\n"
"v_cvt_pk_bf16_f32 v56, v32, v33\n"
"s_waitcnt lgkmcnt(3)\n"
"v_mfma_f32_16x16x32_bf16 a[72:75], v[104:107], v[68:71], a[72:75]\n"
"ds_read_b64_tr_b16 v[128:129], v8 offset:49408\n"
"ds_read_b64_tr_b16 v[130:131], v8 offset:57600\n"
"v_mfma_f32_16x16x32_bf16 a[76:79], v[104:107], v[72:75], a[76:79]\n"
"v_exp_f32 v35, v35\n"
"v_mfma_f32_16x16x32_bf16 a[80:83], v[104:107], v[76:79], a[80:83]\n"
"v_exp_f32 v36, v36\n"
"v_mfma_f32_16x16x32_bf16 a[84:87], v[108:111], v[68:71], a[84:87]\n"
"ds_read_b64_tr_b16 v[132:133], v9 offset:49408\n"
"ds_read_b64_tr_b16 v[134:135], v9 offset:57600\n"
"v_mfma_f32_16x16x32_bf16 a[88:91], v[108:111], v[72:75], a[88:91]\n"
"buffer_load_dwordx4 v17, %1, s42 offen offset:1024 lds\n"
"v_cvt_pk_bf16_f32 v57, v34, v35\n"
"v_mfma_f32_16x16x32_bf16 a[92:95], v[108:111], v[76:79], a[92:95]\n"
"v_exp_f32 v37, v37\n"
"v_mfma_f32_16x16x32_bf16 a[96:99], v[112:115], v[68:71], a[96:99]\n"
"ds_read_b64_tr_b16 v[136:137], v10 offset:49408\n"
"ds_read_b64_tr_b16 v[138:139], v10 offset:57600\n"
"v_mfma_f32_16x16x32_bf16 a[100:103], v[112:115], v[72:75], a[100:103]\n"
"v_exp_f32 v38, v38\n"
"v_mfma_f32_16x16x32_bf16 a[104:107], v[112:115], v[76:79], a[104:107]\n"
"v_exp_f32 v39, v39\n"
"v_mfma_f32_16x16x32_bf16 a[108:111], v[116:119], v[68:71], a[108:111]\n"
"ds_read_b64_tr_b16 v[140:141], v11 offset:49408\n"
"ds_read_b64_tr_b16 v[142:143], v11 offset:57600\n"
"v_mfma_f32_16x16x32_bf16 a[112:115], v[116:119], v[72:75], a[112:115]\n"
"s_memtime s[52:53]\n"
"buffer_load_dwordx4 v18, %1, s42 offen offset:2048 lds\n"
"v_mfma_f32_16x16x32_bf16 a[116:119], v[116:119], v[76:79], a[116:119]\n"
"v_exp_f32 v40, v40\n"
"s_waitcnt lgkmcnt(3)\n"
"v_mfma_f32_16x16x32_bf16 a[120:123], v[120:123], v[68:71], a[120:123]\n"
"v_exp_f32 v41, v41\n"
"v_mfma_f32_16x16x32_bf16 a[124:127], v[120:123], v[72:75], a[124:127]\n"
"v_exp_f32 v42, v42\n"
"v_mfma_f32_16x16x32_bf16 a[128:131], v[120:123], v[76:79], a[128:131]\n"
"buffer_load_dwordx4 v19, %1, s42 offen offset:3072 lds\n"
"v_add_f32 v205, v205, v33\n"
"v_mfma_f32_16x16x32_bf16 a[132:135], v[124:127], v[68:71], a[132:135]\n"
"v_exp_f32 v43, v43\n"
"v_mfma_f32_16x16x32_bf16 a[136:139], v[124:127], v[72:75], a[136:139]\n"
"v_add_f32 v205, v205, v34\n"
"v_add_f32 v205, v205, v35\n"
"v_mfma_f32_16x16x32_bf16 a[140:143], v[124:127], v[76:79], a[140:143]\n"
"v_cvt_pk_bf16_f32 v60, v36, v37\n"
"v_cvt_pk_bf16_f32 v61, v38, v39\n"
"v_mfma_f32_16x16x32_bf16 a[144:147], v[128:131], v[68:71], a[144:147]\n"
"v_add_f32 v206, v206, v36\n"
"v_add_f32 v206, v206, v37\n"
"v_mfma_f32_16x16x32_bf16 a[148:151], v[128:131], v[72:75], a[148:151]\n"
"s_add_u32 s42, s42, 16384\n"
"v_add_f32 v206, v206, v38\n"
"v_mfma_f32_16x16x32_bf16 a[152:155], v[128:131], v[76:79], a[152:155]\n"
"s_waitcnt vmcnt(16) lgkmcnt(0)\n"
"s_barrier\n"
"v_mfma_f32_16x16x32_bf16 a[156:159], v[132:135], v[68:71], a[156:159]\n"
"ds_read_b128 v[80:83], v0 offset:49152\n"
"ds_read_b128 v[84:87], v1 offset:49152\n"
"v_mfma_f32_16x16x32_bf16 a[160:163], v[132:135], v[72:75], a[160:163]\n"
"ds_read_b128 v[88:91], v2 offset:49152\n"
"v_add_f32 v206, v206, v39\n"
"v_mfma_f32_16x16x32_bf16 a[164:167], v[132:135], v[76:79], a[164:167]\n"
"v_cvt_pk_bf16_f32 v64, v40, v41\n"
"v_cvt_pk_bf16_f32 v65, v42, v43\n"
"v_mfma_f32_16x16x32_bf16 a[168:171], v[136:139], v[68:71], a[168:171]\n"
"v_add_f32 v207, v207, v40\n"
"v_add_f32 v207, v207, v41\n"
"v_mfma_f32_16x16x32_bf16 a[172:175], v[136:139], v[72:75], a[172:175]\n"
"v_add_f32 v207, v207, v42\n"
"v_add_f32 v207, v207, v43\n"
"v_mfma_f32_16x16x32_bf16 a[176:179], v[136:139], v[76:79], a[176:179]\n"
"s_sub_u32 s56, s52, s54\n"
"s_mov_b32 s54, s52\n"
"v_mfma_f32_16x16x32_bf16 a[180:183], v[140:143], v[68:71], a[180:183]\n"
"s_sub_u32 s56, s56, s55\n"
"s_cmp_gt_u32 s56, 2100\n"
"v_mfma_f32_16x16x32_bf16 a[184:187], v[140:143], v[72:75], a[184:187]\n"
"s_cselect_b32 s56, 128, -16\n"
"s_add_i32 s55, s55, s56\n"
"v_mfma_f32_16x16x32_bf16 a[188:191], v[140:143], v[76:79], a[188:191]\n"
"s_max_i32 s55, s55, s57\n"
"s_min_i32 s55, s55, 512\n"
"s_lshr_b32 s56, s55, 6\n"
"s_cmp_lg_u32 s56, 0\n"
"s_cbranch_scc1 LY6_%=\n"
"LZ6_%=:\n"
"s_and_b32 s42, s42, 0xfffff\n"
"s_waitcnt lgkmcnt(0)\n"
"v_mfma_f32_16x16x32_bf16 v[32:35], v[80:83], v[144:147], v[20:23]\n"
"ds_read_b128 v[92:95], v3 offset:49152\n"
"ds_read_b128 v[96:99], v0 offset:49408\n"
"v_mfma_f32_16x16x32_bf16 v[36:39], v[80:83], v[160:163], v[24:27]\n"
"v_exp_f32 v44, v44\n"
"v_mfma_f32_16x16x32_bf16 v[40:43], v[80:83], v[176:179], v[28:31]\n"
"ds_read_b128 v[100:103], v1 offset:49408\n"
"v_add_f32 v205, v205, v44\n"
"v_mfma_f32_16x16x32_bf16 v[32:35], v[84:87], v[148:151], v[32:35]\n"
"v_exp_f32 v45, v45\n"
"v_mfma_f32_16x16x32_bf16 v[36:39], v[84:87], v[164:167], v[36:39]\n"
"v_exp_f32 v46, v46\n"
"v_mfma_f32_16x16x32_bf16 v[40:43], v[84:87], v[180:183], v[40:43]\n"
"ds_read_b128 v[104:107], v2 offset:49408\n"
"v_cvt_pk_bf16_f32 v58, v44, v45\n"
"v_mfma_f32_16x16x32_bf16 v[32:35], v[88:91], v[152:155], v[32:35]\n"
"v_exp_f32 v47, v47\n"
"v_mfma_f32_16x16x32_bf16 v[36:39], v[88:91], v[168:171], v[36:39]\n"
"v_exp_f32 v48, v48\n"
"v_mfma_f32_16x16x32_bf16 v[40:43], v[88:91], v[184:187], v[40:43]\n"
"ds_read_b128 v[108:111], v3 offset:49408\n"
"v_cvt_pk_bf16_f32 v59, v46, v47\n"
"s_waitcnt lgkmcnt(2)\n"
"v_mfma_f32_16x16x32_bf16 v[32:35], v[92:95], v[156:159], v[32:35]\n"
"v_exp_f32 v49, v49\n"
"v_mfma_f32_16x16x32_bf16 v[36:39], v[92:95], v[172:175], v[36:39]\n"
"v_exp_f32 v50, v50\n"
"v_mfma_f32_16x16x32_bf16 v[40:43], v[92:95], v[188:191], v[40:43]\n"
"ds_read_b128 v[112:115], v0 offset:57344\n"
"v_add_f32 v205, v205, v45\n"
"v_mfma_f32_16x16x32_bf16 v[32:35], v[96:99], a[204:207], v[32:35]\n"
"v_exp_f32 v51, v51\n"
"v_mfma_f32_16x16x32_bf16 v[36:39], v[96:99], a[220:223], v[36:39]\n"
"v_add_f32 v205, v205, v46\n"
"v_add_f32 v205, v205, v47\n"
"v_mfma_f32_16x16x32_bf16 v[40:43], v[96:99], a[236:239], v[40:43]\n"
"ds_read_b128 v[116:119], v1 offset:57344\n"
"v_cvt_pk_bf16_f32 v62, v48, v49\n"
"v_mfma_f32_16x16x32_bf16 v[32:35], v[100:103], a[208:211], v[32:35]\n"
"v_exp_f32 v52, v52\n"
"v_mfma_f32_16x16x32_bf16 v[36:39], v[100:103], a[224:227], v[36:39]\n"
"v_exp_f32 v53, v53\n"
"v_mfma_f32_16x16x32_bf16 v[40:43], v[100:103], a[240:243], v[40:43]\n"
"ds_read_b128 v[120:123], v2 offset:57344\n"
"v_cvt_pk_bf16_f32 v63, v50, v51\n"
"s_waitcnt lgkmcnt(2)\n"
"v_mfma_f32_16x16x32_bf16 v[32:35], v[104:107], a[212:215], v[32:35]\n"
"v_exp_f32 v54, v54\n"
"v_mfma_f32_16x16x32_bf16 v[36:39], v[104:107], a[228:231], v[36:39]\n"
"v_exp_f32 v55, v55\n"
"v_mfma_f32_16x16x32_bf16 v[40:43], v[104:107], a[244:247], v[40:43]\n"
"v_add_f32 v206, v206, v48\n"
"v_add_f32 v206, v206, v49\n"
"v_mfma_f32_16x16x32_bf16 v[32:35], v[108:111], a[216:219], v[32:35]\n"
"v_add_f32 v206, v206, v50\n"
"v_add_f32 v206, v206, v51\n"
"v_mfma_f32_16x16x32_bf16 v[36:39], v[108:111], a[232:235], v[36:39]\n"
"v_cvt_pk_bf16_f32 v66, v52, v53\n"
"v_cvt_pk_bf16_f32 v67, v54, v55\n"
"v_mfma_f32_16x16x32_bf16 v[40:43], v[108:111], a[248:251], v[40:43]\n"
"v_add_f32 v207, v207, v52\n"
"v_add_f32 v207, v207, v53\n"
"v_mfma_f32_16x16x32_bf16 v[44:47], v[112:115], v[144:147], v[20:23]\n"
"v_add_f32 v207, v207, v54\n"
"v_add_f32 v207, v207, v55\n"
"v_mfma_f32_16x16x32_bf16 v[48:51], v[112:115], v[160:163], v[24:27]\n"
"ds_read_b128 v[124:127], v3 offset:57344\n"
"ds_read_b128 v[128:131], v0 offset:57600\n"
"v_mfma_f32_16x16x32_bf16 v[52:55], v[112:115], v[176:179], v[28:31]\n"
"ds_read_b128 v[132:135], v1 offset:57600\n"
"s_waitcnt lgkmcnt(3)\n"
"v_mfma_f32_16x16x32_bf16 v[44:47], v[116:119], v[148:151], v[44:47]\n"
"s_add_u32 m0, s40, 32768\n"
"v_max3_f32 v196, v32, v33, v34\n"
"v_mfma_f32_16x16x32_bf16 v[48:51], v[116:119], v[164:167], v[48:51]\n"
"v_max3_f32 v196, v196, v35, v36\n"
"v_max3_f32 v196, v196, v37, v38\n"
"v_mfma_f32_16x16x32_bf16 v[52:55], v[116:119], v[180:183], v[52:55]\n"
"ds_read_b128 v[136:139], v2 offset:57600\n"
"v_max3_f32 v196, v196, v39, v40\n"
"v_mfma_f32_16x16x32_bf16 v[44:47], v[120:123], v[152:155], v[44:47]\n"
"v_max3_f32 v196, v196, v41, v42\n"
"v_max3_f32 v196, v196, v43, v43\n"
"v_mfma_f32_16x16x32_bf16 v[48:51], v[120:123], v[168:171], v[48:51]\n"
"ds_read_b64_tr_b16 v[80:81], v4 offset:0\n"
"ds_read_b64_tr_b16 v[82:83], v4 offset:8192\n"
"v_mfma_f32_16x16x32_bf16 v[52:55], v[120:123], v[184:187], v[52:55]\n"
"ds_read_b128 v[140:143], v3 offset:57600\n"
"buffer_load_dwordx4 v12, %0, s41 offen offset:0 lds\n"
"s_waitcnt lgkmcnt(4)\n"
"v_mfma_f32_16x16x32_bf16 v[44:47], v[124:127], v[156:159], v[44:47]\n"
"ds_read_b64_tr_b16 v[84:85], v5 offset:0\n"
"ds_read_b64_tr_b16 v[86:87], v5 offset:8192\n"
"v_mfma_f32_16x16x32_bf16 v[48:51], v[124:127], v[172:175], v[48:51]\n"
"v_mfma_f32_16x16x32_bf16 v[52:55], v[124:127], v[188:191], v[52:55]\n"
"buffer_load_dwordx4 v13, %0, s41 offen offset:1024 lds\n"
"v_mfma_f32_16x16x32_bf16 v[44:47], v[128:131], a[204:207], v[44:47]\n"
"ds_read_b64_tr_b16 v[88:89], v6 offset:0\n"
"ds_read_b64_tr_b16 v[90:91], v6 offset:8192\n"
"v_mfma_f32_16x16x32_bf16 v[48:51], v[128:131], a[220:223], v[48:51]\n"
"v_mfma_f32_16x16x32_bf16 v[52:55], v[128:131], a[236:239], v[52:55]\n"
"v_mfma_f32_16x16x32_bf16 v[44:47], v[132:135], a[208:211], v[44:47]\n"
"buffer_load_dwordx4 v14, %0, s41 offen offset:2048 lds\n"
"ds_read_b64_tr_b16 v[92:93], v7 offset:0\n"
"v_mfma_f32_16x16x32_bf16 v[48:51], v[132:135], a[224:227], v[48:51]\n"
"ds_read_b64_tr_b16 v[94:95], v7 offset:8192\n"
"v_mfma_f32_16x16x32_bf16 v[52:55], v[132:135], a[240:243], v[52:55]\n"
"s_waitcnt lgkmcnt(4)\n"
"v_mfma_f32_16x16x32_bf16 v[44:47], v[136:139], a[212:215], v[44:47]\n"
"ds_read_b64_tr_b16 v[96:97], v8 offset:0\n"
"ds_read_b64_tr_b16 v[98:99], v8 offset:8192\n"
"v_mfma_f32_16x16x32_bf16 v[48:51], v[136:139], a[228:231], v[48:51]\n"
"buffer_load_dwordx4 v15, %0, s41 offen offset:3072 lds\n"
"v_mfma_f32_16x16x32_bf16 v[52:55], v[136:139], a[244:247], v[52:55]\n"
"v_mfma_f32_16x16x32_bf16 v[44:47], v[140:143], a[216:219], v[44:47]\n"
"ds_read_b64_tr_b16 v[100:101], v9 offset:0\n"
"ds_read_b64_tr_b16 v[102:103], v9 offset:8192\n"
"v_mfma_f32_16x16x32_bf16 v[48:51], v[140:143], a[232:235], v[48:51]\n"
"v_mfma_f32_16x16x32_bf16 v[52:55], v[140:143], a[248:251], v[52:55]\n"
"s_add_u32 s41, s41, 16384\n"
"v_mfma_f32_16x16x32_bf16 a[0:3], v[80:83], v[56:59], a[0:3]\n"
"ds_read_b64_tr_b16 v[104:105], v10 offset:0\n"
"ds_read_b64_tr_b16 v[106:107], v10 offset:8192\n"
"v_mfma_f32_16x16x32_bf16 a[4:7], v[80:83], v[60:63], a[4:7]\n"
"v_mfma_f32_16x16x32_bf16 a[8:11], v[80:83], v[64:67], a[8:11]\n"
"v_mfma_f32_16x16x32_bf16 a[12:15], v[84:87], v[56:59], a[12:15]\n"
"s_and_b32 s41, s41, 0xfffff\n"
"ds_read_b64_tr_b16 v[108:109], v11 offset:0\n"
"v_mfma_f32_16x16x32_bf16 a[16:19], v[84:87], v[60:63], a[16:19]\n"
"ds_read_b64_tr_b16 v[110:111], v11 offset:8192\n"
"v_max3_f32 v196, v196, v44, v45\n"
"v_mfma_f32_16x16x32_bf16 a[20:23], v[84:87], v[64:67], a[20:23]\n"
"v_max3_f32 v196, v196, v46, v47\n"
"v_max3_f32 v196, v196, v48, v49\n"
"s_waitcnt lgkmcnt(4)\n"
"v_mfma_f32_16x16x32_bf16 a[24:27], v[88:91], v[56:59], a[24:27]\n"
"v_max3_f32 v196, v196, v50, v51\n"
"v_max3_f32 v196, v196, v52, v53\n"
"v_mfma_f32_16x16x32_bf16 a[28:31], v[88:91], v[60:63], a[28:31]\n"
"v_max3_f32 v196, v196, v54, v55\n"
"v_cmp_lt_f32 vcc, 0x41000000, v196\n"
"v_mfma_f32_16x16x32_bf16 a[32:35], v[88:91], v[64:67], a[32:35]\n"
"s_cbranch_vccnz LSTUB3_%=\n"
"LRET3_%=:\n"
"ds_read_b64_tr_b16 v[112:113], v4 offset:256\n"
"v_mfma_f32_16x16x32_bf16 a[36:39], v[92:95], v[56:59], a[36:39]\n"
"ds_read_b64_tr_b16 v[114:115], v4 offset:8448\n"
"ds_read_b64_tr_b16 v[116:117], v5 offset:256\n"
"v_mfma_f32_16x16x32_bf16 a[40:43], v[92:95], v[60:63], a[40:43]\n"
"ds_read_b64_tr_b16 v[118:119], v5 offset:8448\n"
"v_mfma_f32_16x16x32_bf16 a[44:47], v[92:95], v[64:67], a[44:47]\n"
"v_exp_f32 v32, v32\n"
"v_mfma_f32_16x16x32_bf16 a[48:51], v[96:99], v[56:59], a[48:51]\n"
"s_add_u32 m0, s40, 114688\n"
"ds_read_b64_tr_b16 v[120:121], v6 offset:256\n"
"v_mfma_f32_16x16x32_bf16 a[52:55], v[96:99], v[60:63], a[52:55]\n"
"ds_read_b64_tr_b16 v[122:123], v6 offset:8448\n"
"v_add_f32 v205, v205, v32\n"
"v_mfma_f32_16x16x32_bf16 a[56:59], v[96:99], v[64:67], a[56:59]\n"
"v_exp_f32 v33, v33\n"
"v_mfma_f32_16x16x32_bf16 a[60:63], v[100:103], v[56:59], a[60:63]\n"
"ds_read_b64_tr_b16 v[124:125], v7 offset:256\n"
"ds_read_b64_tr_b16 v[126:127], v7 offset:8448\n"
"v_mfma_f32_16x16x32_bf16 a[64:67], v[100:103], v[60:63], a[64:67]\n"
"v_exp_f32 v34, v34\n"
"v_mfma_f32_16x16x32_bf16 a[68:71], v[100:103], v[64:67], a[68:71]\n"
"buffer_load_dwordx4 v16, %1, s42 offen offset:0 lds\n"
"v_cvt_pk_bf16_f32 v68, v32, v33\n"
"s_waitcnt lgkmcnt(3)\n"
"v_mfma_f32_16x16x32_bf16 a[72:75], v[104:107], v[56:59], a[72:75]\n"
"ds_read_b64_tr_b16 v[128:129], v8 offset:256\n"
"ds_read_b64_tr_b16 v[130:131], v8 offset:8448\n"
"v_mfma_f32_16x16x32_bf16 a[76:79], v[104:107], v[60:63], a[76:79]\n"
"v_exp_f32 v35, v35\n"
"v_mfma_f32_16x16x32_bf16 a[80:83], v[104:107], v[64:67], a[80:83]\n"
"v_exp_f32 v36, v36\n"
"v_mfma_f32_16x16x32_bf16 a[84:87], v[108:111], v[56:59], a[84:87]\n"
"ds_read_b64_tr_b16 v[132:133], v9 offset:256\n"
"ds_read_b64_tr_b16 v[134:135], v9 offset:8448\n"
"v_mfma_f32_16x16x32_bf16 a[88:91], v[108:111], v[60:63], a[88:91]\n"
"buffer_load_dwordx4 v17, %1, s42 offen offset:1024 lds\n"
"v_cvt_pk_bf16_f32 v69, v34, v35\n"
"v_mfma_f32_16x16x32_bf16 a[92:95], v[108:111], v[64:67], a[92:95]\n"
"v_exp_f32 v37, v37\n"
"v_mfma_f32_16x16x32_bf16 a[96:99], v[112:115], v[56:59], a[96:99]\n"
"ds_read_b64_tr_b16 v[136:137], v10 offset:256\n"
"ds_read_b64_tr_b16 v[138:139], v10 offset:8448\n"
"v_mfma_f32_16x16x32_bf16 a[100:103], v[112:115], v[60:63], a[100:103]\n"
"v_exp_f32 v38, v38\n"
"v_mfma_f32_16x16x32_bf16 a[104:107], v[112:115], v[64:67], a[104:107]\n"
"v_exp_f32 v39, v39\n"
"v_mfma_f32_16x16x32_bf16 a[108:111], v[116:119], v[56:59], a[108:111]\n"
"ds_read_b64_tr_b16 v[140:141], v11 offset:256\n"
"ds_read_b64_tr_b16 v[142:143], v11 offset:8448\n"
"v_mfma_f32_16x16x32_bf16 a[112:115], v[116:119], v[60:63], a[112:115]\n"
"s_memtime s[52:53]\n"
"buffer_load_dwordx4 v18, %1, s42 offen offset:2048 lds\n"
"v_mfma_f32_16x16x32_bf16 a[116:119], v[116:119], v[64:67], a[116:119]\n"
"v_exp_f32 v40, v40\n"
"s_waitcnt lgkmcnt(3)\n"
"v_mfma_f32_16x16x32_bf16 a[120:123], v[120:123], v[56:59], a[120:123]\n"
"v_exp_f32 v41, v41\n"
"v_mfma_f32_16x16x32_bf16 a[124:127], v[120:123], v[60:63], a[124:127]\n"
"v_exp_f32 v42, v42\n"
"v_mfma_f32_16x16x32_bf16 a[128:131], v[120:123], v[64:67], a[128:131]\n"
"buffer_load_dwordx4 v19, %1, s42 offen offset:3072 lds\n"
"v_add_f32 v205, v205, v33\n"
"v_mfma_f32_16x16x32_bf16 a[132:135], v[124:127], v[56:59], a[132:135]\n"
"v_exp_f32 v43, v43\n"
"v_mfma_f32_16x16x32_bf16 a[136:139], v[124:127], v[60:63], a[136:139]\n"
"v_add_f32 v205, v205, v34\n"
"v_add_f32 v205, v205, v35\n"
"v_mfma_f32_16x16x32_bf16 a[140:143], v[124:127], v[64:67], a[140:143]\n"
"v_cvt_pk_bf16_f32 v72, v36, v37\n"
"v_cvt_pk_bf16_f32 v73, v38, v39\n"
"v_mfma_f32_16x16x32_bf16 a[144:147], v[128:131], v[56:59], a[144:147]\n"
"v_add_f32 v206, v206, v36\n"
"v_add_f32 v206, v206, v37\n"
"v_mfma_f32_16x16x32_bf16 a[148:151], v[128:131], v[60:63], a[148:151]\n"
"s_add_u32 s42, s42, 16384\n"
"v_add_f32 v206, v206, v38\n"
"v_mfma_f32_16x16x32_bf16 a[152:155], v[128:131], v[64:67], a[152:155]\n"
"s_waitcnt vmcnt(16) lgkmcnt(0)\n"
"s_barrier\n"
"v_mfma_f32_16x16x32_bf16 a[156:159], v[132:135], v[56:59], a[156:159]\n"
"ds_read_b128 v[80:83], v0 offset:0\n"
"ds_read_b128 v[84:87], v1 offset:0\n"
"v_mfma_f32_16x16x32_bf16 a[160:163], v[132:135], v[60:63], a[160:163]\n"
"ds_read_b128 v[88:91], v2 offset:0\n"
"v_add_f32 v206, v206, v39\n"
"v_mfma_f32_16x16x32_bf16 a[164:167], v[132:135], v[64:67], a[164:167]\n"
"v_cvt_pk_bf16_f32 v76, v40, v41\n"
"v_cvt_pk_bf16_f32 v77, v42, v43\n"
"v_mfma_f32_16x16x32_bf16 a[168:171], v[136:139], v[56:59], a[168:171]\n"
"v_add_f32 v207, v207, v40\n"
"v_add_f32 v207, v207, v41\n"
"v_mfma_f32_16x16x32_bf16 a[172:175], v[136:139], v[60:63], a[172:175]\n"
"v_add_f32 v207, v207, v42\n"
"v_add_f32 v207, v207, v43\n"
"v_mfma_f32_16x16x32_bf16 a[176:179], v[136:139], v[64:67], a[176:179]\n"
"s_sub_u32 s56, s52, s54\n"
"s_mov_b32 s54, s52\n"
"v_mfma_f32_16x16x32_bf16 a[180:183], v[140:143], v[56:59], a[180:183]\n"
"s_sub_u32 s56, s56, s55\n"
"s_cmp_gt_u32 s56, 2100\n"
"v_mfma_f32_16x16x32_bf16 a[184:187], v[140:143], v[60:63], a[184:187]\n"
"s_cselect_b32 s56, 128, -16\n"
"s_add_i32 s55, s55, s56\n"
"v_mfma_f32_16x16x32_bf16 a[188:191], v[140:143], v[64:67], a[188:191]\n"
"s_max_i32 s55, s55, s57\n"
"s_min_i32 s55, s55, 512\n"
"s_lshr_b32 s56, s55, 6\n"
"s_cmp_lg_u32 s56, 0\n"
"s_cbranch_scc1 LY7_%=\n"
"LZ7_%=:\n"
"s_and_b32 s42, s42, 0xfffff\n"
"s_waitcnt lgkmcnt(0)\n"
"v_mfma_f32_16x16x32_bf16 v[32:35], v[80:83], v[144:147], v[20:23]\n"
"ds_read_b128 v[92:95], v3 offset:0\n"
"ds_read_b128 v[96:99], v0 offset:256\n"
"v_mfma_f32_16x16x32_bf16 v[36:39], v[80:83], v[160:163], v[24:27]\n"
"v_exp_f32 v44, v44\n"
"v_mfma_f32_16x16x32_bf16 v[40:43], v[80:83], v[176:179], v[28:31]\n"
"ds_read_b128 v[100:103], v1 offset:256\n"
"v_add_f32 v205, v205, v44\n"
"v_mfma_f32_16x16x32_bf16 v[32:35], v[84:87], v[148:151], v[32:35]\n"
"v_exp_f32 v45, v45\n"
"v_mfma_f32_16x16x32_bf16 v[36:39], v[84:87], v[164:167], v[36:39]\n"
"v_exp_f32 v46, v46\n"
"v_mfma_f32_16x16x32_bf16 v[40:43], v[84:87], v[180:183], v[40:43]\n"
"ds_read_b128 v[104:107], v2 offset:256\n"
"v_cvt_pk_bf16_f32 v70, v44, v45\n"
"v_mfma_f32_16x16x32_bf16 v[32:35], v[88:91], v[152:155], v[32:35]\n"
"v_exp_f32 v47, v47\n"
"v_mfma_f32_16x16x32_bf16 v[36:39], v[88:91], v[168:171], v[36:39]\n"
"v_exp_f32 v48, v48\n"
"v_mfma_f32_16x16x32_bf16 v[40:43], v[88:91], v[184:187], v[40:43]\n"
"ds_read_b128 v[108:111], v3 offset:256\n"
"v_cvt_pk_bf16_f32 v71, v46, v47\n"
"s_waitcnt lgkmcnt(2)\n"
"v_mfma_f32_16x16x32_bf16 v[32:35], v[92:95], v[156:159], v[32:35]\n"
"v_exp_f32 v49, v49\n"
"v_mfma_f32_16x16x32_bf16 v[36:39], v[92:95], v[172:175], v[36:39]\n"
"v_exp_f32 v50, v50\n"
"v_mfma_f32_16x16x32_bf16 v[40:43], v[92:95], v[188:191], v[40:43]\n"
"ds_read_b128 v[112:115], v0 offset:8192\n"
"v_add_f32 v205, v205, v45\n"
"v_mfma_f32_16x16x32_bf16 v[32:35], v[96:99], a[204:207], v[32:35]\n"
"v_exp_f32 v51, v51\n"
"v_mfma_f32_16x16x32_bf16 v[36:39], v[96:99], a[220:223], v[36:39]\n"
"v_add_f32 v205, v205, v46\n"
"v_add_f32 v205, v205, v47\n"
"v_mfma_f32_16x16x32_bf16 v[40:43], v[96:99], a[236:239], v[40:43]\n"
"ds_read_b128 v[116:119], v1 offset:8192\n"
"v_cvt_pk_bf16_f32 v74, v48, v49\n"
"v_mfma_f32_16x16x32_bf16 v[32:35], v[100:103], a[208:211], v[32:35]\n"
"v_exp_f32 v52, v52\n"
"v_mfma_f32_16x16x32_bf16 v[36:39], v[100:103], a[224:227], v[36:39]\n"
"v_exp_f32 v53, v53\n"
"v_mfma_f32_16x16x32_bf16 v[40:43], v[100:103], a[240:243], v[40:43]\n"
"ds_read_b128 v[120:123], v2 offset:8192\n"
"v_cvt_pk_bf16_f32 v75, v50, v51\n"
"s_waitcnt lgkmcnt(2)\n"
"v_mfma_f32_16x16x32_bf16 v[32:35], v[104:107], a[212:215], v[32:35]\n"
"v_exp_f32 v54, v54\n"
"v_mfma_f32_16x16x32_bf16 v[36:39], v[104:107], a[228:231], v[36:39]\n"
"v_exp_f32 v55, v55\n"
"v_mfma_f32_16x16x32_bf16 v[40:43], v[104:107], a[244:247], v[40:43]\n"
"v_add_f32 v206, v206, v48\n"
"v_add_f32 v206, v206, v49\n"
"v_mfma_f32_16x16x32_bf16 v[32:35], v[108:111], a[216:219], v[32:35]\n"
"v_add_f32 v206, v206, v50\n"
"v_add_f32 v206, v206, v51\n"
"v_mfma_f32_16x16x32_bf16 v[36:39], v[108:111], a[232:235], v[36:39]\n"
"v_cvt_pk_bf16_f32 v78, v52, v53\n"
"v_cvt_pk_bf16_f32 v79, v54, v55\n"
"v_mfma_f32_16x16x32_bf16 v[40:43], v[108:111], a[248:251], v[40:43]\n"
"v_add_f32 v207, v207, v52\n"
"v_add_f32 v207, v207, v53\n"
"v_mfma_f32_16x16x32_bf16 v[44:47], v[112:115], v[144:147], v[20:23]\n"
"v_add_f32 v207, v207, v54\n"
"v_add_f32 v207, v207, v55\n"
"v_mfma_f32_16x16x32_bf16 v[48:51], v[112:115], v[160:163], v[24:27]\n"
"ds_read_b128 v[124:127], v3 offset:8192\n"
"ds_read_b128 v[128:131], v0 offset:8448\n"
"v_mfma_f32_16x16x32_bf16 v[52:55], v[112:115], v[176:179], v[28:31]\n"
"ds_read_b128 v[132:135], v1 offset:8448\n"
"s_waitcnt lgkmcnt(3)\n"
"v_mfma_f32_16x16x32_bf16 v[44:47], v[116:119], v[148:151], v[44:47]\n"
"s_add_u32 m0, s40, 49152\n"
"v_max3_f32 v196, v32, v33, v34\n"
"v_mfma_f32_16x16x32_bf16 v[48:51], v[116:119], v[164:167], v[48:51]\n"
"v_max3_f32 v196, v196, v35, v36\n"
"v_max3_f32 v196, v196, v37, v38\n"
"v_mfma_f32_16x16x32_bf16 v[52:55], v[116:119], v[180:183], v[52:55]\n"
"ds_read_b128 v[136:139], v2 offset:8448\n"
"v_max3_f32 v196, v196, v39, v40\n"
"v_mfma_f32_16x16x32_bf16 v[44:47], v[120:123], v[152:155], v[44:47]\n"
"v_max3_f32 v196, v196, v41, v42\n"
"v_max3_f32 v196, v196, v43, v43\n"
"v_mfma_f32_16x16x32_bf16 v[48:51], v[120:123], v[168:171], v[48:51]\n"
"ds_read_b64_tr_b16 v[80:81], v4 offset:16384\n"
"ds_read_b64_tr_b16 v[82:83], v4 offset:24576\n"
"v_mfma_f32_16x16x32_bf16 v[52:55], v[120:123], v[184:187], v[52:55]\n"
"ds_read_b128 v[140:143], v3 offset:8448\n"
"buffer_load_dwordx4 v12, %0, s41 offen offset:0 lds\n"
"s_waitcnt lgkmcnt(4)\n"
"v_mfma_f32_16x16x32_bf16 v[44:47], v[124:127], v[156:159], v[44:47]\n"
"ds_read_b64_tr_b16 v[84:85], v5 offset:16384\n"
"ds_read_b64_tr_b16 v[86:87], v5 offset:24576\n"
"v_mfma_f32_16x16x32_bf16 v[48:51], v[124:127], v[172:175], v[48:51]\n"
"v_mfma_f32_16x16x32_bf16 v[52:55], v[124:127], v[188:191], v[52:55]\n"
"buffer_load_dwordx4 v13, %0, s41 offen offset:1024 lds\n"
"v_mfma_f32_16x16x32_bf16 v[44:47], v[128:131], a[204:207], v[44:47]\n"
"ds_read_b64_tr_b16 v[88:89], v6 offset:16384\n"
"ds_read_b64_tr_b16 v[90:91], v6 offset:24576\n"
"v_mfma_f32_16x16x32_bf16 v[48:51], v[128:131], a[220:223], v[48:51]\n"
"v_mfma_f32_16x16x32_bf16 v[52:55], v[128:131], a[236:239], v[52:55]\n"
"v_mfma_f32_16x16x32_bf16 v[44:47], v[132:135], a[208:211], v[44:47]\n"
"buffer_load_dwordx4 v14, %0, s41 offen offset:2048 lds\n"
"ds_read_b64_tr_b16 v[92:93], v7 offset:16384\n"
"v_mfma_f32_16x16x32_bf16 v[48:51], v[132:135], a[224:227], v[48:51]\n"
"ds_read_b64_tr_b16 v[94:95], v7 offset:24576\n"
"v_mfma_f32_16x16x32_bf16 v[52:55], v[132:135], a[240:243], v[52:55]\n"
"s_waitcnt lgkmcnt(4)\n"
"v_mfma_f32_16x16x32_bf16 v[44:47], v[136:139], a[212:215], v[44:47]\n"
"ds_read_b64_tr_b16 v[96:97], v8 offset:16384\n"
"ds_read_b64_tr_b16 v[98:99], v8 offset:24576\n"
"v_mfma_f32_16x16x32_bf16 v[48:51], v[136:139], a[228:231], v[48:51]\n"
"buffer_load_dwordx4 v15, %0, s41 offen offset:3072 lds\n"
"v_mfma_f32_16x16x32_bf16 v[52:55], v[136:139], a[244:247], v[52:55]\n"
"v_mfma_f32_16x16x32_bf16 v[44:47], v[140:143], a[216:219], v[44:47]\n"
"ds_read_b64_tr_b16 v[100:101], v9 offset:16384\n"
"ds_read_b64_tr_b16 v[102:103], v9 offset:24576\n"
"v_mfma_f32_16x16x32_bf16 v[48:51], v[140:143], a[232:235], v[48:51]\n"
"v_mfma_f32_16x16x32_bf16 v[52:55], v[140:143], a[248:251], v[52:55]\n"
"s_add_u32 s41, s41, 16384\n"
"v_mfma_f32_16x16x32_bf16 a[0:3], v[80:83], v[68:71], a[0:3]\n"
"ds_read_b64_tr_b16 v[104:105], v10 offset:16384\n"
"ds_read_b64_tr_b16 v[106:107], v10 offset:24576\n"
"v_mfma_f32_16x16x32_bf16 a[4:7], v[80:83], v[72:75], a[4:7]\n"
"v_mfma_f32_16x16x32_bf16 a[8:11], v[80:83], v[76:79], a[8:11]\n"
"v_mfma_f32_16x16x32_bf16 a[12:15], v[84:87], v[68:71], a[12:15]\n"
"s_and_b32 s41, s41, 0xfffff\n"
"ds_read_b64_tr_b16 v[108:109], v11 offset:16384\n"
"v_mfma_f32_16x16x32_bf16 a[16:19], v[84:87], v[72:75], a[16:19]\n"
"ds_read_b64_tr_b16 v[110:111], v11 offset:24576\n"
"v_max3_f32 v196, v196, v44, v45\n"
"v_mfma_f32_16x16x32_bf16 a[20:23], v[84:87], v[76:79], a[20:23]\n"
"v_max3_f32 v196, v196, v46, v47\n"
"v_max3_f32 v196, v196, v48, v49\n"
"s_waitcnt lgkmcnt(4)\n"
"v_mfma_f32_16x16x32_bf16 a[24:27], v[88:91], v[68:71], a[24:27]\n"
"v_max3_f32 v196, v196, v50, v51\n"
"v_max3_f32 v196, v196, v52, v53\n"
"v_mfma_f32_16x16x32_bf16 a[28:31], v[88:91], v[72:75], a[28:31]\n"
"v_max3_f32 v196, v196, v54, v55\n"
"v_cmp_lt_f32 vcc, 0x41000000, v196\n"
"v_mfma_f32_16x16x32_bf16 a[32:35], v[88:91], v[76:79], a[32:35]\n"
"s_cbranch_vccnz LSTUB4_%=\n"
"LRET4_%=:\n"
"ds_read_b64_tr_b16 v[112:113], v4 offset:16640\n"
"v_mfma_f32_16x16x32_bf16 a[36:39], v[92:95], v[68:71], a[36:39]\n"
"ds_read_b64_tr_b16 v[114:115], v4 offset:24832\n"
"ds_read_b64_tr_b16 v[116:117], v5 offset:16640\n"
"v_mfma_f32_16x16x32_bf16 a[40:43], v[92:95], v[72:75], a[40:43]\n"
"ds_read_b64_tr_b16 v[118:119], v5 offset:24832\n"
"v_mfma_f32_16x16x32_bf16 a[44:47], v[92:95], v[76:79], a[44:47]\n"
"v_exp_f32 v32, v32\n"
"v_mfma_f32_16x16x32_bf16 a[48:51], v[96:99], v[68:71], a[48:51]\n"
"s_add_u32 m0, s40, 65536\n"
"ds_read_b64_tr_b16 v[120:121], v6 offset:16640\n"
"v_mfma_f32_16x16x32_bf16 a[52:55], v[96:99], v[72:75], a[52:55]\n"
"ds_read_b64_tr_b16 v[122:123], v6 offset:24832\n"
"v_add_f32 v205, v205, v32\n"
"v_mfma_f32_16x16x32_bf16 a[56:59], v[96:99], v[76:79], a[56:59]\n"
"v_exp_f32 v33, v33\n"
"v_mfma_f32_16x16x32_bf16 a[60:63], v[100:103], v[68:71], a[60:63]\n"
"ds_read_b64_tr_b16 v[124:125], v7 offset:16640\n"
"ds_read_b64_tr_b16 v[126:127], v7 offset:24832\n"
"v_mfma_f32_16x16x32_bf16 a[64:67], v[100:103], v[72:75], a[64:67]\n"
"v_exp_f32 v34, v34\n"
"v_mfma_f32_16x16x32_bf16 a[68:71], v[100:103], v[76:79], a[68:71]\n"
"buffer_load_dwordx4 v16, %1, s42 offen offset:0 lds\n"
"v_cvt_pk_bf16_f32 v56, v32, v33\n"
"s_waitcnt lgkmcnt(3)\n"
"v_mfma_f32_16x16x32_bf16 a[72:75], v[104:107], v[68:71], a[72:75]\n"
"ds_read_b64_tr_b16 v[128:129], v8 offset:16640\n"
"ds_read_b64_tr_b16 v[130:131], v8 offset:24832\n"
"v_mfma_f32_16x16x32_bf16 a[76:79], v[104:107], v[72:75], a[76:79]\n"
"v_exp_f32 v35, v35\n"
"v_mfma_f32_16x16x32_bf16 a[80:83], v[104:107], v[76:79], a[80:83]\n"
"v_exp_f32 v36, v36\n"
"v_mfma_f32_16x16x32_bf16 a[84:87], v[108:111], v[68:71], a[84:87]\n"
"ds_read_b64_tr_b16 v[132:133], v9 offset:16640\n"
"ds_read_b64_tr_b16 v[134:135], v9 offset:24832\n"
"v_mfma_f32_16x16x32_bf16 a[88:91], v[108:111], v[72:75], a[88:91]\n"
"buffer_load_dwordx4 v17, %1, s42 offen offset:1024 lds\n"
"v_cvt_pk_bf16_f32 v57, v34, v35\n"
"v_mfma_f32_16x16x32_bf16 a[92:95], v[108:111], v[76:79], a[92:95]\n"
"v_exp_f32 v37, v37\n"
"v_mfma_f32_16x16x32_bf16 a[96:99], v[112:115], v[68:71], a[96:99]\n"
"ds_read_b64_tr_b16 v[136:137], v10 offset:16640\n"
"ds_read_b64_tr_b16 v[138:139], v10 offset:24832\n"
"v_mfma_f32_16x16x32_bf16 a[100:103], v[112:115], v[72:75], a[100:103]\n"
"v_exp_f32 v38, v38\n"
"v_mfma_f32_16x16x32_bf16 a[104:107], v[112:115], v[76:79], a[104:107]\n"
"v_exp_f32 v39, v39\n"
"v_mfma_f32_16x16x32_bf16 a[108:111], v[116:119], v[68:71], a[108:111]\n"
"ds_read_b64_tr_b16 v[140:141], v11 offset:16640\n"
"ds_read_b64_tr_b16 v[142:143], v11 offset:24832\n"
"v_mfma_f32_16x16x32_bf16 a[112:115], v[116:119], v[72:75], a[112:115]\n"
"s_memtime s[52:53]\n"
"buffer_load_dwordx4 v18, %1, s42 offen offset:2048 lds\n"
"v_mfma_f32_16x16x32_bf16 a[116:119], v[116:119], v[76:79], a[116:119]\n"
"v_exp_f32 v40, v40\n"
"s_waitcnt lgkmcnt(3)\n"
"v_mfma_f32_16x16x32_bf16 a[120:123], v[120:123], v[68:71], a[120:123]\n"
"v_exp_f32 v41, v41\n"
"v_mfma_f32_16x16x32_bf16 a[124:127], v[120:123], v[72:75], a[124:127]\n"
"v_exp_f32 v42, v42\n"
"v_mfma_f32_16x16x32_bf16 a[128:131], v[120:123], v[76:79], a[128:131]\n"
"buffer_load_dwordx4 v19, %1, s42 offen offset:3072 lds\n"
"v_add_f32 v205, v205, v33\n"
"v_mfma_f32_16x16x32_bf16 a[132:135], v[124:127], v[68:71], a[132:135]\n"
"v_exp_f32 v43, v43\n"
"v_mfma_f32_16x16x32_bf16 a[136:139], v[124:127], v[72:75], a[136:139]\n"
"v_add_f32 v205, v205, v34\n"
"v_add_f32 v205, v205, v35\n"
"v_mfma_f32_16x16x32_bf16 a[140:143], v[124:127], v[76:79], a[140:143]\n"
"v_cvt_pk_bf16_f32 v60, v36, v37\n"
"v_cvt_pk_bf16_f32 v61, v38, v39\n"
"v_mfma_f32_16x16x32_bf16 a[144:147], v[128:131], v[68:71], a[144:147]\n"
"v_add_f32 v206, v206, v36\n"
"v_add_f32 v206, v206, v37\n"
"v_mfma_f32_16x16x32_bf16 a[148:151], v[128:131], v[72:75], a[148:151]\n"
"s_add_u32 s42, s42, 16384\n"
"v_add_f32 v206, v206, v38\n"
"v_mfma_f32_16x16x32_bf16 a[152:155], v[128:131], v[76:79], a[152:155]\n"
"s_waitcnt vmcnt(16) lgkmcnt(0)\n"
"s_barrier\n"
"v_mfma_f32_16x16x32_bf16 a[156:159], v[132:135], v[68:71], a[156:159]\n"
"ds_read_b128 v[80:83], v0 offset:16384\n"
"ds_read_b128 v[84:87], v1 offset:16384\n"
"v_mfma_f32_16x16x32_bf16 a[160:163], v[132:135], v[72:75], a[160:163]\n"
"ds_read_b128 v[88:91], v2 offset:16384\n"
"v_add_f32 v206, v206, v39\n"
"v_mfma_f32_16x16x32_bf16 a[164:167], v[132:135], v[76:79], a[164:167]\n"
"v_cvt_pk_bf16_f32 v64, v40, v41\n"
"v_cvt_pk_bf16_f32 v65, v42, v43\n"
"v_mfma_f32_16x16x32_bf16 a[168:171], v[136:139], v[68:71], a[168:171]\n"
"v_add_f32 v207, v207, v40\n"
"v_add_f32 v207, v207, v41\n"
"v_mfma_f32_16x16x32_bf16 a[172:175], v[136:139], v[72:75], a[172:175]\n"
"v_add_f32 v207, v207, v42\n"
"v_add_f32 v207, v207, v43\n"
"v_mfma_f32_16x16x32_bf16 a[176:179], v[136:139], v[76:79], a[176:179]\n"
"s_sub_u32 s56, s52, s54\n"
"s_mov_b32 s54, s52\n"
"v_mfma_f32_16x16x32_bf16 a[180:183], v[140:143], v[68:71], a[180:183]\n"
"s_sub_u32 s56, s56, s55\n"
"s_cmp_gt_u32 s56, 2100\n"
"v_mfma_f32_16x16x32_bf16 a[184:187], v[140:143], v[72:75], a[184:187]\n"
"s_cselect_b32 s56, 128, -16\n"
"s_add_i32 s55, s55, s56\n"
"v_mfma_f32_16x16x32_bf16 a[188:191], v[140:143], v[76:79], a[188:191]\n"
"s_max_i32 s55, s55, s57\n"
"s_min_i32 s55, s55, 512\n"
"s_lshr_b32 s56, s55, 6\n"
"s_cmp_lg_u32 s56, 0\n"
"s_cbranch_scc1 LY8_%=\n"
"LZ8_%=:\n"
"s_and_b32 s42, s42, 0xfffff\n"
"s_sub_u32 s43, s43, 1\n"
"s_cmp_lg_u32 s43, 0\n"
"s_cbranch_scc1 LLOOPA3_%=\n"
"s_branch LTAIL3_%=\n"
"LLOOPB3_%=:\n"
"s_waitcnt lgkmcnt(0)\n"
"v_mfma_f32_16x16x32_bf16 v[32:35], v[80:83], v[144:147], v[20:23]\n"
"ds_read_b128 v[92:95], v3 offset:16384\n"
"ds_read_b128 v[96:99], v0 offset:16640\n"
"v_mfma_f32_16x16x32_bf16 v[36:39], v[80:83], v[160:163], v[24:27]\n"
"v_exp_f32 v44, v44\n"
"v_mfma_f32_16x16x32_bf16 v[40:43], v[80:83], v[176:179], v[28:31]\n"
"ds_read_b128 v[100:103], v1 offset:16640\n"
"v_add_f32 v205, v205, v44\n"
"v_mfma_f32_16x16x32_bf16 v[32:35], v[84:87], v[148:151], v[32:35]\n"
"v_exp_f32 v45, v45\n"
"v_mfma_f32_16x16x32_bf16 v[36:39], v[84:87], v[164:167], v[36:39]\n"
"v_exp_f32 v46, v46\n"
"v_mfma_f32_16x16x32_bf16 v[40:43], v[84:87], v[180:183], v[40:43]\n"
"ds_read_b128 v[104:107], v2 offset:16640\n"
"v_cvt_pk_bf16_f32 v58, v44, v45\n"
"v_mfma_f32_16x16x32_bf16 v[32:35], v[88:91], v[152:155], v[32:35]\n"
"v_exp_f32 v47, v47\n"
"v_mfma_f32_16x16x32_bf16 v[36:39], v[88:91], v[168:171], v[36:39]\n"
"v_exp_f32 v48, v48\n"
"v_mfma_f32_16x16x32_bf16 v[40:43], v[88:91], v[184:187], v[40:43]\n"
"ds_read_b128 v[108:111], v3 offset:16640\n"
"v_cvt_pk_bf16_f32 v59, v46, v47\n"
"s_waitcnt lgkmcnt(2)\n"
"v_mfma_f32_16x16x32_bf16 v[32:35], v[92:95], v[156:159], v[32:35]\n"
"v_exp_f32 v49, v49\n"
"v_mfma_f32_16x16x32_bf16 v[36:39], v[92:95], v[172:175], v[36:39]\n"
"v_exp_f32 v50, v50\n"
"v_mfma_f32_16x16x32_bf16 v[40:43], v[92:95], v[188:191], v[40:43]\n"
"ds_read_b128 v[112:115], v0 offset:24576\n"
"v_add_f32 v205, v205, v45\n"
"v_mfma_f32_16x16x32_bf16 v[32:35], v[96:99], a[204:207], v[32:35]\n"
"v_exp_f32 v51, v51\n"
"v_mfma_f32_16x16x32_bf16 v[36:39], v[96:99], a[220:223], v[36:39]\n"
"v_add_f32 v205, v205, v46\n"
"v_add_f32 v205, v205, v47\n"
"v_mfma_f32_16x16x32_bf16 v[40:43], v[96:99], a[236:239], v[40:43]\n"
"ds_read_b128 v[116:119], v1 offset:24576\n"
"v_cvt_pk_bf16_f32 v62, v48, v49\n"
"v_mfma_f32_16x16x32_bf16 v[32:35], v[100:103], a[208:211], v[32:35]\n"
"v_exp_f32 v52, v52\n"
"v_mfma_f32_16x16x32_bf16 v[36:39], v[100:103], a[224:227], v[36:39]\n"
"v_exp_f32 v53, v53\n"
"v_mfma_f32_16x16x32_bf16 v[40:43], v[100:103], a[240:243], v[40:43]\n"
"ds_read_b128 v[120:123], v2 offset:24576\n"
"v_cvt_pk_bf16_f32 v63, v50, v51\n"
"s_waitcnt lgkmcnt(2)\n"
"v_mfma_f32_16x16x32_bf16 v[32:35], v[104:107], a[212:215], v[32:35]\n"
"v_exp_f32 v54, v54\n"
"v_mfma_f32_16x16x32_bf16 v[36:39], v[104:107], a[228:231], v[36:39]\n"
"v_exp_f32 v55, v55\n"
"v_mfma_f32_16x16x32_bf16 v[40:43], v[104:107], a[244:247], v[40:43]\n"
"v_add_f32 v206, v206, v48\n"
"v_add_f32 v206, v206, v49\n"
"v_mfma_f32_16x16x32_bf16 v[32:35], v[108:111], a[216:219], v[32:35]\n"
"v_add_f32 v206, v206, v50\n"
"v_add_f32 v206, v206, v51\n"
"v_mfma_f32_16x16x32_bf16 v[36:39], v[108:111], a[232:235], v[36:39]\n"
"v_cvt_pk_bf16_f32 v66, v52, v53\n"
"v_cvt_pk_bf16_f32 v67, v54, v55\n"
"v_mfma_f32_16x16x32_bf16 v[40:43], v[108:111], a[248:251], v[40:43]\n"
"v_add_f32 v207, v207, v52\n"
"v_add_f32 v207, v207, v53\n"
"v_mfma_f32_16x16x32_bf16 v[44:47], v[112:115], v[144:147], v[20:23]\n"
"v_add_f32 v207, v207, v54\n"
"v_add_f32 v207, v207, v55\n"
"v_mfma_f32_16x16x32_bf16 v[48:51], v[112:115], v[160:163], v[24:27]\n"
"ds_read_b128 v[124:127], v3 offset:24576\n"
"ds_read_b128 v[128:131], v0 offset:24832\n"
"v_mfma_f32_16x16x32_bf16 v[52:55], v[112:115], v[176:179], v[28:31]\n"
"ds_read_b128 v[132:135], v1 offset:24832\n"
"s_waitcnt lgkmcnt(3)\n"
"v_mfma_f32_16x16x32_bf16 v[44:47], v[116:119], v[148:151], v[44:47]\n"
"s_add_u32 m0, s40, 0\n"
"v_max3_f32 v196, v32, v33, v34\n"
"v_mfma_f32_16x16x32_bf16 v[48:51], v[116:119], v[164:167], v[48:51]\n"
"v_max3_f32 v196, v196, v35, v36\n"
"v_max3_f32 v196, v196, v37, v38\n"
"v_mfma_f32_16x16x32_bf16 v[52:55], v[116:119], v[180:183], v[52:55]\n"
"ds_read_b128 v[136:139], v2 offset:24832\n"
"v_max3_f32 v196, v196, v39, v40\n"
"v_mfma_f32_16x16x32_bf16 v[44:47], v[120:123], v[152:155], v[44:47]\n"
"v_max3_f32 v196, v196, v41, v42\n"
"v_max3_f32 v196, v196, v43, v43\n"
"v_mfma_f32_16x16x32_bf16 v[48:51], v[120:123], v[168:171], v[48:51]\n"
"ds_read_b64_tr_b16 v[80:81], v4 offset:32768\n"
"ds_read_b64_tr_b16 v[82:83], v4 offset:40960\n"
"v_mfma_f32_16x16x32_bf16 v[52:55], v[120:123], v[184:187], v[52:55]\n"
"ds_read_b128 v[140:143], v3 offset:24832\n"
"buffer_load_dwordx4 v12, %0, s41 offen offset:0 nt lds\n"
"s_waitcnt lgkmcnt(4)\n"
"v_mfma_f32_16x16x32_bf16 v[44:47], v[124:127], v[156:159], v[44:47]\n"
"ds_read_b64_tr_b16 v[84:85], v5 offset:32768\n"
"ds_read_b64_tr_b16 v[86:87], v5 offset:40960\n"
"v_mfma_f32_16x16x32_bf16 v[48:51], v[124:127], v[172:175], v[48:51]\n"
"v_mfma_f32_16x16x32_bf16 v[52:55], v[124:127], v[188:191], v[52:55]\n"
"buffer_load_dwordx4 v13, %0, s41 offen offset:1024 nt lds\n"
"v_mfma_f32_16x16x32_bf16 v[44:47], v[128:131], a[204:207], v[44:47]\n"
"ds_read_b64_tr_b16 v[88:89], v6 offset:32768\n"
"ds_read_b64_tr_b16 v[90:91], v6 offset:40960\n"
"v_mfma_f32_16x16x32_bf16 v[48:51], v[128:131], a[220:223], v[48:51]\n"
"v_mfma_f32_16x16x32_bf16 v[52:55], v[128:131], a[236:239], v[52:55]\n"
"v_mfma_f32_16x16x32_bf16 v[44:47], v[132:135], a[208:211], v[44:47]\n"
"buffer_load_dwordx4 v14, %0, s41 offen offset:2048 nt lds\n"
"ds_read_b64_tr_b16 v[92:93], v7 offset:32768\n"
"v_mfma_f32_16x16x32_bf16 v[48:51], v[132:135], a[224:227], v[48:51]\n"
"ds_read_b64_tr_b16 v[94:95], v7 offset:40960\n"
"v_mfma_f32_16x16x32_bf16 v[52:55], v[132:135], a[240:243], v[52:55]\n"
"s_waitcnt lgkmcnt(4)\n"
"v_mfma_f32_16x16x32_bf16 v[44:47], v[136:139], a[212:215], v[44:47]\n"
"ds_read_b64_tr_b16 v[96:97], v8 offset:32768\n"
"ds_read_b64_tr_b16 v[98:99], v8 offset:40960\n"
"v_mfma_f32_16x16x32_bf16 v[48:51], v[136:139], a[228:231], v[48:51]\n"
"buffer_load_dwordx4 v15, %0, s41 offen offset:3072 nt lds\n"
"v_mfma_f32_16x16x32_bf16 v[52:55], v[136:139], a[244:247], v[52:55]\n"
"v_mfma_f32_16x16x32_bf16 v[44:47], v[140:143], a[216:219], v[44:47]\n"
"ds_read_b64_tr_b16 v[100:101], v9 offset:32768\n"
"ds_read_b64_tr_b16 v[102:103], v9 offset:40960\n"
"v_mfma_f32_16x16x32_bf16 v[48:51], v[140:143], a[232:235], v[48:51]\n"
"v_mfma_f32_16x16x32_bf16 v[52:55], v[140:143], a[248:251], v[52:55]\n"
"s_add_u32 s41, s41, 16384\n"
"v_mfma_f32_16x16x32_bf16 a[0:3], v[80:83], v[56:59], a[0:3]\n"
"ds_read_b64_tr_b16 v[104:105], v10 offset:32768\n"
"ds_read_b64_tr_b16 v[106:107], v10 offset:40960\n"
"v_mfma_f32_16x16x32_bf16 a[4:7], v[80:83], v[60:63], a[4:7]\n"
"v_mfma_f32_16x16x32_bf16 a[8:11], v[80:83], v[64:67], a[8:11]\n"
"v_mfma_f32_16x16x32_bf16 a[12:15], v[84:87], v[56:59], a[12:15]\n"
"s_and_b32 s41, s41, 0xfffff\n"
"ds_read_b64_tr_b16 v[108:109], v11 offset:32768\n"
"v_mfma_f32_16x16x32_bf16 a[16:19], v[84:87], v[60:63], a[16:19]\n"
"ds_read_b64_tr_b16 v[110:111], v11 offset:40960\n"
"v_max3_f32 v196, v196, v44, v45\n"
"v_mfma_f32_16x16x32_bf16 a[20:23], v[84:87], v[64:67], a[20:23]\n"
"v_max3_f32 v196, v196, v46, v47\n"
"v_max3_f32 v196, v196, v48, v49\n"
"s_waitcnt lgkmcnt(4)\n"
"v_mfma_f32_16x16x32_bf16 a[24:27], v[88:91], v[56:59], a[24:27]\n"
"v_max3_f32 v196, v196, v50, v51\n"
"v_max3_f32 v196, v196, v52, v53\n"
"v_mfma_f32_16x16x32_bf16 a[28:31], v[88:91], v[60:63], a[28:31]\n"
"v_max3_f32 v196, v196, v54, v55\n"
"v_cmp_lt_f32 vcc, 0x41000000, v196\n"
"v_mfma_f32_16x16x32_bf16 a[32:35], v[88:91], v[64:67], a[32:35]\n"
"s_cbranch_vccnz LSTUB5_%=\n"
"LRET5_%=:\n"
"ds_read_b64_tr_b16 v[112:113], v4 offset:33024\n"
"v_mfma_f32_16x16x32_bf16 a[36:39], v[92:95], v[56:59], a[36:39]\n"
"ds_read_b64_tr_b16 v[114:115], v4 offset:41216\n"
"ds_read_b64_tr_b16 v[116:117], v5 offset:33024\n"
"v_mfma_f32_16x16x32_bf16 a[40:43], v[92:95], v[60:63], a[40:43]\n"
"ds_read_b64_tr_b16 v[118:119], v5 offset:41216\n"
"v_mfma_f32_16x16x32_bf16 a[44:47], v[92:95], v[64:67], a[44:47]\n"
"v_exp_f32 v32, v32\n"
"v_mfma_f32_16x16x32_bf16 a[48:51], v[96:99], v[56:59], a[48:51]\n"
"s_add_u32 m0, s40, 81920\n"
"ds_read_b64_tr_b16 v[120:121], v6 offset:33024\n"
"v_mfma_f32_16x16x32_bf16 a[52:55], v[96:99], v[60:63], a[52:55]\n"
"ds_read_b64_tr_b16 v[122:123], v6 offset:41216\n"
"v_add_f32 v205, v205, v32\n"
"v_mfma_f32_16x16x32_bf16 a[56:59], v[96:99], v[64:67], a[56:59]\n"
"v_exp_f32 v33, v33\n"
"v_mfma_f32_16x16x32_bf16 a[60:63], v[100:103], v[56:59], a[60:63]\n"
"ds_read_b64_tr_b16 v[124:125], v7 offset:33024\n"
"ds_read_b64_tr_b16 v[126:127], v7 offset:41216\n"
"v_mfma_f32_16x16x32_bf16 a[64:67], v[100:103], v[60:63], a[64:67]\n"
"v_exp_f32 v34, v34\n"
"v_mfma_f32_16x16x32_bf16 a[68:71], v[100:103], v[64:67], a[68:71]\n"
"buffer_load_dwordx4 v16, %1, s42 offen offset:0 nt lds\n"
"v_cvt_pk_bf16_f32 v68, v32, v33\n"
"s_waitcnt lgkmcnt(3)\n"
"v_mfma_f32_16x16x32_bf16 a[72:75], v[104:107], v[56:59], a[72:75]\n"
"ds_read_b64_tr_b16 v[128:129], v8 offset:33024\n"
"ds_read_b64_tr_b16 v[130:131], v8 offset:41216\n"
"v_mfma_f32_16x16x32_bf16 a[76:79], v[104:107], v[60:63], a[76:79]\n"
"v_exp_f32 v35, v35\n"
"v_mfma_f32_16x16x32_bf16 a[80:83], v[104:107], v[64:67], a[80:83]\n"
"v_exp_f32 v36, v36\n"
"v_mfma_f32_16x16x32_bf16 a[84:87], v[108:111], v[56:59], a[84:87]\n"
"ds_read_b64_tr_b16 v[132:133], v9 offset:33024\n"
"ds_read_b64_tr_b16 v[134:135], v9 offset:41216\n"
"v_mfma_f32_16x16x32_bf16 a[88:91], v[108:111], v[60:63], a[88:91]\n"
"buffer_load_dwordx4 v17, %1, s42 offen offset:1024 nt lds\n"
"v_cvt_pk_bf16_f32 v69, v34, v35\n"
"v_mfma_f32_16x16x32_bf16 a[92:95], v[108:111], v[64:67], a[92:95]\n"
"v_exp_f32 v37, v37\n"
"v_mfma_f32_16x16x32_bf16 a[96:99], v[112:115], v[56:59], a[96:99]\n"
"ds_read_b64_tr_b16 v[136:137], v10 offset:33024\n"
"ds_read_b64_tr_b16 v[138:139], v10 offset:41216\n"
"v_mfma_f32_16x16x32_bf16 a[100:103], v[112:115], v[60:63], a[100:103]\n"
"v_exp_f32 v38, v38\n"
"v_mfma_f32_16x16x32_bf16 a[104:107], v[112:115], v[64:67], a[104:107]\n"
"v_exp_f32 v39, v39\n"
"v_mfma_f32_16x16x32_bf16 a[108:111], v[116:119], v[56:59], a[108:111]\n"
"ds_read_b64_tr_b16 v[140:141], v11 offset:33024\n"
"ds_read_b64_tr_b16 v[142:143], v11 offset:41216\n"
"v_mfma_f32_16x16x32_bf16 a[112:115], v[116:119], v[60:63], a[112:115]\n"
"s_memtime s[52:53]\n"
"buffer_load_dwordx4 v18, %1, s42 offen offset:2048 nt lds\n"
"v_mfma_f32_16x16x32_bf16 a[116:119], v[116:119], v[64:67], a[116:119]\n"
"v_exp_f32 v40, v40\n"
"s_waitcnt lgkmcnt(3)\n"
"v_mfma_f32_16x16x32_bf16 a[120:123], v[120:123], v[56:59], a[120:123]\n"
"v_exp_f32 v41, v41\n"
"v_mfma_f32_16x16x32_bf16 a[124:127], v[120:123], v[60:63], a[124:127]\n"
"v_exp_f32 v42, v42\n"
"v_mfma_f32_16x16x32_bf16 a[128:131], v[120:123], v[64:67], a[128:131]\n"
"buffer_load_dwordx4 v19, %1, s42 offen offset:3072 nt lds\n"
"v_add_f32 v205, v205, v33\n"
"v_mfma_f32_16x16x32_bf16 a[132:135], v[124:127], v[56:59], a[132:135]\n"
"v_exp_f32 v43, v43\n"
"v_mfma_f32_16x16x32_bf16 a[136:139], v[124:127], v[60:63], a[136:139]\n"
"v_add_f32 v205, v205, v34\n"
"v_add_f32 v205, v205, v35\n"
"v_mfma_f32_16x16x32_bf16 a[140:143], v[124:127], v[64:67], a[140:143]\n"
"v_cvt_pk_bf16_f32 v72, v36, v37\n"
"v_cvt_pk_bf16_f32 v73, v38, v39\n"
"v_mfma_f32_16x16x32_bf16 a[144:147], v[128:131], v[56:59], a[144:147]\n"
"v_add_f32 v206, v206, v36\n"
"v_add_f32 v206, v206, v37\n"
"v_mfma_f32_16x16x32_bf16 a[148:151], v[128:131], v[60:63], a[148:151]\n"
"s_add_u32 s42, s42, 16384\n"
"v_add_f32 v206, v206, v38\n"
"v_mfma_f32_16x16x32_bf16 a[152:155], v[128:131], v[64:67], a[152:155]\n"
"s_waitcnt vmcnt(16) lgkmcnt(0)\n"
"s_barrier\n"
"v_mfma_f32_16x16x32_bf16 a[156:159], v[132:135], v[56:59], a[156:159]\n"
"ds_read_b128 v[80:83], v0 offset:32768\n"
"ds_read_b128 v[84:87], v1 offset:32768\n"
"v_mfma_f32_16x16x32_bf16 a[160:163], v[132:135], v[60:63], a[160:163]\n"
"ds_read_b128 v[88:91], v2 offset:32768\n"
"v_add_f32 v206, v206, v39\n"
"v_mfma_f32_16x16x32_bf16 a[164:167], v[132:135], v[64:67], a[164:167]\n"
"v_cvt_pk_bf16_f32 v76, v40, v41\n"
"v_cvt_pk_bf16_f32 v77, v42, v43\n"
"v_mfma_f32_16x16x32_bf16 a[168:171], v[136:139], v[56:59], a[168:171]\n"
"v_add_f32 v207, v207, v40\n"
"v_add_f32 v207, v207, v41\n"
"v_mfma_f32_16x16x32_bf16 a[172:175], v[136:139], v[60:63], a[172:175]\n"
"v_add_f32 v207, v207, v42\n"
"v_add_f32 v207, v207, v43\n"
"v_mfma_f32_16x16x32_bf16 a[176:179], v[136:139], v[64:67], a[176:179]\n"
"s_sub_u32 s56, s52, s54\n"
"s_mov_b32 s54, s52\n"
"v_mfma_f32_16x16x32_bf16 a[180:183], v[140:143], v[56:59], a[180:183]\n"
"s_sub_u32 s56, s56, s55\n"
"s_cmp_gt_u32 s56, 2100\n"
"v_mfma_f32_16x16x32_bf16 a[184:187], v[140:143], v[60:63], a[184:187]\n"
"s_cselect_b32 s56, 128, -16\n"
"s_add_i32 s55, s55, s56\n"
"v_mfma_f32_16x16x32_bf16 a[188:191], v[140:143], v[64:67], a[188:191]\n"
"s_max_i32 s55, s55, s57\n"
"s_min_i32 s55, s55, 512\n"
"s_lshr_b32 s56, s55, 6\n"
"s_cmp_lg_u32 s56, 0\n"
"s_cbranch_scc1 LY9_%=\n"
"LZ9_%=:\n"
"s_and_b32 s42, s42, 0xfffff\n"
"s_waitcnt lgkmcnt(0)\n"
"v_mfma_f32_16x16x32_bf16 v[32:35], v[80:83], v[144:147], v[20:23]\n"
"ds_read_b128 v[92:95], v3 offset:32768\n"
"ds_read_b128 v[96:99], v0 offset:33024\n"
"v_mfma_f32_16x16x32_bf16 v[36:39], v[80:83], v[160:163], v[24:27]\n"
"v_exp_f32 v44, v44\n"
"v_mfma_f32_16x16x32_bf16 v[40:43], v[80:83], v[176:179], v[28:31]\n"
"ds_read_b128 v[100:103], v1 offset:33024\n"
"v_add_f32 v205, v205, v44\n"
"v_mfma_f32_16x16x32_bf16 v[32:35], v[84:87], v[148:151], v[32:35]\n"
"v_exp_f32 v45, v45\n"
"v_mfma_f32_16x16x32_bf16 v[36:39], v[84:87], v[164:167], v[36:39]\n"
"v_exp_f32 v46, v46\n"
"v_mfma_f32_16x16x32_bf16 v[40:43], v[84:87], v[180:183], v[40:43]\n"
"ds_read_b128 v[104:107], v2 offset:33024\n"
"v_cvt_pk_bf16_f32 v70, v44, v45\n"
"v_mfma_f32_16x16x32_bf16 v[32:35], v[88:91], v[152:155], v[32:35]\n"
"v_exp_f32 v47, v47\n"
"v_mfma_f32_16x16x32_bf16 v[36:39], v[88:91], v[168:171], v[36:39]\n"
"v_exp_f32 v48, v48\n"
"v_mfma_f32_16x16x32_bf16 v[40:43], v[88:91], v[184:187], v[40:43]\n"
"ds_read_b128 v[108:111], v3 offset:33024\n"
"v_cvt_pk_bf16_f32 v71, v46, v47\n"
"s_waitcnt lgkmcnt(2)\n"
"v_mfma_f32_16x16x32_bf16 v[32:35], v[92:95], v[156:159], v[32:35]\n"
"v_exp_f32 v49, v49\n"
"v_mfma_f32_16x16x32_bf16 v[36:39], v[92:95], v[172:175], v[36:39]\n"
"v_exp_f32 v50, v50\n"
"v_mfma_f32_16x16x32_bf16 v[40:43], v[92:95], v[188:191], v[40:43]\n"
"ds_read_b128 v[112:115], v0 offset:40960\n"
"v_add_f32 v205, v205, v45\n"
"v_mfma_f32_16x16x32_bf16 v[32:35], v[96:99], a[204:207], v[32:35]\n"
"v_exp_f32 v51, v51\n"
"v_mfma_f32_16x16x32_bf16 v[36:39], v[96:99], a[220:223], v[36:39]\n"
"v_add_f32 v205, v205, v46\n"
"v_add_f32 v205, v205, v47\n"
"v_mfma_f32_16x16x32_bf16 v[40:43], v[96:99], a[236:239], v[40:43]\n"
"ds_read_b128 v[116:119], v1 offset:40960\n"
"v_cvt_pk_bf16_f32 v74, v48, v49\n"
"v_mfma_f32_16x16x32_bf16 v[32:35], v[100:103], a[208:211], v[32:35]\n"
"v_exp_f32 v52, v52\n"
"v_mfma_f32_16x16x32_bf16 v[36:39], v[100:103], a[224:227], v[36:39]\n"
"v_exp_f32 v53, v53\n"
"v_mfma_f32_16x16x32_bf16 v[40:43], v[100:103], a[240:243], v[40:43]\n"
"ds_read_b128 v[120:123], v2 offset:40960\n"
"v_cvt_pk_bf16_f32 v75, v50, v51\n"
"s_waitcnt lgkmcnt(2)\n"
"v_mfma_f32_16x16x32_bf16 v[32:35], v[104:107], a[212:215], v[32:35]\n"
"v_exp_f32 v54, v54\n"
"v_mfma_f32_16x16x32_bf16 v[36:39], v[104:107], a[228:231], v[36:39]\n"
"v_exp_f32 v55, v55\n"
"v_mfma_f32_16x16x32_bf16 v[40:43], v[104:107], a[244:247], v[40:43]\n"
"v_add_f32 v206, v206, v48\n"
"v_add_f32 v206, v206, v49\n"
"v_mfma_f32_16x16x32_bf16 v[32:35], v[108:111], a[216:219], v[32:35]\n"
"v_add_f32 v206, v206, v50\n"
"v_add_f32 v206, v206, v51\n"
"v_mfma_f32_16x16x32_bf16 v[36:39], v[108:111], a[232:235], v[36:39]\n"
"v_cvt_pk_bf16_f32 v78, v52, v53\n"
"v_cvt_pk_bf16_f32 v79, v54, v55\n"
"v_mfma_f32_16x16x32_bf16 v[40:43], v[108:111], a[248:251], v[40:43]\n"
"v_add_f32 v207, v207, v52\n"
"v_add_f32 v207, v207, v53\n"
"v_mfma_f32_16x16x32_bf16 v[44:47], v[112:115], v[144:147], v[20:23]\n"
"v_add_f32 v207, v207, v54\n"
"v_add_f32 v207, v207, v55\n"
"v_mfma_f32_16x16x32_bf16 v[48:51], v[112:115], v[160:163], v[24:27]\n"
"ds_read_b128 v[124:127], v3 offset:40960\n"
"ds_read_b128 v[128:131], v0 offset:41216\n"
"v_mfma_f32_16x16x32_bf16 v[52:55], v[112:115], v[176:179], v[28:31]\n"
"ds_read_b128 v[132:135], v1 offset:41216\n"
"s_waitcnt lgkmcnt(3)\n"
"v_mfma_f32_16x16x32_bf16 v[44:47], v[116:119], v[148:151], v[44:47]\n"
"s_add_u32 m0, s40, 16384\n"
"v_max3_f32 v196, v32, v33, v34\n"
"v_mfma_f32_16x16x32_bf16 v[48:51], v[116:119], v[164:167], v[48:51]\n"
"v_max3_f32 v196, v196, v35, v36\n"
"v_max3_f32 v196, v196, v37, v38\n"
"v_mfma_f32_16x16x32_bf16 v[52:55], v[116:119], v[180:183], v[52:55]\n"
"ds_read_b128 v[136:139], v2 offset:41216\n"
"v_max3_f32 v196, v196, v39, v40\n"
"v_mfma_f32_16x16x32_bf16 v[44:47], v[120:123], v[152:155], v[44:47]\n"
"v_max3_f32 v196, v196, v41, v42\n"
"v_max3_f32 v196, v196, v43, v43\n"
"v_mfma_f32_16x16x32_bf16 v[48:51], v[120:123], v[168:171], v[48:51]\n"
"ds_read_b64_tr_b16 v[80:81], v4 offset:49152\n"
"ds_read_b64_tr_b16 v[82:83], v4 offset:57344\n"
"v_mfma_f32_16x16x32_bf16 v[52:55], v[120:123], v[184:187], v[52:55]\n"
"ds_read_b128 v[140:143], v3 offset:41216\n"
"buffer_load_dwordx4 v12, %0, s41 offen offset:0 nt lds\n"
"s_waitcnt lgkmcnt(4)\n"
"v_mfma_f32_16x16x32_bf16 v[44:47], v[124:127], v[156:159], v[44:47]\n"
"ds_read_b64_tr_b16 v[84:85], v5 offset:49152\n"
"ds_read_b64_tr_b16 v[86:87], v5 offset:57344\n"
"v_mfma_f32_16x16x32_bf16 v[48:51], v[124:127], v[172:175], v[48:51]\n"
"v_mfma_f32_16x16x32_bf16 v[52:55], v[124:127], v[188:191], v[52:55]\n"
"buffer_load_dwordx4 v13, %0, s41 offen offset:1024 nt lds\n"
"v_mfma_f32_16x16x32_bf16 v[44:47], v[128:131], a[204:207], v[44:47]\n"
"ds_read_b64_tr_b16 v[88:89], v6 offset:49152\n"
"ds_read_b64_tr_b16 v[90:91], v6 offset:57344\n"
"v_mfma_f32_16x16x32_bf16 v[48:51], v[128:131], a[220:223], v[48:51]\n"
"v_mfma_f32_16x16x32_bf16 v[52:55], v[128:131], a[236:239], v[52:55]\n"
"v_mfma_f32_16x16x32_bf16 v[44:47], v[132:135], a[208:211], v[44:47]\n"
"buffer_load_dwordx4 v14, %0, s41 offen offset:2048 nt lds\n"
"ds_read_b64_tr_b16 v[92:93], v7 offset:49152\n"
"v_mfma_f32_16x16x32_bf16 v[48:51], v[132:135], a[224:227], v[48:51]\n"
"ds_read_b64_tr_b16 v[94:95], v7 offset:57344\n"
"v_mfma_f32_16x16x32_bf16 v[52:55], v[132:135], a[240:243], v[52:55]\n"
"s_waitcnt lgkmcnt(4)\n"
"v_mfma_f32_16x16x32_bf16 v[44:47], v[136:139], a[212:215], v[44:47]\n"
"ds_read_b64_tr_b16 v[96:97], v8 offset:49152\n"
"ds_read_b64_tr_b16 v[98:99], v8 offset:57344\n"
"v_mfma_f32_16x16x32_bf16 v[48:51], v[136:139], a[228:231], v[48:51]\n"
"buffer_load_dwordx4 v15, %0, s41 offen offset:3072 nt lds\n"
"v_mfma_f32_16x16x32_bf16 v[52:55], v[136:139], a[244:247], v[52:55]\n"
"v_mfma_f32_16x16x32_bf16 v[44:47], v[140:143], a[216:219], v[44:47]\n"
"ds_read_b64_tr_b16 v[100:101], v9 offset:49152\n"
"ds_read_b64_tr_b16 v[102:103], v9 offset:57344\n"
"v_mfma_f32_16x16x32_bf16 v[48:51], v[140:143], a[232:235], v[48:51]\n"
"v_mfma_f32_16x16x32_bf16 v[52:55], v[140:143], a[248:251], v[52:55]\n"
"s_add_u32 s41, s41, 16384\n"
"v_mfma_f32_16x16x32_bf16 a[0:3], v[80:83], v[68:71], a[0:3]\n"
"ds_read_b64_tr_b16 v[104:105], v10 offset:49152\n"
"ds_read_b64_tr_b16 v[106:107], v10 offset:57344\n"
"v_mfma_f32_16x16x32_bf16 a[4:7], v[80:83], v[72:75], a[4:7]\n"
"v_mfma_f32_16x16x32_bf16 a[8:11], v[80:83], v[76:79], a[8:11]\n"
"v_mfma_f32_16x16x32_bf16 a[12:15], v[84:87], v[68:71], a[12:15]\n"
"s_and_b32 s41, s41, 0xfffff\n"
"ds_read_b64_tr_b16 v[108:109], v11 offset:49152\n"
"v_mfma_f32_16x16x32_bf16 a[16:19], v[84:87], v[72:75], a[16:19]\n"
"ds_read_b64_tr_b16 v[110:111], v11 offset:57344\n"
"v_max3_f32 v196, v196, v44, v45\n"
"v_mfma_f32_16x16x32_bf16 a[20:23], v[84:87], v[76:79], a[20:23]\n"
"v_max3_f32 v196, v196, v46, v47\n"
"v_max3_f32 v196, v196, v48, v49\n"
"s_waitcnt lgkmcnt(4)\n"
"v_mfma_f32_16x16x32_bf16 a[24:27], v[88:91], v[68:71], a[24:27]\n"
"v_max3_f32 v196, v196, v50, v51\n"
"v_max3_f32 v196, v196, v52, v53\n"
"v_mfma_f32_16x16x32_bf16 a[28:31], v[88:91], v[72:75], a[28:31]\n"
"v_max3_f32 v196, v196, v54, v55\n"
"v_cmp_lt_f32 vcc, 0x41000000, v196\n"
"v_mfma_f32_16x16x32_bf16 a[32:35], v[88:91], v[76:79], a[32:35]\n"
"s_cbranch_vccnz LSTUB6_%=\n"
"LRET6_%=:\n"
"ds_read_b64_tr_b16 v[112:113], v4 offset:49408\n"
"v_mfma_f32_16x16x32_bf16 a[36:39], v[92:95], v[68:71], a[36:39]\n"
"ds_read_b64_tr_b16 v[114:115], v4 offset:57600\n"
"ds_read_b64_tr_b16 v[116:117], v5 offset:49408\n"
"v_mfma_f32_16x16x32_bf16 a[40:43], v[92:95], v[72:75], a[40:43]\n"
"ds_read_b64_tr_b16 v[118:119], v5 offset:57600\n"
"v_mfma_f32_16x16x32_bf16 a[44:47], v[92:95], v[76:79], a[44:47]\n"
"v_exp_f32 v32, v32\n"
"v_mfma_f32_16x16x32_bf16 a[48:51], v[96:99], v[68:71], a[48:51]\n"
"s_add_u32 m0, s40, 98304\n"
"ds_read_b64_tr_b16 v[120:121], v6 offset:49408\n"
"v_mfma_f32_16x16x32_bf16 a[52:55], v[96:99], v[72:75], a[52:55]\n"
"ds_read_b64_tr_b16 v[122:123], v6 offset:57600\n"
"v_add_f32 v205, v205, v32\n"
"v_mfma_f32_16x16x32_bf16 a[56:59], v[96:99], v[76:79], a[56:59]\n"
"v_exp_f32 v33, v33\n"
"v_mfma_f32_16x16x32_bf16 a[60:63], v[100:103], v[68:71], a[60:63]\n"
"ds_read_b64_tr_b16 v[124:125], v7 offset:49408\n"
"ds_read_b64_tr_b16 v[126:127], v7 offset:57600\n"
"v_mfma_f32_16x16x32_bf16 a[64:67], v[100:103], v[72:75], a[64:67]\n"
"v_exp_f32 v34, v34\n"
"v_mfma_f32_16x16x32_bf16 a[68:71], v[100:103], v[76:79], a[68:71]\n"
"buffer_load_dwordx4 v16, %1, s42 offen offset:0 nt lds\n"
"v_cvt_pk_bf16_f32 v56, v32, v33\n"
"s_waitcnt lgkmcnt(3)\n"
"v_mfma_f32_16x16x32_bf16 a[72:75], v[104:107], v[68:71], a[72:75]\n"
"ds_read_b64_tr_b16 v[128:129], v8 offset:49408\n"
"ds_read_b64_tr_b16 v[130:131], v8 offset:57600\n"
"v_mfma_f32_16x16x32_bf16 a[76:79], v[104:107], v[72:75], a[76:79]\n"
"v_exp_f32 v35, v35\n"
"v_mfma_f32_16x16x32_bf16 a[80:83], v[104:107], v[76:79], a[80:83]\n"
"v_exp_f32 v36, v36\n"
"v_mfma_f32_16x16x32_bf16 a[84:87], v[108:111], v[68:71], a[84:87]\n"
"ds_read_b64_tr_b16 v[132:133], v9 offset:49408\n"
"ds_read_b64_tr_b16 v[134:135], v9 offset:57600\n"
"v_mfma_f32_16x16x32_bf16 a[88:91], v[108:111], v[72:75], a[88:91]\n"
"buffer_load_dwordx4 v17, %1, s42 offen offset:1024 nt lds\n"
"v_cvt_pk_bf16_f32 v57, v34, v35\n"
"v_mfma_f32_16x16x32_bf16 a[92:95], v[108:111], v[76:79], a[92:95]\n"
"v_exp_f32 v37, v37\n"
"v_mfma_f32_16x16x32_bf16 a[96:99], v[112:115], v[68:71], a[96:99]\n"
"ds_read_b64_tr_b16 v[136:137], v10 offset:49408\n"
"ds_read_b64_tr_b16 v[138:139], v10 offset:57600\n"
"v_mfma_f32_16x16x32_bf16 a[100:103], v[112:115], v[72:75], a[100:103]\n"
"v_exp_f32 v38, v38\n"
"v_mfma_f32_16x16x32_bf16 a[104:107], v[112:115], v[76:79], a[104:107]\n"
"v_exp_f32 v39, v39\n"
"v_mfma_f32_16x16x32_bf16 a[108:111], v[116:119], v[68:71], a[108:111]\n"
"ds_read_b64_tr_b16 v[140:141], v11 offset:49408\n"
"ds_read_b64_tr_b16 v[142:143], v11 offset:57600\n"
"v_mfma_f32_16x16x32_bf16 a[112:115], v[116:119], v[72:75], a[112:115]\n"
"s_memtime s[52:53]\n"
"buffer_load_dwordx4 v18, %1, s42 offen offset:2048 nt lds\n"
"v_mfma_f32_16x16x32_bf16 a[116:119], v[116:119], v[76:79], a[116:119]\n"
"v_exp_f32 v40, v40\n"
"s_waitcnt lgkmcnt(3)\n"
"v_mfma_f32_16x16x32_bf16 a[120:123], v[120:123], v[68:71], a[120:123]\n"
"v_exp_f32 v41, v41\n"
"v_mfma_f32_16x16x32_bf16 a[124:127], v[120:123], v[72:75], a[124:127]\n"
"v_exp_f32 v42, v42\n"
"v_mfma_f32_16x16x32_bf16 a[128:131], v[120:123], v[76:79], a[128:131]\n"
"buffer_load_dwordx4 v19, %1, s42 offen offset:3072 nt lds\n"
"v_add_f32 v205, v205, v33\n"
"v_mfma_f32_16x16x32_bf16 a[132:135], v[124:127], v[68:71], a[132:135]\n"
"v_exp_f32 v43, v43\n"
"v_mfma_f32_16x16x32_bf16 a[136:139], v[124:127], v[72:75], a[136:139]\n"
"v_add_f32 v205, v205, v34\n"
"v_add_f32 v205, v205, v35\n"
"v_mfma_f32_16x16x32_bf16 a[140:143], v[124:127], v[76:79], a[140:143]\n"
"v_cvt_pk_bf16_f32 v60, v36, v37\n"
"v_cvt_pk_bf16_f32 v61, v38, v39\n"
"v_mfma_f32_16x16x32_bf16 a[144:147], v[128:131], v[68:71], a[144:147]\n"
"v_add_f32 v206, v206, v36\n"
"v_add_f32 v206, v206, v37\n"
"v_mfma_f32_16x16x32_bf16 a[148:151], v[128:131], v[72:75], a[148:151]\n"
"s_add_u32 s42, s42, 16384\n"
"v_add_f32 v206, v206, v38\n"
"v_mfma_f32_16x16x32_bf16 a[152:155], v[128:131], v[76:79], a[152:155]\n"
"s_waitcnt vmcnt(16) lgkmcnt(0)\n"
"s_barrier\n"
"v_mfma_f32_16x16x32_bf16 a[156:159], v[132:135], v[68:71], a[156:159]\n"
"ds_read_b128 v[80:83], v0 offset:49152\n"
"ds_read_b128 v[84:87], v1 offset:49152\n"
"v_mfma_f32_16x16x32_bf16 a[160:163], v[132:135], v[72:75], a[160:163]\n"
"ds_read_b128 v[88:91], v2 offset:49152\n"
"v_add_f32 v206, v206, v39\n"
"v_mfma_f32_16x16x32_bf16 a[164:167], v[132:135], v[76:79], a[164:167]\n"
"v_cvt_pk_bf16_f32 v64, v40, v41\n"
"v_cvt_pk_bf16_f32 v65, v42, v43\n"
"v_mfma_f32_16x16x32_bf16 a[168:171], v[136:139], v[68:71], a[168:171]\n"
"v_add_f32 v207, v207, v40\n"
"v_add_f32 v207, v207, v41\n"
"v_mfma_f32_16x16x32_bf16 a[172:175], v[136:139], v[72:75], a[172:175]\n"
"v_add_f32 v207, v207, v42\n"
"v_add_f32 v207, v207, v43\n"
"v_mfma_f32_16x16x32_bf16 a[176:179], v[136:139], v[76:79], a[176:179]\n"
"s_sub_u32 s56, s52, s54\n"
"s_mov_b32 s54, s52\n"
"v_mfma_f32_16x16x32_bf16 a[180:183], v[140:143], v[68:71], a[180:183]\n"
"s_sub_u32 s56, s56, s55\n"
"s_cmp_gt_u32 s56, 2100\n"
"v_mfma_f32_16x16x32_bf16 a[184:187], v[140:143], v[72:75], a[184:187]\n"
"s_cselect_b32 s56, 128, -16\n"
"s_add_i32 s55, s55, s56\n"
"v_mfma_f32_16x16x32_bf16 a[188:191], v[140:143], v[76:79], a[188:191]\n"
"s_max_i32 s55, s55, s57\n"
"s_min_i32 s55, s55, 512\n"
"s_lshr_b32 s56, s55, 6\n"
"s_cmp_lg_u32 s56, 0\n"
"s_cbranch_scc1 LY10_%=\n"
"LZ10_%=:\n"
"s_and_b32 s42, s42, 0xfffff\n"
"s_waitcnt lgkmcnt(0)\n"
"v_mfma_f32_16x16x32_bf16 v[32:35], v[80:83], v[144:147], v[20:23]\n"
"ds_read_b128 v[92:95], v3 offset:49152\n"
"ds_read_b128 v[96:99], v0 offset:49408\n"
"v_mfma_f32_16x16x32_bf16 v[36:39], v[80:83], v[160:163], v[24:27]\n"
"v_exp_f32 v44, v44\n"
"v_mfma_f32_16x16x32_bf16 v[40:43], v[80:83], v[176:179], v[28:31]\n"
"ds_read_b128 v[100:103], v1 offset:49408\n"
"v_add_f32 v205, v205, v44\n"
"v_mfma_f32_16x16x32_bf16 v[32:35], v[84:87], v[148:151], v[32:35]\n"
"v_exp_f32 v45, v45\n"
"v_mfma_f32_16x16x32_bf16 v[36:39], v[84:87], v[164:167], v[36:39]\n"
"v_exp_f32 v46, v46\n"
"v_mfma_f32_16x16x32_bf16 v[40:43], v[84:87], v[180:183], v[40:43]\n"
"ds_read_b128 v[104:107], v2 offset:49408\n"
"v_cvt_pk_bf16_f32 v58, v44, v45\n"
"v_mfma_f32_16x16x32_bf16 v[32:35], v[88:91], v[152:155], v[32:35]\n"
"v_exp_f32 v47, v47\n"
"v_mfma_f32_16x16x32_bf16 v[36:39], v[88:91], v[168:171], v[36:39]\n"
"v_exp_f32 v48, v48\n"
"v_mfma_f32_16x16x32_bf16 v[40:43], v[88:91], v[184:187], v[40:43]\n"
"ds_read_b128 v[108:111], v3 offset:49408\n"
"v_cvt_pk_bf16_f32 v59, v46, v47\n"
"s_waitcnt lgkmcnt(2)\n"
"v_mfma_f32_16x16x32_bf16 v[32:35], v[92:95], v[156:159], v[32:35]\n"
"v_exp_f32 v49, v49\n"
"v_mfma_f32_16x16x32_bf16 v[36:39], v[92:95], v[172:175], v[36:39]\n"
"v_exp_f32 v50, v50\n"
"v_mfma_f32_16x16x32_bf16 v[40:43], v[92:95], v[188:191], v[40:43]\n"
"ds_read_b128 v[112:115], v0 offset:57344\n"
"v_add_f32 v205, v205, v45\n"
"v_mfma_f32_16x16x32_bf16 v[32:35], v[96:99], a[204:207], v[32:35]\n"
"v_exp_f32 v51, v51\n"
"v_mfma_f32_16x16x32_bf16 v[36:39], v[96:99], a[220:223], v[36:39]\n"
"v_add_f32 v205, v205, v46\n"
"v_add_f32 v205, v205, v47\n"
"v_mfma_f32_16x16x32_bf16 v[40:43], v[96:99], a[236:239], v[40:43]\n"
"ds_read_b128 v[116:119], v1 offset:57344\n"
"v_cvt_pk_bf16_f32 v62, v48, v49\n"
"v_mfma_f32_16x16x32_bf16 v[32:35], v[100:103], a[208:211], v[32:35]\n"
"v_exp_f32 v52, v52\n"
"v_mfma_f32_16x16x32_bf16 v[36:39], v[100:103], a[224:227], v[36:39]\n"
"v_exp_f32 v53, v53\n"
"v_mfma_f32_16x16x32_bf16 v[40:43], v[100:103], a[240:243], v[40:43]\n"
"ds_read_b128 v[120:123], v2 offset:57344\n"
"v_cvt_pk_bf16_f32 v63, v50, v51\n"
"s_waitcnt lgkmcnt(2)\n"
"v_mfma_f32_16x16x32_bf16 v[32:35], v[104:107], a[212:215], v[32:35]\n"
"v_exp_f32 v54, v54\n"
"v_mfma_f32_16x16x32_bf16 v[36:39], v[104:107], a[228:231], v[36:39]\n"
"v_exp_f32 v55, v55\n"
"v_mfma_f32_16x16x32_bf16 v[40:43], v[104:107], a[244:247], v[40:43]\n"
"v_add_f32 v206, v206, v48\n"
"v_add_f32 v206, v206, v49\n"
"v_mfma_f32_16x16x32_bf16 v[32:35], v[108:111], a[216:219], v[32:35]\n"
"v_add_f32 v206, v206, v50\n"
"v_add_f32 v206, v206, v51\n"
"v_mfma_f32_16x16x32_bf16 v[36:39], v[108:111], a[232:235], v[36:39]\n"
"v_cvt_pk_bf16_f32 v66, v52, v53\n"
"v_cvt_pk_bf16_f32 v67, v54, v55\n"
"v_mfma_f32_16x16x32_bf16 v[40:43], v[108:111], a[248:251], v[40:43]\n"
"v_add_f32 v207, v207, v52\n"
"v_add_f32 v207, v207, v53\n"
"v_mfma_f32_16x16x32_bf16 v[44:47], v[112:115], v[144:147], v[20:23]\n"
"v_add_f32 v207, v207, v54\n"
"v_add_f32 v207, v207, v55\n"
"v_mfma_f32_16x16x32_bf16 v[48:51], v[112:115], v[160:163], v[24:27]\n"
"ds_read_b128 v[124:127], v3 offset:57344\n"
"ds_read_b128 v[128:131], v0 offset:57600\n"
"v_mfma_f32_16x16x32_bf16 v[52:55], v[112:115], v[176:179], v[28:31]\n"
"ds_read_b128 v[132:135], v1 offset:57600\n"
"s_waitcnt lgkmcnt(3)\n"
"v_mfma_f32_16x16x32_bf16 v[44:47], v[116:119], v[148:151], v[44:47]\n"
"s_add_u32 m0, s40, 32768\n"
"v_max3_f32 v196, v32, v33, v34\n"
"v_mfma_f32_16x16x32_bf16 v[48:51], v[116:119], v[164:167], v[48:51]\n"
"v_max3_f32 v196, v196, v35, v36\n"
"v_max3_f32 v196, v196, v37, v38\n"
"v_mfma_f32_16x16x32_bf16 v[52:55], v[116:119], v[180:183], v[52:55]\n"
"ds_read_b128 v[136:139], v2 offset:57600\n"
"v_max3_f32 v196, v196, v39, v40\n"
"v_mfma_f32_16x16x32_bf16 v[44:47], v[120:123], v[152:155], v[44:47]\n"
"v_max3_f32 v196, v196, v41, v42\n"
"v_max3_f32 v196, v196, v43, v43\n"
"v_mfma_f32_16x16x32_bf16 v[48:51], v[120:123], v[168:171], v[48:51]\n"
"ds_read_b64_tr_b16 v[80:81], v4 offset:0\n"
"ds_read_b64_tr_b16 v[82:83], v4 offset:8192\n"
"v_mfma_f32_16x16x32_bf16 v[52:55], v[120:123], v[184:187], v[52:55]\n"
"ds_read_b128 v[140:143], v3 offset:57600\n"
"buffer_load_dwordx4 v12, %0, s41 offen offset:0 nt lds\n"
"s_waitcnt lgkmcnt(4)\n"
"v_mfma_f32_16x16x32_bf16 v[44:47], v[124:127], v[156:159], v[44:47]\n"
"ds_read_b64_tr_b16 v[84:85], v5 offset:0\n"
"ds_read_b64_tr_b16 v[86:87], v5 offset:8192\n"
"v_mfma_f32_16x16x32_bf16 v[48:51], v[124:127], v[172:175], v[48:51]\n"
"v_mfma_f32_16x16x32_bf16 v[52:55], v[124:127], v[188:191], v[52:55]\n"
"buffer_load_dwordx4 v13, %0, s41 offen offset:1024 nt lds\n"
"v_mfma_f32_16x16x32_bf16 v[44:47], v[128:131], a[204:207], v[44:47]\n"
"ds_read_b64_tr_b16 v[88:89], v6 offset:0\n"
"ds_read_b64_tr_b16 v[90:91], v6 offset:8192\n"
"v_mfma_f32_16x16x32_bf16 v[48:51], v[128:131], a[220:223], v[48:51]\n"
"v_mfma_f32_16x16x32_bf16 v[52:55], v[128:131], a[236:239], v[52:55]\n"
"v_mfma_f32_16x16x32_bf16 v[44:47], v[132:135], a[208:211], v[44:47]\n"
"buffer_load_dwordx4 v14, %0, s41 offen offset:2048 nt lds\n"
"ds_read_b64_tr_b16 v[92:93], v7 offset:0\n"
"v_mfma_f32_16x16x32_bf16 v[48:51], v[132:135], a[224:227], v[48:51]\n"
"ds_read_b64_tr_b16 v[94:95], v7 offset:8192\n"
"v_mfma_f32_16x16x32_bf16 v[52:55], v[132:135], a[240:243], v[52:55]\n"
"s_waitcnt lgkmcnt(4)\n"
"v_mfma_f32_16x16x32_bf16 v[44:47], v[136:139], a[212:215], v[44:47]\n"
"ds_read_b64_tr_b16 v[96:97], v8 offset:0\n"
"ds_read_b64_tr_b16 v[98:99], v8 offset:8192\n"
"v_mfma_f32_16x16x32_bf16 v[48:51], v[136:139], a[228:231], v[48:51]\n"
"buffer_load_dwordx4 v15, %0, s41 offen offset:3072 nt lds\n"
"v_mfma_f32_16x16x32_bf16 v[52:55], v[136:139], a[244:247], v[52:55]\n"
"v_mfma_f32_16x16x32_bf16 v[44:47], v[140:143], a[216:219], v[44:47]\n"
"ds_read_b64_tr_b16 v[100:101], v9 offset:0\n"
"ds_read_b64_tr_b16 v[102:103], v9 offset:8192\n"
"v_mfma_f32_16x16x32_bf16 v[48:51], v[140:143], a[232:235], v[48:51]\n"
"v_mfma_f32_16x16x32_bf16 v[52:55], v[140:143], a[248:251], v[52:55]\n"
"s_add_u32 s41, s41, 16384\n"
"v_mfma_f32_16x16x32_bf16 a[0:3], v[80:83], v[56:59], a[0:3]\n"
"ds_read_b64_tr_b16 v[104:105], v10 offset:0\n"
"ds_read_b64_tr_b16 v[106:107], v10 offset:8192\n"
"v_mfma_f32_16x16x32_bf16 a[4:7], v[80:83], v[60:63], a[4:7]\n"
"v_mfma_f32_16x16x32_bf16 a[8:11], v[80:83], v[64:67], a[8:11]\n"
"v_mfma_f32_16x16x32_bf16 a[12:15], v[84:87], v[56:59], a[12:15]\n"
"s_and_b32 s41, s41, 0xfffff\n"
"ds_read_b64_tr_b16 v[108:109], v11 offset:0\n"
"v_mfma_f32_16x16x32_bf16 a[16:19], v[84:87], v[60:63], a[16:19]\n"
"ds_read_b64_tr_b16 v[110:111], v11 offset:8192\n"
"v_max3_f32 v196, v196, v44, v45\n"
"v_mfma_f32_16x16x32_bf16 a[20:23], v[84:87], v[64:67], a[20:23]\n"
"v_max3_f32 v196, v196, v46, v47\n"
"v_max3_f32 v196, v196, v48, v49\n"
"s_waitcnt lgkmcnt(4)\n"
"v_mfma_f32_16x16x32_bf16 a[24:27], v[88:91], v[56:59], a[24:27]\n"
"v_max3_f32 v196, v196, v50, v51\n"
"v_max3_f32 v196, v196, v52, v53\n"
"v_mfma_f32_16x16x32_bf16 a[28:31], v[88:91], v[60:63], a[28:31]\n"
"v_max3_f32 v196, v196, v54, v55\n"
"v_cmp_lt_f32 vcc, 0x41000000, v196\n"
"v_mfma_f32_16x16x32_bf16 a[32:35], v[88:91], v[64:67], a[32:35]\n"
"s_cbranch_vccnz LSTUB7_%=\n"
"LRET7_%=:\n"
"ds_read_b64_tr_b16 v[112:113], v4 offset:256\n"
"v_mfma_f32_16x16x32_bf16 a[36:39], v[92:95], v[56:59], a[36:39]\n"
"ds_read_b64_tr_b16 v[114:115], v4 offset:8448\n"
"ds_read_b64_tr_b16 v[116:117], v5 offset:256\n"
"v_mfma_f32_16x16x32_bf16 a[40:43], v[92:95], v[60:63], a[40:43]\n"
"ds_read_b64_tr_b16 v[118:119], v5 offset:8448\n"
"v_mfma_f32_16x16x32_bf16 a[44:47], v[92:95], v[64:67], a[44:47]\n"
"v_exp_f32 v32, v32\n"
"v_mfma_f32_16x16x32_bf16 a[48:51], v[96:99], v[56:59], a[48:51]\n"
"s_add_u32 m0, s40, 114688\n"
"ds_read_b64_tr_b16 v[120:121], v6 offset:256\n"
"v_mfma_f32_16x16x32_bf16 a[52:55], v[96:99], v[60:63], a[52:55]\n"
"ds_read_b64_tr_b16 v[122:123], v6 offset:8448\n"
"v_add_f32 v205, v205, v32\n"
"v_mfma_f32_16x16x32_bf16 a[56:59], v[96:99], v[64:67], a[56:59]\n"
"v_exp_f32 v33, v33\n"
"v_mfma_f32_16x16x32_bf16 a[60:63], v[100:103], v[56:59], a[60:63]\n"
"ds_read_b64_tr_b16 v[124:125], v7 offset:256\n"
"ds_read_b64_tr_b16 v[126:127], v7 offset:8448\n"
"v_mfma_f32_16x16x32_bf16 a[64:67], v[100:103], v[60:63], a[64:67]\n"
"v_exp_f32 v34, v34\n"
"v_mfma_f32_16x16x32_bf16 a[68:71], v[100:103], v[64:67], a[68:71]\n"
"buffer_load_dwordx4 v16, %1, s42 offen offset:0 nt lds\n"
"v_cvt_pk_bf16_f32 v68, v32, v33\n"
"s_waitcnt lgkmcnt(3)\n"
"v_mfma_f32_16x16x32_bf16 a[72:75], v[104:107], v[56:59], a[72:75]\n"
"ds_read_b64_tr_b16 v[128:129], v8 offset:256\n"
"ds_read_b64_tr_b16 v[130:131], v8 offset:8448\n"
"v_mfma_f32_16x16x32_bf16 a[76:79], v[104:107], v[60:63], a[76:79]\n"
"v_exp_f32 v35, v35\n"
"v_mfma_f32_16x16x32_bf16 a[80:83], v[104:107], v[64:67], a[80:83]\n"
"v_exp_f32 v36, v36\n"
"v_mfma_f32_16x16x32_bf16 a[84:87], v[108:111], v[56:59], a[84:87]\n"
"ds_read_b64_tr_b16 v[132:133], v9 offset:256\n"
"ds_read_b64_tr_b16 v[134:135], v9 offset:8448\n"
"v_mfma_f32_16x16x32_bf16 a[88:91], v[108:111], v[60:63], a[88:91]\n"
"buffer_load_dwordx4 v17, %1, s42 offen offset:1024 nt lds\n"
"v_cvt_pk_bf16_f32 v69, v34, v35\n"
"v_mfma_f32_16x16x32_bf16 a[92:95], v[108:111], v[64:67], a[92:95]\n"
"v_exp_f32 v37, v37\n"
"v_mfma_f32_16x16x32_bf16 a[96:99], v[112:115], v[56:59], a[96:99]\n"
"ds_read_b64_tr_b16 v[136:137], v10 offset:256\n"
"ds_read_b64_tr_b16 v[138:139], v10 offset:8448\n"
"v_mfma_f32_16x16x32_bf16 a[100:103], v[112:115], v[60:63], a[100:103]\n"
"v_exp_f32 v38, v38\n"
"v_mfma_f32_16x16x32_bf16 a[104:107], v[112:115], v[64:67], a[104:107]\n"
"v_exp_f32 v39, v39\n"
"v_mfma_f32_16x16x32_bf16 a[108:111], v[116:119], v[56:59], a[108:111]\n"
"ds_read_b64_tr_b16 v[140:141], v11 offset:256\n"
"ds_read_b64_tr_b16 v[142:143], v11 offset:8448\n"
"v_mfma_f32_16x16x32_bf16 a[112:115], v[116:119], v[60:63], a[112:115]\n"
"s_memtime s[52:53]\n"
"buffer_load_dwordx4 v18, %1, s42 offen offset:2048 nt lds\n"
"v_mfma_f32_16x16x32_bf16 a[116:119], v[116:119], v[64:67], a[116:119]\n"
"v_exp_f32 v40, v40\n"
"s_waitcnt lgkmcnt(3)\n"
"v_mfma_f32_16x16x32_bf16 a[120:123], v[120:123], v[56:59], a[120:123]\n"
"v_exp_f32 v41, v41\n"
"v_mfma_f32_16x16x32_bf16 a[124:127], v[120:123], v[60:63], a[124:127]\n"
"v_exp_f32 v42, v42\n"
"v_mfma_f32_16x16x32_bf16 a[128:131], v[120:123], v[64:67], a[128:131]\n"
"buffer_load_dwordx4 v19, %1, s42 offen offset:3072 nt lds\n"
"v_add_f32 v205, v205, v33\n"
"v_mfma_f32_16x16x32_bf16 a[132:135], v[124:127], v[56:59], a[132:135]\n"
"v_exp_f32 v43, v43\n"
"v_mfma_f32_16x16x32_bf16 a[136:139], v[124:127], v[60:63], a[136:139]\n"
"v_add_f32 v205, v205, v34\n"
"v_add_f32 v205, v205, v35\n"
"v_mfma_f32_16x16x32_bf16 a[140:143], v[124:127], v[64:67], a[140:143]\n"
"v_cvt_pk_bf16_f32 v72, v36, v37\n"
"v_cvt_pk_bf16_f32 v73, v38, v39\n"
"v_mfma_f32_16x16x32_bf16 a[144:147], v[128:131], v[56:59], a[144:147]\n"
"v_add_f32 v206, v206, v36\n"
"v_add_f32 v206, v206, v37\n"
"v_mfma_f32_16x16x32_bf16 a[148:151], v[128:131], v[60:63], a[148:151]\n"
"s_add_u32 s42, s42, 16384\n"
"v_add_f32 v206, v206, v38\n"
"v_mfma_f32_16x16x32_bf16 a[152:155], v[128:131], v[64:67], a[152:155]\n"
"s_waitcnt vmcnt(16) lgkmcnt(0)\n"
"s_barrier\n"
"v_mfma_f32_16x16x32_bf16 a[156:159], v[132:135], v[56:59], a[156:159]\n"
"ds_read_b128 v[80:83], v0 offset:0\n"
"ds_read_b128 v[84:87], v1 offset:0\n"
"v_mfma_f32_16x16x32_bf16 a[160:163], v[132:135], v[60:63], a[160:163]\n"
"ds_read_b128 v[88:91], v2 offset:0\n"
"v_add_f32 v206, v206, v39\n"
"v_mfma_f32_16x16x32_bf16 a[164:167], v[132:135], v[64:67], a[164:167]\n"
"v_cvt_pk_bf16_f32 v76, v40, v41\n"
"v_cvt_pk_bf16_f32 v77, v42, v43\n"
"v_mfma_f32_16x16x32_bf16 a[168:171], v[136:139], v[56:59], a[168:171]\n"
"v_add_f32 v207, v207, v40\n"
"v_add_f32 v207, v207, v41\n"
"v_mfma_f32_16x16x32_bf16 a[172:175], v[136:139], v[60:63], a[172:175]\n"
"v_add_f32 v207, v207, v42\n"
"v_add_f32 v207, v207, v43\n"
"v_mfma_f32_16x16x32_bf16 a[176:179], v[136:139], v[64:67], a[176:179]\n"
"s_sub_u32 s56, s52, s54\n"
"s_mov_b32 s54, s52\n"
"v_mfma_f32_16x16x32_bf16 a[180:183], v[140:143], v[56:59], a[180:183]\n"
"s_sub_u32 s56, s56, s55\n"
"s_cmp_gt_u32 s56, 2100\n"
"v_mfma_f32_16x16x32_bf16 a[184:187], v[140:143], v[60:63], a[184:187]\n"
"s_cselect_b32 s56, 128, -16\n"
"s_add_i32 s55, s55, s56\n"
"v_mfma_f32_16x16x32_bf16 a[188:191], v[140:143], v[64:67], a[188:191]\n"
"s_max_i32 s55, s55, s57\n"
"s_min_i32 s55, s55, 512\n"
"s_lshr_b32 s56, s55, 6\n"
"s_cmp_lg_u32 s56, 0\n"
"s_cbranch_scc1 LY11_%=\n"
"LZ11_%=:\n"
"s_and_b32 s42, s42, 0xfffff\n"
"s_waitcnt lgkmcnt(0)\n"
"v_mfma_f32_16x16x32_bf16 v[32:35], v[80:83], v[144:147], v[20:23]\n"
"ds_read_b128 v[92:95], v3 offset:0\n"
"ds_read_b128 v[96:99], v0 offset:256\n"
"v_mfma_f32_16x16x32_bf16 v[36:39], v[80:83], v[160:163], v[24:27]\n"
"v_exp_f32 v44, v44\n"
"v_mfma_f32_16x16x32_bf16 v[40:43], v[80:83], v[176:179], v[28:31]\n"
"ds_read_b128 v[100:103], v1 offset:256\n"
"v_add_f32 v205, v205, v44\n"
"v_mfma_f32_16x16x32_bf16 v[32:35], v[84:87], v[148:151], v[32:35]\n"
"v_exp_f32 v45, v45\n"
"v_mfma_f32_16x16x32_bf16 v[36:39], v[84:87], v[164:167], v[36:39]\n"
"v_exp_f32 v46, v46\n"
"v_mfma_f32_16x16x32_bf16 v[40:43], v[84:87], v[180:183], v[40:43]\n"
"ds_read_b128 v[104:107], v2 offset:256\n"
"v_cvt_pk_bf16_f32 v70, v44, v45\n"
"v_mfma_f32_16x16x32_bf16 v[32:35], v[88:91], v[152:155], v[32:35]\n"
"v_exp_f32 v47, v47\n"
"v_mfma_f32_16x16x32_bf16 v[36:39], v[88:91], v[168:171], v[36:39]\n"
"v_exp_f32 v48, v48\n"
"v_mfma_f32_16x16x32_bf16 v[40:43], v[88:91], v[184:187], v[40:43]\n"
"ds_read_b128 v[108:111], v3 offset:256\n"
"v_cvt_pk_bf16_f32 v71, v46, v47\n"
"s_waitcnt lgkmcnt(2)\n"
"v_mfma_f32_16x16x32_bf16 v[32:35], v[92:95], v[156:159], v[32:35]\n"
"v_exp_f32 v49, v49\n"
"v_mfma_f32_16x16x32_bf16 v[36:39], v[92:95], v[172:175], v[36:39]\n"
"v_exp_f32 v50, v50\n"
"v_mfma_f32_16x16x32_bf16 v[40:43], v[92:95], v[188:191], v[40:43]\n"
"ds_read_b128 v[112:115], v0 offset:8192\n"
"v_add_f32 v205, v205, v45\n"
"v_mfma_f32_16x16x32_bf16 v[32:35], v[96:99], a[204:207], v[32:35]\n"
"v_exp_f32 v51, v51\n"
"v_mfma_f32_16x16x32_bf16 v[36:39], v[96:99], a[220:223], v[36:39]\n"
"v_add_f32 v205, v205, v46\n"
"v_add_f32 v205, v205, v47\n"
"v_mfma_f32_16x16x32_bf16 v[40:43], v[96:99], a[236:239], v[40:43]\n"
"ds_read_b128 v[116:119], v1 offset:8192\n"
"v_cvt_pk_bf16_f32 v74, v48, v49\n"
"v_mfma_f32_16x16x32_bf16 v[32:35], v[100:103], a[208:211], v[32:35]\n"
"v_exp_f32 v52, v52\n"
"v_mfma_f32_16x16x32_bf16 v[36:39], v[100:103], a[224:227], v[36:39]\n"
"v_exp_f32 v53, v53\n"
"v_mfma_f32_16x16x32_bf16 v[40:43], v[100:103], a[240:243], v[40:43]\n"
"ds_read_b128 v[120:123], v2 offset:8192\n"
"v_cvt_pk_bf16_f32 v75, v50, v51\n"
"s_waitcnt lgkmcnt(2)\n"
"v_mfma_f32_16x16x32_bf16 v[32:35], v[104:107], a[212:215], v[32:35]\n"
"v_exp_f32 v54, v54\n"
"v_mfma_f32_16x16x32_bf16 v[36:39], v[104:107], a[228:231], v[36:39]\n"
"v_exp_f32 v55, v55\n"
"v_mfma_f32_16x16x32_bf16 v[40:43], v[104:107], a[244:247], v[40:43]\n"
"v_add_f32 v206, v206, v48\n"
"v_add_f32 v206, v206, v49\n"
"v_mfma_f32_16x16x32_bf16 v[32:35], v[108:111], a[216:219], v[32:35]\n"
"v_add_f32 v206, v206, v50\n"
"v_add_f32 v206, v206, v51\n"
"v_mfma_f32_16x16x32_bf16 v[36:39], v[108:111], a[232:235], v[36:39]\n"
"v_cvt_pk_bf16_f32 v78, v52, v53\n"
"v_cvt_pk_bf16_f32 v79, v54, v55\n"
"v_mfma_f32_16x16x32_bf16 v[40:43], v[108:111], a[248:251], v[40:43]\n"
"v_add_f32 v207, v207, v52\n"
"v_add_f32 v207, v207, v53\n"
"v_mfma_f32_16x16x32_bf16 v[44:47], v[112:115], v[144:147], v[20:23]\n"
"v_add_f32 v207, v207, v54\n"
"v_add_f32 v207, v207, v55\n"
"v_mfma_f32_16x16x32_bf16 v[48:51], v[112:115], v[160:163], v[24:27]\n"
"ds_read_b128 v[124:127], v3 offset:8192\n"
"ds_read_b128 v[128:131], v0 offset:8448\n"
"v_mfma_f32_16x16x32_bf16 v[52:55], v[112:115], v[176:179], v[28:31]\n"
"ds_read_b128 v[132:135], v1 offset:8448\n"
"s_waitcnt lgkmcnt(3)\n"
"v_mfma_f32_16x16x32_bf16 v[44:47], v[116:119], v[148:151], v[44:47]\n"
"s_add_u32 m0, s40, 49152\n"
"v_max3_f32 v196, v32, v33, v34\n"
"v_mfma_f32_16x16x32_bf16 v[48:51], v[116:119], v[164:167], v[48:51]\n"
"v_max3_f32 v196, v196, v35, v36\n"
"v_max3_f32 v196, v196, v37, v38\n"
"v_mfma_f32_16x16x32_bf16 v[52:55], v[116:119], v[180:183], v[52:55]\n"
"ds_read_b128 v[136:139], v2 offset:8448\n"
"v_max3_f32 v196, v196, v39, v40\n"
"v_mfma_f32_16x16x32_bf16 v[44:47], v[120:123], v[152:155], v[44:47]\n"
"v_max3_f32 v196, v196, v41, v42\n"
"v_max3_f32 v196, v196, v43, v43\n"
"v_mfma_f32_16x16x32_bf16 v[48:51], v[120:123], v[168:171], v[48:51]\n"
"ds_read_b64_tr_b16 v[80:81], v4 offset:16384\n"
"ds_read_b64_tr_b16 v[82:83], v4 offset:24576\n"
"v_mfma_f32_16x16x32_bf16 v[52:55], v[120:123], v[184:187], v[52:55]\n"
"ds_read_b128 v[140:143], v3 offset:8448\n"
"buffer_load_dwordx4 v12, %0, s41 offen offset:0 nt lds\n"
"s_waitcnt lgkmcnt(4)\n"
"v_mfma_f32_16x16x32_bf16 v[44:47], v[124:127], v[156:159], v[44:47]\n"
"ds_read_b64_tr_b16 v[84:85], v5 offset:16384\n"
"ds_read_b64_tr_b16 v[86:87], v5 offset:24576\n"
"v_mfma_f32_16x16x32_bf16 v[48:51], v[124:127], v[172:175], v[48:51]\n"
"v_mfma_f32_16x16x32_bf16 v[52:55], v[124:127], v[188:191], v[52:55]\n"
"buffer_load_dwordx4 v13, %0, s41 offen offset:1024 nt lds\n"
"v_mfma_f32_16x16x32_bf16 v[44:47], v[128:131], a[204:207], v[44:47]\n"
"ds_read_b64_tr_b16 v[88:89], v6 offset:16384\n"
"ds_read_b64_tr_b16 v[90:91], v6 offset:24576\n"
"v_mfma_f32_16x16x32_bf16 v[48:51], v[128:131], a[220:223], v[48:51]\n"
"v_mfma_f32_16x16x32_bf16 v[52:55], v[128:131], a[236:239], v[52:55]\n"
"v_mfma_f32_16x16x32_bf16 v[44:47], v[132:135], a[208:211], v[44:47]\n"
"buffer_load_dwordx4 v14, %0, s41 offen offset:2048 nt lds\n"
"ds_read_b64_tr_b16 v[92:93], v7 offset:16384\n"
"v_mfma_f32_16x16x32_bf16 v[48:51], v[132:135], a[224:227], v[48:51]\n"
"ds_read_b64_tr_b16 v[94:95], v7 offset:24576\n"
"v_mfma_f32_16x16x32_bf16 v[52:55], v[132:135], a[240:243], v[52:55]\n"
"s_waitcnt lgkmcnt(4)\n"
"v_mfma_f32_16x16x32_bf16 v[44:47], v[136:139], a[212:215], v[44:47]\n"
"ds_read_b64_tr_b16 v[96:97], v8 offset:16384\n"
"ds_read_b64_tr_b16 v[98:99], v8 offset:24576\n"
"v_mfma_f32_16x16x32_bf16 v[48:51], v[136:139], a[228:231], v[48:51]\n"
"buffer_load_dwordx4 v15, %0, s41 offen offset:3072 nt lds\n"
"v_mfma_f32_16x16x32_bf16 v[52:55], v[136:139], a[244:247], v[52:55]\n"
"v_mfma_f32_16x16x32_bf16 v[44:47], v[140:143], a[216:219], v[44:47]\n"
"ds_read_b64_tr_b16 v[100:101], v9 offset:16384\n"
"ds_read_b64_tr_b16 v[102:103], v9 offset:24576\n"
"v_mfma_f32_16x16x32_bf16 v[48:51], v[140:143], a[232:235], v[48:51]\n"
"v_mfma_f32_16x16x32_bf16 v[52:55], v[140:143], a[248:251], v[52:55]\n"
"s_add_u32 s41, s41, 16384\n"
"v_mfma_f32_16x16x32_bf16 a[0:3], v[80:83], v[68:71], a[0:3]\n"
"ds_read_b64_tr_b16 v[104:105], v10 offset:16384\n"
"ds_read_b64_tr_b16 v[106:107], v10 offset:24576\n"
"v_mfma_f32_16x16x32_bf16 a[4:7], v[80:83], v[72:75], a[4:7]\n"
"v_mfma_f32_16x16x32_bf16 a[8:11], v[80:83], v[76:79], a[8:11]\n"
"v_mfma_f32_16x16x32_bf16 a[12:15], v[84:87], v[68:71], a[12:15]\n"
"s_and_b32 s41, s41, 0xfffff\n"
"ds_read_b64_tr_b16 v[108:109], v11 offset:16384\n"
"v_mfma_f32_16x16x32_bf16 a[16:19], v[84:87], v[72:75], a[16:19]\n"
"ds_read_b64_tr_b16 v[110:111], v11 offset:24576\n"
"v_max3_f32 v196, v196, v44, v45\n"
"v_mfma_f32_16x16x32_bf16 a[20:23], v[84:87], v[76:79], a[20:23]\n"
"v_max3_f32 v196, v196, v46, v47\n"
"v_max3_f32 v196, v196, v48, v49\n"
"s_waitcnt lgkmcnt(4)\n"
"v_mfma_f32_16x16x32_bf16 a[24:27], v[88:91], v[68:71], a[24:27]\n"
"v_max3_f32 v196, v196, v50, v51\n"
"v_max3_f32 v196, v196, v52, v53\n"
"v_mfma_f32_16x16x32_bf16 a[28:31], v[88:91], v[72:75], a[28:31]\n"
"v_max3_f32 v196, v196, v54, v55\n"
"v_cmp_lt_f32 vcc, 0x41000000, v196\n"
"v_mfma_f32_16x16x32_bf16 a[32:35], v[88:91], v[76:79], a[32:35]\n"
"s_cbranch_vccnz LSTUB8_%=\n"
"LRET8_%=:\n"
"ds_read_b64_tr_b16 v[112:113], v4 offset:16640\n"
"v_mfma_f32_16x16x32_bf16 a[36:39], v[92:95], v[68:71], a[36:39]\n"
"ds_read_b64_tr_b16 v[114:115], v4 offset:24832\n"
"ds_read_b64_tr_b16 v[116:117], v5 offset:16640\n"
"v_mfma_f32_16x16x32_bf16 a[40:43], v[92:95], v[72:75], a[40:43]\n"
"ds_read_b64_tr_b16 v[118:119], v5 offset:24832\n"
"v_mfma_f32_16x16x32_bf16 a[44:47], v[92:95], v[76:79], a[44:47]\n"
"v_exp_f32 v32, v32\n"
"v_mfma_f32_16x16x32_bf16 a[48:51], v[96:99], v[68:71], a[48:51]\n"
"s_add_u32 m0, s40, 65536\n"
"ds_read_b64_tr_b16 v[120:121], v6 offset:16640\n"
"v_mfma_f32_16x16x32_bf16 a[52:55], v[96:99], v[72:75], a[52:55]\n"
"ds_read_b64_tr_b16 v[122:123], v6 offset:24832\n"
"v_add_f32 v205, v205, v32\n"
"v_mfma_f32_16x16x32_bf16 a[56:59], v[96:99], v[76:79], a[56:59]\n"
"v_exp_f32 v33, v33\n"
"v_mfma_f32_16x16x32_bf16 a[60:63], v[100:103], v[68:71], a[60:63]\n"
"ds_read_b64_tr_b16 v[124:125], v7 offset:16640\n"
"ds_read_b64_tr_b16 v[126:127], v7 offset:24832\n"
"v_mfma_f32_16x16x32_bf16 a[64:67], v[100:103], v[72:75], a[64:67]\n"
"v_exp_f32 v34, v34\n"
"v_mfma_f32_16x16x32_bf16 a[68:71], v[100:103], v[76:79], a[68:71]\n"
"buffer_load_dwordx4 v16, %1, s42 offen offset:0 nt lds\n"
"v_cvt_pk_bf16_f32 v56, v32, v33\n"
"s_waitcnt lgkmcnt(3)\n"
"v_mfma_f32_16x16x32_bf16 a[72:75], v[104:107], v[68:71], a[72:75]\n"
"ds_read_b64_tr_b16 v[128:129], v8 offset:16640\n"
"ds_read_b64_tr_b16 v[130:131], v8 offset:24832\n"
"v_mfma_f32_16x16x32_bf16 a[76:79], v[104:107], v[72:75], a[76:79]\n"
"v_exp_f32 v35, v35\n"
"v_mfma_f32_16x16x32_bf16 a[80:83], v[104:107], v[76:79], a[80:83]\n"
"v_exp_f32 v36, v36\n"
"v_mfma_f32_16x16x32_bf16 a[84:87], v[108:111], v[68:71], a[84:87]\n"
"ds_read_b64_tr_b16 v[132:133], v9 offset:16640\n"
"ds_read_b64_tr_b16 v[134:135], v9 offset:24832\n"
"v_mfma_f32_16x16x32_bf16 a[88:91], v[108:111], v[72:75], a[88:91]\n"
"buffer_load_dwordx4 v17, %1, s42 offen offset:1024 nt lds\n"
"v_cvt_pk_bf16_f32 v57, v34, v35\n"
"v_mfma_f32_16x16x32_bf16 a[92:95], v[108:111], v[76:79], a[92:95]\n"
"v_exp_f32 v37, v37\n"
"v_mfma_f32_16x16x32_bf16 a[96:99], v[112:115], v[68:71], a[96:99]\n"
"ds_read_b64_tr_b16 v[136:137], v10 offset:16640\n"
"ds_read_b64_tr_b16 v[138:139], v10 offset:24832\n"
"v_mfma_f32_16x16x32_bf16 a[100:103], v[112:115], v[72:75], a[100:103]\n"
"v_exp_f32 v38, v38\n"
"v_mfma_f32_16x16x32_bf16 a[104:107], v[112:115], v[76:79], a[104:107]\n"
"v_exp_f32 v39, v39\n"
"v_mfma_f32_16x16x32_bf16 a[108:111], v[116:119], v[68:71], a[108:111]\n"
"ds_read_b64_tr_b16 v[140:141], v11 offset:16640\n"
"ds_read_b64_tr_b16 v[142:143], v11 offset:24832\n"
"v_mfma_f32_16x16x32_bf16 a[112:115], v[116:119], v[72:75], a[112:115]\n"
"s_memtime s[52:53]\n"
"buffer_load_dwordx4 v18, %1, s42 offen offset:2048 nt lds\n"
"v_mfma_f32_16x16x32_bf16 a[116:119], v[116:119], v[76:79], a[116:119]\n"
"v_exp_f32 v40, v40\n"
"s_waitcnt lgkmcnt(3)\n"
"v_mfma_f32_16x16x32_bf16 a[120:123], v[120:123], v[68:71], a[120:123]\n"
"v_exp_f32 v41, v41\n"
"v_mfma_f32_16x16x32_bf16 a[124:127], v[120:123], v[72:75], a[124:127]\n"
"v_exp_f32 v42, v42\n"
"v_mfma_f32_16x16x32_bf16 a[128:131], v[120:123], v[76:79], a[128:131]\n"
"buffer_load_dwordx4 v19, %1, s42 offen offset:3072 nt lds\n"
"v_add_f32 v205, v205, v33\n"
"v_mfma_f32_16x16x32_bf16 a[132:135], v[124:127], v[68:71], a[132:135]\n"
"v_exp_f32 v43, v43\n"
"v_mfma_f32_16x16x32_bf16 a[136:139], v[124:127], v[72:75], a[136:139]\n"
"v_add_f32 v205, v205, v34\n"
"v_add_f32 v205, v205, v35\n"
"v_mfma_f32_16x16x32_bf16 a[140:143], v[124:127], v[76:79], a[140:143]\n"
"v_cvt_pk_bf16_f32 v60, v36, v37\n"
"v_cvt_pk_bf16_f32 v61, v38, v39\n"
"v_mfma_f32_16x16x32_bf16 a[144:147], v[128:131], v[68:71], a[144:147]\n"
"v_add_f32 v206, v206, v36\n"
"v_add_f32 v206, v206, v37\n"
"v_mfma_f32_16x16x32_bf16 a[148:151], v[128:131], v[72:75], a[148:151]\n"
"s_add_u32 s42, s42, 16384\n"
"v_add_f32 v206, v206, v38\n"
"v_mfma_f32_16x16x32_bf16 a[152:155], v[128:131], v[76:79], a[152:155]\n"
"s_waitcnt vmcnt(16) lgkmcnt(0)\n"
"s_barrier\n"
"v_mfma_f32_16x16x32_bf16 a[156:159], v[132:135], v[68:71], a[156:159]\n"
"ds_read_b128 v[80:83], v0 offset:16384\n"
"ds_read_b128 v[84:87], v1 offset:16384\n"
"v_mfma_f32_16x16x32_bf16 a[160:163], v[132:135], v[72:75], a[160:163]\n"
"ds_read_b128 v[88:91], v2 offset:16384\n"
"v_add_f32 v206, v206, v39\n"
"v_mfma_f32_16x16x32_bf16 a[164:167], v[132:135], v[76:79], a[164:167]\n"
"v_cvt_pk_bf16_f32 v64, v40, v41\n"
"v_cvt_pk_bf16_f32 v65, v42, v43\n"
"v_mfma_f32_16x16x32_bf16 a[168:171], v[136:139], v[68:71], a[168:171]\n"
"v_add_f32 v207, v207, v40\n"
"v_add_f32 v207, v207, v41\n"
"v_mfma_f32_16x16x32_bf16 a[172:175], v[136:139], v[72:75], a[172:175]\n"
"v_add_f32 v207, v207, v42\n"
"v_add_f32 v207, v207, v43\n"
"v_mfma_f32_16x16x32_bf16 a[176:179], v[136:139], v[76:79], a[176:179]\n"
"s_sub_u32 s56, s52, s54\n"
"s_mov_b32 s54, s52\n"
"v_mfma_f32_16x16x32_bf16 a[180:183], v[140:143], v[68:71], a[180:183]\n"
"s_sub_u32 s56, s56, s55\n"
"s_cmp_gt_u32 s56, 2100\n"
"v_mfma_f32_16x16x32_bf16 a[184:187], v[140:143], v[72:75], a[184:187]\n"
"s_cselect_b32 s56, 128, -16\n"
"s_add_i32 s55, s55, s56\n"
"v_mfma_f32_16x16x32_bf16 a[188:191], v[140:143], v[76:79], a[188:191]\n"
"s_max_i32 s55, s55, s57\n"
"s_min_i32 s55, s55, 512\n"
"s_lshr_b32 s56, s55, 6\n"
"s_cmp_lg_u32 s56, 0\n"
"s_cbranch_scc1 LY12_%=\n"
"LZ12_%=:\n"
"s_and_b32 s42, s42, 0xfffff\n"
"s_sub_u32 s43, s43, 1\n"
"s_cmp_lg_u32 s43, 0\n"
"s_cbranch_scc1 LLOOPB3_%=\n"
"LTAIL3_%=:\n"
"s_waitcnt lgkmcnt(0)\n"
"v_mfma_f32_16x16x32_bf16 v[32:35], v[80:83], v[144:147], v[20:23]\n"
"ds_read_b128 v[92:95], v3 offset:16384\n"
"ds_read_b128 v[96:99], v0 offset:16640\n"
"v_mfma_f32_16x16x32_bf16 v[36:39], v[80:83], v[160:163], v[24:27]\n"
"v_exp_f32 v44, v44\n"
"v_mfma_f32_16x16x32_bf16 v[40:43], v[80:83], v[176:179], v[28:31]\n"
"ds_read_b128 v[100:103], v1 offset:16640\n"
"v_add_f32 v205, v205, v44\n"
"v_mfma_f32_16x16x32_bf16 v[32:35], v[84:87], v[148:151], v[32:35]\n"
"v_exp_f32 v45, v45\n"
"v_mfma_f32_16x16x32_bf16 v[36:39], v[84:87], v[164:167], v[36:39]\n"
"v_exp_f32 v46, v46\n"
"v_mfma_f32_16x16x32_bf16 v[40:43], v[84:87], v[180:183], v[40:43]\n"
"ds_read_b128 v[104:107], v2 offset:16640\n"
"v_cvt_pk_bf16_f32 v58, v44, v45\n"
"v_mfma_f32_16x16x32_bf16 v[32:35], v[88:91], v[152:155], v[32:35]\n"
"v_exp_f32 v47, v47\n"
"v_mfma_f32_16x16x32_bf16 v[36:39], v[88:91], v[168:171], v[36:39]\n"
"v_exp_f32 v48, v48\n"
"v_mfma_f32_16x16x32_bf16 v[40:43], v[88:91], v[184:187], v[40:43]\n"
"ds_read_b128 v[108:111], v3 offset:16640\n"
"v_cvt_pk_bf16_f32 v59, v46, v47\n"
"s_waitcnt lgkmcnt(2)\n"
"v_mfma_f32_16x16x32_bf16 v[32:35], v[92:95], v[156:159], v[32:35]\n"
"v_exp_f32 v49, v49\n"
"v_mfma_f32_16x16x32_bf16 v[36:39], v[92:95], v[172:175], v[36:39]\n"
"v_exp_f32 v50, v50\n"
"v_mfma_f32_16x16x32_bf16 v[40:43], v[92:95], v[188:191], v[40:43]\n"
"ds_read_b128 v[112:115], v0 offset:24576\n"
"v_add_f32 v205, v205, v45\n"
"v_mfma_f32_16x16x32_bf16 v[32:35], v[96:99], a[204:207], v[32:35]\n"
"v_exp_f32 v51, v51\n"
"v_mfma_f32_16x16x32_bf16 v[36:39], v[96:99], a[220:223], v[36:39]\n"
"v_add_f32 v205, v205, v46\n"
"v_add_f32 v205, v205, v47\n"
"v_mfma_f32_16x16x32_bf16 v[40:43], v[96:99], a[236:239], v[40:43]\n"
"ds_read_b128 v[116:119], v1 offset:24576\n"
"v_cvt_pk_bf16_f32 v62, v48, v49\n"
"v_mfma_f32_16x16x32_bf16 v[32:35], v[100:103], a[208:211], v[32:35]\n"
"v_exp_f32 v52, v52\n"
"v_mfma_f32_16x16x32_bf16 v[36:39], v[100:103], a[224:227], v[36:39]\n"
"v_exp_f32 v53, v53\n"
"v_mfma_f32_16x16x32_bf16 v[40:43], v[100:103], a[240:243], v[40:43]\n"
"ds_read_b128 v[120:123], v2 offset:24576\n"
"v_cvt_pk_bf16_f32 v63, v50, v51\n"
"s_waitcnt lgkmcnt(2)\n"
"v_mfma_f32_16x16x32_bf16 v[32:35], v[104:107], a[212:215], v[32:35]\n"
"v_exp_f32 v54, v54\n"
"v_mfma_f32_16x16x32_bf16 v[36:39], v[104:107], a[228:231], v[36:39]\n"
"v_exp_f32 v55, v55\n"
"v_mfma_f32_16x16x32_bf16 v[40:43], v[104:107], a[244:247], v[40:43]\n"
"v_add_f32 v206, v206, v48\n"
"v_add_f32 v206, v206, v49\n"
"v_mfma_f32_16x16x32_bf16 v[32:35], v[108:111], a[216:219], v[32:35]\n"
"v_add_f32 v206, v206, v50\n"
"v_add_f32 v206, v206, v51\n"
"v_mfma_f32_16x16x32_bf16 v[36:39], v[108:111], a[232:235], v[36:39]\n"
"s_add_u32 m0, s40, 81920\n"
"v_cvt_pk_bf16_f32 v66, v52, v53\n"
"v_mfma_f32_16x16x32_bf16 v[40:43], v[108:111], a[248:251], v[40:43]\n"
"v_cvt_pk_bf16_f32 v67, v54, v55\n"
"v_add_f32 v207, v207, v52\n"
"v_mfma_f32_16x16x32_bf16 v[44:47], v[112:115], v[144:147], v[20:23]\n"
"v_add_f32 v207, v207, v53\n"
"v_add_f32 v207, v207, v54\n"
"v_mfma_f32_16x16x32_bf16 v[48:51], v[112:115], v[160:163], v[24:27]\n"
"v_add_f32 v207, v207, v55\n"
"ds_read_b128 v[124:127], v3 offset:24576\n"
"v_mfma_f32_16x16x32_bf16 v[52:55], v[112:115], v[176:179], v[28:31]\n"
"buffer_load_dwordx4 v16, %1, s42 offen offset:0 lds\n"
"ds_read_b128 v[128:131], v0 offset:24832\n"
"s_waitcnt lgkmcnt(2)\n"
"v_mfma_f32_16x16x32_bf16 v[44:47], v[116:119], v[148:151], v[44:47]\n"
"buffer_load_dwordx4 v17, %1, s42 offen offset:1024 lds\n"
"ds_read_b128 v[132:135], v1 offset:24832\n"
"v_mfma_f32_16x16x32_bf16 v[48:51], v[116:119], v[164:167], v[48:51]\n"
"buffer_load_dwordx4 v18, %1, s42 offen offset:2048 lds\n"
"buffer_load_dwordx4 v19, %1, s42 offen offset:3072 lds\n"
"v_mfma_f32_16x16x32_bf16 v[52:55], v[116:119], v[180:183], v[52:55]\n"
"ds_read_b128 v[136:139], v2 offset:24832\n"
"v_max3_f32 v196, v32, v33, v34\n"
"v_mfma_f32_16x16x32_bf16 v[44:47], v[120:123], v[152:155], v[44:47]\n"
"v_max3_f32 v196, v196, v35, v36\n"
"v_max3_f32 v196, v196, v37, v38\n"
"v_mfma_f32_16x16x32_bf16 v[48:51], v[120:123], v[168:171], v[48:51]\n"
"v_max3_f32 v196, v196, v39, v40\n"
"v_max3_f32 v196, v196, v41, v42\n"
"v_mfma_f32_16x16x32_bf16 v[52:55], v[120:123], v[184:187], v[52:55]\n"
"ds_read_b128 v[140:143], v3 offset:24832\n"
"v_max3_f32 v196, v196, v43, v43\n"
"s_waitcnt lgkmcnt(2)\n"
"v_mfma_f32_16x16x32_bf16 v[44:47], v[124:127], v[156:159], v[44:47]\n"
"ds_read_b64_tr_b16 v[80:81], v4 offset:32768\n"
"ds_read_b64_tr_b16 v[82:83], v4 offset:40960\n"
"v_mfma_f32_16x16x32_bf16 v[48:51], v[124:127], v[172:175], v[48:51]\n"
"s_add_u32 s42, s42, 16384\n"
"ds_read_b64_tr_b16 v[84:85], v5 offset:32768\n"
"v_mfma_f32_16x16x32_bf16 v[52:55], v[124:127], v[188:191], v[52:55]\n"
"ds_read_b64_tr_b16 v[86:87], v5 offset:40960\n"
"s_and_b32 s42, s42, 0xfffff\n"
"v_mfma_f32_16x16x32_bf16 v[44:47], v[128:131], a[204:207], v[44:47]\n"
"ds_read_b64_tr_b16 v[88:89], v6 offset:32768\n"
"ds_read_b64_tr_b16 v[90:91], v6 offset:40960\n"
"v_mfma_f32_16x16x32_bf16 v[48:51], v[128:131], a[220:223], v[48:51]\n"
"v_mfma_f32_16x16x32_bf16 v[52:55], v[128:131], a[236:239], v[52:55]\n"
"v_mfma_f32_16x16x32_bf16 v[44:47], v[132:135], a[208:211], v[44:47]\n"
"s_mov_b32 s47, 0\n"
"s_add_u32 m0, s48, 131072\n"
"v_mfma_f32_16x16x32_bf16 v[48:51], v[132:135], a[224:227], v[48:51]\n"
"buffer_load_dwordx4 v208, %4, s47 offen offset:0 lds\n"
"ds_read_b64_tr_b16 v[92:93], v7 offset:32768\n"
"v_mfma_f32_16x16x32_bf16 v[52:55], v[132:135], a[240:243], v[52:55]\n"
"ds_read_b64_tr_b16 v[94:95], v7 offset:40960\n"
"s_waitcnt lgkmcnt(4)\n"
"v_mfma_f32_16x16x32_bf16 v[44:47], v[136:139], a[212:215], v[44:47]\n"
"ds_read_b64_tr_b16 v[96:97], v8 offset:32768\n"
"ds_read_b64_tr_b16 v[98:99], v8 offset:40960\n"
"v_mfma_f32_16x16x32_bf16 v[48:51], v[136:139], a[228:231], v[48:51]\n"
"v_mfma_f32_16x16x32_bf16 v[52:55], v[136:139], a[244:247], v[52:55]\n"
"v_mfma_f32_16x16x32_bf16 v[44:47], v[140:143], a[216:219], v[44:47]\n"
"buffer_load_dwordx4 v209, %4, s47 offen offset:1024 lds\n"
"ds_read_b64_tr_b16 v[100:101], v9 offset:32768\n"
"v_mfma_f32_16x16x32_bf16 v[48:51], v[140:143], a[232:235], v[48:51]\n"
"ds_read_b64_tr_b16 v[102:103], v9 offset:40960\n"
"v_mfma_f32_16x16x32_bf16 v[52:55], v[140:143], a[248:251], v[52:55]\n"
"v_mfma_f32_16x16x32_bf16 a[0:3], v[80:83], v[56:59], a[0:3]\n"
"ds_read_b64_tr_b16 v[104:105], v10 offset:32768\n"
"ds_read_b64_tr_b16 v[106:107], v10 offset:40960\n"
"v_mfma_f32_16x16x32_bf16 a[4:7], v[80:83], v[60:63], a[4:7]\n"
"v_mfma_f32_16x16x32_bf16 a[8:11], v[80:83], v[64:67], a[8:11]\n"
"v_mfma_f32_16x16x32_bf16 a[12:15], v[84:87], v[56:59], a[12:15]\n"
"buffer_load_dwordx4 v210, %4, s47 offen offset:2048 lds\n"
"ds_read_b64_tr_b16 v[108:109], v11 offset:32768\n"
"v_mfma_f32_16x16x32_bf16 a[16:19], v[84:87], v[60:63], a[16:19]\n"
"ds_read_b64_tr_b16 v[110:111], v11 offset:40960\n"
"v_max3_f32 v196, v196, v44, v45\n"
"v_mfma_f32_16x16x32_bf16 a[20:23], v[84:87], v[64:67], a[20:23]\n"
"v_max3_f32 v196, v196, v46, v47\n"
"v_max3_f32 v196, v196, v48, v49\n"
"s_waitcnt lgkmcnt(4)\n"
"v_mfma_f32_16x16x32_bf16 a[24:27], v[88:91], v[56:59], a[24:27]\n"
"v_max3_f32 v196, v196, v50, v51\n"
"v_max3_f32 v196, v196, v52, v53\n"
"v_mfma_f32_16x16x32_bf16 a[28:31], v[88:91], v[60:63], a[28:31]\n"
"v_max3_f32 v196, v196, v54, v55\n"
"v_cmp_lt_f32 vcc, 0x41000000, v196\n"
"v_mfma_f32_16x16x32_bf16 a[32:35], v[88:91], v[64:67], a[32:35]\n"
"buffer_load_dwordx4 v211, %4, s47 offen offset:3072 lds\n"
"s_cbranch_vccnz LSTUB9_%=\n"
"LRET9_%=:\n"
"v_mfma_f32_16x16x32_bf16 a[36:39], v[92:95], v[56:59], a[36:39]\n"
"ds_read_b64_tr_b16 v[112:113], v4 offset:33024\n"
"ds_read_b64_tr_b16 v[114:115], v4 offset:41216\n"
"v_mfma_f32_16x16x32_bf16 a[40:43], v[92:95], v[60:63], a[40:43]\n"
"ds_read_b64_tr_b16 v[116:117], v5 offset:33024\n"
"ds_read_b64_tr_b16 v[118:119], v5 offset:41216\n"
"v_mfma_f32_16x16x32_bf16 a[44:47], v[92:95], v[64:67], a[44:47]\n"
"v_exp_f32 v32, v32\n"
"v_mfma_f32_16x16x32_bf16 a[48:51], v[96:99], v[56:59], a[48:51]\n"
"ds_read_b64_tr_b16 v[120:121], v6 offset:33024\n"
"ds_read_b64_tr_b16 v[122:123], v6 offset:41216\n"
"v_mfma_f32_16x16x32_bf16 a[52:55], v[96:99], v[60:63], a[52:55]\n"
"s_mov_b32 s47, 28672\n"
"s_add_u32 m0, s48, 135168\n"
"v_mfma_f32_16x16x32_bf16 a[56:59], v[96:99], v[64:67], a[56:59]\n"
"buffer_load_dwordx4 v208, %4, s47 offen offset:0 lds\n"
"v_add_f32 v205, v205, v32\n"
"v_mfma_f32_16x16x32_bf16 a[60:63], v[100:103], v[56:59], a[60:63]\n"
"ds_read_b64_tr_b16 v[124:125], v7 offset:33024\n"
"ds_read_b64_tr_b16 v[126:127], v7 offset:41216\n"
"v_mfma_f32_16x16x32_bf16 a[64:67], v[100:103], v[60:63], a[64:67]\n"
"v_exp_f32 v33, v33\n"
"v_mfma_f32_16x16x32_bf16 a[68:71], v[100:103], v[64:67], a[68:71]\n"
"v_exp_f32 v34, v34\n"
"s_waitcnt lgkmcnt(2)\n"
"v_mfma_f32_16x16x32_bf16 a[72:75], v[104:107], v[56:59], a[72:75]\n"
"ds_read_b64_tr_b16 v[128:129], v8 offset:33024\n"
"ds_read_b64_tr_b16 v[130:131], v8 offset:41216\n"
"v_mfma_f32_16x16x32_bf16 a[76:79], v[104:107], v[60:63], a[76:79]\n"
"v_exp_f32 v35, v35\n"
"v_mfma_f32_16x16x32_bf16 a[80:83], v[104:107], v[64:67], a[80:83]\n"
"buffer_load_dwordx4 v209, %4, s47 offen offset:1024 lds\n"
"v_cvt_pk_bf16_f32 v68, v32, v33\n"
"v_mfma_f32_16x16x32_bf16 a[84:87], v[108:111], v[56:59], a[84:87]\n"
"ds_read_b64_tr_b16 v[132:133], v9 offset:33024\n"
"ds_read_b64_tr_b16 v[134:135], v9 offset:41216\n"
"v_mfma_f32_16x16x32_bf16 a[88:91], v[108:111], v[60:63], a[88:91]\n"
"v_exp_f32 v36, v36\n"
"v_mfma_f32_16x16x32_bf16 a[92:95], v[108:111], v[64:67], a[92:95]\n"
"v_exp_f32 v37, v37\n"
"v_mfma_f32_16x16x32_bf16 a[96:99], v[112:115], v[56:59], a[96:99]\n"
"ds_read_b64_tr_b16 v[136:137], v10 offset:33024\n"
"ds_read_b64_tr_b16 v[138:139], v10 offset:41216\n"
"v_mfma_f32_16x16x32_bf16 a[100:103], v[112:115], v[60:63], a[100:103]\n"
"buffer_load_dwordx4 v210, %4, s47 offen offset:2048 lds\n"
"v_cvt_pk_bf16_f32 v69, v34, v35\n"
"v_mfma_f32_16x16x32_bf16 a[104:107], v[112:115], v[64:67], a[104:107]\n"
"v_exp_f32 v38, v38\n"
"v_mfma_f32_16x16x32_bf16 a[108:111], v[116:119], v[56:59], a[108:111]\n"
"ds_read_b64_tr_b16 v[140:141], v11 offset:33024\n"
"ds_read_b64_tr_b16 v[142:143], v11 offset:41216\n"
"v_mfma_f32_16x16x32_bf16 a[112:115], v[116:119], v[60:63], a[112:115]\n"
"v_exp_f32 v39, v39\n"
"v_mfma_f32_16x16x32_bf16 a[116:119], v[116:119], v[64:67], a[116:119]\n"
"v_exp_f32 v40, v40\n"
"v_mfma_f32_16x16x32_bf16 a[120:123], v[120:123], v[56:59], a[120:123]\n"
"v_exp_f32 v41, v41\n"
"v_mfma_f32_16x16x32_bf16 a[124:127], v[120:123], v[60:63], a[124:127]\n"
"buffer_load_dwordx4 v211, %4, s47 offen offset:3072 lds\n"
"v_add_f32 v205, v205, v33\n"
"v_mfma_f32_16x16x32_bf16 a[128:131], v[120:123], v[64:67], a[128:131]\n"
"v_exp_f32 v42, v42\n"
"s_waitcnt lgkmcnt(2)\n"
"v_mfma_f32_16x16x32_bf16 a[132:135], v[124:127], v[56:59], a[132:135]\n"
"v_exp_f32 v43, v43\n"
"v_mfma_f32_16x16x32_bf16 a[136:139], v[124:127], v[60:63], a[136:139]\n"
"v_add_f32 v205, v205, v34\n"
"v_add_f32 v205, v205, v35\n"
"v_mfma_f32_16x16x32_bf16 a[140:143], v[124:127], v[64:67], a[140:143]\n"
"v_cvt_pk_bf16_f32 v72, v36, v37\n"
"v_cvt_pk_bf16_f32 v73, v38, v39\n"
"v_mfma_f32_16x16x32_bf16 a[144:147], v[128:131], v[56:59], a[144:147]\n"
"v_add_f32 v206, v206, v36\n"
"v_add_f32 v206, v206, v37\n"
"v_mfma_f32_16x16x32_bf16 a[148:151], v[128:131], v[60:63], a[148:151]\n"
"v_add_f32 v206, v206, v38\n"
"v_add_f32 v206, v206, v39\n"
"v_mfma_f32_16x16x32_bf16 a[152:155], v[128:131], v[64:67], a[152:155]\n"
"s_waitcnt vmcnt(20) lgkmcnt(0)\n"
"s_barrier\n"
"v_mfma_f32_16x16x32_bf16 a[156:159], v[132:135], v[56:59], a[156:159]\n"
"ds_read_b128 v[80:83], v0 offset:32768\n"
"ds_read_b128 v[84:87], v1 offset:32768\n"
"v_mfma_f32_16x16x32_bf16 a[160:163], v[132:135], v[60:63], a[160:163]\n"
"ds_read_b128 v[88:91], v2 offset:32768\n"
"v_cvt_pk_bf16_f32 v76, v40, v41\n"
"v_mfma_f32_16x16x32_bf16 a[164:167], v[132:135], v[64:67], a[164:167]\n"
"v_cvt_pk_bf16_f32 v77, v42, v43\n"
"v_add_f32 v207, v207, v40\n"
"v_mfma_f32_16x16x32_bf16 a[168:171], v[136:139], v[56:59], a[168:171]\n"
"v_add_f32 v207, v207, v41\n"
"v_add_f32 v207, v207, v42\n"
"v_mfma_f32_16x16x32_bf16 a[172:175], v[136:139], v[60:63], a[172:175]\n"
"v_add_f32 v207, v207, v43\n"
"v_mfma_f32_16x16x32_bf16 a[176:179], v[136:139], v[64:67], a[176:179]\n"
"v_mfma_f32_16x16x32_bf16 a[180:183], v[140:143], v[56:59], a[180:183]\n"
"v_mfma_f32_16x16x32_bf16 a[184:187], v[140:143], v[60:63], a[184:187]\n"
"v_mfma_f32_16x16x32_bf16 a[188:191], v[140:143], v[64:67], a[188:191]\n"
"s_waitcnt lgkmcnt(0)\n"
"v_mfma_f32_16x16x32_bf16 v[32:35], v[80:83], v[144:147], v[20:23]\n"
"ds_read_b128 v[92:95], v3 offset:32768\n"
"ds_read_b128 v[96:99], v0 offset:33024\n"
"v_mfma_f32_16x16x32_bf16 v[36:39], v[80:83], v[160:163], v[24:27]\n"
"v_exp_f32 v44, v44\n"
"v_mfma_f32_16x16x32_bf16 v[40:43], v[80:83], v[176:179], v[28:31]\n"
"ds_read_b128 v[100:103], v1 offset:33024\n"
"v_add_f32 v205, v205, v44\n"
"v_mfma_f32_16x16x32_bf16 v[32:35], v[84:87], v[148:151], v[32:35]\n"
"s_mov_b32 s47, 128\n"
"s_add_u32 m0, s48, 0\n"
"v_mfma_f32_16x16x32_bf16 v[36:39], v[84:87], v[164:167], v[36:39]\n"
"buffer_load_dwordx4 v208, %4, s47 offen offset:0 lds\n"
"v_mfma_f32_16x16x32_bf16 v[40:43], v[84:87], v[180:183], v[40:43]\n"
"ds_read_b128 v[104:107], v2 offset:33024\n"
"v_mfma_f32_16x16x32_bf16 v[32:35], v[88:91], v[152:155], v[32:35]\n"
"v_exp_f32 v45, v45\n"
"v_mfma_f32_16x16x32_bf16 v[36:39], v[88:91], v[168:171], v[36:39]\n"
"v_exp_f32 v46, v46\n"
"v_mfma_f32_16x16x32_bf16 v[40:43], v[88:91], v[184:187], v[40:43]\n"
"ds_read_b128 v[108:111], v3 offset:33024\n"
"v_cvt_pk_bf16_f32 v70, v44, v45\n"
"s_waitcnt lgkmcnt(2)\n"
"v_mfma_f32_16x16x32_bf16 v[32:35], v[92:95], v[156:159], v[32:35]\n"
"v_exp_f32 v47, v47\n"
"v_mfma_f32_16x16x32_bf16 v[36:39], v[92:95], v[172:175], v[36:39]\n"
"v_exp_f32 v48, v48\n"
"v_mfma_f32_16x16x32_bf16 v[40:43], v[92:95], v[188:191], v[40:43]\n"
"ds_read_b128 v[112:115], v0 offset:40960\n"
"v_cvt_pk_bf16_f32 v71, v46, v47\n"
"v_mfma_f32_16x16x32_bf16 v[32:35], v[96:99], a[204:207], v[32:35]\n"
"v_exp_f32 v49, v49\n"
"v_mfma_f32_16x16x32_bf16 v[36:39], v[96:99], a[220:223], v[36:39]\n"
"v_exp_f32 v50, v50\n"
"v_mfma_f32_16x16x32_bf16 v[40:43], v[96:99], a[236:239], v[40:43]\n"
"buffer_load_dwordx4 v209, %4, s47 offen offset:1024 lds\n"
"ds_read_b128 v[116:119], v1 offset:40960\n"
"v_mfma_f32_16x16x32_bf16 v[32:35], v[100:103], a[208:211], v[32:35]\n"
"v_exp_f32 v51, v51\n"
"v_mfma_f32_16x16x32_bf16 v[36:39], v[100:103], a[224:227], v[36:39]\n"
"v_add_f32 v205, v205, v45\n"
"v_add_f32 v205, v205, v46\n"
"v_mfma_f32_16x16x32_bf16 v[40:43], v[100:103], a[240:243], v[40:43]\n"
"ds_read_b128 v[120:123], v2 offset:40960\n"
"v_add_f32 v205, v205, v47\n"
"s_waitcnt lgkmcnt(2)\n"
"v_mfma_f32_16x16x32_bf16 v[32:35], v[104:107], a[212:215], v[32:35]\n"
"v_exp_f32 v52, v52\n"
"v_mfma_f32_16x16x32_bf16 v[36:39], v[104:107], a[228:231], v[36:39]\n"
"v_exp_f32 v53, v53\n"
"v_mfma_f32_16x16x32_bf16 v[40:43], v[104:107], a[244:247], v[40:43]\n"
"v_exp_f32 v54, v54\n"
"v_mfma_f32_16x16x32_bf16 v[32:35], v[108:111], a[216:219], v[32:35]\n"
"v_exp_f32 v55, v55\n"
"v_mfma_f32_16x16x32_bf16 v[36:39], v[108:111], a[232:235], v[36:39]\n"
"v_cvt_pk_bf16_f32 v74, v48, v49\n"
"v_cvt_pk_bf16_f32 v75, v50, v51\n"
"v_mfma_f32_16x16x32_bf16 v[40:43], v[108:111], a[248:251], v[40:43]\n"
"v_add_f32 v206, v206, v48\n"
"v_add_f32 v206, v206, v49\n"
"v_mfma_f32_16x16x32_bf16 v[44:47], v[112:115], v[144:147], v[20:23]\n"
"v_add_f32 v206, v206, v50\n"
"v_add_f32 v206, v206, v51\n"
"v_mfma_f32_16x16x32_bf16 v[48:51], v[112:115], v[160:163], v[24:27]\n"
"v_cvt_pk_bf16_f32 v78, v52, v53\n"
"v_cvt_pk_bf16_f32 v79, v54, v55\n"
"v_add_f32 v207, v207, v52\n"
"v_add_f32 v207, v207, v53\n"
"v_add_f32 v207, v207, v54\n"
"v_add_f32 v207, v207, v55\n"
"ds_read_b128 v[124:127], v3 offset:40960\n"
"v_mfma_f32_16x16x32_bf16 v[52:55], v[112:115], v[176:179], v[28:31]\n"
"ds_read_b128 v[128:131], v0 offset:41216\n"
"buffer_load_dwordx4 v210, %4, s47 offen offset:2048 lds\n"
"s_waitcnt lgkmcnt(2)\n"
"v_mfma_f32_16x16x32_bf16 v[44:47], v[116:119], v[148:151], v[44:47]\n"
"ds_read_b128 v[132:135], v1 offset:41216\n"
"v_max3_f32 v196, v32, v33, v34\n"
"v_mfma_f32_16x16x32_bf16 v[48:51], v[116:119], v[164:167], v[48:51]\n"
"v_max3_f32 v196, v196, v35, v36\n"
"v_max3_f32 v196, v196, v37, v38\n"
"v_mfma_f32_16x16x32_bf16 v[52:55], v[116:119], v[180:183], v[52:55]\n"
"ds_read_b128 v[136:139], v2 offset:41216\n"
"v_max3_f32 v196, v196, v39, v40\n"
"v_mfma_f32_16x16x32_bf16 v[44:47], v[120:123], v[152:155], v[44:47]\n"
"v_max3_f32 v196, v196, v41, v42\n"
"v_max3_f32 v196, v196, v43, v43\n"
"v_mfma_f32_16x16x32_bf16 v[48:51], v[120:123], v[168:171], v[48:51]\n"
"ds_read_b64_tr_b16 v[80:81], v4 offset:49152\n"
"ds_read_b64_tr_b16 v[82:83], v4 offset:57344\n"
"v_mfma_f32_16x16x32_bf16 v[52:55], v[120:123], v[184:187], v[52:55]\n"
"ds_read_b128 v[140:143], v3 offset:41216\n"
"s_waitcnt lgkmcnt(4)\n"
"v_mfma_f32_16x16x32_bf16 v[44:47], v[124:127], v[156:159], v[44:47]\n"
"ds_read_b64_tr_b16 v[84:85], v5 offset:49152\n"
"ds_read_b64_tr_b16 v[86:87], v5 offset:57344\n"
"v_mfma_f32_16x16x32_bf16 v[48:51], v[124:127], v[172:175], v[48:51]\n"
"buffer_load_dwordx4 v211, %4, s47 offen offset:3072 lds\n"
"v_mfma_f32_16x16x32_bf16 v[52:55], v[124:127], v[188:191], v[52:55]\n"
"v_mfma_f32_16x16x32_bf16 v[44:47], v[128:131], a[204:207], v[44:47]\n"
"ds_read_b64_tr_b16 v[88:89], v6 offset:49152\n"
"ds_read_b64_tr_b16 v[90:91], v6 offset:57344\n"
"v_mfma_f32_16x16x32_bf16 v[48:51], v[128:131], a[220:223], v[48:51]\n"
"v_mfma_f32_16x16x32_bf16 v[52:55], v[128:131], a[236:239], v[52:55]\n"
"v_mfma_f32_16x16x32_bf16 v[44:47], v[132:135], a[208:211], v[44:47]\n"
"ds_read_b64_tr_b16 v[92:93], v7 offset:49152\n"
"ds_read_b64_tr_b16 v[94:95], v7 offset:57344\n"
"v_mfma_f32_16x16x32_bf16 v[48:51], v[132:135], a[224:227], v[48:51]\n"
"v_mfma_f32_16x16x32_bf16 v[52:55], v[132:135], a[240:243], v[52:55]\n"
"s_waitcnt lgkmcnt(4)\n"
"v_mfma_f32_16x16x32_bf16 v[44:47], v[136:139], a[212:215], v[44:47]\n"
"ds_read_b64_tr_b16 v[96:97], v8 offset:49152\n"
"ds_read_b64_tr_b16 v[98:99], v8 offset:57344\n"
"v_mfma_f32_16x16x32_bf16 v[48:51], v[136:139], a[228:231], v[48:51]\n"
"s_mov_b32 s47, 28800\n"
"s_add_u32 m0, s48, 4096\n"
"v_mfma_f32_16x16x32_bf16 v[52:55], v[136:139], a[244:247], v[52:55]\n"
"buffer_load_dwordx4 v208, %4, s47 offen offset:0 lds\n"
"v_mfma_f32_16x16x32_bf16 v[44:47], v[140:143], a[216:219], v[44:47]\n"
"ds_read_b64_tr_b16 v[100:101], v9 offset:49152\n"
"ds_read_b64_tr_b16 v[102:103], v9 offset:57344\n"
"v_mfma_f32_16x16x32_bf16 v[48:51], v[140:143], a[232:235], v[48:51]\n"
"v_mfma_f32_16x16x32_bf16 v[52:55], v[140:143], a[248:251], v[52:55]\n"
"v_mfma_f32_16x16x32_bf16 a[0:3], v[80:83], v[68:71], a[0:3]\n"
"ds_read_b64_tr_b16 v[104:105], v10 offset:49152\n"
"ds_read_b64_tr_b16 v[106:107], v10 offset:57344\n"
"v_mfma_f32_16x16x32_bf16 a[4:7], v[80:83], v[72:75], a[4:7]\n"
"v_mfma_f32_16x16x32_bf16 a[8:11], v[80:83], v[76:79], a[8:11]\n"
"v_mfma_f32_16x16x32_bf16 a[12:15], v[84:87], v[68:71], a[12:15]\n"
"ds_read_b64_tr_b16 v[108:109], v11 offset:49152\n"
"ds_read_b64_tr_b16 v[110:111], v11 offset:57344\n"
"v_mfma_f32_16x16x32_bf16 a[16:19], v[84:87], v[72:75], a[16:19]\n"
"v_max3_f32 v196, v196, v44, v45\n"
"v_max3_f32 v196, v196, v46, v47\n"
"v_mfma_f32_16x16x32_bf16 a[20:23], v[84:87], v[76:79], a[20:23]\n"
"v_max3_f32 v196, v196, v48, v49\n"
"v_max3_f32 v196, v196, v50, v51\n"
"s_waitcnt lgkmcnt(4)\n"
"v_mfma_f32_16x16x32_bf16 a[24:27], v[88:91], v[68:71], a[24:27]\n"
"buffer_load_dwordx4 v209, %4, s47 offen offset:1024 lds\n"
"v_max3_f32 v196, v196, v52, v53\n"
"v_mfma_f32_16x16x32_bf16 a[28:31], v[88:91], v[72:75], a[28:31]\n"
"v_max3_f32 v196, v196, v54, v55\n"
"v_cmp_lt_f32 vcc, 0x41000000, v196\n"
"v_mfma_f32_16x16x32_bf16 a[32:35], v[88:91], v[76:79], a[32:35]\n"
"s_cbranch_vccnz LSTUB10_%=\n"
"LRET10_%=:\n"
"ds_read_b64_tr_b16 v[112:113], v4 offset:49408\n"
"v_mfma_f32_16x16x32_bf16 a[36:39], v[92:95], v[68:71], a[36:39]\n"
"ds_read_b64_tr_b16 v[114:115], v4 offset:57600\n"
"ds_read_b64_tr_b16 v[116:117], v5 offset:49408\n"
"v_mfma_f32_16x16x32_bf16 a[40:43], v[92:95], v[72:75], a[40:43]\n"
"ds_read_b64_tr_b16 v[118:119], v5 offset:57600\n"
"v_mfma_f32_16x16x32_bf16 a[44:47], v[92:95], v[76:79], a[44:47]\n"
"v_exp_f32 v32, v32\n"
"v_mfma_f32_16x16x32_bf16 a[48:51], v[96:99], v[68:71], a[48:51]\n"
"ds_read_b64_tr_b16 v[120:121], v6 offset:49408\n"
"ds_read_b64_tr_b16 v[122:123], v6 offset:57600\n"
"v_mfma_f32_16x16x32_bf16 a[52:55], v[96:99], v[72:75], a[52:55]\n"
"v_exp_f32 v33, v33\n"
"v_mfma_f32_16x16x32_bf16 a[56:59], v[96:99], v[76:79], a[56:59]\n"
"v_exp_f32 v34, v34\n"
"v_mfma_f32_16x16x32_bf16 a[60:63], v[100:103], v[68:71], a[60:63]\n"
"ds_read_b64_tr_b16 v[124:125], v7 offset:49408\n"
"ds_read_b64_tr_b16 v[126:127], v7 offset:57600\n"
"v_mfma_f32_16x16x32_bf16 a[64:67], v[100:103], v[72:75], a[64:67]\n"
"buffer_load_dwordx4 v210, %4, s47 offen offset:2048 lds\n"
"v_cvt_pk_bf16_f32 v56, v32, v33\n"
"v_mfma_f32_16x16x32_bf16 a[68:71], v[100:103], v[76:79], a[68:71]\n"
"v_exp_f32 v35, v35\n"
"s_waitcnt lgkmcnt(3)\n"
"v_mfma_f32_16x16x32_bf16 a[72:75], v[104:107], v[68:71], a[72:75]\n"
"ds_read_b64_tr_b16 v[128:129], v8 offset:49408\n"
"ds_read_b64_tr_b16 v[130:131], v8 offset:57600\n"
"v_mfma_f32_16x16x32_bf16 a[76:79], v[104:107], v[72:75], a[76:79]\n"
"v_exp_f32 v36, v36\n"
"v_mfma_f32_16x16x32_bf16 a[80:83], v[104:107], v[76:79], a[80:83]\n"
"v_exp_f32 v37, v37\n"
"v_mfma_f32_16x16x32_bf16 a[84:87], v[108:111], v[68:71], a[84:87]\n"
"ds_read_b64_tr_b16 v[132:133], v9 offset:49408\n"
"ds_read_b64_tr_b16 v[134:135], v9 offset:57600\n"
"v_mfma_f32_16x16x32_bf16 a[88:91], v[108:111], v[72:75], a[88:91]\n"
"v_exp_f32 v38, v38\n"
"v_mfma_f32_16x16x32_bf16 a[92:95], v[108:111], v[76:79], a[92:95]\n"
"v_exp_f32 v39, v39\n"
"v_mfma_f32_16x16x32_bf16 a[96:99], v[112:115], v[68:71], a[96:99]\n"
"ds_read_b64_tr_b16 v[136:137], v10 offset:49408\n"
"ds_read_b64_tr_b16 v[138:139], v10 offset:57600\n"
"v_mfma_f32_16x16x32_bf16 a[100:103], v[112:115], v[72:75], a[100:103]\n"
"v_exp_f32 v40, v40\n"
"v_mfma_f32_16x16x32_bf16 a[104:107], v[112:115], v[76:79], a[104:107]\n"
"buffer_load_dwordx4 v211, %4, s47 offen offset:3072 lds\n"
"v_cvt_pk_bf16_f32 v57, v34, v35\n"
"v_mfma_f32_16x16x32_bf16 a[108:111], v[116:119], v[68:71], a[108:111]\n"
"ds_read_b64_tr_b16 v[140:141], v11 offset:49408\n"
"ds_read_b64_tr_b16 v[142:143], v11 offset:57600\n"
"v_mfma_f32_16x16x32_bf16 a[112:115], v[116:119], v[72:75], a[112:115]\n"
"v_exp_f32 v41, v41\n"
"v_mfma_f32_16x16x32_bf16 a[116:119], v[116:119], v[76:79], a[116:119]\n"
"v_exp_f32 v42, v42\n"
"s_waitcnt lgkmcnt(3)\n"
"v_mfma_f32_16x16x32_bf16 a[120:123], v[120:123], v[68:71], a[120:123]\n"
"v_exp_f32 v43, v43\n"
"v_mfma_f32_16x16x32_bf16 a[124:127], v[120:123], v[72:75], a[124:127]\n"
"v_add_f32 v205, v205, v32\n"
"v_add_f32 v205, v205, v33\n"
"v_mfma_f32_16x16x32_bf16 a[128:131], v[120:123], v[76:79], a[128:131]\n"
"v_add_f32 v205, v205, v34\n"
"v_add_f32 v205, v205, v35\n"
"v_mfma_f32_16x16x32_bf16 a[132:135], v[124:127], v[68:71], a[132:135]\n"
"v_cvt_pk_bf16_f32 v60, v36, v37\n"
"v_cvt_pk_bf16_f32 v61, v38, v39\n"
"v_mfma_f32_16x16x32_bf16 a[136:139], v[124:127], v[72:75], a[136:139]\n"
"v_add_f32 v206, v206, v36\n"
"v_add_f32 v206, v206, v37\n"
"v_mfma_f32_16x16x32_bf16 a[140:143], v[124:127], v[76:79], a[140:143]\n"
"v_add_f32 v206, v206, v38\n"
"v_add_f32 v206, v206, v39\n"
"v_mfma_f32_16x16x32_bf16 a[144:147], v[128:131], v[68:71], a[144:147]\n"
"v_cvt_pk_bf16_f32 v64, v40, v41\n"
"v_cvt_pk_bf16_f32 v65, v42, v43\n"
"v_mfma_f32_16x16x32_bf16 a[148:151], v[128:131], v[72:75], a[148:151]\n"
"v_add_f32 v207, v207, v40\n"
"v_add_f32 v207, v207, v41\n"
"v_mfma_f32_16x16x32_bf16 a[152:155], v[128:131], v[76:79], a[152:155]\n"
"s_waitcnt vmcnt(20) lgkmcnt(0)\n"
"s_barrier\n"
"v_mfma_f32_16x16x32_bf16 a[156:159], v[132:135], v[68:71], a[156:159]\n"
"ds_read_b128 v[80:83], v0 offset:49152\n"
"ds_read_b128 v[84:87], v1 offset:49152\n"
"v_mfma_f32_16x16x32_bf16 a[160:163], v[132:135], v[72:75], a[160:163]\n"
"ds_read_b128 v[88:91], v2 offset:49152\n"
"v_add_f32 v207, v207, v42\n"
"v_mfma_f32_16x16x32_bf16 a[164:167], v[132:135], v[76:79], a[164:167]\n"
"v_add_f32 v207, v207, v43\n"
"v_mfma_f32_16x16x32_bf16 a[168:171], v[136:139], v[68:71], a[168:171]\n"
"v_mfma_f32_16x16x32_bf16 a[172:175], v[136:139], v[72:75], a[172:175]\n"
"v_mfma_f32_16x16x32_bf16 a[176:179], v[136:139], v[76:79], a[176:179]\n"
"v_mfma_f32_16x16x32_bf16 a[180:183], v[140:143], v[68:71], a[180:183]\n"
"v_mfma_f32_16x16x32_bf16 a[184:187], v[140:143], v[72:75], a[184:187]\n"
"v_mfma_f32_16x16x32_bf16 a[188:191], v[140:143], v[76:79], a[188:191]\n"
"s_waitcnt lgkmcnt(2)\n"
"v_mfma_f32_16x16x32_bf16 v[32:35], v[80:83], v[144:147], v[20:23]\n"
"ds_read_b128 v[92:95], v3 offset:49152\n"
"ds_read_b128 v[96:99], v0 offset:49408\n"
"v_mfma_f32_16x16x32_bf16 v[36:39], v[80:83], v[160:163], v[24:27]\n"
"v_exp_f32 v44, v44\n"
"v_mfma_f32_16x16x32_bf16 v[40:43], v[80:83], v[176:179], v[28:31]\n"
"ds_read_b128 v[100:103], v1 offset:49408\n"
"v_add_f32 v205, v205, v44\n"
"s_waitcnt lgkmcnt(3)\n"
"v_mfma_f32_16x16x32_bf16 v[32:35], v[84:87], v[148:151], v[32:35]\n"
"s_mov_b32 s47, 256\n"
"s_add_u32 m0, s48, 98304\n"
"v_mfma_f32_16x16x32_bf16 v[36:39], v[84:87], v[164:167], v[36:39]\n"
"buffer_load_dwordx4 v208, %4, s47 offen offset:0 lds\n"
"v_mfma_f32_16x16x32_bf16 v[40:43], v[84:87], v[180:183], v[40:43]\n"
"ds_read_b128 v[104:107], v2 offset:49408\n"
"v_mfma_f32_16x16x32_bf16 v[32:35], v[88:91], v[152:155], v[32:35]\n"
"v_exp_f32 v45, v45\n"
"v_mfma_f32_16x16x32_bf16 v[36:39], v[88:91], v[168:171], v[36:39]\n"
"v_exp_f32 v46, v46\n"
"v_mfma_f32_16x16x32_bf16 v[40:43], v[88:91], v[184:187], v[40:43]\n"
"ds_read_b128 v[108:111], v3 offset:49408\n"
"v_cvt_pk_bf16_f32 v58, v44, v45\n"
"s_waitcnt lgkmcnt(2)\n"
"v_mfma_f32_16x16x32_bf16 v[32:35], v[92:95], v[156:159], v[32:35]\n"
"v_exp_f32 v47, v47\n"
"v_mfma_f32_16x16x32_bf16 v[36:39], v[92:95], v[172:175], v[36:39]\n"
"v_exp_f32 v48, v48\n"
"v_mfma_f32_16x16x32_bf16 v[40:43], v[92:95], v[188:191], v[40:43]\n"
"ds_read_b128 v[112:115], v0 offset:57344\n"
"v_cvt_pk_bf16_f32 v59, v46, v47\n"
"v_mfma_f32_16x16x32_bf16 v[32:35], v[96:99], a[204:207], v[32:35]\n"
"v_exp_f32 v49, v49\n"
"v_mfma_f32_16x16x32_bf16 v[36:39], v[96:99], a[220:223], v[36:39]\n"
"v_exp_f32 v50, v50\n"
"v_mfma_f32_16x16x32_bf16 v[40:43], v[96:99], a[236:239], v[40:43]\n"
"buffer_load_dwordx4 v209, %4, s47 offen offset:1024 lds\n"
"ds_read_b128 v[116:119], v1 offset:57344\n"
"v_mfma_f32_16x16x32_bf16 v[32:35], v[100:103], a[208:211], v[32:35]\n"
"v_exp_f32 v51, v51\n"
"v_mfma_f32_16x16x32_bf16 v[36:39], v[100:103], a[224:227], v[36:39]\n"
"v_add_f32 v205, v205, v45\n"
"v_add_f32 v205, v205, v46\n"
"v_mfma_f32_16x16x32_bf16 v[40:43], v[100:103], a[240:243], v[40:43]\n"
"ds_read_b128 v[120:123], v2 offset:57344\n"
"v_add_f32 v205, v205, v47\n"
"s_waitcnt lgkmcnt(2)\n"
"v_mfma_f32_16x16x32_bf16 v[32:35], v[104:107], a[212:215], v[32:35]\n"
"v_exp_f32 v52, v52\n"
"v_mfma_f32_16x16x32_bf16 v[36:39], v[104:107], a[228:231], v[36:39]\n"
"v_exp_f32 v53, v53\n"
"v_mfma_f32_16x16x32_bf16 v[40:43], v[104:107], a[244:247], v[40:43]\n"
"v_exp_f32 v54, v54\n"
"v_mfma_f32_16x16x32_bf16 v[32:35], v[108:111], a[216:219], v[32:35]\n"
"v_exp_f32 v55, v55\n"
"v_mfma_f32_16x16x32_bf16 v[36:39], v[108:111], a[232:235], v[36:39]\n"
"v_cvt_pk_bf16_f32 v62, v48, v49\n"
"v_cvt_pk_bf16_f32 v63, v50, v51\n"
"v_mfma_f32_16x16x32_bf16 v[40:43], v[108:111], a[248:251], v[40:43]\n"
"v_add_f32 v206, v206, v48\n"
"v_add_f32 v206, v206, v49\n"
"v_mfma_f32_16x16x32_bf16 v[44:47], v[112:115], v[144:147], v[20:23]\n"
"v_add_f32 v206, v206, v50\n"
"v_add_f32 v206, v206, v51\n"
"v_mfma_f32_16x16x32_bf16 v[48:51], v[112:115], v[160:163], v[24:27]\n"
"v_cvt_pk_bf16_f32 v66, v52, v53\n"
"v_cvt_pk_bf16_f32 v67, v54, v55\n"
"v_add_f32 v207, v207, v52\n"
"v_add_f32 v207, v207, v53\n"
"v_add_f32 v207, v207, v54\n"
"v_add_f32 v207, v207, v55\n"
"ds_read_b128 v[124:127], v3 offset:57344\n"
"v_mfma_f32_16x16x32_bf16 v[52:55], v[112:115], v[176:179], v[28:31]\n"
"ds_read_b128 v[128:131], v0 offset:57600\n"
"buffer_load_dwordx4 v210, %4, s47 offen offset:2048 lds\n"
"s_waitcnt lgkmcnt(2)\n"
"v_mfma_f32_16x16x32_bf16 v[44:47], v[116:119], v[148:151], v[44:47]\n"
"ds_read_b128 v[132:135], v1 offset:57600\n"
"v_max3_f32 v196, v32, v33, v34\n"
"v_mfma_f32_16x16x32_bf16 v[48:51], v[116:119], v[164:167], v[48:51]\n"
"v_max3_f32 v196, v196, v35, v36\n"
"v_max3_f32 v196, v196, v37, v38\n"
"v_mfma_f32_16x16x32_bf16 v[52:55], v[116:119], v[180:183], v[52:55]\n"
"ds_read_b128 v[136:139], v2 offset:57600\n"
"v_max3_f32 v196, v196, v39, v40\n"
"v_mfma_f32_16x16x32_bf16 v[44:47], v[120:123], v[152:155], v[44:47]\n"
"v_max3_f32 v196, v196, v41, v42\n"
"v_max3_f32 v196, v196, v43, v43\n"
"v_mfma_f32_16x16x32_bf16 v[48:51], v[120:123], v[168:171], v[48:51]\n"
"ds_read_b64_tr_b16 v[80:81], v4 offset:0\n"
"ds_read_b64_tr_b16 v[82:83], v4 offset:8192\n"
"v_mfma_f32_16x16x32_bf16 v[52:55], v[120:123], v[184:187], v[52:55]\n"
"ds_read_b128 v[140:143], v3 offset:57600\n"
"s_waitcnt lgkmcnt(4)\n"
"v_mfma_f32_16x16x32_bf16 v[44:47], v[124:127], v[156:159], v[44:47]\n"
"ds_read_b64_tr_b16 v[84:85], v5 offset:0\n"
"ds_read_b64_tr_b16 v[86:87], v5 offset:8192\n"
"v_mfma_f32_16x16x32_bf16 v[48:51], v[124:127], v[172:175], v[48:51]\n"
"buffer_load_dwordx4 v211, %4, s47 offen offset:3072 lds\n"
"v_mfma_f32_16x16x32_bf16 v[52:55], v[124:127], v[188:191], v[52:55]\n"
"v_mfma_f32_16x16x32_bf16 v[44:47], v[128:131], a[204:207], v[44:47]\n"
"ds_read_b64_tr_b16 v[88:89], v6 offset:0\n"
"ds_read_b64_tr_b16 v[90:91], v6 offset:8192\n"
"v_mfma_f32_16x16x32_bf16 v[48:51], v[128:131], a[220:223], v[48:51]\n"
"v_mfma_f32_16x16x32_bf16 v[52:55], v[128:131], a[236:239], v[52:55]\n"
"v_mfma_f32_16x16x32_bf16 v[44:47], v[132:135], a[208:211], v[44:47]\n"
"ds_read_b64_tr_b16 v[92:93], v7 offset:0\n"
"ds_read_b64_tr_b16 v[94:95], v7 offset:8192\n"
"v_mfma_f32_16x16x32_bf16 v[48:51], v[132:135], a[224:227], v[48:51]\n"
"v_mfma_f32_16x16x32_bf16 v[52:55], v[132:135], a[240:243], v[52:55]\n"
"s_waitcnt lgkmcnt(4)\n"
"v_mfma_f32_16x16x32_bf16 v[44:47], v[136:139], a[212:215], v[44:47]\n"
"ds_read_b64_tr_b16 v[96:97], v8 offset:0\n"
"ds_read_b64_tr_b16 v[98:99], v8 offset:8192\n"
"v_mfma_f32_16x16x32_bf16 v[48:51], v[136:139], a[228:231], v[48:51]\n"
"s_mov_b32 s47, 28928\n"
"s_add_u32 m0, s48, 102400\n"
"v_mfma_f32_16x16x32_bf16 v[52:55], v[136:139], a[244:247], v[52:55]\n"
"buffer_load_dwordx4 v208, %4, s47 offen offset:0 lds\n"
"v_mfma_f32_16x16x32_bf16 v[44:47], v[140:143], a[216:219], v[44:47]\n"
"ds_read_b64_tr_b16 v[100:101], v9 offset:0\n"
"ds_read_b64_tr_b16 v[102:103], v9 offset:8192\n"
"v_mfma_f32_16x16x32_bf16 v[48:51], v[140:143], a[232:235], v[48:51]\n"
"v_mfma_f32_16x16x32_bf16 v[52:55], v[140:143], a[248:251], v[52:55]\n"
"v_mfma_f32_16x16x32_bf16 a[0:3], v[80:83], v[56:59], a[0:3]\n"
"ds_read_b64_tr_b16 v[104:105], v10 offset:0\n"
"ds_read_b64_tr_b16 v[106:107], v10 offset:8192\n"
"v_mfma_f32_16x16x32_bf16 a[4:7], v[80:83], v[60:63], a[4:7]\n"
"v_mfma_f32_16x16x32_bf16 a[8:11], v[80:83], v[64:67], a[8:11]\n"
"v_mfma_f32_16x16x32_bf16 a[12:15], v[84:87], v[56:59], a[12:15]\n"
"ds_read_b64_tr_b16 v[108:109], v11 offset:0\n"
"ds_read_b64_tr_b16 v[110:111], v11 offset:8192\n"
"v_mfma_f32_16x16x32_bf16 a[16:19], v[84:87], v[60:63], a[16:19]\n"
"v_max3_f32 v196, v196, v44, v45\n"
"v_max3_f32 v196, v196, v46, v47\n"
"v_mfma_f32_16x16x32_bf16 a[20:23], v[84:87], v[64:67], a[20:23]\n"
"v_max3_f32 v196, v196, v48, v49\n"
"v_max3_f32 v196, v196, v50, v51\n"
"s_waitcnt lgkmcnt(4)\n"
"v_mfma_f32_16x16x32_bf16 a[24:27], v[88:91], v[56:59], a[24:27]\n"
"buffer_load_dwordx4 v209, %4, s47 offen offset:1024 lds\n"
"v_max3_f32 v196, v196, v52, v53\n"
"v_mfma_f32_16x16x32_bf16 a[28:31], v[88:91], v[60:63], a[28:31]\n"
"v_max3_f32 v196, v196, v54, v55\n"
"v_cmp_lt_f32 vcc, 0x41000000, v196\n"
"v_mfma_f32_16x16x32_bf16 a[32:35], v[88:91], v[64:67], a[32:35]\n"
"s_cbranch_vccnz LSTUB11_%=\n"
"LRET11_%=:\n"
"ds_read_b64_tr_b16 v[112:113], v4 offset:256\n"
"v_mfma_f32_16x16x32_bf16 a[36:39], v[92:95], v[56:59], a[36:39]\n"
"ds_read_b64_tr_b16 v[114:115], v4 offset:8448\n"
"ds_read_b64_tr_b16 v[116:117], v5 offset:256\n"
"v_mfma_f32_16x16x32_bf16 a[40:43], v[92:95], v[60:63], a[40:43]\n"
"ds_read_b64_tr_b16 v[118:119], v5 offset:8448\n"
"v_mfma_f32_16x16x32_bf16 a[44:47], v[92:95], v[64:67], a[44:47]\n"
"v_exp_f32 v32, v32\n"
"v_mfma_f32_16x16x32_bf16 a[48:51], v[96:99], v[56:59], a[48:51]\n"
"ds_read_b64_tr_b16 v[120:121], v6 offset:256\n"
"ds_read_b64_tr_b16 v[122:123], v6 offset:8448\n"
"v_mfma_f32_16x16x32_bf16 a[52:55], v[96:99], v[60:63], a[52:55]\n"
"v_exp_f32 v33, v33\n"
"v_mfma_f32_16x16x32_bf16 a[56:59], v[96:99], v[64:67], a[56:59]\n"
"v_exp_f32 v34, v34\n"
"v_mfma_f32_16x16x32_bf16 a[60:63], v[100:103], v[56:59], a[60:63]\n"
"ds_read_b64_tr_b16 v[124:125], v7 offset:256\n"
"ds_read_b64_tr_b16 v[126:127], v7 offset:8448\n"
"v_mfma_f32_16x16x32_bf16 a[64:67], v[100:103], v[60:63], a[64:67]\n"
"buffer_load_dwordx4 v210, %4, s47 offen offset:2048 lds\n"
"v_cvt_pk_bf16_f32 v68, v32, v33\n"
"v_mfma_f32_16x16x32_bf16 a[68:71], v[100:103], v[64:67], a[68:71]\n"
"v_exp_f32 v35, v35\n"
"s_waitcnt lgkmcnt(3)\n"
"v_mfma_f32_16x16x32_bf16 a[72:75], v[104:107], v[56:59], a[72:75]\n"
"ds_read_b64_tr_b16 v[128:129], v8 offset:256\n"
"ds_read_b64_tr_b16 v[130:131], v8 offset:8448\n"
"v_mfma_f32_16x16x32_bf16 a[76:79], v[104:107], v[60:63], a[76:79]\n"
"v_exp_f32 v36, v36\n"
"v_mfma_f32_16x16x32_bf16 a[80:83], v[104:107], v[64:67], a[80:83]\n"
"v_exp_f32 v37, v37\n"
"v_mfma_f32_16x16x32_bf16 a[84:87], v[108:111], v[56:59], a[84:87]\n"
"ds_read_b64_tr_b16 v[132:133], v9 offset:256\n"
"ds_read_b64_tr_b16 v[134:135], v9 offset:8448\n"
"v_mfma_f32_16x16x32_bf16 a[88:91], v[108:111], v[60:63], a[88:91]\n"
"v_exp_f32 v38, v38\n"
"v_mfma_f32_16x16x32_bf16 a[92:95], v[108:111], v[64:67], a[92:95]\n"
"v_exp_f32 v39, v39\n"
"v_mfma_f32_16x16x32_bf16 a[96:99], v[112:115], v[56:59], a[96:99]\n"
"ds_read_b64_tr_b16 v[136:137], v10 offset:256\n"
"ds_read_b64_tr_b16 v[138:139], v10 offset:8448\n"
"v_mfma_f32_16x16x32_bf16 a[100:103], v[112:115], v[60:63], a[100:103]\n"
"v_exp_f32 v40, v40\n"
"v_mfma_f32_16x16x32_bf16 a[104:107], v[112:115], v[64:67], a[104:107]\n"
"buffer_load_dwordx4 v211, %4, s47 offen offset:3072 lds\n"
"v_cvt_pk_bf16_f32 v69, v34, v35\n"
"v_mfma_f32_16x16x32_bf16 a[108:111], v[116:119], v[56:59], a[108:111]\n"
"ds_read_b64_tr_b16 v[140:141], v11 offset:256\n"
"ds_read_b64_tr_b16 v[142:143], v11 offset:8448\n"
"v_mfma_f32_16x16x32_bf16 a[112:115], v[116:119], v[60:63], a[112:115]\n"
"v_exp_f32 v41, v41\n"
"v_mfma_f32_16x16x32_bf16 a[116:119], v[116:119], v[64:67], a[116:119]\n"
"v_exp_f32 v42, v42\n"
"s_waitcnt lgkmcnt(3)\n"
"v_mfma_f32_16x16x32_bf16 a[120:123], v[120:123], v[56:59], a[120:123]\n"
"v_exp_f32 v43, v43\n"
"v_mfma_f32_16x16x32_bf16 a[124:127], v[120:123], v[60:63], a[124:127]\n"
"v_add_f32 v205, v205, v32\n"
"v_add_f32 v205, v205, v33\n"
"v_mfma_f32_16x16x32_bf16 a[128:131], v[120:123], v[64:67], a[128:131]\n"
"v_add_f32 v205, v205, v34\n"
"v_add_f32 v205, v205, v35\n"
"v_mfma_f32_16x16x32_bf16 a[132:135], v[124:127], v[56:59], a[132:135]\n"
"v_cvt_pk_bf16_f32 v72, v36, v37\n"
"v_cvt_pk_bf16_f32 v73, v38, v39\n"
"v_mfma_f32_16x16x32_bf16 a[136:139], v[124:127], v[60:63], a[136:139]\n"
"v_add_f32 v206, v206, v36\n"
"v_add_f32 v206, v206, v37\n"
"v_mfma_f32_16x16x32_bf16 a[140:143], v[124:127], v[64:67], a[140:143]\n"
"v_add_f32 v206, v206, v38\n"
"v_add_f32 v206, v206, v39\n"
"v_mfma_f32_16x16x32_bf16 a[144:147], v[128:131], v[56:59], a[144:147]\n"
"v_cvt_pk_bf16_f32 v76, v40, v41\n"
"v_cvt_pk_bf16_f32 v77, v42, v43\n"
"v_mfma_f32_16x16x32_bf16 a[148:151], v[128:131], v[60:63], a[148:151]\n"
"v_add_f32 v207, v207, v40\n"
"v_add_f32 v207, v207, v41\n"
"v_mfma_f32_16x16x32_bf16 a[152:155], v[128:131], v[64:67], a[152:155]\n"
"s_waitcnt vmcnt(24) lgkmcnt(0)\n"
"s_barrier\n"
"v_mfma_f32_16x16x32_bf16 a[156:159], v[132:135], v[56:59], a[156:159]\n"
"v_add_f32 v207, v207, v42\n"
"v_add_f32 v207, v207, v43\n"
"v_mfma_f32_16x16x32_bf16 a[160:163], v[132:135], v[60:63], a[160:163]\n"
"v_mfma_f32_16x16x32_bf16 a[164:167], v[132:135], v[64:67], a[164:167]\n"
"v_mfma_f32_16x16x32_bf16 a[168:171], v[136:139], v[56:59], a[168:171]\n"
"v_mfma_f32_16x16x32_bf16 a[172:175], v[136:139], v[60:63], a[172:175]\n"
"v_mfma_f32_16x16x32_bf16 a[176:179], v[136:139], v[64:67], a[176:179]\n"
"v_mfma_f32_16x16x32_bf16 a[180:183], v[140:143], v[56:59], a[180:183]\n"
"v_mfma_f32_16x16x32_bf16 a[184:187], v[140:143], v[60:63], a[184:187]\n"
"v_mfma_f32_16x16x32_bf16 a[188:191], v[140:143], v[64:67], a[188:191]\n"
"s_mov_b32 s47, 384\n"
"s_add_u32 m0, s48, 32768\n"
"buffer_load_dwordx4 v208, %4, s47 offen offset:0 lds\n"
"v_exp_f32 v44, v44\n"
"v_exp_f32 v45, v45\n"
"v_exp_f32 v46, v46\n"
"v_exp_f32 v47, v47\n"
"v_exp_f32 v48, v48\n"
"v_exp_f32 v49, v49\n"
"v_exp_f32 v50, v50\n"
"v_exp_f32 v51, v51\n"
"v_cvt_pk_bf16_f32 v70, v44, v45\n"
"v_cvt_pk_bf16_f32 v71, v46, v47\n"
"buffer_load_dwordx4 v209, %4, s47 offen offset:1024 lds\n"
"v_add_f32 v205, v205, v44\n"
"v_add_f32 v205, v205, v45\n"
"v_add_f32 v205, v205, v46\n"
"v_add_f32 v205, v205, v47\n"
"v_cvt_pk_bf16_f32 v74, v48, v49\n"
"v_exp_f32 v52, v52\n"
"v_exp_f32 v53, v53\n"
"v_exp_f32 v54, v54\n"
"v_exp_f32 v55, v55\n"
"v_cvt_pk_bf16_f32 v75, v50, v51\n"
"v_add_f32 v206, v206, v48\n"
"v_add_f32 v206, v206, v49\n"
"v_add_f32 v206, v206, v50\n"
"v_add_f32 v206, v206, v51\n"
"v_cvt_pk_bf16_f32 v78, v52, v53\n"
"v_cvt_pk_bf16_f32 v79, v54, v55\n"
"v_add_f32 v207, v207, v52\n"
"v_add_f32 v207, v207, v53\n"
"v_add_f32 v207, v207, v54\n"
"v_add_f32 v207, v207, v55\n"
"buffer_load_dwordx4 v210, %4, s47 offen offset:2048 lds\n"
"ds_read_b64_tr_b16 v[80:81], v4 offset:16384\n"
"ds_read_b64_tr_b16 v[82:83], v4 offset:24576\n"
"buffer_load_dwordx4 v211, %4, s47 offen offset:3072 lds\n"
"ds_read_b64_tr_b16 v[84:85], v5 offset:16384\n"
"ds_read_b64_tr_b16 v[86:87], v5 offset:24576\n"
"ds_read_b64_tr_b16 v[88:89], v6 offset:16384\n"
"ds_read_b64_tr_b16 v[90:91], v6 offset:24576\n"
"ds_read_b64_tr_b16 v[92:93], v7 offset:16384\n"
"ds_read_b64_tr_b16 v[94:95], v7 offset:24576\n"
"ds_read_b64_tr_b16 v[96:97], v8 offset:16384\n"
"ds_read_b64_tr_b16 v[98:99], v8 offset:24576\n"
"s_mov_b32 s47, 29056\n"
"s_add_u32 m0, s48, 36864\n"
"buffer_load_dwordx4 v208, %4, s47 offen offset:0 lds\n"
"ds_read_b64_tr_b16 v[100:101], v9 offset:16384\n"
"ds_read_b64_tr_b16 v[102:103], v9 offset:24576\n"
"s_waitcnt lgkmcnt(10)\n"
"v_mfma_f32_16x16x32_bf16 a[0:3], v[80:83], v[68:71], a[0:3]\n"
"ds_read_b64_tr_b16 v[104:105], v10 offset:16384\n"
"ds_read_b64_tr_b16 v[106:107], v10 offset:24576\n"
"v_mfma_f32_16x16x32_bf16 a[4:7], v[80:83], v[72:75], a[4:7]\n"
"v_mfma_f32_16x16x32_bf16 a[8:11], v[80:83], v[76:79], a[8:11]\n"
"s_waitcnt lgkmcnt(6)\n"
"v_mfma_f32_16x16x32_bf16 a[12:15], v[84:87], v[68:71], a[12:15]\n"
"ds_read_b64_tr_b16 v[108:109], v11 offset:16384\n"
"ds_read_b64_tr_b16 v[110:111], v11 offset:24576\n"
"v_mfma_f32_16x16x32_bf16 a[16:19], v[84:87], v[72:75], a[16:19]\n"
"v_mfma_f32_16x16x32_bf16 a[20:23], v[84:87], v[76:79], a[20:23]\n"
"v_mfma_f32_16x16x32_bf16 a[24:27], v[88:91], v[68:71], a[24:27]\n"
"ds_read_b64_tr_b16 v[112:113], v4 offset:16640\n"
"ds_read_b64_tr_b16 v[114:115], v4 offset:24832\n"
"v_mfma_f32_16x16x32_bf16 a[28:31], v[88:91], v[72:75], a[28:31]\n"
"v_mfma_f32_16x16x32_bf16 a[32:35], v[88:91], v[76:79], a[32:35]\n"
"buffer_load_dwordx4 v209, %4, s47 offen offset:1024 lds\n"
"v_mfma_f32_16x16x32_bf16 a[36:39], v[92:95], v[68:71], a[36:39]\n"
"ds_read_b64_tr_b16 v[116:117], v5 offset:16640\n"
"ds_read_b64_tr_b16 v[118:119], v5 offset:24832\n"
"v_mfma_f32_16x16x32_bf16 a[40:43], v[92:95], v[72:75], a[40:43]\n"
"v_mfma_f32_16x16x32_bf16 a[44:47], v[92:95], v[76:79], a[44:47]\n"
"s_waitcnt lgkmcnt(4)\n"
"v_mfma_f32_16x16x32_bf16 a[48:51], v[96:99], v[68:71], a[48:51]\n"
"ds_read_b64_tr_b16 v[120:121], v6 offset:16640\n"
"ds_read_b64_tr_b16 v[122:123], v6 offset:24832\n"
"v_mfma_f32_16x16x32_bf16 a[52:55], v[96:99], v[72:75], a[52:55]\n"
"v_mfma_f32_16x16x32_bf16 a[56:59], v[96:99], v[76:79], a[56:59]\n"
"v_mfma_f32_16x16x32_bf16 a[60:63], v[100:103], v[68:71], a[60:63]\n"
"ds_read_b64_tr_b16 v[124:125], v7 offset:16640\n"
"ds_read_b64_tr_b16 v[126:127], v7 offset:24832\n"
"v_mfma_f32_16x16x32_bf16 a[64:67], v[100:103], v[72:75], a[64:67]\n"
"v_mfma_f32_16x16x32_bf16 a[68:71], v[100:103], v[76:79], a[68:71]\n"
"v_mfma_f32_16x16x32_bf16 a[72:75], v[104:107], v[68:71], a[72:75]\n"
"ds_read_b64_tr_b16 v[128:129], v8 offset:16640\n"
"ds_read_b64_tr_b16 v[130:131], v8 offset:24832\n"
"v_mfma_f32_16x16x32_bf16 a[76:79], v[104:107], v[72:75], a[76:79]\n"
"buffer_load_dwordx4 v210, %4, s47 offen offset:2048 lds\n"
"v_mfma_f32_16x16x32_bf16 a[80:83], v[104:107], v[76:79], a[80:83]\n"
"v_mfma_f32_16x16x32_bf16 a[84:87], v[108:111], v[68:71], a[84:87]\n"
"ds_read_b64_tr_b16 v[132:133], v9 offset:16640\n"
"ds_read_b64_tr_b16 v[134:135], v9 offset:24832\n"
"v_mfma_f32_16x16x32_bf16 a[88:91], v[108:111], v[72:75], a[88:91]\n"
"v_mfma_f32_16x16x32_bf16 a[92:95], v[108:111], v[76:79], a[92:95]\n"
"s_waitcnt lgkmcnt(4)\n"
"v_mfma_f32_16x16x32_bf16 a[96:99], v[112:115], v[68:71], a[96:99]\n"
"ds_read_b64_tr_b16 v[136:137], v10 offset:16640\n"
"ds_read_b64_tr_b16 v[138:139], v10 offset:24832\n"
"v_mfma_f32_16x16x32_bf16 a[100:103], v[112:115], v[72:75], a[100:103]\n"
"v_mfma_f32_16x16x32_bf16 a[104:107], v[112:115], v[76:79], a[104:107]\n"
"v_mfma_f32_16x16x32_bf16 a[108:111], v[116:119], v[68:71], a[108:111]\n"
"ds_read_b64_tr_b16 v[140:141], v11 offset:16640\n"
"ds_read_b64_tr_b16 v[142:143], v11 offset:24832\n"
"v_mfma_f32_16x16x32_bf16 a[112:115], v[116:119], v[72:75], a[112:115]\n"
"v_mfma_f32_16x16x32_bf16 a[116:119], v[116:119], v[76:79], a[116:119]\n"
"v_mfma_f32_16x16x32_bf16 a[120:123], v[120:123], v[68:71], a[120:123]\n"
"buffer_load_dwordx4 v211, %4, s47 offen offset:3072 lds\n"
"v_mfma_f32_16x16x32_bf16 a[124:127], v[120:123], v[72:75], a[124:127]\n"
"v_mfma_f32_16x16x32_bf16 a[128:131], v[120:123], v[76:79], a[128:131]\n"
"v_mfma_f32_16x16x32_bf16 a[132:135], v[124:127], v[68:71], a[132:135]\n"
"v_mfma_f32_16x16x32_bf16 a[136:139], v[124:127], v[72:75], a[136:139]\n"
"v_mfma_f32_16x16x32_bf16 a[140:143], v[124:127], v[76:79], a[140:143]\n"
"s_waitcnt lgkmcnt(2)\n"
"v_mfma_f32_16x16x32_bf16 a[144:147], v[128:131], v[68:71], a[144:147]\n"
"v_mfma_f32_16x16x32_bf16 a[148:151], v[128:131], v[72:75], a[148:151]\n"
"v_mfma_f32_16x16x32_bf16 a[152:155], v[128:131], v[76:79], a[152:155]\n"
"v_mfma_f32_16x16x32_bf16 a[156:159], v[132:135], v[68:71], a[156:159]\n"
"v_mfma_f32_16x16x32_bf16 a[160:163], v[132:135], v[72:75], a[160:163]\n"
"v_mfma_f32_16x16x32_bf16 a[164:167], v[132:135], v[76:79], a[164:167]\n"
"v_mfma_f32_16x16x32_bf16 a[168:171], v[136:139], v[68:71], a[168:171]\n"
"v_mfma_f32_16x16x32_bf16 a[172:175], v[136:139], v[72:75], a[172:175]\n"
"v_mfma_f32_16x16x32_bf16 a[176:179], v[136:139], v[76:79], a[176:179]\n"
"s_waitcnt lgkmcnt(0)\n"
"v_mfma_f32_16x16x32_bf16 a[180:183], v[140:143], v[68:71], a[180:183]\n"
"v_mfma_f32_16x16x32_bf16 a[184:187], v[140:143], v[72:75], a[184:187]\n"
"v_mfma_f32_16x16x32_bf16 a[188:191], v[140:143], v[76:79], a[188:191]\n"
"s_waitcnt lgkmcnt(0)\n"
"s_barrier\n"
"s_mov_b32 s47, 512\n"
"s_add_u32 m0, s48, 65536\n"
"s_nop 0\n"
"buffer_load_dwordx4 v208, %4, s47 offen offset:0 lds\n"
"buffer_load_dwordx4 v209, %4, s47 offen offset:1024 lds\n"
"buffer_load_dwordx4 v210, %4, s47 offen offset:2048 lds\n"
"buffer_load_dwordx4 v211, %4, s47 offen offset:3072 lds\n"
"s_mov_b32 s47, 29184\n"
"s_add_u32 m0, s48, 69632\n"
"s_nop 0\n"
"buffer_load_dwordx4 v208, %4, s47 offen offset:0 lds\n"
"buffer_load_dwordx4 v209, %4, s47 offen offset:1024 lds\n"
"buffer_load_dwordx4 v210, %4, s47 offen offset:2048 lds\n"
"buffer_load_dwordx4 v211, %4, s47 offen offset:3072 lds\n"
"v_mov_b32 v4, v208\n"
"v_mov_b32 v11, v209\n"
"v_mov_b32 v30, v210\n"
"v_mov_b32 v31, v211\n"
"v_mbcnt_lo_u32_b32 v20, -1, 0\n"
"v_mbcnt_hi_u32_b32 v20, -1, v20\n"
"v_and_b32 v21, 15, v20\n"
"v_lshrrev_b32 v22, 4, v20\n"
"v_bfe_u32 v23, v20, 2, 2\n"
"v_and_b32 v24, 3, v20\n"
"v_lshrrev_b32 v25, 5, v20\n"
"v_and_b32 v26, 31, v20\n"
"v_lshl_add_u32 v29, v22, 2, v23\n"
"v_bfe_u32 v28, v29, 1, 2\n"
"v_lshlrev_b32 v29, 7, v29\n"
"v_lshl_add_u32 v29, v24, 3, v29\n"
"v_add_u32 v29, %7, v29\n"
"v_add_u32 v29, 131072, v29\n"
"v_xor_b32 v27, 0, v28\n"
"v_lshl_add_u32 v0, v27, 5, v29\n"
"v_xor_b32 v27, 1, v28\n"
"v_lshl_add_u32 v1, v27, 5, v29\n"
"v_xor_b32 v27, 2, v28\n"
"v_lshl_add_u32 v2, v27, 5, v29\n"
"v_xor_b32 v27, 3, v28\n"
"v_lshl_add_u32 v3, v27, 5, v29\n"
"s_mul_i32 s46, %6, 43008\n"
"v_lshlrev_b32 v27, 4, v20\n"
"v_add_u32 v27, s46, v27\n"
"v_add_u32 v5, 0, v27\n"
"v_add_u32 v6, 14336, v27\n"
"v_add_u32 v7, 28672, v27\n"
"s_nop 4\n"
"v_xor_b32 v27, 16, v20\n"
"v_lshlrev_b32 v27, 2, v27\n"
"v_xor_b32 v28, 32, v20\n"
"v_lshlrev_b32 v28, 2, v28\n"
"v_mov_b32 v8, v205\n"
"v_mov_b32 v9, v206\n"
"v_mov_b32 v10, v207\n"
"ds_bpermute_b32 v12, v27, v8\n"
"ds_bpermute_b32 v13, v27, v9\n"
"ds_bpermute_b32 v14, v27, v10\n"
"s_waitcnt lgkmcnt(0)\n"
"v_add_f32 v8, v8, v12\n"
"v_add_f32 v9, v9, v13\n"
"v_add_f32 v10, v10, v14\n"
"ds_bpermute_b32 v12, v28, v8\n"
"ds_bpermute_b32 v13, v28, v9\n"
"ds_bpermute_b32 v14, v28, v10\n"
"s_waitcnt lgkmcnt(0)\n"
"v_add_f32 v8, v8, v12\n"
"v_add_f32 v9, v9, v13\n"
"v_add_f32 v10, v10, v14\n"
"s_nop 0\n"
"v_rcp_f32 v8, v8\n"
"v_rcp_f32 v9, v9\n"
"v_rcp_f32 v10, v10\n"
"v_accvgpr_read_b32 v12, a0\n"
"v_accvgpr_read_b32 v13, a1\n"
"v_accvgpr_read_b32 v14, a2\n"
"v_accvgpr_read_b32 v15, a3\n"
"v_accvgpr_read_b32 v16, a12\n"
"v_accvgpr_read_b32 v17, a13\n"
"v_accvgpr_read_b32 v18, a14\n"
"v_accvgpr_read_b32 v19, a15\n"
"v_cvt_pk_bf16_f32 v128, v12, v13\n"
"v_cvt_pk_bf16_f32 v129, v14, v15\n"
"v_cvt_pk_bf16_f32 v130, v16, v17\n"
"v_cvt_pk_bf16_f32 v131, v18, v19\n"
"v_accvgpr_read_b32 v20, a4\n"
"v_accvgpr_read_b32 v21, a5\n"
"v_accvgpr_read_b32 v22, a6\n"
"v_accvgpr_read_b32 v23, a7\n"
"v_accvgpr_read_b32 v24, a16\n"
"v_accvgpr_read_b32 v25, a17\n"
"v_accvgpr_read_b32 v26, a18\n"
"v_accvgpr_read_b32 v27, a19\n"
"v_cvt_pk_bf16_f32 v132, v20, v21\n"
"v_cvt_pk_bf16_f32 v133, v22, v23\n"
"v_cvt_pk_bf16_f32 v134, v24, v25\n"
"v_cvt_pk_bf16_f32 v135, v26, v27\n"
"v_accvgpr_read_b32 v12, a8\n"
"v_accvgpr_read_b32 v13, a9\n"
"v_accvgpr_read_b32 v14, a10\n"
"v_accvgpr_read_b32 v15, a11\n"
"v_accvgpr_read_b32 v16, a20\n"
"v_accvgpr_read_b32 v17, a21\n"
"v_accvgpr_read_b32 v18, a22\n"
"v_accvgpr_read_b32 v19, a23\n"
"v_cvt_pk_bf16_f32 v136, v12, v13\n"
"v_cvt_pk_bf16_f32 v137, v14, v15\n"
"v_cvt_pk_bf16_f32 v138, v16, v17\n"
"v_cvt_pk_bf16_f32 v139, v18, v19\n"
"s_waitcnt vmcnt(32)\n"
"s_barrier\n"
"s_waitcnt lgkmcnt(0)\n"
"ds_read_b64_tr_b16 v[224:225], v0 offset:0\n"
"ds_read_b64_tr_b16 v[226:227], v0 offset:2048\n"
"ds_read_b64_tr_b16 v[228:229], v1 offset:0\n"
"ds_read_b64_tr_b16 v[230:231], v1 offset:2048\n"
"ds_read_b64_tr_b16 v[232:233], v2 offset:0\n"
"ds_read_b64_tr_b16 v[234:235], v2 offset:2048\n"
"ds_read_b64_tr_b16 v[236:237], v3 offset:0\n"
"ds_read_b64_tr_b16 v[238:239], v3 offset:2048\n"
"s_waitcnt lgkmcnt(6)\n"
"v_mfma_f32_16x16x32_bf16 v[32:35], v[224:227], v[128:131], 0\n"
"ds_read_b64_tr_b16 v[240:241], v0 offset:4096\n"
"ds_read_b64_tr_b16 v[242:243], v0 offset:6144\n"
"v_accvgpr_read_b32 v20, a24\n"
"v_accvgpr_read_b32 v21, a25\n"
"v_mfma_f32_16x16x32_bf16 v[36:39], v[224:227], v[132:135], 0\n"
"ds_read_b64_tr_b16 v[244:245], v1 offset:4096\n"
"ds_read_b64_tr_b16 v[246:247], v1 offset:6144\n"
"v_accvgpr_read_b32 v22, a26\n"
"v_accvgpr_read_b32 v23, a27\n"
"v_mfma_f32_16x16x32_bf16 v[40:43], v[224:227], v[136:139], 0\n"
"v_accvgpr_read_b32 v24, a36\n"
"v_accvgpr_read_b32 v25, a37\n"
"v_accvgpr_read_b32 v26, a38\n"
"v_accvgpr_read_b32 v27, a39\n"
"s_waitcnt lgkmcnt(4)\n"
"v_mfma_f32_16x16x32_bf16 v[44:47], v[228:231], v[128:131], 0\n"
"v_cvt_pk_bf16_f32 v140, v20, v21\n"
"v_cvt_pk_bf16_f32 v141, v22, v23\n"
"v_cvt_pk_bf16_f32 v142, v24, v25\n"
"v_cvt_pk_bf16_f32 v143, v26, v27\n"
"v_mfma_f32_16x16x32_bf16 v[48:51], v[228:231], v[132:135], 0\n"
"v_accvgpr_read_b32 v12, a28\n"
"v_accvgpr_read_b32 v13, a29\n"
"v_accvgpr_read_b32 v14, a30\n"
"v_accvgpr_read_b32 v15, a31\n"
"v_mfma_f32_16x16x32_bf16 v[52:55], v[228:231], v[136:139], 0\n"
"v_accvgpr_read_b32 v16, a40\n"
"v_accvgpr_read_b32 v17, a41\n"
"v_accvgpr_read_b32 v18, a42\n"
"v_accvgpr_read_b32 v19, a43\n"
"v_mfma_f32_16x16x32_bf16 v[56:59], v[232:235], v[128:131], 0\n"
"v_cvt_pk_bf16_f32 v144, v12, v13\n"
"v_cvt_pk_bf16_f32 v145, v14, v15\n"
"v_cvt_pk_bf16_f32 v146, v16, v17\n"
"ds_read_b64_tr_b16 v[248:249], v2 offset:4096\n"
"v_mfma_f32_16x16x32_bf16 v[60:63], v[232:235], v[132:135], 0\n"
"v_cvt_pk_bf16_f32 v147, v18, v19\n"
"v_accvgpr_read_b32 v20, a32\n"
"v_accvgpr_read_b32 v21, a33\n"
"v_accvgpr_read_b32 v22, a34\n"
"v_mfma_f32_16x16x32_bf16 v[64:67], v[232:235], v[136:139], 0\n"
"v_accvgpr_read_b32 v23, a35\n"
"v_accvgpr_read_b32 v24, a44\n"
"v_accvgpr_read_b32 v25, a45\n"
"ds_read_b64_tr_b16 v[250:251], v2 offset:6144\n"
"v_mfma_f32_16x16x32_bf16 v[68:71], v[236:239], v[128:131], 0\n"
"v_accvgpr_read_b32 v26, a46\n"
"v_accvgpr_read_b32 v27, a47\n"
"v_cvt_pk_bf16_f32 v148, v20, v21\n"
"ds_read_b64_tr_b16 v[252:253], v3 offset:4096\n"
"v_mfma_f32_16x16x32_bf16 v[72:75], v[236:239], v[132:135], 0\n"
"v_cvt_pk_bf16_f32 v149, v22, v23\n"
"v_cvt_pk_bf16_f32 v150, v24, v25\n"
"v_cvt_pk_bf16_f32 v151, v26, v27\n"
"ds_read_b64_tr_b16 v[254:255], v3 offset:6144\n"
"v_mfma_f32_16x16x32_bf16 v[76:79], v[236:239], v[136:139], 0\n"
"ds_read_b64_tr_b16 v[224:225], v0 offset:8192\n"
"ds_read_b64_tr_b16 v[226:227], v0 offset:10240\n"
"s_waitcnt lgkmcnt(5)\n"
"v_mfma_f32_16x16x32_bf16 v[32:35], v[240:243], v[140:143], v[32:35]\n"
"v_accvgpr_read_b32 v12, a48\n"
"v_accvgpr_read_b32 v13, a49\n"
"v_accvgpr_read_b32 v14, a50\n"
"v_accvgpr_read_b32 v15, a51\n"
"v_mfma_f32_16x16x32_bf16 v[36:39], v[240:243], v[144:147], v[36:39]\n"
"ds_read_b64_tr_b16 v[228:229], v1 offset:8192\n"
"ds_read_b64_tr_b16 v[230:231], v1 offset:10240\n"
"v_accvgpr_read_b32 v16, a60\n"
"v_accvgpr_read_b32 v17, a61\n"
"v_mfma_f32_16x16x32_bf16 v[40:43], v[240:243], v[148:151], v[40:43]\n"
"v_accvgpr_read_b32 v18, a62\n"
"v_accvgpr_read_b32 v19, a63\n"
"v_cvt_pk_bf16_f32 v152, v12, v13\n"
"v_cvt_pk_bf16_f32 v153, v14, v15\n"
"v_mfma_f32_16x16x32_bf16 v[44:47], v[244:247], v[140:143], v[44:47]\n"
"v_cvt_pk_bf16_f32 v154, v16, v17\n"
"v_cvt_pk_bf16_f32 v155, v18, v19\n"
"v_accvgpr_read_b32 v20, a52\n"
"v_accvgpr_read_b32 v21, a53\n"
"v_mfma_f32_16x16x32_bf16 v[48:51], v[244:247], v[144:147], v[48:51]\n"
"v_accvgpr_read_b32 v22, a54\n"
"v_accvgpr_read_b32 v23, a55\n"
"v_accvgpr_read_b32 v24, a64\n"
"ds_read_b64_tr_b16 v[232:233], v2 offset:8192\n"
"v_mfma_f32_16x16x32_bf16 v[52:55], v[244:247], v[148:151], v[52:55]\n"
"v_accvgpr_read_b32 v25, a65\n"
"v_accvgpr_read_b32 v26, a66\n"
"v_accvgpr_read_b32 v27, a67\n"
"ds_read_b64_tr_b16 v[234:235], v2 offset:10240\n"
"s_waitcnt lgkmcnt(2)\n"
"v_mfma_f32_16x16x32_bf16 v[56:59], v[248:251], v[140:143], v[56:59]\n"
"v_cvt_pk_bf16_f32 v156, v20, v21\n"
"v_cvt_pk_bf16_f32 v157, v22, v23\n"
"v_cvt_pk_bf16_f32 v158, v24, v25\n"
"v_mfma_f32_16x16x32_bf16 v[60:63], v[248:251], v[144:147], v[60:63]\n"
"v_cvt_pk_bf16_f32 v159, v26, v27\n"
"v_accvgpr_read_b32 v12, a56\n"
"v_accvgpr_read_b32 v13, a57\n"
"v_accvgpr_read_b32 v14, a58\n"
"v_mfma_f32_16x16x32_bf16 v[64:67], v[248:251], v[148:151], v[64:67]\n"
"v_accvgpr_read_b32 v15, a59\n"
"v_accvgpr_read_b32 v16, a68\n"
"v_accvgpr_read_b32 v17, a69\n"
"ds_read_b64_tr_b16 v[236:237], v3 offset:8192\n"
"v_mfma_f32_16x16x32_bf16 v[68:71], v[252:255], v[140:143], v[68:71]\n"
"v_accvgpr_read_b32 v18, a70\n"
"v_accvgpr_read_b32 v19, a71\n"
"v_cvt_pk_bf16_f32 v160, v12, v13\n"
"ds_read_b64_tr_b16 v[238:239], v3 offset:10240\n"
"v_mfma_f32_16x16x32_bf16 v[72:75], v[252:255], v[144:147], v[72:75]\n"
"v_cvt_pk_bf16_f32 v161, v14, v15\n"
"v_cvt_pk_bf16_f32 v162, v16, v17\n"
"v_cvt_pk_bf16_f32 v163, v18, v19\n"
"ds_read_b64_tr_b16 v[240:241], v0 offset:12288\n"
"v_mfma_f32_16x16x32_bf16 v[76:79], v[252:255], v[148:151], v[76:79]\n"
"ds_read_b64_tr_b16 v[242:243], v0 offset:14336\n"
"v_mfma_f32_16x16x32_bf16 v[32:35], v[224:227], v[152:155], v[32:35]\n"
"v_accvgpr_read_b32 v20, a72\n"
"v_accvgpr_read_b32 v21, a73\n"
"v_accvgpr_read_b32 v22, a74\n"
"v_accvgpr_read_b32 v23, a75\n"
"v_mfma_f32_16x16x32_bf16 v[36:39], v[224:227], v[156:159], v[36:39]\n"
"ds_read_b64_tr_b16 v[244:245], v1 offset:12288\n"
"ds_read_b64_tr_b16 v[246:247], v1 offset:14336\n"
"v_accvgpr_read_b32 v24, a84\n"
"v_accvgpr_read_b32 v25, a85\n"
"v_mfma_f32_16x16x32_bf16 v[40:43], v[224:227], v[160:163], v[40:43]\n"
"v_accvgpr_read_b32 v26, a86\n"
"v_accvgpr_read_b32 v27, a87\n"
"v_cvt_pk_bf16_f32 v164, v20, v21\n"
"v_cvt_pk_bf16_f32 v165, v22, v23\n"
"v_mfma_f32_16x16x32_bf16 v[44:47], v[228:231], v[152:155], v[44:47]\n"
"v_cvt_pk_bf16_f32 v166, v24, v25\n"
"v_cvt_pk_bf16_f32 v167, v26, v27\n"
"v_accvgpr_read_b32 v12, a76\n"
"v_accvgpr_read_b32 v13, a77\n"
"v_mfma_f32_16x16x32_bf16 v[48:51], v[228:231], v[156:159], v[48:51]\n"
"v_accvgpr_read_b32 v14, a78\n"
"v_accvgpr_read_b32 v15, a79\n"
"v_accvgpr_read_b32 v16, a88\n"
"ds_read_b64_tr_b16 v[248:249], v2 offset:12288\n"
"v_mfma_f32_16x16x32_bf16 v[52:55], v[228:231], v[160:163], v[52:55]\n"
"v_accvgpr_read_b32 v17, a89\n"
"v_accvgpr_read_b32 v18, a90\n"
"v_accvgpr_read_b32 v19, a91\n"
"ds_read_b64_tr_b16 v[250:251], v2 offset:14336\n"
"s_waitcnt lgkmcnt(2)\n"
"v_mfma_f32_16x16x32_bf16 v[56:59], v[232:235], v[152:155], v[56:59]\n"
"v_cvt_pk_bf16_f32 v168, v12, v13\n"
"v_cvt_pk_bf16_f32 v169, v14, v15\n"
"v_cvt_pk_bf16_f32 v170, v16, v17\n"
"v_mfma_f32_16x16x32_bf16 v[60:63], v[232:235], v[156:159], v[60:63]\n"
"v_cvt_pk_bf16_f32 v171, v18, v19\n"
"v_accvgpr_read_b32 v20, a80\n"
"v_accvgpr_read_b32 v21, a81\n"
"v_accvgpr_read_b32 v22, a82\n"
"v_mfma_f32_16x16x32_bf16 v[64:67], v[232:235], v[160:163], v[64:67]\n"
"v_accvgpr_read_b32 v23, a83\n"
"v_accvgpr_read_b32 v24, a92\n"
"v_accvgpr_read_b32 v25, a93\n"
"ds_read_b64_tr_b16 v[252:253], v3 offset:12288\n"
"v_mfma_f32_16x16x32_bf16 v[68:71], v[236:239], v[152:155], v[68:71]\n"
"v_accvgpr_read_b32 v26, a94\n"
"v_accvgpr_read_b32 v27, a95\n"
"v_cvt_pk_bf16_f32 v172, v20, v21\n"
"ds_read_b64_tr_b16 v[254:255], v3 offset:14336\n"
"v_mfma_f32_16x16x32_bf16 v[72:75], v[236:239], v[156:159], v[72:75]\n"
"v_cvt_pk_bf16_f32 v173, v22, v23\n"
"v_cvt_pk_bf16_f32 v174, v24, v25\n"
"v_cvt_pk_bf16_f32 v175, v26, v27\n"
"ds_read_b64_tr_b16 v[224:225], v0 offset:16384\n"
"v_mfma_f32_16x16x32_bf16 v[76:79], v[236:239], v[160:163], v[76:79]\n"
"ds_read_b64_tr_b16 v[226:227], v0 offset:18432\n"
"v_mfma_f32_16x16x32_bf16 v[32:35], v[240:243], v[164:167], v[32:35]\n"
"v_accvgpr_read_b32 v12, a96\n"
"v_accvgpr_read_b32 v13, a97\n"
"v_accvgpr_read_b32 v14, a98\n"
"v_accvgpr_read_b32 v15, a99\n"
"v_mfma_f32_16x16x32_bf16 v[36:39], v[240:243], v[168:171], v[36:39]\n"
"ds_read_b64_tr_b16 v[228:229], v1 offset:16384\n"
"ds_read_b64_tr_b16 v[230:231], v1 offset:18432\n"
"v_accvgpr_read_b32 v16, a108\n"
"v_accvgpr_read_b32 v17, a109\n"
"v_mfma_f32_16x16x32_bf16 v[40:43], v[240:243], v[172:175], v[40:43]\n"
"v_accvgpr_read_b32 v18, a110\n"
"v_accvgpr_read_b32 v19, a111\n"
"v_cvt_pk_bf16_f32 v176, v12, v13\n"
"v_cvt_pk_bf16_f32 v177, v14, v15\n"
"v_mfma_f32_16x16x32_bf16 v[44:47], v[244:247], v[164:167], v[44:47]\n"
"v_cvt_pk_bf16_f32 v178, v16, v17\n"
"v_cvt_pk_bf16_f32 v179, v18, v19\n"
"v_accvgpr_read_b32 v20, a100\n"
"v_accvgpr_read_b32 v21, a101\n"
"v_mfma_f32_16x16x32_bf16 v[48:51], v[244:247], v[168:171], v[48:51]\n"
"v_accvgpr_read_b32 v22, a102\n"
"v_accvgpr_read_b32 v23, a103\n"
"v_accvgpr_read_b32 v24, a112\n"
"ds_read_b64_tr_b16 v[232:233], v2 offset:16384\n"
"v_mfma_f32_16x16x32_bf16 v[52:55], v[244:247], v[172:175], v[52:55]\n"
"v_accvgpr_read_b32 v25, a113\n"
"v_accvgpr_read_b32 v26, a114\n"
"v_accvgpr_read_b32 v27, a115\n"
"ds_read_b64_tr_b16 v[234:235], v2 offset:18432\n"
"s_waitcnt lgkmcnt(2)\n"
"v_mfma_f32_16x16x32_bf16 v[56:59], v[248:251], v[164:167], v[56:59]\n"
"v_cvt_pk_bf16_f32 v180, v20, v21\n"
"v_cvt_pk_bf16_f32 v181, v22, v23\n"
"v_cvt_pk_bf16_f32 v182, v24, v25\n"
"v_mfma_f32_16x16x32_bf16 v[60:63], v[248:251], v[168:171], v[60:63]\n"
"v_cvt_pk_bf16_f32 v183, v26, v27\n"
"v_accvgpr_read_b32 v12, a104\n"
"v_accvgpr_read_b32 v13, a105\n"
"v_accvgpr_read_b32 v14, a106\n"
"v_mfma_f32_16x16x32_bf16 v[64:67], v[248:251], v[172:175], v[64:67]\n"
"v_accvgpr_read_b32 v15, a107\n"
"v_accvgpr_read_b32 v16, a116\n"
"v_accvgpr_read_b32 v17, a117\n"
"ds_read_b64_tr_b16 v[236:237], v3 offset:16384\n"
"v_mfma_f32_16x16x32_bf16 v[68:71], v[252:255], v[164:167], v[68:71]\n"
"v_accvgpr_read_b32 v18, a118\n"
"v_accvgpr_read_b32 v19, a119\n"
"v_cvt_pk_bf16_f32 v184, v12, v13\n"
"ds_read_b64_tr_b16 v[238:239], v3 offset:18432\n"
"v_mfma_f32_16x16x32_bf16 v[72:75], v[252:255], v[168:171], v[72:75]\n"
"v_cvt_pk_bf16_f32 v185, v14, v15\n"
"v_cvt_pk_bf16_f32 v186, v16, v17\n"
"v_cvt_pk_bf16_f32 v187, v18, v19\n"
"ds_read_b64_tr_b16 v[240:241], v0 offset:20480\n"
"v_mfma_f32_16x16x32_bf16 v[76:79], v[252:255], v[172:175], v[76:79]\n"
"ds_read_b64_tr_b16 v[242:243], v0 offset:22528\n"
"v_mfma_f32_16x16x32_bf16 v[32:35], v[224:227], v[176:179], v[32:35]\n"
"v_accvgpr_read_b32 v20, a120\n"
"v_accvgpr_read_b32 v21, a121\n"
"v_accvgpr_read_b32 v22, a122\n"
"v_accvgpr_read_b32 v23, a123\n"
"v_mfma_f32_16x16x32_bf16 v[36:39], v[224:227], v[180:183], v[36:39]\n"
"ds_read_b64_tr_b16 v[244:245], v1 offset:20480\n"
"ds_read_b64_tr_b16 v[246:247], v1 offset:22528\n"
"v_accvgpr_read_b32 v24, a132\n"
"v_accvgpr_read_b32 v25, a133\n"
"v_mfma_f32_16x16x32_bf16 v[40:43], v[224:227], v[184:187], v[40:43]\n"
"v_accvgpr_read_b32 v26, a134\n"
"v_accvgpr_read_b32 v27, a135\n"
"v_cvt_pk_bf16_f32 v188, v20, v21\n"
"v_cvt_pk_bf16_f32 v189, v22, v23\n"
"v_mfma_f32_16x16x32_bf16 v[44:47], v[228:231], v[176:179], v[44:47]\n"
"v_cvt_pk_bf16_f32 v190, v24, v25\n"
"v_cvt_pk_bf16_f32 v191, v26, v27\n"
"v_accvgpr_read_b32 v12, a124\n"
"v_accvgpr_read_b32 v13, a125\n"
"v_mfma_f32_16x16x32_bf16 v[48:51], v[228:231], v[180:183], v[48:51]\n"
"v_accvgpr_read_b32 v14, a126\n"
"v_accvgpr_read_b32 v15, a127\n"
"v_accvgpr_read_b32 v16, a136\n"
"ds_read_b64_tr_b16 v[248:249], v2 offset:20480\n"
"v_mfma_f32_16x16x32_bf16 v[52:55], v[228:231], v[184:187], v[52:55]\n"
"v_accvgpr_read_b32 v17, a137\n"
"v_accvgpr_read_b32 v18, a138\n"
"v_accvgpr_read_b32 v19, a139\n"
"ds_read_b64_tr_b16 v[250:251], v2 offset:22528\n"
"s_waitcnt lgkmcnt(2)\n"
"v_mfma_f32_16x16x32_bf16 v[56:59], v[232:235], v[176:179], v[56:59]\n"
"v_cvt_pk_bf16_f32 v192, v12, v13\n"
"v_cvt_pk_bf16_f32 v193, v14, v15\n"
"v_cvt_pk_bf16_f32 v194, v16, v17\n"
"v_mfma_f32_16x16x32_bf16 v[60:63], v[232:235], v[180:183], v[60:63]\n"
"v_cvt_pk_bf16_f32 v195, v18, v19\n"
"v_accvgpr_read_b32 v20, a128\n"
"v_accvgpr_read_b32 v21, a129\n"
"v_accvgpr_read_b32 v22, a130\n"
"v_mfma_f32_16x16x32_bf16 v[64:67], v[232:235], v[184:187], v[64:67]\n"
"v_accvgpr_read_b32 v23, a131\n"
"v_accvgpr_read_b32 v24, a140\n"
"v_accvgpr_read_b32 v25, a141\n"
"ds_read_b64_tr_b16 v[252:253], v3 offset:20480\n"
"v_mfma_f32_16x16x32_bf16 v[68:71], v[236:239], v[176:179], v[68:71]\n"
"v_accvgpr_read_b32 v26, a142\n"
"v_accvgpr_read_b32 v27, a143\n"
"v_cvt_pk_bf16_f32 v196, v20, v21\n"
"ds_read_b64_tr_b16 v[254:255], v3 offset:22528\n"
"v_mfma_f32_16x16x32_bf16 v[72:75], v[236:239], v[180:183], v[72:75]\n"
"v_cvt_pk_bf16_f32 v197, v22, v23\n"
"v_cvt_pk_bf16_f32 v198, v24, v25\n"
"v_cvt_pk_bf16_f32 v199, v26, v27\n"
"ds_read_b64_tr_b16 v[224:225], v0 offset:24576\n"
"v_mfma_f32_16x16x32_bf16 v[76:79], v[236:239], v[184:187], v[76:79]\n"
"ds_read_b64_tr_b16 v[226:227], v0 offset:26624\n"
"v_mfma_f32_16x16x32_bf16 v[32:35], v[240:243], v[188:191], v[32:35]\n"
"v_accvgpr_read_b32 v12, a144\n"
"v_accvgpr_read_b32 v13, a145\n"
"v_accvgpr_read_b32 v14, a146\n"
"v_accvgpr_read_b32 v15, a147\n"
"v_mfma_f32_16x16x32_bf16 v[36:39], v[240:243], v[192:195], v[36:39]\n"
"ds_read_b64_tr_b16 v[228:229], v1 offset:24576\n"
"ds_read_b64_tr_b16 v[230:231], v1 offset:26624\n"
"v_accvgpr_read_b32 v16, a156\n"
"v_accvgpr_read_b32 v17, a157\n"
"v_mfma_f32_16x16x32_bf16 v[40:43], v[240:243], v[196:199], v[40:43]\n"
"v_accvgpr_read_b32 v18, a158\n"
"v_accvgpr_read_b32 v19, a159\n"
"v_cvt_pk_bf16_f32 v200, v12, v13\n"
"v_cvt_pk_bf16_f32 v201, v14, v15\n"
"v_mfma_f32_16x16x32_bf16 v[44:47], v[244:247], v[188:191], v[44:47]\n"
"v_cvt_pk_bf16_f32 v202, v16, v17\n"
"v_cvt_pk_bf16_f32 v203, v18, v19\n"
"v_accvgpr_read_b32 v20, a148\n"
"v_accvgpr_read_b32 v21, a149\n"
"v_mfma_f32_16x16x32_bf16 v[48:51], v[244:247], v[192:195], v[48:51]\n"
"v_accvgpr_read_b32 v22, a150\n"
"v_accvgpr_read_b32 v23, a151\n"
"v_accvgpr_read_b32 v24, a160\n"
"ds_read_b64_tr_b16 v[232:233], v2 offset:24576\n"
"v_mfma_f32_16x16x32_bf16 v[52:55], v[244:247], v[196:199], v[52:55]\n"
"v_accvgpr_read_b32 v25, a161\n"
"v_accvgpr_read_b32 v26, a162\n"
"v_accvgpr_read_b32 v27, a163\n"
"ds_read_b64_tr_b16 v[234:235], v2 offset:26624\n"
"s_waitcnt lgkmcnt(2)\n"
"v_mfma_f32_16x16x32_bf16 v[56:59], v[248:251], v[188:191], v[56:59]\n"
"v_cvt_pk_bf16_f32 v204, v20, v21\n"
"v_cvt_pk_bf16_f32 v205, v22, v23\n"
"v_cvt_pk_bf16_f32 v206, v24, v25\n"
"v_mfma_f32_16x16x32_bf16 v[60:63], v[248:251], v[192:195], v[60:63]\n"
"v_cvt_pk_bf16_f32 v207, v26, v27\n"
"v_accvgpr_read_b32 v12, a152\n"
"v_accvgpr_read_b32 v13, a153\n"
"v_accvgpr_read_b32 v14, a154\n"
"v_mfma_f32_16x16x32_bf16 v[64:67], v[248:251], v[196:199], v[64:67]\n"
"v_accvgpr_read_b32 v15, a155\n"
"v_accvgpr_read_b32 v16, a164\n"
"v_accvgpr_read_b32 v17, a165\n"
"ds_read_b64_tr_b16 v[236:237], v3 offset:24576\n"
"v_mfma_f32_16x16x32_bf16 v[68:71], v[252:255], v[188:191], v[68:71]\n"
"v_accvgpr_read_b32 v18, a166\n"
"v_accvgpr_read_b32 v19, a167\n"
"v_cvt_pk_bf16_f32 v208, v12, v13\n"
"ds_read_b64_tr_b16 v[238:239], v3 offset:26624\n"
"v_mfma_f32_16x16x32_bf16 v[72:75], v[252:255], v[192:195], v[72:75]\n"
"v_cvt_pk_bf16_f32 v209, v14, v15\n"
"v_cvt_pk_bf16_f32 v210, v16, v17\n"
"v_cvt_pk_bf16_f32 v211, v18, v19\n"
"ds_read_b64_tr_b16 v[240:241], v0 offset:28672\n"
"v_mfma_f32_16x16x32_bf16 v[76:79], v[252:255], v[196:199], v[76:79]\n"
"ds_read_b64_tr_b16 v[242:243], v0 offset:30720\n"
"v_mfma_f32_16x16x32_bf16 v[32:35], v[224:227], v[200:203], v[32:35]\n"
"v_accvgpr_read_b32 v20, a168\n"
"v_accvgpr_read_b32 v21, a169\n"
"v_accvgpr_read_b32 v22, a170\n"
"v_accvgpr_read_b32 v23, a171\n"
"v_mfma_f32_16x16x32_bf16 v[36:39], v[224:227], v[204:207], v[36:39]\n"
"ds_read_b64_tr_b16 v[244:245], v1 offset:28672\n"
"ds_read_b64_tr_b16 v[246:247], v1 offset:30720\n"
"v_accvgpr_read_b32 v24, a180\n"
"v_accvgpr_read_b32 v25, a181\n"
"v_mfma_f32_16x16x32_bf16 v[40:43], v[224:227], v[208:211], v[40:43]\n"
"v_accvgpr_read_b32 v26, a182\n"
"v_accvgpr_read_b32 v27, a183\n"
"v_cvt_pk_bf16_f32 v212, v20, v21\n"
"v_cvt_pk_bf16_f32 v213, v22, v23\n"
"v_mfma_f32_16x16x32_bf16 v[44:47], v[228:231], v[200:203], v[44:47]\n"
"v_cvt_pk_bf16_f32 v214, v24, v25\n"
"v_cvt_pk_bf16_f32 v215, v26, v27\n"
"v_accvgpr_read_b32 v12, a172\n"
"v_accvgpr_read_b32 v13, a173\n"
"v_mfma_f32_16x16x32_bf16 v[48:51], v[228:231], v[204:207], v[48:51]\n"
"ds_read_b64_tr_b16 v[248:249], v2 offset:28672\n"
"ds_read_b64_tr_b16 v[250:251], v2 offset:30720\n"
"v_accvgpr_read_b32 v14, a174\n"
"v_accvgpr_read_b32 v15, a175\n"
"v_mfma_f32_16x16x32_bf16 v[52:55], v[228:231], v[208:211], v[52:55]\n"
"ds_read_b64_tr_b16 v[252:253], v3 offset:28672\n"
"ds_read_b64_tr_b16 v[254:255], v3 offset:30720\n"
"v_accvgpr_read_b32 v16, a184\n"
"v_accvgpr_read_b32 v17, a185\n"
"s_waitcnt lgkmcnt(4)\n"
"v_mfma_f32_16x16x32_bf16 v[56:59], v[232:235], v[200:203], v[56:59]\n"
"v_accvgpr_read_b32 v18, a186\n"
"v_accvgpr_read_b32 v19, a187\n"
"v_cvt_pk_bf16_f32 v216, v12, v13\n"
"v_cvt_pk_bf16_f32 v217, v14, v15\n"
"v_mfma_f32_16x16x32_bf16 v[60:63], v[232:235], v[204:207], v[60:63]\n"
"v_cvt_pk_bf16_f32 v218, v16, v17\n"
"v_cvt_pk_bf16_f32 v219, v18, v19\n"
"v_accvgpr_read_b32 v20, a176\n"
"v_accvgpr_read_b32 v21, a177\n"
"v_mfma_f32_16x16x32_bf16 v[64:67], v[232:235], v[208:211], v[64:67]\n"
"v_accvgpr_read_b32 v22, a178\n"
"v_accvgpr_read_b32 v23, a179\n"
"v_accvgpr_read_b32 v24, a188\n"
"v_accvgpr_read_b32 v25, a189\n"
"v_mfma_f32_16x16x32_bf16 v[68:71], v[236:239], v[200:203], v[68:71]\n"
"v_accvgpr_read_b32 v26, a190\n"
"v_accvgpr_read_b32 v27, a191\n"
"v_cvt_pk_bf16_f32 v220, v20, v21\n"
"v_mfma_f32_16x16x32_bf16 v[72:75], v[236:239], v[204:207], v[72:75]\n"
"v_cvt_pk_bf16_f32 v221, v22, v23\n"
"v_cvt_pk_bf16_f32 v222, v24, v25\n"
"v_cvt_pk_bf16_f32 v223, v26, v27\n"
"v_mfma_f32_16x16x32_bf16 v[76:79], v[236:239], v[208:211], v[76:79]\n"
"v_mfma_f32_16x16x32_bf16 v[32:35], v[240:243], v[212:215], v[32:35]\n"
"v_mfma_f32_16x16x32_bf16 v[36:39], v[240:243], v[216:219], v[36:39]\n"
"v_mfma_f32_16x16x32_bf16 v[40:43], v[240:243], v[220:223], v[40:43]\n"
"s_waitcnt vmcnt(24) lgkmcnt(0)\n"
"s_barrier\n"
"v_subrev_u32 v0, 131072, v0\n"
"v_subrev_u32 v1, 131072, v1\n"
"v_mfma_f32_16x16x32_bf16 v[44:47], v[244:247], v[212:215], v[44:47]\n"
"v_subrev_u32 v2, 131072, v2\n"
"v_subrev_u32 v3, 131072, v3\n"
"ds_read_b64_tr_b16 v[224:225], v0 offset:0\n"
"ds_read_b64_tr_b16 v[226:227], v0 offset:2048\n"
"v_mfma_f32_16x16x32_bf16 v[48:51], v[244:247], v[216:219], v[48:51]\n"
"ds_read_b64_tr_b16 v[228:229], v1 offset:0\n"
"ds_read_b64_tr_b16 v[230:231], v1 offset:2048\n"
"ds_read_b64_tr_b16 v[232:233], v2 offset:0\n"
"ds_read_b64_tr_b16 v[234:235], v2 offset:2048\n"
"v_mfma_f32_16x16x32_bf16 v[52:55], v[244:247], v[220:223], v[52:55]\n"
"ds_read_b64_tr_b16 v[236:237], v3 offset:0\n"
"ds_read_b64_tr_b16 v[238:239], v3 offset:2048\n"
"v_mfma_f32_16x16x32_bf16 v[56:59], v[248:251], v[212:215], v[56:59]\n"
"v_mfma_f32_16x16x32_bf16 v[60:63], v[248:251], v[216:219], v[60:63]\n"
"v_mfma_f32_16x16x32_bf16 v[64:67], v[248:251], v[220:223], v[64:67]\n"
"v_mfma_f32_16x16x32_bf16 v[68:71], v[252:255], v[212:215], v[68:71]\n"
"v_mfma_f32_16x16x32_bf16 v[72:75], v[252:255], v[216:219], v[72:75]\n"
"v_mfma_f32_16x16x32_bf16 v[76:79], v[252:255], v[220:223], v[76:79]\n"
"s_waitcnt lgkmcnt(6)\n"
"v_mfma_f32_16x16x32_bf16 v[80:83], v[224:227], v[128:131], 0\n"
"ds_read_b64_tr_b16 v[240:241], v0 offset:4096\n"
"ds_read_b64_tr_b16 v[242:243], v0 offset:6144\n"
"v_mfma_f32_16x16x32_bf16 v[84:87], v[224:227], v[132:135], 0\n"
"s_mov_b32 s47, 640\n"
"s_add_u32 m0, s48, 131072\n"
"v_mfma_f32_16x16x32_bf16 v[88:91], v[224:227], v[136:139], 0\n"
"buffer_load_dwordx4 v4, %4, s47 offen offset:0 lds\n"
"ds_read_b64_tr_b16 v[244:245], v1 offset:4096\n"
"s_waitcnt lgkmcnt(3)\n"
"v_mfma_f32_16x16x32_bf16 v[92:95], v[228:231], v[128:131], 0\n"
"ds_read_b64_tr_b16 v[246:247], v1 offset:6144\n"
"v_mfma_f32_16x16x32_bf16 v[96:99], v[228:231], v[132:135], 0\n"
"ds_read_b64_tr_b16 v[248:249], v2 offset:4096\n"
"ds_read_b64_tr_b16 v[250:251], v2 offset:6144\n"
"v_mfma_f32_16x16x32_bf16 v[100:103], v[228:231], v[136:139], 0\n"
"v_mfma_f32_16x16x32_bf16 v[104:107], v[232:235], v[128:131], 0\n"
"s_mov_b32 s46, 0\n"
"v_mfma_f32_16x16x32_bf16 v[108:111], v[232:235], v[132:135], 0\n"
"ds_read_b64_tr_b16 v[252:253], v3 offset:4096\n"
"ds_read_b64_tr_b16 v[254:255], v3 offset:6144\n"
"v_mfma_f32_16x16x32_bf16 v[112:115], v[232:235], v[136:139], 0\n"
"v_mul_f32 v32, v32, v8\n"
"v_mul_f32 v33, v33, v8\n"
"v_mfma_f32_16x16x32_bf16 v[116:119], v[236:239], v[128:131], 0\n"
"v_mul_f32 v34, v34, v8\n"
"v_mfma_f32_16x16x32_bf16 v[120:123], v[236:239], v[132:135], 0\n"
"ds_read_b64_tr_b16 v[224:225], v0 offset:8192\n"
"ds_read_b64_tr_b16 v[226:227], v0 offset:10240\n"
"v_mfma_f32_16x16x32_bf16 v[124:127], v[236:239], v[136:139], 0\n"
"buffer_load_dwordx4 v11, %4, s47 offen offset:1024 lds\n"
"v_mul_f32 v35, v35, v8\n"
"s_waitcnt lgkmcnt(4)\n"
"v_mfma_f32_16x16x32_bf16 v[80:83], v[240:243], v[140:143], v[80:83]\n"
"v_mul_f32 v44, v44, v8\n"
"v_mul_f32 v45, v45, v8\n"
"v_mfma_f32_16x16x32_bf16 v[84:87], v[240:243], v[144:147], v[84:87]\n"
"ds_read_b64_tr_b16 v[228:229], v1 offset:8192\n"
"ds_read_b64_tr_b16 v[230:231], v1 offset:10240\n"
"v_mfma_f32_16x16x32_bf16 v[88:91], v[240:243], v[148:151], v[88:91]\n"
"v_mul_f32 v46, v46, v8\n"
"v_mul_f32 v47, v47, v8\n"
"v_mfma_f32_16x16x32_bf16 v[92:95], v[244:247], v[140:143], v[92:95]\n"
"v_cvt_pk_bf16_f32 v32, v32, v33\n"
"v_mfma_f32_16x16x32_bf16 v[96:99], v[244:247], v[144:147], v[96:99]\n"
"ds_read_b64_tr_b16 v[232:233], v2 offset:8192\n"
"ds_read_b64_tr_b16 v[234:235], v2 offset:10240\n"
"v_mfma_f32_16x16x32_bf16 v[100:103], v[244:247], v[148:151], v[100:103]\n"
"v_cvt_pk_bf16_f32 v33, v34, v35\n"
"v_cvt_pk_bf16_f32 v34, v44, v45\n"
"v_mfma_f32_16x16x32_bf16 v[104:107], v[248:251], v[140:143], v[104:107]\n"
"v_cvt_pk_bf16_f32 v35, v46, v47\n"
"v_mfma_f32_16x16x32_bf16 v[108:111], v[248:251], v[144:147], v[108:111]\n"
"ds_read_b64_tr_b16 v[236:237], v3 offset:8192\n"
"ds_read_b64_tr_b16 v[238:239], v3 offset:10240\n"
"v_mfma_f32_16x16x32_bf16 v[112:115], v[248:251], v[148:151], v[112:115]\n"
"buffer_store_dwordx4 v[32:35], v5, %5, s46 offen offset:0 sc0 sc1\n"
"v_mul_f32 v36, v36, v9\n"
"s_waitcnt lgkmcnt(4)\n"
"v_mfma_f32_16x16x32_bf16 v[116:119], v[252:255], v[140:143], v[116:119]\n"
"buffer_load_dwordx4 v30, %4, s47 offen offset:2048 lds\n"
"v_mul_f32 v37, v37, v9\n"
"v_mfma_f32_16x16x32_bf16 v[120:123], v[252:255], v[144:147], v[120:123]\n"
"ds_read_b64_tr_b16 v[240:241], v0 offset:12288\n"
"ds_read_b64_tr_b16 v[242:243], v0 offset:14336\n"
"v_mfma_f32_16x16x32_bf16 v[124:127], v[252:255], v[148:151], v[124:127]\n"
"v_mul_f32 v38, v38, v9\n"
"v_mul_f32 v39, v39, v9\n"
"v_mfma_f32_16x16x32_bf16 v[80:83], v[224:227], v[152:155], v[80:83]\n"
"v_mul_f32 v48, v48, v9\n"
"v_mfma_f32_16x16x32_bf16 v[84:87], v[224:227], v[156:159], v[84:87]\n"
"ds_read_b64_tr_b16 v[244:245], v1 offset:12288\n"
"ds_read_b64_tr_b16 v[246:247], v1 offset:14336\n"
"v_mfma_f32_16x16x32_bf16 v[88:91], v[224:227], v[160:163], v[88:91]\n"
"v_mul_f32 v49, v49, v9\n"
"v_mul_f32 v50, v50, v9\n"
"v_mfma_f32_16x16x32_bf16 v[92:95], v[228:231], v[152:155], v[92:95]\n"
"v_mul_f32 v51, v51, v9\n"
"v_mfma_f32_16x16x32_bf16 v[96:99], v[228:231], v[156:159], v[96:99]\n"
"ds_read_b64_tr_b16 v[248:249], v2 offset:12288\n"
"ds_read_b64_tr_b16 v[250:251], v2 offset:14336\n"
"v_mfma_f32_16x16x32_bf16 v[100:103], v[228:231], v[160:163], v[100:103]\n"
"v_cvt_pk_bf16_f32 v36, v36, v37\n"
"v_cvt_pk_bf16_f32 v37, v38, v39\n"
"s_waitcnt lgkmcnt(4)\n"
"v_mfma_f32_16x16x32_bf16 v[104:107], v[232:235], v[152:155], v[104:107]\n"
"v_cvt_pk_bf16_f32 v38, v48, v49\n"
"v_mfma_f32_16x16x32_bf16 v[108:111], v[232:235], v[156:159], v[108:111]\n"
"buffer_load_dwordx4 v31, %4, s47 offen offset:3072 lds\n"
"ds_read_b64_tr_b16 v[252:253], v3 offset:12288\n"
"v_mfma_f32_16x16x32_bf16 v[112:115], v[232:235], v[160:163], v[112:115]\n"
"ds_read_b64_tr_b16 v[254:255], v3 offset:14336\n"
"v_cvt_pk_bf16_f32 v39, v50, v51\n"
"v_mfma_f32_16x16x32_bf16 v[116:119], v[236:239], v[152:155], v[116:119]\n"
"buffer_store_dwordx4 v[36:39], v6, %5, s46 offen offset:0 sc0 sc1\n"
"v_mul_f32 v40, v40, v10\n"
"v_mfma_f32_16x16x32_bf16 v[120:123], v[236:239], v[156:159], v[120:123]\n"
"ds_read_b64_tr_b16 v[224:225], v0 offset:16384\n"
"ds_read_b64_tr_b16 v[226:227], v0 offset:18432\n"
"v_mfma_f32_16x16x32_bf16 v[124:127], v[236:239], v[160:163], v[124:127]\n"
"v_mul_f32 v41, v41, v10\n"
"v_mul_f32 v42, v42, v10\n"
"v_mfma_f32_16x16x32_bf16 v[80:83], v[240:243], v[164:167], v[80:83]\n"
"v_mul_f32 v43, v43, v10\n"
"v_mfma_f32_16x16x32_bf16 v[84:87], v[240:243], v[168:171], v[84:87]\n"
"ds_read_b64_tr_b16 v[228:229], v1 offset:16384\n"
"ds_read_b64_tr_b16 v[230:231], v1 offset:18432\n"
"v_mfma_f32_16x16x32_bf16 v[88:91], v[240:243], v[172:175], v[88:91]\n"
"v_mul_f32 v52, v52, v10\n"
"v_mul_f32 v53, v53, v10\n"
"s_waitcnt lgkmcnt(4)\n"
"v_mfma_f32_16x16x32_bf16 v[92:95], v[244:247], v[164:167], v[92:95]\n"
"v_mul_f32 v54, v54, v10\n"
"v_mfma_f32_16x16x32_bf16 v[96:99], v[244:247], v[168:171], v[96:99]\n"
"s_mov_b32 s47, 29312\n"
"s_add_u32 m0, s48, 135168\n"
"v_mfma_f32_16x16x32_bf16 v[100:103], v[244:247], v[172:175], v[100:103]\n"
"buffer_load_dwordx4 v4, %4, s47 offen offset:0 lds\n"
"ds_read_b64_tr_b16 v[232:233], v2 offset:16384\n"
"v_mfma_f32_16x16x32_bf16 v[104:107], v[248:251], v[164:167], v[104:107]\n"
"ds_read_b64_tr_b16 v[234:235], v2 offset:18432\n"
"v_mul_f32 v55, v55, v10\n"
"v_mfma_f32_16x16x32_bf16 v[108:111], v[248:251], v[168:171], v[108:111]\n"
"ds_read_b64_tr_b16 v[236:237], v3 offset:16384\n"
"ds_read_b64_tr_b16 v[238:239], v3 offset:18432\n"
"v_mfma_f32_16x16x32_bf16 v[112:115], v[248:251], v[172:175], v[112:115]\n"
"v_cvt_pk_bf16_f32 v40, v40, v41\n"
"v_cvt_pk_bf16_f32 v41, v42, v43\n"
"v_mfma_f32_16x16x32_bf16 v[116:119], v[252:255], v[164:167], v[116:119]\n"
"v_cvt_pk_bf16_f32 v42, v52, v53\n"
"v_cvt_pk_bf16_f32 v43, v54, v55\n"
"v_mfma_f32_16x16x32_bf16 v[120:123], v[252:255], v[168:171], v[120:123]\n"
"ds_read_b64_tr_b16 v[240:241], v0 offset:20480\n"
"ds_read_b64_tr_b16 v[242:243], v0 offset:22528\n"
"v_mfma_f32_16x16x32_bf16 v[124:127], v[252:255], v[172:175], v[124:127]\n"
"buffer_store_dwordx4 v[40:43], v7, %5, s46 offen offset:0 sc0 sc1\n"
"v_mul_f32 v56, v56, v8\n"
"s_waitcnt lgkmcnt(4)\n"
"v_mfma_f32_16x16x32_bf16 v[80:83], v[224:227], v[176:179], v[80:83]\n"
"v_mul_f32 v57, v57, v8\n"
"v_mul_f32 v58, v58, v8\n"
"v_mfma_f32_16x16x32_bf16 v[84:87], v[224:227], v[180:183], v[84:87]\n"
"ds_read_b64_tr_b16 v[244:245], v1 offset:20480\n"
"ds_read_b64_tr_b16 v[246:247], v1 offset:22528\n"
"v_mfma_f32_16x16x32_bf16 v[88:91], v[224:227], v[184:187], v[88:91]\n"
"buffer_load_dwordx4 v11, %4, s47 offen offset:1024 lds\n"
"v_mul_f32 v59, v59, v8\n"
"v_mfma_f32_16x16x32_bf16 v[92:95], v[228:231], v[176:179], v[92:95]\n"
"v_mul_f32 v68, v68, v8\n"
"v_mul_f32 v69, v69, v8\n"
"v_mfma_f32_16x16x32_bf16 v[96:99], v[228:231], v[180:183], v[96:99]\n"
"ds_read_b64_tr_b16 v[248:249], v2 offset:20480\n"
"ds_read_b64_tr_b16 v[250:251], v2 offset:22528\n"
"v_mfma_f32_16x16x32_bf16 v[100:103], v[228:231], v[184:187], v[100:103]\n"
"v_mul_f32 v70, v70, v8\n"
"v_mul_f32 v71, v71, v8\n"
"v_mfma_f32_16x16x32_bf16 v[104:107], v[232:235], v[176:179], v[104:107]\n"
"v_cvt_pk_bf16_f32 v56, v56, v57\n"
"v_mfma_f32_16x16x32_bf16 v[108:111], v[232:235], v[180:183], v[108:111]\n"
"ds_read_b64_tr_b16 v[252:253], v3 offset:20480\n"
"ds_read_b64_tr_b16 v[254:255], v3 offset:22528\n"
"v_mfma_f32_16x16x32_bf16 v[112:115], v[232:235], v[184:187], v[112:115]\n"
"v_cvt_pk_bf16_f32 v57, v58, v59\n"
"v_cvt_pk_bf16_f32 v58, v68, v69\n"
"s_waitcnt lgkmcnt(4)\n"
"v_mfma_f32_16x16x32_bf16 v[116:119], v[236:239], v[176:179], v[116:119]\n"
"v_cvt_pk_bf16_f32 v59, v70, v71\n"
"v_mfma_f32_16x16x32_bf16 v[120:123], v[236:239], v[180:183], v[120:123]\n"
"ds_read_b64_tr_b16 v[224:225], v0 offset:24576\n"
"ds_read_b64_tr_b16 v[226:227], v0 offset:26624\n"
"v_mfma_f32_16x16x32_bf16 v[124:127], v[236:239], v[184:187], v[124:127]\n"
"buffer_store_dwordx4 v[56:59], v5, %5, s46 offen offset:1024 sc0 sc1\n"
"v_mul_f32 v60, v60, v9\n"
"v_mfma_f32_16x16x32_bf16 v[80:83], v[240:243], v[188:191], v[80:83]\n"
"buffer_load_dwordx4 v30, %4, s47 offen offset:2048 lds\n"
"v_mul_f32 v61, v61, v9\n"
"v_mfma_f32_16x16x32_bf16 v[84:87], v[240:243], v[192:195], v[84:87]\n"
"ds_read_b64_tr_b16 v[228:229], v1 offset:24576\n"
"ds_read_b64_tr_b16 v[230:231], v1 offset:26624\n"
"v_mfma_f32_16x16x32_bf16 v[88:91], v[240:243], v[196:199], v[88:91]\n"
"v_mul_f32 v62, v62, v9\n"
"v_mul_f32 v63, v63, v9\n"
"v_mfma_f32_16x16x32_bf16 v[92:95], v[244:247], v[188:191], v[92:95]\n"
"v_mul_f32 v72, v72, v9\n"
"v_mfma_f32_16x16x32_bf16 v[96:99], v[244:247], v[192:195], v[96:99]\n"
"ds_read_b64_tr_b16 v[232:233], v2 offset:24576\n"
"ds_read_b64_tr_b16 v[234:235], v2 offset:26624\n"
"v_mfma_f32_16x16x32_bf16 v[100:103], v[244:247], v[196:199], v[100:103]\n"
"v_mul_f32 v73, v73, v9\n"
"v_mul_f32 v74, v74, v9\n"
"s_waitcnt lgkmcnt(4)\n"
"v_mfma_f32_16x16x32_bf16 v[104:107], v[248:251], v[188:191], v[104:107]\n"
"v_mul_f32 v75, v75, v9\n"
"v_mfma_f32_16x16x32_bf16 v[108:111], v[248:251], v[192:195], v[108:111]\n"
"ds_read_b64_tr_b16 v[236:237], v3 offset:24576\n"
"ds_read_b64_tr_b16 v[238:239], v3 offset:26624\n"
"v_mfma_f32_16x16x32_bf16 v[112:115], v[248:251], v[196:199], v[112:115]\n"
"v_cvt_pk_bf16_f32 v60, v60, v61\n"
"v_cvt_pk_bf16_f32 v61, v62, v63\n"
"v_mfma_f32_16x16x32_bf16 v[116:119], v[252:255], v[188:191], v[116:119]\n"
"v_cvt_pk_bf16_f32 v62, v72, v73\n"
"v_mfma_f32_16x16x32_bf16 v[120:123], v[252:255], v[192:195], v[120:123]\n"
"buffer_load_dwordx4 v31, %4, s47 offen offset:3072 lds\n"
"ds_read_b64_tr_b16 v[240:241], v0 offset:28672\n"
"v_mfma_f32_16x16x32_bf16 v[124:127], v[252:255], v[196:199], v[124:127]\n"
"ds_read_b64_tr_b16 v[242:243], v0 offset:30720\n"
"v_cvt_pk_bf16_f32 v63, v74, v75\n"
"v_mfma_f32_16x16x32_bf16 v[80:83], v[224:227], v[200:203], v[80:83]\n"
"buffer_store_dwordx4 v[60:63], v6, %5, s46 offen offset:1024 sc0 sc1\n"
"v_mul_f32 v64, v64, v10\n"
"v_mfma_f32_16x16x32_bf16 v[84:87], v[224:227], v[204:207], v[84:87]\n"
"ds_read_b64_tr_b16 v[244:245], v1 offset:28672\n"
"ds_read_b64_tr_b16 v[246:247], v1 offset:30720\n"
"v_mfma_f32_16x16x32_bf16 v[88:91], v[224:227], v[208:211], v[88:91]\n"
"v_mul_f32 v65, v65, v10\n"
"v_mul_f32 v66, v66, v10\n"
"s_waitcnt lgkmcnt(4)\n"
"v_mfma_f32_16x16x32_bf16 v[92:95], v[228:231], v[200:203], v[92:95]\n"
"v_mul_f32 v67, v67, v10\n"
"v_mfma_f32_16x16x32_bf16 v[96:99], v[228:231], v[204:207], v[96:99]\n"
"ds_read_b64_tr_b16 v[248:249], v2 offset:28672\n"
"ds_read_b64_tr_b16 v[250:251], v2 offset:30720\n"
"v_mfma_f32_16x16x32_bf16 v[100:103], v[228:231], v[208:211], v[100:103]\n"
"ds_read_b64_tr_b16 v[252:253], v3 offset:28672\n"
"ds_read_b64_tr_b16 v[254:255], v3 offset:30720\n"
"v_mfma_f32_16x16x32_bf16 v[104:107], v[232:235], v[200:203], v[104:107]\n"
"v_mul_f32 v76, v76, v10\n"
"v_mul_f32 v77, v77, v10\n"
"v_mfma_f32_16x16x32_bf16 v[108:111], v[232:235], v[204:207], v[108:111]\n"
"v_mul_f32 v78, v78, v10\n"
"v_mul_f32 v79, v79, v10\n"
"v_mfma_f32_16x16x32_bf16 v[112:115], v[232:235], v[208:211], v[112:115]\n"
"v_cvt_pk_bf16_f32 v64, v64, v65\n"
"v_mfma_f32_16x16x32_bf16 v[116:119], v[236:239], v[200:203], v[116:119]\n"
"v_cvt_pk_bf16_f32 v65, v66, v67\n"
"v_mfma_f32_16x16x32_bf16 v[120:123], v[236:239], v[204:207], v[120:123]\n"
"v_cvt_pk_bf16_f32 v66, v76, v77\n"
"v_mfma_f32_16x16x32_bf16 v[124:127], v[236:239], v[208:211], v[124:127]\n"
"v_cvt_pk_bf16_f32 v67, v78, v79\n"
"s_waitcnt lgkmcnt(1)\n"
"v_mfma_f32_16x16x32_bf16 v[80:83], v[240:243], v[212:215], v[80:83]\n"
"buffer_store_dwordx4 v[64:67], v7, %5, s46 offen offset:1024 sc0 sc1\n"
"v_mfma_f32_16x16x32_bf16 v[84:87], v[240:243], v[216:219], v[84:87]\n"
"v_mfma_f32_16x16x32_bf16 v[88:91], v[240:243], v[220:223], v[88:91]\n"
"s_waitcnt vmcnt(30) lgkmcnt(0)\n"
"s_barrier\n"
"v_mfma_f32_16x16x32_bf16 v[92:95], v[244:247], v[212:215], v[92:95]\n"
"v_add_u32 v0, 98304, v0\n"
"v_add_u32 v1, 98304, v1\n"
"v_mfma_f32_16x16x32_bf16 v[96:99], v[244:247], v[216:219], v[96:99]\n"
"v_add_u32 v2, 98304, v2\n"
"v_add_u32 v3, 98304, v3\n"
"v_mfma_f32_16x16x32_bf16 v[100:103], v[244:247], v[220:223], v[100:103]\n"
"ds_read_b64_tr_b16 v[224:225], v0 offset:0\n"
"ds_read_b64_tr_b16 v[226:227], v0 offset:2048\n"
"v_mfma_f32_16x16x32_bf16 v[104:107], v[248:251], v[212:215], v[104:107]\n"
"ds_read_b64_tr_b16 v[228:229], v1 offset:0\n"
"ds_read_b64_tr_b16 v[230:231], v1 offset:2048\n"
"v_mfma_f32_16x16x32_bf16 v[108:111], v[248:251], v[216:219], v[108:111]\n"
"ds_read_b64_tr_b16 v[232:233], v2 offset:0\n"
"ds_read_b64_tr_b16 v[234:235], v2 offset:2048\n"
"v_mfma_f32_16x16x32_bf16 v[112:115], v[248:251], v[220:223], v[112:115]\n"
"ds_read_b64_tr_b16 v[236:237], v3 offset:0\n"
"ds_read_b64_tr_b16 v[238:239], v3 offset:2048\n"
"v_mfma_f32_16x16x32_bf16 v[116:119], v[252:255], v[212:215], v[116:119]\n"
"v_mfma_f32_16x16x32_bf16 v[120:123], v[252:255], v[216:219], v[120:123]\n"
"v_mfma_f32_16x16x32_bf16 v[124:127], v[252:255], v[220:223], v[124:127]\n"
"s_waitcnt lgkmcnt(6)\n"
"v_mfma_f32_16x16x32_bf16 v[32:35], v[224:227], v[128:131], 0\n"
"ds_read_b64_tr_b16 v[240:241], v0 offset:4096\n"
"ds_read_b64_tr_b16 v[242:243], v0 offset:6144\n"
"v_mfma_f32_16x16x32_bf16 v[36:39], v[224:227], v[132:135], 0\n"
"s_mov_b32 s47, 768\n"
"s_add_u32 m0, s48, 0\n"
"v_mfma_f32_16x16x32_bf16 v[40:43], v[224:227], v[136:139], 0\n"
"buffer_load_dwordx4 v4, %4, s47 offen offset:0 lds\n"
"ds_read_b64_tr_b16 v[244:245], v1 offset:4096\n"
"s_waitcnt lgkmcnt(5)\n"
"v_mfma_f32_16x16x32_bf16 v[44:47], v[228:231], v[128:131], 0\n"
"ds_read_b64_tr_b16 v[246:247], v1 offset:6144\n"
"v_mfma_f32_16x16x32_bf16 v[48:51], v[228:231], v[132:135], 0\n"
"ds_read_b64_tr_b16 v[248:249], v2 offset:4096\n"
"ds_read_b64_tr_b16 v[250:251], v2 offset:6144\n"
"v_mfma_f32_16x16x32_bf16 v[52:55], v[228:231], v[136:139], 0\n"
"v_mfma_f32_16x16x32_bf16 v[56:59], v[232:235], v[128:131], 0\n"
"s_mov_b32 s46, 2048\n"
"v_mfma_f32_16x16x32_bf16 v[60:63], v[232:235], v[132:135], 0\n"
"ds_read_b64_tr_b16 v[252:253], v3 offset:4096\n"
"ds_read_b64_tr_b16 v[254:255], v3 offset:6144\n"
"v_mfma_f32_16x16x32_bf16 v[64:67], v[232:235], v[136:139], 0\n"
"v_mul_f32 v80, v80, v8\n"
"v_mul_f32 v81, v81, v8\n"
"s_waitcnt lgkmcnt(5)\n"
"v_mfma_f32_16x16x32_bf16 v[68:71], v[236:239], v[128:131], 0\n"
"v_mul_f32 v82, v82, v8\n"
"v_mfma_f32_16x16x32_bf16 v[72:75], v[236:239], v[132:135], 0\n"
"ds_read_b64_tr_b16 v[224:225], v0 offset:8192\n"
"ds_read_b64_tr_b16 v[226:227], v0 offset:10240\n"
"v_mfma_f32_16x16x32_bf16 v[76:79], v[236:239], v[136:139], 0\n"
"buffer_load_dwordx4 v11, %4, s47 offen offset:1024 lds\n"
"v_mul_f32 v83, v83, v8\n"
"v_mfma_f32_16x16x32_bf16 v[32:35], v[240:243], v[140:143], v[32:35]\n"
"v_mul_f32 v92, v92, v8\n"
"v_mul_f32 v93, v93, v8\n"
"v_mfma_f32_16x16x32_bf16 v[36:39], v[240:243], v[144:147], v[36:39]\n"
"ds_read_b64_tr_b16 v[228:229], v1 offset:8192\n"
"ds_read_b64_tr_b16 v[230:231], v1 offset:10240\n"
"v_mfma_f32_16x16x32_bf16 v[40:43], v[240:243], v[148:151], v[40:43]\n"
"v_mul_f32 v94, v94, v8\n"
"v_mul_f32 v95, v95, v8\n"
"s_waitcnt lgkmcnt(4)\n"
"v_mfma_f32_16x16x32_bf16 v[44:47], v[244:247], v[140:143], v[44:47]\n"
"v_cvt_pk_bf16_f32 v80, v80, v81\n"
"v_mfma_f32_16x16x32_bf16 v[48:51], v[244:247], v[144:147], v[48:51]\n"
"ds_read_b64_tr_b16 v[232:233], v2 offset:8192\n"
"ds_read_b64_tr_b16 v[234:235], v2 offset:10240\n"
"v_mfma_f32_16x16x32_bf16 v[52:55], v[244:247], v[148:151], v[52:55]\n"
"v_cvt_pk_bf16_f32 v81, v82, v83\n"
"v_cvt_pk_bf16_f32 v82, v92, v93\n"
"v_mfma_f32_16x16x32_bf16 v[56:59], v[248:251], v[140:143], v[56:59]\n"
"v_cvt_pk_bf16_f32 v83, v94, v95\n"
"v_mfma_f32_16x16x32_bf16 v[60:63], v[248:251], v[144:147], v[60:63]\n"
"ds_read_b64_tr_b16 v[236:237], v3 offset:8192\n"
"ds_read_b64_tr_b16 v[238:239], v3 offset:10240\n"
"v_mfma_f32_16x16x32_bf16 v[64:67], v[248:251], v[148:151], v[64:67]\n"
"buffer_store_dwordx4 v[80:83], v5, %5, s46 offen offset:0 sc0 sc1\n"
"v_mul_f32 v84, v84, v9\n"
"v_mfma_f32_16x16x32_bf16 v[68:71], v[252:255], v[140:143], v[68:71]\n"
"buffer_load_dwordx4 v30, %4, s47 offen offset:2048 lds\n"
"v_mul_f32 v85, v85, v9\n"
"v_mfma_f32_16x16x32_bf16 v[72:75], v[252:255], v[144:147], v[72:75]\n"
"ds_read_b64_tr_b16 v[240:241], v0 offset:12288\n"
"ds_read_b64_tr_b16 v[242:243], v0 offset:14336\n"
"v_mfma_f32_16x16x32_bf16 v[76:79], v[252:255], v[148:151], v[76:79]\n"
"v_mul_f32 v86, v86, v9\n"
"v_mul_f32 v87, v87, v9\n"
"s_waitcnt lgkmcnt(4)\n"
"v_mfma_f32_16x16x32_bf16 v[32:35], v[224:227], v[152:155], v[32:35]\n"
"v_mul_f32 v96, v96, v9\n"
"v_mfma_f32_16x16x32_bf16 v[36:39], v[224:227], v[156:159], v[36:39]\n"
"ds_read_b64_tr_b16 v[244:245], v1 offset:12288\n"
"ds_read_b64_tr_b16 v[246:247], v1 offset:14336\n"
"v_mfma_f32_16x16x32_bf16 v[40:43], v[224:227], v[160:163], v[40:43]\n"
"v_mul_f32 v97, v97, v9\n"
"v_mul_f32 v98, v98, v9\n"
"v_mfma_f32_16x16x32_bf16 v[44:47], v[228:231], v[152:155], v[44:47]\n"
"v_mul_f32 v99, v99, v9\n"
"v_mfma_f32_16x16x32_bf16 v[48:51], v[228:231], v[156:159], v[48:51]\n"
"ds_read_b64_tr_b16 v[248:249], v2 offset:12288\n"
"ds_read_b64_tr_b16 v[250:251], v2 offset:14336\n"
"v_mfma_f32_16x16x32_bf16 v[52:55], v[228:231], v[160:163], v[52:55]\n"
"v_cvt_pk_bf16_f32 v84, v84, v85\n"
"v_cvt_pk_bf16_f32 v85, v86, v87\n"
"v_mfma_f32_16x16x32_bf16 v[56:59], v[232:235], v[152:155], v[56:59]\n"
"v_cvt_pk_bf16_f32 v86, v96, v97\n"
"v_mfma_f32_16x16x32_bf16 v[60:63], v[232:235], v[156:159], v[60:63]\n"
"buffer_load_dwordx4 v31, %4, s47 offen offset:3072 lds\n"
"ds_read_b64_tr_b16 v[252:253], v3 offset:12288\n"
"v_mfma_f32_16x16x32_bf16 v[64:67], v[232:235], v[160:163], v[64:67]\n"
"ds_read_b64_tr_b16 v[254:255], v3 offset:14336\n"
"v_cvt_pk_bf16_f32 v87, v98, v99\n"
"s_waitcnt lgkmcnt(4)\n"
"v_mfma_f32_16x16x32_bf16 v[68:71], v[236:239], v[152:155], v[68:71]\n"
"buffer_store_dwordx4 v[84:87], v6, %5, s46 offen offset:0 sc0 sc1\n"
"v_mul_f32 v88, v88, v10\n"
"v_mfma_f32_16x16x32_bf16 v[72:75], v[236:239], v[156:159], v[72:75]\n"
"ds_read_b64_tr_b16 v[224:225], v0 offset:16384\n"
"ds_read_b64_tr_b16 v[226:227], v0 offset:18432\n"
"v_mfma_f32_16x16x32_bf16 v[76:79], v[236:239], v[160:163], v[76:79]\n"
"v_mul_f32 v89, v89, v10\n"
"v_mul_f32 v90, v90, v10\n"
"v_mfma_f32_16x16x32_bf16 v[32:35], v[240:243], v[164:167], v[32:35]\n"
"v_mul_f32 v91, v91, v10\n"
"v_mfma_f32_16x16x32_bf16 v[36:39], v[240:243], v[168:171], v[36:39]\n"
"ds_read_b64_tr_b16 v[228:229], v1 offset:16384\n"
"ds_read_b64_tr_b16 v[230:231], v1 offset:18432\n"
"v_mfma_f32_16x16x32_bf16 v[40:43], v[240:243], v[172:175], v[40:43]\n"
"v_mul_f32 v100, v100, v10\n"
"v_mul_f32 v101, v101, v10\n"
"v_mfma_f32_16x16x32_bf16 v[44:47], v[244:247], v[164:167], v[44:47]\n"
"v_mul_f32 v102, v102, v10\n"
"v_mfma_f32_16x16x32_bf16 v[48:51], v[244:247], v[168:171], v[48:51]\n"
"s_mov_b32 s47, 29440\n"
"s_add_u32 m0, s48, 4096\n"
"v_mfma_f32_16x16x32_bf16 v[52:55], v[244:247], v[172:175], v[52:55]\n"
"buffer_load_dwordx4 v4, %4, s47 offen offset:0 lds\n"
"ds_read_b64_tr_b16 v[232:233], v2 offset:16384\n"
"s_waitcnt lgkmcnt(3)\n"
"v_mfma_f32_16x16x32_bf16 v[56:59], v[248:251], v[164:167], v[56:59]\n"
"ds_read_b64_tr_b16 v[234:235], v2 offset:18432\n"
"v_mul_f32 v103, v103, v10\n"
"v_mfma_f32_16x16x32_bf16 v[60:63], v[248:251], v[168:171], v[60:63]\n"
"ds_read_b64_tr_b16 v[236:237], v3 offset:16384\n"
"ds_read_b64_tr_b16 v[238:239], v3 offset:18432\n"
"v_mfma_f32_16x16x32_bf16 v[64:67], v[248:251], v[172:175], v[64:67]\n"
"v_cvt_pk_bf16_f32 v88, v88, v89\n"
"v_cvt_pk_bf16_f32 v89, v90, v91\n"
"v_mfma_f32_16x16x32_bf16 v[68:71], v[252:255], v[164:167], v[68:71]\n"
"v_cvt_pk_bf16_f32 v90, v100, v101\n"
"v_cvt_pk_bf16_f32 v91, v102, v103\n"
"v_mfma_f32_16x16x32_bf16 v[72:75], v[252:255], v[168:171], v[72:75]\n"
"ds_read_b64_tr_b16 v[240:241], v0 offset:20480\n"
"ds_read_b64_tr_b16 v[242:243], v0 offset:22528\n"
"v_mfma_f32_16x16x32_bf16 v[76:79], v[252:255], v[172:175], v[76:79]\n"
"buffer_store_dwordx4 v[88:91], v7, %5, s46 offen offset:0 sc0 sc1\n"
"v_mul_f32 v104, v104, v8\n"
"v_mfma_f32_16x16x32_bf16 v[32:35], v[224:227], v[176:179], v[32:35]\n"
"v_mul_f32 v105, v105, v8\n"
"v_mul_f32 v106, v106, v8\n"
"v_mfma_f32_16x16x32_bf16 v[36:39], v[224:227], v[180:183], v[36:39]\n"
"ds_read_b64_tr_b16 v[244:245], v1 offset:20480\n"
"ds_read_b64_tr_b16 v[246:247], v1 offset:22528\n"
"v_mfma_f32_16x16x32_bf16 v[40:43], v[224:227], v[184:187], v[40:43]\n"
"buffer_load_dwordx4 v11, %4, s47 offen offset:1024 lds\n"
"v_mul_f32 v107, v107, v8\n"
"s_waitcnt lgkmcnt(4)\n"
"v_mfma_f32_16x16x32_bf16 v[44:47], v[228:231], v[176:179], v[44:47]\n"
"v_mul_f32 v116, v116, v8\n"
"v_mul_f32 v117, v117, v8\n"
"v_mfma_f32_16x16x32_bf16 v[48:51], v[228:231], v[180:183], v[48:51]\n"
"ds_read_b64_tr_b16 v[248:249], v2 offset:20480\n"
"ds_read_b64_tr_b16 v[250:251], v2 offset:22528\n"
"v_mfma_f32_16x16x32_bf16 v[52:55], v[228:231], v[184:187], v[52:55]\n"
"v_mul_f32 v118, v118, v8\n"
"v_mul_f32 v119, v119, v8\n"
"v_mfma_f32_16x16x32_bf16 v[56:59], v[232:235], v[176:179], v[56:59]\n"
"v_cvt_pk_bf16_f32 v104, v104, v105\n"
"v_mfma_f32_16x16x32_bf16 v[60:63], v[232:235], v[180:183], v[60:63]\n"
"ds_read_b64_tr_b16 v[252:253], v3 offset:20480\n"
"ds_read_b64_tr_b16 v[254:255], v3 offset:22528\n"
"v_mfma_f32_16x16x32_bf16 v[64:67], v[232:235], v[184:187], v[64:67]\n"
"v_cvt_pk_bf16_f32 v105, v106, v107\n"
"v_cvt_pk_bf16_f32 v106, v116, v117\n"
"v_mfma_f32_16x16x32_bf16 v[68:71], v[236:239], v[176:179], v[68:71]\n"
"v_cvt_pk_bf16_f32 v107, v118, v119\n"
"v_mfma_f32_16x16x32_bf16 v[72:75], v[236:239], v[180:183], v[72:75]\n"
"ds_read_b64_tr_b16 v[224:225], v0 offset:24576\n"
"ds_read_b64_tr_b16 v[226:227], v0 offset:26624\n"
"v_mfma_f32_16x16x32_bf16 v[76:79], v[236:239], v[184:187], v[76:79]\n"
"buffer_store_dwordx4 v[104:107], v5, %5, s46 offen offset:1024 sc0 sc1\n"
"v_mul_f32 v108, v108, v9\n"
"s_waitcnt lgkmcnt(4)\n"
"v_mfma_f32_16x16x32_bf16 v[32:35], v[240:243], v[188:191], v[32:35]\n"
"buffer_load_dwordx4 v30, %4, s47 offen offset:2048 lds\n"
"v_mul_f32 v109, v109, v9\n"
"v_mfma_f32_16x16x32_bf16 v[36:39], v[240:243], v[192:195], v[36:39]\n"
"ds_read_b64_tr_b16 v[228:229], v1 offset:24576\n"
"ds_read_b64_tr_b16 v[230:231], v1 offset:26624\n"
"v_mfma_f32_16x16x32_bf16 v[40:43], v[240:243], v[196:199], v[40:43]\n"
"v_mul_f32 v110, v110, v9\n"
"v_mul_f32 v111, v111, v9\n"
"v_mfma_f32_16x16x32_bf16 v[44:47], v[244:247], v[188:191], v[44:47]\n"
"v_mul_f32 v120, v120, v9\n"
"v_mfma_f32_16x16x32_bf16 v[48:51], v[244:247], v[192:195], v[48:51]\n"
"ds_read_b64_tr_b16 v[232:233], v2 offset:24576\n"
"ds_read_b64_tr_b16 v[234:235], v2 offset:26624\n"
"v_mfma_f32_16x16x32_bf16 v[52:55], v[244:247], v[196:199], v[52:55]\n"
"v_mul_f32 v121, v121, v9\n"
"v_mul_f32 v122, v122, v9\n"
"v_mfma_f32_16x16x32_bf16 v[56:59], v[248:251], v[188:191], v[56:59]\n"
"v_mul_f32 v123, v123, v9\n"
"v_mfma_f32_16x16x32_bf16 v[60:63], v[248:251], v[192:195], v[60:63]\n"
"ds_read_b64_tr_b16 v[236:237], v3 offset:24576\n"
"ds_read_b64_tr_b16 v[238:239], v3 offset:26624\n"
"v_mfma_f32_16x16x32_bf16 v[64:67], v[248:251], v[196:199], v[64:67]\n"
"v_cvt_pk_bf16_f32 v108, v108, v109\n"
"v_cvt_pk_bf16_f32 v109, v110, v111\n"
"s_waitcnt lgkmcnt(4)\n"
"v_mfma_f32_16x16x32_bf16 v[68:71], v[252:255], v[188:191], v[68:71]\n"
"v_cvt_pk_bf16_f32 v110, v120, v121\n"
"v_mfma_f32_16x16x32_bf16 v[72:75], v[252:255], v[192:195], v[72:75]\n"
"buffer_load_dwordx4 v31, %4, s47 offen offset:3072 lds\n"
"ds_read_b64_tr_b16 v[240:241], v0 offset:28672\n"
"v_mfma_f32_16x16x32_bf16 v[76:79], v[252:255], v[196:199], v[76:79]\n"
"ds_read_b64_tr_b16 v[242:243], v0 offset:30720\n"
"v_cvt_pk_bf16_f32 v111, v122, v123\n"
"v_mfma_f32_16x16x32_bf16 v[32:35], v[224:227], v[200:203], v[32:35]\n"
"buffer_store_dwordx4 v[108:111], v6, %5, s46 offen offset:1024 sc0 sc1\n"
"v_mul_f32 v112, v112, v10\n"
"v_mfma_f32_16x16x32_bf16 v[36:39], v[224:227], v[204:207], v[36:39]\n"
"ds_read_b64_tr_b16 v[244:245], v1 offset:28672\n"
"ds_read_b64_tr_b16 v[246:247], v1 offset:30720\n"
"v_mfma_f32_16x16x32_bf16 v[40:43], v[224:227], v[208:211], v[40:43]\n"
"v_mul_f32 v113, v113, v10\n"
"v_mul_f32 v114, v114, v10\n"
"v_mfma_f32_16x16x32_bf16 v[44:47], v[228:231], v[200:203], v[44:47]\n"
"v_mul_f32 v115, v115, v10\n"
"v_mfma_f32_16x16x32_bf16 v[48:51], v[228:231], v[204:207], v[48:51]\n"
"ds_read_b64_tr_b16 v[248:249], v2 offset:28672\n"
"ds_read_b64_tr_b16 v[250:251], v2 offset:30720\n"
"v_mfma_f32_16x16x32_bf16 v[52:55], v[228:231], v[208:211], v[52:55]\n"
"ds_read_b64_tr_b16 v[252:253], v3 offset:28672\n"
"ds_read_b64_tr_b16 v[254:255], v3 offset:30720\n"
"s_waitcnt lgkmcnt(6)\n"
"v_mfma_f32_16x16x32_bf16 v[56:59], v[232:235], v[200:203], v[56:59]\n"
"v_mul_f32 v124, v124, v10\n"
"v_mul_f32 v125, v125, v10\n"
"v_mfma_f32_16x16x32_bf16 v[60:63], v[232:235], v[204:207], v[60:63]\n"
"v_mul_f32 v126, v126, v10\n"
"v_mul_f32 v127, v127, v10\n"
"v_mfma_f32_16x16x32_bf16 v[64:67], v[232:235], v[208:211], v[64:67]\n"
"v_cvt_pk_bf16_f32 v112, v112, v113\n"
"v_mfma_f32_16x16x32_bf16 v[68:71], v[236:239], v[200:203], v[68:71]\n"
"v_cvt_pk_bf16_f32 v113, v114, v115\n"
"v_mfma_f32_16x16x32_bf16 v[72:75], v[236:239], v[204:207], v[72:75]\n"
"v_cvt_pk_bf16_f32 v114, v124, v125\n"
"v_mfma_f32_16x16x32_bf16 v[76:79], v[236:239], v[208:211], v[76:79]\n"
"v_cvt_pk_bf16_f32 v115, v126, v127\n"
"v_mfma_f32_16x16x32_bf16 v[32:35], v[240:243], v[212:215], v[32:35]\n"
"buffer_store_dwordx4 v[112:115], v7, %5, s46 offen offset:1024 sc0 sc1\n"
"v_mfma_f32_16x16x32_bf16 v[36:39], v[240:243], v[216:219], v[36:39]\n"
"v_mfma_f32_16x16x32_bf16 v[40:43], v[240:243], v[220:223], v[40:43]\n"
"s_waitcnt vmcnt(14) lgkmcnt(0)\n"
"s_barrier\n"
"v_mfma_f32_16x16x32_bf16 v[44:47], v[244:247], v[212:215], v[44:47]\n"
"v_subrev_u32 v0, 65536, v0\n"
"v_subrev_u32 v1, 65536, v1\n"
"v_mfma_f32_16x16x32_bf16 v[48:51], v[244:247], v[216:219], v[48:51]\n"
"v_subrev_u32 v2, 65536, v2\n"
"v_subrev_u32 v3, 65536, v3\n"
"v_mfma_f32_16x16x32_bf16 v[52:55], v[244:247], v[220:223], v[52:55]\n"
"ds_read_b64_tr_b16 v[224:225], v0 offset:0\n"
"ds_read_b64_tr_b16 v[226:227], v0 offset:2048\n"
"v_mfma_f32_16x16x32_bf16 v[56:59], v[248:251], v[212:215], v[56:59]\n"
"ds_read_b64_tr_b16 v[228:229], v1 offset:0\n"
"ds_read_b64_tr_b16 v[230:231], v1 offset:2048\n"
"v_mfma_f32_16x16x32_bf16 v[60:63], v[248:251], v[216:219], v[60:63]\n"
"ds_read_b64_tr_b16 v[232:233], v2 offset:0\n"
"ds_read_b64_tr_b16 v[234:235], v2 offset:2048\n"
"v_mfma_f32_16x16x32_bf16 v[64:67], v[248:251], v[220:223], v[64:67]\n"
"ds_read_b64_tr_b16 v[236:237], v3 offset:0\n"
"ds_read_b64_tr_b16 v[238:239], v3 offset:2048\n"
"v_mfma_f32_16x16x32_bf16 v[68:71], v[252:255], v[212:215], v[68:71]\n"
"v_mfma_f32_16x16x32_bf16 v[72:75], v[252:255], v[216:219], v[72:75]\n"
"v_mfma_f32_16x16x32_bf16 v[76:79], v[252:255], v[220:223], v[76:79]\n"
"s_waitcnt lgkmcnt(6)\n"
"v_mfma_f32_16x16x32_bf16 v[80:83], v[224:227], v[128:131], 0\n"
"ds_read_b64_tr_b16 v[240:241], v0 offset:4096\n"
"ds_read_b64_tr_b16 v[242:243], v0 offset:6144\n"
"v_mfma_f32_16x16x32_bf16 v[84:87], v[224:227], v[132:135], 0\n"
"ds_read_b64_tr_b16 v[244:245], v1 offset:4096\n"
"ds_read_b64_tr_b16 v[246:247], v1 offset:6144\n"
"v_mfma_f32_16x16x32_bf16 v[88:91], v[224:227], v[136:139], 0\n"
"s_waitcnt lgkmcnt(7)\n"
"v_mfma_f32_16x16x32_bf16 v[92:95], v[228:231], v[128:131], 0\n"
"v_mfma_f32_16x16x32_bf16 v[96:99], v[228:231], v[132:135], 0\n"
"ds_read_b64_tr_b16 v[248:249], v2 offset:4096\n"
"ds_read_b64_tr_b16 v[250:251], v2 offset:6144\n"
"v_mfma_f32_16x16x32_bf16 v[100:103], v[228:231], v[136:139], 0\n"
"s_waitcnt lgkmcnt(6)\n"
"v_mfma_f32_16x16x32_bf16 v[104:107], v[232:235], v[128:131], 0\n"
"s_mov_b32 s46, 4096\n"
"v_mfma_f32_16x16x32_bf16 v[108:111], v[232:235], v[132:135], 0\n"
"ds_read_b64_tr_b16 v[252:253], v3 offset:4096\n"
"ds_read_b64_tr_b16 v[254:255], v3 offset:6144\n"
"v_mfma_f32_16x16x32_bf16 v[112:115], v[232:235], v[136:139], 0\n"
"v_mul_f32 v32, v32, v8\n"
"v_mul_f32 v33, v33, v8\n"
"v_mfma_f32_16x16x32_bf16 v[116:119], v[236:239], v[128:131], 0\n"
"v_mul_f32 v34, v34, v8\n"
"v_mfma_f32_16x16x32_bf16 v[120:123], v[236:239], v[132:135], 0\n"
"ds_read_b64_tr_b16 v[224:225], v0 offset:8192\n"
"ds_read_b64_tr_b16 v[226:227], v0 offset:10240\n"
"v_mfma_f32_16x16x32_bf16 v[124:127], v[236:239], v[136:139], 0\n"
"v_mul_f32 v35, v35, v8\n"
"v_mul_f32 v44, v44, v8\n"
"s_waitcnt lgkmcnt(4)\n"
"v_mfma_f32_16x16x32_bf16 v[80:83], v[240:243], v[140:143], v[80:83]\n"
"v_mul_f32 v45, v45, v8\n"
"v_mfma_f32_16x16x32_bf16 v[84:87], v[240:243], v[144:147], v[84:87]\n"
"ds_read_b64_tr_b16 v[228:229], v1 offset:8192\n"
"ds_read_b64_tr_b16 v[230:231], v1 offset:10240\n"
"v_mfma_f32_16x16x32_bf16 v[88:91], v[240:243], v[148:151], v[88:91]\n"
"v_mul_f32 v46, v46, v8\n"
"v_mul_f32 v47, v47, v8\n"
"v_mfma_f32_16x16x32_bf16 v[92:95], v[244:247], v[140:143], v[92:95]\n"
"v_cvt_pk_bf16_f32 v32, v32, v33\n"
"v_mfma_f32_16x16x32_bf16 v[96:99], v[244:247], v[144:147], v[96:99]\n"
"ds_read_b64_tr_b16 v[232:233], v2 offset:8192\n"
"ds_read_b64_tr_b16 v[234:235], v2 offset:10240\n"
"v_mfma_f32_16x16x32_bf16 v[100:103], v[244:247], v[148:151], v[100:103]\n"
"v_cvt_pk_bf16_f32 v33, v34, v35\n"
"v_cvt_pk_bf16_f32 v34, v44, v45\n"
"v_mfma_f32_16x16x32_bf16 v[104:107], v[248:251], v[140:143], v[104:107]\n"
"v_cvt_pk_bf16_f32 v35, v46, v47\n"
"v_mfma_f32_16x16x32_bf16 v[108:111], v[248:251], v[144:147], v[108:111]\n"
"ds_read_b64_tr_b16 v[236:237], v3 offset:8192\n"
"ds_read_b64_tr_b16 v[238:239], v3 offset:10240\n"
"v_mfma_f32_16x16x32_bf16 v[112:115], v[248:251], v[148:151], v[112:115]\n"
"buffer_store_dwordx4 v[32:35], v5, %5, s46 offen offset:0 sc0 sc1\n"
"v_mul_f32 v36, v36, v9\n"
"s_waitcnt lgkmcnt(4)\n"
"v_mfma_f32_16x16x32_bf16 v[116:119], v[252:255], v[140:143], v[116:119]\n"
"v_mul_f32 v37, v37, v9\n"
"v_mfma_f32_16x16x32_bf16 v[120:123], v[252:255], v[144:147], v[120:123]\n"
"ds_read_b64_tr_b16 v[240:241], v0 offset:12288\n"
"ds_read_b64_tr_b16 v[242:243], v0 offset:14336\n"
"v_mfma_f32_16x16x32_bf16 v[124:127], v[252:255], v[148:151], v[124:127]\n"
"v_mul_f32 v38, v38, v9\n"
"v_mul_f32 v39, v39, v9\n"
"v_mfma_f32_16x16x32_bf16 v[80:83], v[224:227], v[152:155], v[80:83]\n"
"v_mul_f32 v48, v48, v9\n"
"v_mfma_f32_16x16x32_bf16 v[84:87], v[224:227], v[156:159], v[84:87]\n"
"ds_read_b64_tr_b16 v[244:245], v1 offset:12288\n"
"ds_read_b64_tr_b16 v[246:247], v1 offset:14336\n"
"v_mfma_f32_16x16x32_bf16 v[88:91], v[224:227], v[160:163], v[88:91]\n"
"v_mul_f32 v49, v49, v9\n"
"v_mul_f32 v50, v50, v9\n"
"v_mfma_f32_16x16x32_bf16 v[92:95], v[228:231], v[152:155], v[92:95]\n"
"v_mul_f32 v51, v51, v9\n"
"v_mfma_f32_16x16x32_bf16 v[96:99], v[228:231], v[156:159], v[96:99]\n"
"ds_read_b64_tr_b16 v[248:249], v2 offset:12288\n"
"ds_read_b64_tr_b16 v[250:251], v2 offset:14336\n"
"v_mfma_f32_16x16x32_bf16 v[100:103], v[228:231], v[160:163], v[100:103]\n"
"v_cvt_pk_bf16_f32 v36, v36, v37\n"
"v_cvt_pk_bf16_f32 v37, v38, v39\n"
"s_waitcnt lgkmcnt(4)\n"
"v_mfma_f32_16x16x32_bf16 v[104:107], v[232:235], v[152:155], v[104:107]\n"
"v_cvt_pk_bf16_f32 v38, v48, v49\n"
"v_mfma_f32_16x16x32_bf16 v[108:111], v[232:235], v[156:159], v[108:111]\n"
"ds_read_b64_tr_b16 v[252:253], v3 offset:12288\n"
"ds_read_b64_tr_b16 v[254:255], v3 offset:14336\n"
"v_mfma_f32_16x16x32_bf16 v[112:115], v[232:235], v[160:163], v[112:115]\n"
"v_cvt_pk_bf16_f32 v39, v50, v51\n"
"buffer_store_dwordx4 v[36:39], v6, %5, s46 offen offset:0 sc0 sc1\n"
"v_mfma_f32_16x16x32_bf16 v[116:119], v[236:239], v[152:155], v[116:119]\n"
"v_mul_f32 v40, v40, v10\n"
"v_mfma_f32_16x16x32_bf16 v[120:123], v[236:239], v[156:159], v[120:123]\n"
"ds_read_b64_tr_b16 v[224:225], v0 offset:16384\n"
"ds_read_b64_tr_b16 v[226:227], v0 offset:18432\n"
"v_mfma_f32_16x16x32_bf16 v[124:127], v[236:239], v[160:163], v[124:127]\n"
"v_mul_f32 v41, v41, v10\n"
"v_mul_f32 v42, v42, v10\n"
"v_mfma_f32_16x16x32_bf16 v[80:83], v[240:243], v[164:167], v[80:83]\n"
"v_mul_f32 v43, v43, v10\n"
"v_mfma_f32_16x16x32_bf16 v[84:87], v[240:243], v[168:171], v[84:87]\n"
"ds_read_b64_tr_b16 v[228:229], v1 offset:16384\n"
"ds_read_b64_tr_b16 v[230:231], v1 offset:18432\n"
"v_mfma_f32_16x16x32_bf16 v[88:91], v[240:243], v[172:175], v[88:91]\n"
"v_mul_f32 v52, v52, v10\n"
"v_mul_f32 v53, v53, v10\n"
"s_waitcnt lgkmcnt(4)\n"
"v_mfma_f32_16x16x32_bf16 v[92:95], v[244:247], v[164:167], v[92:95]\n"
"v_mul_f32 v54, v54, v10\n"
"v_mfma_f32_16x16x32_bf16 v[96:99], v[244:247], v[168:171], v[96:99]\n"
"ds_read_b64_tr_b16 v[232:233], v2 offset:16384\n"
"ds_read_b64_tr_b16 v[234:235], v2 offset:18432\n"
"v_mfma_f32_16x16x32_bf16 v[100:103], v[244:247], v[172:175], v[100:103]\n"
"v_mul_f32 v55, v55, v10\n"
"v_cvt_pk_bf16_f32 v40, v40, v41\n"
"v_mfma_f32_16x16x32_bf16 v[104:107], v[248:251], v[164:167], v[104:107]\n"
"v_cvt_pk_bf16_f32 v41, v42, v43\n"
"v_mfma_f32_16x16x32_bf16 v[108:111], v[248:251], v[168:171], v[108:111]\n"
"ds_read_b64_tr_b16 v[236:237], v3 offset:16384\n"
"ds_read_b64_tr_b16 v[238:239], v3 offset:18432\n"
"v_mfma_f32_16x16x32_bf16 v[112:115], v[248:251], v[172:175], v[112:115]\n"
"v_cvt_pk_bf16_f32 v42, v52, v53\n"
"v_cvt_pk_bf16_f32 v43, v54, v55\n"
"v_mfma_f32_16x16x32_bf16 v[116:119], v[252:255], v[164:167], v[116:119]\n"
"buffer_store_dwordx4 v[40:43], v7, %5, s46 offen offset:0 sc0 sc1\n"
"v_mfma_f32_16x16x32_bf16 v[120:123], v[252:255], v[168:171], v[120:123]\n"
"ds_read_b64_tr_b16 v[240:241], v0 offset:20480\n"
"ds_read_b64_tr_b16 v[242:243], v0 offset:22528\n"
"v_mfma_f32_16x16x32_bf16 v[124:127], v[252:255], v[172:175], v[124:127]\n"
"v_mul_f32 v56, v56, v8\n"
"v_mul_f32 v57, v57, v8\n"
"s_waitcnt lgkmcnt(4)\n"
"v_mfma_f32_16x16x32_bf16 v[80:83], v[224:227], v[176:179], v[80:83]\n"
"v_mul_f32 v58, v58, v8\n"
"v_mfma_f32_16x16x32_bf16 v[84:87], v[224:227], v[180:183], v[84:87]\n"
"ds_read_b64_tr_b16 v[244:245], v1 offset:20480\n"
"ds_read_b64_tr_b16 v[246:247], v1 offset:22528\n"
"v_mfma_f32_16x16x32_bf16 v[88:91], v[224:227], v[184:187], v[88:91]\n"
"v_mul_f32 v59, v59, v8\n"
"v_mul_f32 v68, v68, v8\n"
"v_mfma_f32_16x16x32_bf16 v[92:95], v[228:231], v[176:179], v[92:95]\n"
"v_mul_f32 v69, v69, v8\n"
"v_mfma_f32_16x16x32_bf16 v[96:99], v[228:231], v[180:183], v[96:99]\n"
"ds_read_b64_tr_b16 v[248:249], v2 offset:20480\n"
"ds_read_b64_tr_b16 v[250:251], v2 offset:22528\n"
"v_mfma_f32_16x16x32_bf16 v[100:103], v[228:231], v[184:187], v[100:103]\n"
"v_mul_f32 v70, v70, v8\n"
"v_mul_f32 v71, v71, v8\n"
"v_mfma_f32_16x16x32_bf16 v[104:107], v[232:235], v[176:179], v[104:107]\n"
"v_cvt_pk_bf16_f32 v56, v56, v57\n"
"v_mfma_f32_16x16x32_bf16 v[108:111], v[232:235], v[180:183], v[108:111]\n"
"ds_read_b64_tr_b16 v[252:253], v3 offset:20480\n"
"ds_read_b64_tr_b16 v[254:255], v3 offset:22528\n"
"v_mfma_f32_16x16x32_bf16 v[112:115], v[232:235], v[184:187], v[112:115]\n"
"v_cvt_pk_bf16_f32 v57, v58, v59\n"
"v_cvt_pk_bf16_f32 v58, v68, v69\n"
"s_waitcnt lgkmcnt(4)\n"
"v_mfma_f32_16x16x32_bf16 v[116:119], v[236:239], v[176:179], v[116:119]\n"
"v_cvt_pk_bf16_f32 v59, v70, v71\n"
"v_mfma_f32_16x16x32_bf16 v[120:123], v[236:239], v[180:183], v[120:123]\n"
"ds_read_b64_tr_b16 v[224:225], v0 offset:24576\n"
"ds_read_b64_tr_b16 v[226:227], v0 offset:26624\n"
"v_mfma_f32_16x16x32_bf16 v[124:127], v[236:239], v[184:187], v[124:127]\n"
"buffer_store_dwordx4 v[56:59], v5, %5, s46 offen offset:1024 sc0 sc1\n"
"v_mul_f32 v60, v60, v9\n"
"v_mfma_f32_16x16x32_bf16 v[80:83], v[240:243], v[188:191], v[80:83]\n"
"v_mul_f32 v61, v61, v9\n"
"v_mfma_f32_16x16x32_bf16 v[84:87], v[240:243], v[192:195], v[84:87]\n"
"ds_read_b64_tr_b16 v[228:229], v1 offset:24576\n"
"ds_read_b64_tr_b16 v[230:231], v1 offset:26624\n"
"v_mfma_f32_16x16x32_bf16 v[88:91], v[240:243], v[196:199], v[88:91]\n"
"v_mul_f32 v62, v62, v9\n"
"v_mul_f32 v63, v63, v9\n"
"v_mfma_f32_16x16x32_bf16 v[92:95], v[244:247], v[188:191], v[92:95]\n"
"v_mul_f32 v72, v72, v9\n"
"v_mfma_f32_16x16x32_bf16 v[96:99], v[244:247], v[192:195], v[96:99]\n"
"ds_read_b64_tr_b16 v[232:233], v2 offset:24576\n"
"ds_read_b64_tr_b16 v[234:235], v2 offset:26624\n"
"v_mfma_f32_16x16x32_bf16 v[100:103], v[244:247], v[196:199], v[100:103]\n"
"v_mul_f32 v73, v73, v9\n"
"v_mul_f32 v74, v74, v9\n"
"s_waitcnt lgkmcnt(4)\n"
"v_mfma_f32_16x16x32_bf16 v[104:107], v[248:251], v[188:191], v[104:107]\n"
"v_mul_f32 v75, v75, v9\n"
"v_mfma_f32_16x16x32_bf16 v[108:111], v[248:251], v[192:195], v[108:111]\n"
"ds_read_b64_tr_b16 v[236:237], v3 offset:24576\n"
"ds_read_b64_tr_b16 v[238:239], v3 offset:26624\n"
"v_mfma_f32_16x16x32_bf16 v[112:115], v[248:251], v[196:199], v[112:115]\n"
"v_cvt_pk_bf16_f32 v60, v60, v61\n"
"v_cvt_pk_bf16_f32 v61, v62, v63\n"
"v_mfma_f32_16x16x32_bf16 v[116:119], v[252:255], v[188:191], v[116:119]\n"
"v_cvt_pk_bf16_f32 v62, v72, v73\n"
"v_mfma_f32_16x16x32_bf16 v[120:123], v[252:255], v[192:195], v[120:123]\n"
"ds_read_b64_tr_b16 v[240:241], v0 offset:28672\n"
"ds_read_b64_tr_b16 v[242:243], v0 offset:30720\n"
"v_mfma_f32_16x16x32_bf16 v[124:127], v[252:255], v[196:199], v[124:127]\n"
"v_cvt_pk_bf16_f32 v63, v74, v75\n"
"buffer_store_dwordx4 v[60:63], v6, %5, s46 offen offset:1024 sc0 sc1\n"
"v_mfma_f32_16x16x32_bf16 v[80:83], v[224:227], v[200:203], v[80:83]\n"
"v_mul_f32 v64, v64, v10\n"
"v_mfma_f32_16x16x32_bf16 v[84:87], v[224:227], v[204:207], v[84:87]\n"
"ds_read_b64_tr_b16 v[244:245], v1 offset:28672\n"
"ds_read_b64_tr_b16 v[246:247], v1 offset:30720\n"
"v_mfma_f32_16x16x32_bf16 v[88:91], v[224:227], v[208:211], v[88:91]\n"
"v_mul_f32 v65, v65, v10\n"
"v_mul_f32 v66, v66, v10\n"
"s_waitcnt lgkmcnt(4)\n"
"v_mfma_f32_16x16x32_bf16 v[92:95], v[228:231], v[200:203], v[92:95]\n"
"v_mul_f32 v67, v67, v10\n"
"v_mfma_f32_16x16x32_bf16 v[96:99], v[228:231], v[204:207], v[96:99]\n"
"ds_read_b64_tr_b16 v[248:249], v2 offset:28672\n"
"ds_read_b64_tr_b16 v[250:251], v2 offset:30720\n"
"v_mfma_f32_16x16x32_bf16 v[100:103], v[228:231], v[208:211], v[100:103]\n"
"ds_read_b64_tr_b16 v[252:253], v3 offset:28672\n"
"ds_read_b64_tr_b16 v[254:255], v3 offset:30720\n"
"v_mfma_f32_16x16x32_bf16 v[104:107], v[232:235], v[200:203], v[104:107]\n"
"v_mul_f32 v76, v76, v10\n"
"v_mul_f32 v77, v77, v10\n"
"v_mfma_f32_16x16x32_bf16 v[108:111], v[232:235], v[204:207], v[108:111]\n"
"v_mul_f32 v78, v78, v10\n"
"v_mul_f32 v79, v79, v10\n"
"v_mfma_f32_16x16x32_bf16 v[112:115], v[232:235], v[208:211], v[112:115]\n"
"v_cvt_pk_bf16_f32 v64, v64, v65\n"
"v_mfma_f32_16x16x32_bf16 v[116:119], v[236:239], v[200:203], v[116:119]\n"
"v_cvt_pk_bf16_f32 v65, v66, v67\n"
"v_mfma_f32_16x16x32_bf16 v[120:123], v[236:239], v[204:207], v[120:123]\n"
"v_cvt_pk_bf16_f32 v66, v76, v77\n"
"v_mfma_f32_16x16x32_bf16 v[124:127], v[236:239], v[208:211], v[124:127]\n"
"v_cvt_pk_bf16_f32 v67, v78, v79\n"
"s_waitcnt lgkmcnt(1)\n"
"v_mfma_f32_16x16x32_bf16 v[80:83], v[240:243], v[212:215], v[80:83]\n"
"buffer_store_dwordx4 v[64:67], v7, %5, s46 offen offset:1024 sc0 sc1\n"
"v_mfma_f32_16x16x32_bf16 v[84:87], v[240:243], v[216:219], v[84:87]\n"
"v_mfma_f32_16x16x32_bf16 v[88:91], v[240:243], v[220:223], v[88:91]\n"
"s_waitcnt vmcnt(6) lgkmcnt(0)\n"
"s_barrier\n"
"v_mfma_f32_16x16x32_bf16 v[92:95], v[244:247], v[212:215], v[92:95]\n"
"v_add_u32 v0, 32768, v0\n"
"v_add_u32 v1, 32768, v1\n"
"v_mfma_f32_16x16x32_bf16 v[96:99], v[244:247], v[216:219], v[96:99]\n"
"v_add_u32 v2, 32768, v2\n"
"v_add_u32 v3, 32768, v3\n"
"v_mfma_f32_16x16x32_bf16 v[100:103], v[244:247], v[220:223], v[100:103]\n"
"ds_read_b64_tr_b16 v[224:225], v0 offset:0\n"
"ds_read_b64_tr_b16 v[226:227], v0 offset:2048\n"
"v_mfma_f32_16x16x32_bf16 v[104:107], v[248:251], v[212:215], v[104:107]\n"
"ds_read_b64_tr_b16 v[228:229], v1 offset:0\n"
"ds_read_b64_tr_b16 v[230:231], v1 offset:2048\n"
"v_mfma_f32_16x16x32_bf16 v[108:111], v[248:251], v[216:219], v[108:111]\n"
"ds_read_b64_tr_b16 v[232:233], v2 offset:0\n"
"ds_read_b64_tr_b16 v[234:235], v2 offset:2048\n"
"v_mfma_f32_16x16x32_bf16 v[112:115], v[248:251], v[220:223], v[112:115]\n"
"ds_read_b64_tr_b16 v[236:237], v3 offset:0\n"
"ds_read_b64_tr_b16 v[238:239], v3 offset:2048\n"
"v_mfma_f32_16x16x32_bf16 v[116:119], v[252:255], v[212:215], v[116:119]\n"
"v_mfma_f32_16x16x32_bf16 v[120:123], v[252:255], v[216:219], v[120:123]\n"
"v_mfma_f32_16x16x32_bf16 v[124:127], v[252:255], v[220:223], v[124:127]\n"
"s_waitcnt lgkmcnt(6)\n"
"v_mfma_f32_16x16x32_bf16 v[32:35], v[224:227], v[128:131], 0\n"
"ds_read_b64_tr_b16 v[240:241], v0 offset:4096\n"
"ds_read_b64_tr_b16 v[242:243], v0 offset:6144\n"
"v_mfma_f32_16x16x32_bf16 v[36:39], v[224:227], v[132:135], 0\n"
"ds_read_b64_tr_b16 v[244:245], v1 offset:4096\n"
"ds_read_b64_tr_b16 v[246:247], v1 offset:6144\n"
"v_mfma_f32_16x16x32_bf16 v[40:43], v[224:227], v[136:139], 0\n"
"s_waitcnt lgkmcnt(7)\n"
"v_mfma_f32_16x16x32_bf16 v[44:47], v[228:231], v[128:131], 0\n"
"v_mfma_f32_16x16x32_bf16 v[48:51], v[228:231], v[132:135], 0\n"
"ds_read_b64_tr_b16 v[248:249], v2 offset:4096\n"
"ds_read_b64_tr_b16 v[250:251], v2 offset:6144\n"
"v_mfma_f32_16x16x32_bf16 v[52:55], v[228:231], v[136:139], 0\n"
"s_waitcnt lgkmcnt(6)\n"
"v_mfma_f32_16x16x32_bf16 v[56:59], v[232:235], v[128:131], 0\n"
"s_mov_b32 s46, 6144\n"
"v_mfma_f32_16x16x32_bf16 v[60:63], v[232:235], v[132:135], 0\n"
"ds_read_b64_tr_b16 v[252:253], v3 offset:4096\n"
"ds_read_b64_tr_b16 v[254:255], v3 offset:6144\n"
"v_mfma_f32_16x16x32_bf16 v[64:67], v[232:235], v[136:139], 0\n"
"v_mul_f32 v80, v80, v8\n"
"v_mul_f32 v81, v81, v8\n"
"v_mfma_f32_16x16x32_bf16 v[68:71], v[236:239], v[128:131], 0\n"
"v_mul_f32 v82, v82, v8\n"
"v_mfma_f32_16x16x32_bf16 v[72:75], v[236:239], v[132:135], 0\n"
"ds_read_b64_tr_b16 v[224:225], v0 offset:8192\n"
"ds_read_b64_tr_b16 v[226:227], v0 offset:10240\n"
"v_mfma_f32_16x16x32_bf16 v[76:79], v[236:239], v[136:139], 0\n"
"v_mul_f32 v83, v83, v8\n"
"v_mul_f32 v92, v92, v8\n"
"s_waitcnt lgkmcnt(4)\n"
"v_mfma_f32_16x16x32_bf16 v[32:35], v[240:243], v[140:143], v[32:35]\n"
"v_mul_f32 v93, v93, v8\n"
"v_mfma_f32_16x16x32_bf16 v[36:39], v[240:243], v[144:147], v[36:39]\n"
"ds_read_b64_tr_b16 v[228:229], v1 offset:8192\n"
"ds_read_b64_tr_b16 v[230:231], v1 offset:10240\n"
"v_mfma_f32_16x16x32_bf16 v[40:43], v[240:243], v[148:151], v[40:43]\n"
"v_mul_f32 v94, v94, v8\n"
"v_mul_f32 v95, v95, v8\n"
"v_mfma_f32_16x16x32_bf16 v[44:47], v[244:247], v[140:143], v[44:47]\n"
"v_cvt_pk_bf16_f32 v80, v80, v81\n"
"v_mfma_f32_16x16x32_bf16 v[48:51], v[244:247], v[144:147], v[48:51]\n"
"ds_read_b64_tr_b16 v[232:233], v2 offset:8192\n"
"ds_read_b64_tr_b16 v[234:235], v2 offset:10240\n"
"v_mfma_f32_16x16x32_bf16 v[52:55], v[244:247], v[148:151], v[52:55]\n"
"v_cvt_pk_bf16_f32 v81, v82, v83\n"
"v_cvt_pk_bf16_f32 v82, v92, v93\n"
"v_mfma_f32_16x16x32_bf16 v[56:59], v[248:251], v[140:143], v[56:59]\n"
"v_cvt_pk_bf16_f32 v83, v94, v95\n"
"v_mfma_f32_16x16x32_bf16 v[60:63], v[248:251], v[144:147], v[60:63]\n"
"ds_read_b64_tr_b16 v[236:237], v3 offset:8192\n"
"ds_read_b64_tr_b16 v[238:239], v3 offset:10240\n"
"v_mfma_f32_16x16x32_bf16 v[64:67], v[248:251], v[148:151], v[64:67]\n"
"buffer_store_dwordx4 v[80:83], v5, %5, s46 offen offset:0 sc0 sc1\n"
"v_mul_f32 v84, v84, v9\n"
"s_waitcnt lgkmcnt(4)\n"
"v_mfma_f32_16x16x32_bf16 v[68:71], v[252:255], v[140:143], v[68:71]\n"
"v_mul_f32 v85, v85, v9\n"
"v_mfma_f32_16x16x32_bf16 v[72:75], v[252:255], v[144:147], v[72:75]\n"
"ds_read_b64_tr_b16 v[240:241], v0 offset:12288\n"
"ds_read_b64_tr_b16 v[242:243], v0 offset:14336\n"
"v_mfma_f32_16x16x32_bf16 v[76:79], v[252:255], v[148:151], v[76:79]\n"
"v_mul_f32 v86, v86, v9\n"
"v_mul_f32 v87, v87, v9\n"
"v_mfma_f32_16x16x32_bf16 v[32:35], v[224:227], v[152:155], v[32:35]\n"
"v_mul_f32 v96, v96, v9\n"
"v_mfma_f32_16x16x32_bf16 v[36:39], v[224:227], v[156:159], v[36:39]\n"
"ds_read_b64_tr_b16 v[244:245], v1 offset:12288\n"
"ds_read_b64_tr_b16 v[246:247], v1 offset:14336\n"
"v_mfma_f32_16x16x32_bf16 v[40:43], v[224:227], v[160:163], v[40:43]\n"
"v_mul_f32 v97, v97, v9\n"
"v_mul_f32 v98, v98, v9\n"
"v_mfma_f32_16x16x32_bf16 v[44:47], v[228:231], v[152:155], v[44:47]\n"
"v_mul_f32 v99, v99, v9\n"
"v_mfma_f32_16x16x32_bf16 v[48:51], v[228:231], v[156:159], v[48:51]\n"
"ds_read_b64_tr_b16 v[248:249], v2 offset:12288\n"
"ds_read_b64_tr_b16 v[250:251], v2 offset:14336\n"
"v_mfma_f32_16x16x32_bf16 v[52:55], v[228:231], v[160:163], v[52:55]\n"
"v_cvt_pk_bf16_f32 v84, v84, v85\n"
"v_cvt_pk_bf16_f32 v85, v86, v87\n"
"s_waitcnt lgkmcnt(4)\n"
"v_mfma_f32_16x16x32_bf16 v[56:59], v[232:235], v[152:155], v[56:59]\n"
"v_cvt_pk_bf16_f32 v86, v96, v97\n"
"v_mfma_f32_16x16x32_bf16 v[60:63], v[232:235], v[156:159], v[60:63]\n"
"ds_read_b64_tr_b16 v[252:253], v3 offset:12288\n"
"ds_read_b64_tr_b16 v[254:255], v3 offset:14336\n"
"v_mfma_f32_16x16x32_bf16 v[64:67], v[232:235], v[160:163], v[64:67]\n"
"v_cvt_pk_bf16_f32 v87, v98, v99\n"
"buffer_store_dwordx4 v[84:87], v6, %5, s46 offen offset:0 sc0 sc1\n"
"v_mfma_f32_16x16x32_bf16 v[68:71], v[236:239], v[152:155], v[68:71]\n"
"v_mul_f32 v88, v88, v10\n"
"v_mfma_f32_16x16x32_bf16 v[72:75], v[236:239], v[156:159], v[72:75]\n"
"ds_read_b64_tr_b16 v[224:225], v0 offset:16384\n"
"ds_read_b64_tr_b16 v[226:227], v0 offset:18432\n"
"v_mfma_f32_16x16x32_bf16 v[76:79], v[236:239], v[160:163], v[76:79]\n"
"v_mul_f32 v89, v89, v10\n"
"v_mul_f32 v90, v90, v10\n"
"v_mfma_f32_16x16x32_bf16 v[32:35], v[240:243], v[164:167], v[32:35]\n"
"v_mul_f32 v91, v91, v10\n"
"v_mfma_f32_16x16x32_bf16 v[36:39], v[240:243], v[168:171], v[36:39]\n"
"ds_read_b64_tr_b16 v[228:229], v1 offset:16384\n"
"ds_read_b64_tr_b16 v[230:231], v1 offset:18432\n"
"v_mfma_f32_16x16x32_bf16 v[40:43], v[240:243], v[172:175], v[40:43]\n"
"v_mul_f32 v100, v100, v10\n"
"v_mul_f32 v101, v101, v10\n"
"s_waitcnt lgkmcnt(4)\n"
"v_mfma_f32_16x16x32_bf16 v[44:47], v[244:247], v[164:167], v[44:47]\n"
"v_mul_f32 v102, v102, v10\n"
"v_mfma_f32_16x16x32_bf16 v[48:51], v[244:247], v[168:171], v[48:51]\n"
"ds_read_b64_tr_b16 v[232:233], v2 offset:16384\n"
"ds_read_b64_tr_b16 v[234:235], v2 offset:18432\n"
"v_mfma_f32_16x16x32_bf16 v[52:55], v[244:247], v[172:175], v[52:55]\n"
"v_mul_f32 v103, v103, v10\n"
"v_cvt_pk_bf16_f32 v88, v88, v89\n"
"v_mfma_f32_16x16x32_bf16 v[56:59], v[248:251], v[164:167], v[56:59]\n"
"v_cvt_pk_bf16_f32 v89, v90, v91\n"
"v_mfma_f32_16x16x32_bf16 v[60:63], v[248:251], v[168:171], v[60:63]\n"
"ds_read_b64_tr_b16 v[236:237], v3 offset:16384\n"
"ds_read_b64_tr_b16 v[238:239], v3 offset:18432\n"
"v_mfma_f32_16x16x32_bf16 v[64:67], v[248:251], v[172:175], v[64:67]\n"
"v_cvt_pk_bf16_f32 v90, v100, v101\n"
"v_cvt_pk_bf16_f32 v91, v102, v103\n"
"v_mfma_f32_16x16x32_bf16 v[68:71], v[252:255], v[164:167], v[68:71]\n"
"buffer_store_dwordx4 v[88:91], v7, %5, s46 offen offset:0 sc0 sc1\n"
"v_mfma_f32_16x16x32_bf16 v[72:75], v[252:255], v[168:171], v[72:75]\n"
"ds_read_b64_tr_b16 v[240:241], v0 offset:20480\n"
"ds_read_b64_tr_b16 v[242:243], v0 offset:22528\n"
"v_mfma_f32_16x16x32_bf16 v[76:79], v[252:255], v[172:175], v[76:79]\n"
"v_mul_f32 v104, v104, v8\n"
"v_mul_f32 v105, v105, v8\n"
"s_waitcnt lgkmcnt(4)\n"
"v_mfma_f32_16x16x32_bf16 v[32:35], v[224:227], v[176:179], v[32:35]\n"
"v_mul_f32 v106, v106, v8\n"
"v_mfma_f32_16x16x32_bf16 v[36:39], v[224:227], v[180:183], v[36:39]\n"
"ds_read_b64_tr_b16 v[244:245], v1 offset:20480\n"
"ds_read_b64_tr_b16 v[246:247], v1 offset:22528\n"
"v_mfma_f32_16x16x32_bf16 v[40:43], v[224:227], v[184:187], v[40:43]\n"
"v_mul_f32 v107, v107, v8\n"
"v_mul_f32 v116, v116, v8\n"
"v_mfma_f32_16x16x32_bf16 v[44:47], v[228:231], v[176:179], v[44:47]\n"
"v_mul_f32 v117, v117, v8\n"
"v_mfma_f32_16x16x32_bf16 v[48:51], v[228:231], v[180:183], v[48:51]\n"
"ds_read_b64_tr_b16 v[248:249], v2 offset:20480\n"
"ds_read_b64_tr_b16 v[250:251], v2 offset:22528\n"
"v_mfma_f32_16x16x32_bf16 v[52:55], v[228:231], v[184:187], v[52:55]\n"
"v_mul_f32 v118, v118, v8\n"
"v_mul_f32 v119, v119, v8\n"
"v_mfma_f32_16x16x32_bf16 v[56:59], v[232:235], v[176:179], v[56:59]\n"
"v_cvt_pk_bf16_f32 v104, v104, v105\n"
"v_mfma_f32_16x16x32_bf16 v[60:63], v[232:235], v[180:183], v[60:63]\n"
"ds_read_b64_tr_b16 v[252:253], v3 offset:20480\n"
"ds_read_b64_tr_b16 v[254:255], v3 offset:22528\n"
"v_mfma_f32_16x16x32_bf16 v[64:67], v[232:235], v[184:187], v[64:67]\n"
"v_cvt_pk_bf16_f32 v105, v106, v107\n"
"v_cvt_pk_bf16_f32 v106, v116, v117\n"
"s_waitcnt lgkmcnt(4)\n"
"v_mfma_f32_16x16x32_bf16 v[68:71], v[236:239], v[176:179], v[68:71]\n"
"v_cvt_pk_bf16_f32 v107, v118, v119\n"
"v_mfma_f32_16x16x32_bf16 v[72:75], v[236:239], v[180:183], v[72:75]\n"
"ds_read_b64_tr_b16 v[224:225], v0 offset:24576\n"
"ds_read_b64_tr_b16 v[226:227], v0 offset:26624\n"
"v_mfma_f32_16x16x32_bf16 v[76:79], v[236:239], v[184:187], v[76:79]\n"
"buffer_store_dwordx4 v[104:107], v5, %5, s46 offen offset:1024 sc0 sc1\n"
"v_mul_f32 v108, v108, v9\n"
"v_mfma_f32_16x16x32_bf16 v[32:35], v[240:243], v[188:191], v[32:35]\n"
"v_mul_f32 v109, v109, v9\n"
"v_mfma_f32_16x16x32_bf16 v[36:39], v[240:243], v[192:195], v[36:39]\n"
"ds_read_b64_tr_b16 v[228:229], v1 offset:24576\n"
"ds_read_b64_tr_b16 v[230:231], v1 offset:26624\n"
"v_mfma_f32_16x16x32_bf16 v[40:43], v[240:243], v[196:199], v[40:43]\n"
"v_mul_f32 v110, v110, v9\n"
"v_mul_f32 v111, v111, v9\n"
"v_mfma_f32_16x16x32_bf16 v[44:47], v[244:247], v[188:191], v[44:47]\n"
"v_mul_f32 v120, v120, v9\n"
"v_mfma_f32_16x16x32_bf16 v[48:51], v[244:247], v[192:195], v[48:51]\n"
"ds_read_b64_tr_b16 v[232:233], v2 offset:24576\n"
"ds_read_b64_tr_b16 v[234:235], v2 offset:26624\n"
"v_mfma_f32_16x16x32_bf16 v[52:55], v[244:247], v[196:199], v[52:55]\n"
"v_mul_f32 v121, v121, v9\n"
"v_mul_f32 v122, v122, v9\n"
"s_waitcnt lgkmcnt(4)\n"
"v_mfma_f32_16x16x32_bf16 v[56:59], v[248:251], v[188:191], v[56:59]\n"
"v_mul_f32 v123, v123, v9\n"
"v_mfma_f32_16x16x32_bf16 v[60:63], v[248:251], v[192:195], v[60:63]\n"
"ds_read_b64_tr_b16 v[236:237], v3 offset:24576\n"
"ds_read_b64_tr_b16 v[238:239], v3 offset:26624\n"
"v_mfma_f32_16x16x32_bf16 v[64:67], v[248:251], v[196:199], v[64:67]\n"
"v_cvt_pk_bf16_f32 v108, v108, v109\n"
"v_cvt_pk_bf16_f32 v109, v110, v111\n"
"v_mfma_f32_16x16x32_bf16 v[68:71], v[252:255], v[188:191], v[68:71]\n"
"v_cvt_pk_bf16_f32 v110, v120, v121\n"
"v_mfma_f32_16x16x32_bf16 v[72:75], v[252:255], v[192:195], v[72:75]\n"
"ds_read_b64_tr_b16 v[240:241], v0 offset:28672\n"
"ds_read_b64_tr_b16 v[242:243], v0 offset:30720\n"
"v_mfma_f32_16x16x32_bf16 v[76:79], v[252:255], v[196:199], v[76:79]\n"
"v_cvt_pk_bf16_f32 v111, v122, v123\n"
"buffer_store_dwordx4 v[108:111], v6, %5, s46 offen offset:1024 sc0 sc1\n"
"v_mfma_f32_16x16x32_bf16 v[32:35], v[224:227], v[200:203], v[32:35]\n"
"v_mul_f32 v112, v112, v10\n"
"v_mfma_f32_16x16x32_bf16 v[36:39], v[224:227], v[204:207], v[36:39]\n"
"ds_read_b64_tr_b16 v[244:245], v1 offset:28672\n"
"ds_read_b64_tr_b16 v[246:247], v1 offset:30720\n"
"v_mfma_f32_16x16x32_bf16 v[40:43], v[224:227], v[208:211], v[40:43]\n"
"v_mul_f32 v113, v113, v10\n"
"v_mul_f32 v114, v114, v10\n"
"s_waitcnt lgkmcnt(4)\n"
"v_mfma_f32_16x16x32_bf16 v[44:47], v[228:231], v[200:203], v[44:47]\n"
"v_mul_f32 v115, v115, v10\n"
"v_mfma_f32_16x16x32_bf16 v[48:51], v[228:231], v[204:207], v[48:51]\n"
"ds_read_b64_tr_b16 v[248:249], v2 offset:28672\n"
"ds_read_b64_tr_b16 v[250:251], v2 offset:30720\n"
"v_mfma_f32_16x16x32_bf16 v[52:55], v[228:231], v[208:211], v[52:55]\n"
"ds_read_b64_tr_b16 v[252:253], v3 offset:28672\n"
"ds_read_b64_tr_b16 v[254:255], v3 offset:30720\n"
"v_mfma_f32_16x16x32_bf16 v[56:59], v[232:235], v[200:203], v[56:59]\n"
"v_mul_f32 v124, v124, v10\n"
"v_mul_f32 v125, v125, v10\n"
"v_mfma_f32_16x16x32_bf16 v[60:63], v[232:235], v[204:207], v[60:63]\n"
"v_mul_f32 v126, v126, v10\n"
"v_mul_f32 v127, v127, v10\n"
"v_mfma_f32_16x16x32_bf16 v[64:67], v[232:235], v[208:211], v[64:67]\n"
"v_cvt_pk_bf16_f32 v112, v112, v113\n"
"v_mfma_f32_16x16x32_bf16 v[68:71], v[236:239], v[200:203], v[68:71]\n"
"v_cvt_pk_bf16_f32 v113, v114, v115\n"
"v_mfma_f32_16x16x32_bf16 v[72:75], v[236:239], v[204:207], v[72:75]\n"
"v_cvt_pk_bf16_f32 v114, v124, v125\n"
"v_mfma_f32_16x16x32_bf16 v[76:79], v[236:239], v[208:211], v[76:79]\n"
"v_cvt_pk_bf16_f32 v115, v126, v127\n"
"s_waitcnt lgkmcnt(1)\n"
"v_mfma_f32_16x16x32_bf16 v[32:35], v[240:243], v[212:215], v[32:35]\n"
"buffer_store_dwordx4 v[112:115], v7, %5, s46 offen offset:1024 sc0 sc1\n"
"v_mfma_f32_16x16x32_bf16 v[36:39], v[240:243], v[216:219], v[36:39]\n"
"v_mfma_f32_16x16x32_bf16 v[40:43], v[240:243], v[220:223], v[40:43]\n"
"s_waitcnt vmcnt(6) lgkmcnt(0)\n"
"s_barrier\n"
"v_mfma_f32_16x16x32_bf16 v[44:47], v[244:247], v[212:215], v[44:47]\n"
"v_add_u32 v0, 65536, v0\n"
"v_add_u32 v1, 65536, v1\n"
"v_mfma_f32_16x16x32_bf16 v[48:51], v[244:247], v[216:219], v[48:51]\n"
"v_add_u32 v2, 65536, v2\n"
"v_add_u32 v3, 65536, v3\n"
"v_mfma_f32_16x16x32_bf16 v[52:55], v[244:247], v[220:223], v[52:55]\n"
"ds_read_b64_tr_b16 v[224:225], v0 offset:0\n"
"ds_read_b64_tr_b16 v[226:227], v0 offset:2048\n"
"v_mfma_f32_16x16x32_bf16 v[56:59], v[248:251], v[212:215], v[56:59]\n"
"ds_read_b64_tr_b16 v[228:229], v1 offset:0\n"
"ds_read_b64_tr_b16 v[230:231], v1 offset:2048\n"
"v_mfma_f32_16x16x32_bf16 v[60:63], v[248:251], v[216:219], v[60:63]\n"
"ds_read_b64_tr_b16 v[232:233], v2 offset:0\n"
"ds_read_b64_tr_b16 v[234:235], v2 offset:2048\n"
"v_mfma_f32_16x16x32_bf16 v[64:67], v[248:251], v[220:223], v[64:67]\n"
"ds_read_b64_tr_b16 v[236:237], v3 offset:0\n"
"ds_read_b64_tr_b16 v[238:239], v3 offset:2048\n"
"v_mfma_f32_16x16x32_bf16 v[68:71], v[252:255], v[212:215], v[68:71]\n"
"v_mfma_f32_16x16x32_bf16 v[72:75], v[252:255], v[216:219], v[72:75]\n"
"v_mfma_f32_16x16x32_bf16 v[76:79], v[252:255], v[220:223], v[76:79]\n"
"s_waitcnt lgkmcnt(6)\n"
"v_mfma_f32_16x16x32_bf16 v[80:83], v[224:227], v[128:131], 0\n"
"ds_read_b64_tr_b16 v[240:241], v0 offset:4096\n"
"ds_read_b64_tr_b16 v[242:243], v0 offset:6144\n"
"v_mfma_f32_16x16x32_bf16 v[84:87], v[224:227], v[132:135], 0\n"
"ds_read_b64_tr_b16 v[244:245], v1 offset:4096\n"
"ds_read_b64_tr_b16 v[246:247], v1 offset:6144\n"
"v_mfma_f32_16x16x32_bf16 v[88:91], v[224:227], v[136:139], 0\n"
"s_waitcnt lgkmcnt(7)\n"
"v_mfma_f32_16x16x32_bf16 v[92:95], v[228:231], v[128:131], 0\n"
"v_mfma_f32_16x16x32_bf16 v[96:99], v[228:231], v[132:135], 0\n"
"ds_read_b64_tr_b16 v[248:249], v2 offset:4096\n"
"ds_read_b64_tr_b16 v[250:251], v2 offset:6144\n"
"v_mfma_f32_16x16x32_bf16 v[100:103], v[228:231], v[136:139], 0\n"
"s_waitcnt lgkmcnt(6)\n"
"v_mfma_f32_16x16x32_bf16 v[104:107], v[232:235], v[128:131], 0\n"
"s_mov_b32 s46, 8192\n"
"v_mfma_f32_16x16x32_bf16 v[108:111], v[232:235], v[132:135], 0\n"
"ds_read_b64_tr_b16 v[252:253], v3 offset:4096\n"
"ds_read_b64_tr_b16 v[254:255], v3 offset:6144\n"
"v_mfma_f32_16x16x32_bf16 v[112:115], v[232:235], v[136:139], 0\n"
"v_mul_f32 v32, v32, v8\n"
"v_mul_f32 v33, v33, v8\n"
"v_mfma_f32_16x16x32_bf16 v[116:119], v[236:239], v[128:131], 0\n"
"v_mul_f32 v34, v34, v8\n"
"v_mfma_f32_16x16x32_bf16 v[120:123], v[236:239], v[132:135], 0\n"
"ds_read_b64_tr_b16 v[224:225], v0 offset:8192\n"
"ds_read_b64_tr_b16 v[226:227], v0 offset:10240\n"
"v_mfma_f32_16x16x32_bf16 v[124:127], v[236:239], v[136:139], 0\n"
"v_mul_f32 v35, v35, v8\n"
"v_mul_f32 v44, v44, v8\n"
"s_waitcnt lgkmcnt(4)\n"
"v_mfma_f32_16x16x32_bf16 v[80:83], v[240:243], v[140:143], v[80:83]\n"
"v_mul_f32 v45, v45, v8\n"
"v_mfma_f32_16x16x32_bf16 v[84:87], v[240:243], v[144:147], v[84:87]\n"
"ds_read_b64_tr_b16 v[228:229], v1 offset:8192\n"
"ds_read_b64_tr_b16 v[230:231], v1 offset:10240\n"
"v_mfma_f32_16x16x32_bf16 v[88:91], v[240:243], v[148:151], v[88:91]\n"
"v_mul_f32 v46, v46, v8\n"
"v_mul_f32 v47, v47, v8\n"
"v_mfma_f32_16x16x32_bf16 v[92:95], v[244:247], v[140:143], v[92:95]\n"
"v_cvt_pk_bf16_f32 v32, v32, v33\n"
"v_mfma_f32_16x16x32_bf16 v[96:99], v[244:247], v[144:147], v[96:99]\n"
"ds_read_b64_tr_b16 v[232:233], v2 offset:8192\n"
"ds_read_b64_tr_b16 v[234:235], v2 offset:10240\n"
"v_mfma_f32_16x16x32_bf16 v[100:103], v[244:247], v[148:151], v[100:103]\n"
"v_cvt_pk_bf16_f32 v33, v34, v35\n"
"v_cvt_pk_bf16_f32 v34, v44, v45\n"
"v_mfma_f32_16x16x32_bf16 v[104:107], v[248:251], v[140:143], v[104:107]\n"
"v_cvt_pk_bf16_f32 v35, v46, v47\n"
"v_mfma_f32_16x16x32_bf16 v[108:111], v[248:251], v[144:147], v[108:111]\n"
"ds_read_b64_tr_b16 v[236:237], v3 offset:8192\n"
"ds_read_b64_tr_b16 v[238:239], v3 offset:10240\n"
"v_mfma_f32_16x16x32_bf16 v[112:115], v[248:251], v[148:151], v[112:115]\n"
"buffer_store_dwordx4 v[32:35], v5, %5, s46 offen offset:0 sc0 sc1\n"
"v_mul_f32 v36, v36, v9\n"
"s_waitcnt lgkmcnt(4)\n"
"v_mfma_f32_16x16x32_bf16 v[116:119], v[252:255], v[140:143], v[116:119]\n"
"v_mul_f32 v37, v37, v9\n"
"v_mfma_f32_16x16x32_bf16 v[120:123], v[252:255], v[144:147], v[120:123]\n"
"ds_read_b64_tr_b16 v[240:241], v0 offset:12288\n"
"ds_read_b64_tr_b16 v[242:243], v0 offset:14336\n"
"v_mfma_f32_16x16x32_bf16 v[124:127], v[252:255], v[148:151], v[124:127]\n"
"v_mul_f32 v38, v38, v9\n"
"v_mul_f32 v39, v39, v9\n"
"v_mfma_f32_16x16x32_bf16 v[80:83], v[224:227], v[152:155], v[80:83]\n"
"v_mul_f32 v48, v48, v9\n"
"v_mfma_f32_16x16x32_bf16 v[84:87], v[224:227], v[156:159], v[84:87]\n"
"ds_read_b64_tr_b16 v[244:245], v1 offset:12288\n"
"ds_read_b64_tr_b16 v[246:247], v1 offset:14336\n"
"v_mfma_f32_16x16x32_bf16 v[88:91], v[224:227], v[160:163], v[88:91]\n"
"v_mul_f32 v49, v49, v9\n"
"v_mul_f32 v50, v50, v9\n"
"v_mfma_f32_16x16x32_bf16 v[92:95], v[228:231], v[152:155], v[92:95]\n"
"v_mul_f32 v51, v51, v9\n"
"v_mfma_f32_16x16x32_bf16 v[96:99], v[228:231], v[156:159], v[96:99]\n"
"ds_read_b64_tr_b16 v[248:249], v2 offset:12288\n"
"ds_read_b64_tr_b16 v[250:251], v2 offset:14336\n"
"v_mfma_f32_16x16x32_bf16 v[100:103], v[228:231], v[160:163], v[100:103]\n"
"v_cvt_pk_bf16_f32 v36, v36, v37\n"
"v_cvt_pk_bf16_f32 v37, v38, v39\n"
"s_waitcnt lgkmcnt(4)\n"
"v_mfma_f32_16x16x32_bf16 v[104:107], v[232:235], v[152:155], v[104:107]\n"
"v_cvt_pk_bf16_f32 v38, v48, v49\n"
"v_mfma_f32_16x16x32_bf16 v[108:111], v[232:235], v[156:159], v[108:111]\n"
"ds_read_b64_tr_b16 v[252:253], v3 offset:12288\n"
"ds_read_b64_tr_b16 v[254:255], v3 offset:14336\n"
"v_mfma_f32_16x16x32_bf16 v[112:115], v[232:235], v[160:163], v[112:115]\n"
"v_cvt_pk_bf16_f32 v39, v50, v51\n"
"buffer_store_dwordx4 v[36:39], v6, %5, s46 offen offset:0 sc0 sc1\n"
"v_mfma_f32_16x16x32_bf16 v[116:119], v[236:239], v[152:155], v[116:119]\n"
"v_mul_f32 v40, v40, v10\n"
"v_mfma_f32_16x16x32_bf16 v[120:123], v[236:239], v[156:159], v[120:123]\n"
"ds_read_b64_tr_b16 v[224:225], v0 offset:16384\n"
"ds_read_b64_tr_b16 v[226:227], v0 offset:18432\n"
"v_mfma_f32_16x16x32_bf16 v[124:127], v[236:239], v[160:163], v[124:127]\n"
"v_mul_f32 v41, v41, v10\n"
"v_mul_f32 v42, v42, v10\n"
"v_mfma_f32_16x16x32_bf16 v[80:83], v[240:243], v[164:167], v[80:83]\n"
"v_mul_f32 v43, v43, v10\n"
"v_mfma_f32_16x16x32_bf16 v[84:87], v[240:243], v[168:171], v[84:87]\n"
"ds_read_b64_tr_b16 v[228:229], v1 offset:16384\n"
"ds_read_b64_tr_b16 v[230:231], v1 offset:18432\n"
"v_mfma_f32_16x16x32_bf16 v[88:91], v[240:243], v[172:175], v[88:91]\n"
"v_mul_f32 v52, v52, v10\n"
"v_mul_f32 v53, v53, v10\n"
"s_waitcnt lgkmcnt(4)\n"
"v_mfma_f32_16x16x32_bf16 v[92:95], v[244:247], v[164:167], v[92:95]\n"
"v_mul_f32 v54, v54, v10\n"
"v_mfma_f32_16x16x32_bf16 v[96:99], v[244:247], v[168:171], v[96:99]\n"
"ds_read_b64_tr_b16 v[232:233], v2 offset:16384\n"
"ds_read_b64_tr_b16 v[234:235], v2 offset:18432\n"
"v_mfma_f32_16x16x32_bf16 v[100:103], v[244:247], v[172:175], v[100:103]\n"
"v_mul_f32 v55, v55, v10\n"
"v_cvt_pk_bf16_f32 v40, v40, v41\n"
"v_mfma_f32_16x16x32_bf16 v[104:107], v[248:251], v[164:167], v[104:107]\n"
"v_cvt_pk_bf16_f32 v41, v42, v43\n"
"v_mfma_f32_16x16x32_bf16 v[108:111], v[248:251], v[168:171], v[108:111]\n"
"ds_read_b64_tr_b16 v[236:237], v3 offset:16384\n"
"ds_read_b64_tr_b16 v[238:239], v3 offset:18432\n"
"v_mfma_f32_16x16x32_bf16 v[112:115], v[248:251], v[172:175], v[112:115]\n"
"v_cvt_pk_bf16_f32 v42, v52, v53\n"
"v_cvt_pk_bf16_f32 v43, v54, v55\n"
"v_mfma_f32_16x16x32_bf16 v[116:119], v[252:255], v[164:167], v[116:119]\n"
"buffer_store_dwordx4 v[40:43], v7, %5, s46 offen offset:0 sc0 sc1\n"
"v_mfma_f32_16x16x32_bf16 v[120:123], v[252:255], v[168:171], v[120:123]\n"
"ds_read_b64_tr_b16 v[240:241], v0 offset:20480\n"
"ds_read_b64_tr_b16 v[242:243], v0 offset:22528\n"
"v_mfma_f32_16x16x32_bf16 v[124:127], v[252:255], v[172:175], v[124:127]\n"
"v_mul_f32 v56, v56, v8\n"
"v_mul_f32 v57, v57, v8\n"
"s_waitcnt lgkmcnt(4)\n"
"v_mfma_f32_16x16x32_bf16 v[80:83], v[224:227], v[176:179], v[80:83]\n"
"v_mul_f32 v58, v58, v8\n"
"v_mfma_f32_16x16x32_bf16 v[84:87], v[224:227], v[180:183], v[84:87]\n"
"ds_read_b64_tr_b16 v[244:245], v1 offset:20480\n"
"ds_read_b64_tr_b16 v[246:247], v1 offset:22528\n"
"v_mfma_f32_16x16x32_bf16 v[88:91], v[224:227], v[184:187], v[88:91]\n"
"v_mul_f32 v59, v59, v8\n"
"v_mul_f32 v68, v68, v8\n"
"v_mfma_f32_16x16x32_bf16 v[92:95], v[228:231], v[176:179], v[92:95]\n"
"v_mul_f32 v69, v69, v8\n"
"v_mfma_f32_16x16x32_bf16 v[96:99], v[228:231], v[180:183], v[96:99]\n"
"ds_read_b64_tr_b16 v[248:249], v2 offset:20480\n"
"ds_read_b64_tr_b16 v[250:251], v2 offset:22528\n"
"v_mfma_f32_16x16x32_bf16 v[100:103], v[228:231], v[184:187], v[100:103]\n"
"v_mul_f32 v70, v70, v8\n"
"v_mul_f32 v71, v71, v8\n"
"v_mfma_f32_16x16x32_bf16 v[104:107], v[232:235], v[176:179], v[104:107]\n"
"v_cvt_pk_bf16_f32 v56, v56, v57\n"
"v_mfma_f32_16x16x32_bf16 v[108:111], v[232:235], v[180:183], v[108:111]\n"
"ds_read_b64_tr_b16 v[252:253], v3 offset:20480\n"
"ds_read_b64_tr_b16 v[254:255], v3 offset:22528\n"
"v_mfma_f32_16x16x32_bf16 v[112:115], v[232:235], v[184:187], v[112:115]\n"
"v_cvt_pk_bf16_f32 v57, v58, v59\n"
"v_cvt_pk_bf16_f32 v58, v68, v69\n"
"s_waitcnt lgkmcnt(4)\n"
"v_mfma_f32_16x16x32_bf16 v[116:119], v[236:239], v[176:179], v[116:119]\n"
"v_cvt_pk_bf16_f32 v59, v70, v71\n"
"v_mfma_f32_16x16x32_bf16 v[120:123], v[236:239], v[180:183], v[120:123]\n"
"ds_read_b64_tr_b16 v[224:225], v0 offset:24576\n"
"ds_read_b64_tr_b16 v[226:227], v0 offset:26624\n"
"v_mfma_f32_16x16x32_bf16 v[124:127], v[236:239], v[184:187], v[124:127]\n"
"buffer_store_dwordx4 v[56:59], v5, %5, s46 offen offset:1024 sc0 sc1\n"
"v_mul_f32 v60, v60, v9\n"
"v_mfma_f32_16x16x32_bf16 v[80:83], v[240:243], v[188:191], v[80:83]\n"
"v_mul_f32 v61, v61, v9\n"
"v_mfma_f32_16x16x32_bf16 v[84:87], v[240:243], v[192:195], v[84:87]\n"
"ds_read_b64_tr_b16 v[228:229], v1 offset:24576\n"
"ds_read_b64_tr_b16 v[230:231], v1 offset:26624\n"
"v_mfma_f32_16x16x32_bf16 v[88:91], v[240:243], v[196:199], v[88:91]\n"
"v_mul_f32 v62, v62, v9\n"
"v_mul_f32 v63, v63, v9\n"
"v_mfma_f32_16x16x32_bf16 v[92:95], v[244:247], v[188:191], v[92:95]\n"
"v_mul_f32 v72, v72, v9\n"
"v_mfma_f32_16x16x32_bf16 v[96:99], v[244:247], v[192:195], v[96:99]\n"
"ds_read_b64_tr_b16 v[232:233], v2 offset:24576\n"
"ds_read_b64_tr_b16 v[234:235], v2 offset:26624\n"
"v_mfma_f32_16x16x32_bf16 v[100:103], v[244:247], v[196:199], v[100:103]\n"
"v_mul_f32 v73, v73, v9\n"
"v_mul_f32 v74, v74, v9\n"
"s_waitcnt lgkmcnt(4)\n"
"v_mfma_f32_16x16x32_bf16 v[104:107], v[248:251], v[188:191], v[104:107]\n"
"v_mul_f32 v75, v75, v9\n"
"v_mfma_f32_16x16x32_bf16 v[108:111], v[248:251], v[192:195], v[108:111]\n"
"ds_read_b64_tr_b16 v[236:237], v3 offset:24576\n"
"ds_read_b64_tr_b16 v[238:239], v3 offset:26624\n"
"v_mfma_f32_16x16x32_bf16 v[112:115], v[248:251], v[196:199], v[112:115]\n"
"v_cvt_pk_bf16_f32 v60, v60, v61\n"
"v_cvt_pk_bf16_f32 v61, v62, v63\n"
"v_mfma_f32_16x16x32_bf16 v[116:119], v[252:255], v[188:191], v[116:119]\n"
"v_cvt_pk_bf16_f32 v62, v72, v73\n"
"v_mfma_f32_16x16x32_bf16 v[120:123], v[252:255], v[192:195], v[120:123]\n"
"ds_read_b64_tr_b16 v[240:241], v0 offset:28672\n"
"ds_read_b64_tr_b16 v[242:243], v0 offset:30720\n"
"v_mfma_f32_16x16x32_bf16 v[124:127], v[252:255], v[196:199], v[124:127]\n"
"v_cvt_pk_bf16_f32 v63, v74, v75\n"
"buffer_store_dwordx4 v[60:63], v6, %5, s46 offen offset:1024 sc0 sc1\n"
"v_mfma_f32_16x16x32_bf16 v[80:83], v[224:227], v[200:203], v[80:83]\n"
"v_mul_f32 v64, v64, v10\n"
"v_mfma_f32_16x16x32_bf16 v[84:87], v[224:227], v[204:207], v[84:87]\n"
"ds_read_b64_tr_b16 v[244:245], v1 offset:28672\n"
"ds_read_b64_tr_b16 v[246:247], v1 offset:30720\n"
"v_mfma_f32_16x16x32_bf16 v[88:91], v[224:227], v[208:211], v[88:91]\n"
"v_mul_f32 v65, v65, v10\n"
"v_mul_f32 v66, v66, v10\n"
"s_waitcnt lgkmcnt(4)\n"
"v_mfma_f32_16x16x32_bf16 v[92:95], v[228:231], v[200:203], v[92:95]\n"
"v_mul_f32 v67, v67, v10\n"
"v_mfma_f32_16x16x32_bf16 v[96:99], v[228:231], v[204:207], v[96:99]\n"
"ds_read_b64_tr_b16 v[248:249], v2 offset:28672\n"
"ds_read_b64_tr_b16 v[250:251], v2 offset:30720\n"
"v_mfma_f32_16x16x32_bf16 v[100:103], v[228:231], v[208:211], v[100:103]\n"
"ds_read_b64_tr_b16 v[252:253], v3 offset:28672\n"
"ds_read_b64_tr_b16 v[254:255], v3 offset:30720\n"
"v_mfma_f32_16x16x32_bf16 v[104:107], v[232:235], v[200:203], v[104:107]\n"
"v_mul_f32 v76, v76, v10\n"
"v_mul_f32 v77, v77, v10\n"
"v_mfma_f32_16x16x32_bf16 v[108:111], v[232:235], v[204:207], v[108:111]\n"
"v_mul_f32 v78, v78, v10\n"
"v_mul_f32 v79, v79, v10\n"
"v_mfma_f32_16x16x32_bf16 v[112:115], v[232:235], v[208:211], v[112:115]\n"
"v_cvt_pk_bf16_f32 v64, v64, v65\n"
"v_mfma_f32_16x16x32_bf16 v[116:119], v[236:239], v[200:203], v[116:119]\n"
"v_cvt_pk_bf16_f32 v65, v66, v67\n"
"v_mfma_f32_16x16x32_bf16 v[120:123], v[236:239], v[204:207], v[120:123]\n"
"v_cvt_pk_bf16_f32 v66, v76, v77\n"
"v_mfma_f32_16x16x32_bf16 v[124:127], v[236:239], v[208:211], v[124:127]\n"
"v_cvt_pk_bf16_f32 v67, v78, v79\n"
"s_waitcnt lgkmcnt(1)\n"
"v_mfma_f32_16x16x32_bf16 v[80:83], v[240:243], v[212:215], v[80:83]\n"
"buffer_store_dwordx4 v[64:67], v7, %5, s46 offen offset:1024 sc0 sc1\n"
"v_mfma_f32_16x16x32_bf16 v[84:87], v[240:243], v[216:219], v[84:87]\n"
"v_mfma_f32_16x16x32_bf16 v[88:91], v[240:243], v[220:223], v[88:91]\n"
"s_waitcnt vmcnt(6) lgkmcnt(0)\n"
"s_barrier\n"
"v_mfma_f32_16x16x32_bf16 v[92:95], v[244:247], v[212:215], v[92:95]\n"
"v_subrev_u32 v0, 131072, v0\n"
"v_subrev_u32 v1, 131072, v1\n"
"v_mfma_f32_16x16x32_bf16 v[96:99], v[244:247], v[216:219], v[96:99]\n"
"v_subrev_u32 v2, 131072, v2\n"
"v_subrev_u32 v3, 131072, v3\n"
"v_mfma_f32_16x16x32_bf16 v[100:103], v[244:247], v[220:223], v[100:103]\n"
"ds_read_b64_tr_b16 v[224:225], v0 offset:0\n"
"ds_read_b64_tr_b16 v[226:227], v0 offset:2048\n"
"v_mfma_f32_16x16x32_bf16 v[104:107], v[248:251], v[212:215], v[104:107]\n"
"ds_read_b64_tr_b16 v[228:229], v1 offset:0\n"
"ds_read_b64_tr_b16 v[230:231], v1 offset:2048\n"
"v_mfma_f32_16x16x32_bf16 v[108:111], v[248:251], v[216:219], v[108:111]\n"
"ds_read_b64_tr_b16 v[232:233], v2 offset:0\n"
"ds_read_b64_tr_b16 v[234:235], v2 offset:2048\n"
"v_mfma_f32_16x16x32_bf16 v[112:115], v[248:251], v[220:223], v[112:115]\n"
"ds_read_b64_tr_b16 v[236:237], v3 offset:0\n"
"ds_read_b64_tr_b16 v[238:239], v3 offset:2048\n"
"v_mfma_f32_16x16x32_bf16 v[116:119], v[252:255], v[212:215], v[116:119]\n"
"v_mfma_f32_16x16x32_bf16 v[120:123], v[252:255], v[216:219], v[120:123]\n"
"v_mfma_f32_16x16x32_bf16 v[124:127], v[252:255], v[220:223], v[124:127]\n"
"s_waitcnt lgkmcnt(6)\n"
"v_mfma_f32_16x16x32_bf16 v[32:35], v[224:227], v[128:131], 0\n"
"ds_read_b64_tr_b16 v[240:241], v0 offset:4096\n"
"ds_read_b64_tr_b16 v[242:243], v0 offset:6144\n"
"v_mfma_f32_16x16x32_bf16 v[36:39], v[224:227], v[132:135], 0\n"
"ds_read_b64_tr_b16 v[244:245], v1 offset:4096\n"
"ds_read_b64_tr_b16 v[246:247], v1 offset:6144\n"
"v_mfma_f32_16x16x32_bf16 v[40:43], v[224:227], v[136:139], 0\n"
"s_waitcnt lgkmcnt(7)\n"
"v_mfma_f32_16x16x32_bf16 v[44:47], v[228:231], v[128:131], 0\n"
"v_mfma_f32_16x16x32_bf16 v[48:51], v[228:231], v[132:135], 0\n"
"ds_read_b64_tr_b16 v[248:249], v2 offset:4096\n"
"ds_read_b64_tr_b16 v[250:251], v2 offset:6144\n"
"v_mfma_f32_16x16x32_bf16 v[52:55], v[228:231], v[136:139], 0\n"
"s_waitcnt lgkmcnt(6)\n"
"v_mfma_f32_16x16x32_bf16 v[56:59], v[232:235], v[128:131], 0\n"
"s_mov_b32 s46, 10240\n"
"v_mfma_f32_16x16x32_bf16 v[60:63], v[232:235], v[132:135], 0\n"
"ds_read_b64_tr_b16 v[252:253], v3 offset:4096\n"
"ds_read_b64_tr_b16 v[254:255], v3 offset:6144\n"
"v_mfma_f32_16x16x32_bf16 v[64:67], v[232:235], v[136:139], 0\n"
"v_mul_f32 v80, v80, v8\n"
"v_mul_f32 v81, v81, v8\n"
"v_mfma_f32_16x16x32_bf16 v[68:71], v[236:239], v[128:131], 0\n"
"v_mul_f32 v82, v82, v8\n"
"v_mfma_f32_16x16x32_bf16 v[72:75], v[236:239], v[132:135], 0\n"
"ds_read_b64_tr_b16 v[224:225], v0 offset:8192\n"
"ds_read_b64_tr_b16 v[226:227], v0 offset:10240\n"
"v_mfma_f32_16x16x32_bf16 v[76:79], v[236:239], v[136:139], 0\n"
"v_mul_f32 v83, v83, v8\n"
"v_mul_f32 v92, v92, v8\n"
"s_waitcnt lgkmcnt(4)\n"
"v_mfma_f32_16x16x32_bf16 v[32:35], v[240:243], v[140:143], v[32:35]\n"
"v_mul_f32 v93, v93, v8\n"
"v_mfma_f32_16x16x32_bf16 v[36:39], v[240:243], v[144:147], v[36:39]\n"
"ds_read_b64_tr_b16 v[228:229], v1 offset:8192\n"
"ds_read_b64_tr_b16 v[230:231], v1 offset:10240\n"
"v_mfma_f32_16x16x32_bf16 v[40:43], v[240:243], v[148:151], v[40:43]\n"
"v_mul_f32 v94, v94, v8\n"
"v_mul_f32 v95, v95, v8\n"
"v_mfma_f32_16x16x32_bf16 v[44:47], v[244:247], v[140:143], v[44:47]\n"
"v_cvt_pk_bf16_f32 v80, v80, v81\n"
"v_mfma_f32_16x16x32_bf16 v[48:51], v[244:247], v[144:147], v[48:51]\n"
"ds_read_b64_tr_b16 v[232:233], v2 offset:8192\n"
"ds_read_b64_tr_b16 v[234:235], v2 offset:10240\n"
"v_mfma_f32_16x16x32_bf16 v[52:55], v[244:247], v[148:151], v[52:55]\n"
"v_cvt_pk_bf16_f32 v81, v82, v83\n"
"v_cvt_pk_bf16_f32 v82, v92, v93\n"
"v_mfma_f32_16x16x32_bf16 v[56:59], v[248:251], v[140:143], v[56:59]\n"
"v_cvt_pk_bf16_f32 v83, v94, v95\n"
"v_mfma_f32_16x16x32_bf16 v[60:63], v[248:251], v[144:147], v[60:63]\n"
"ds_read_b64_tr_b16 v[236:237], v3 offset:8192\n"
"ds_read_b64_tr_b16 v[238:239], v3 offset:10240\n"
"v_mfma_f32_16x16x32_bf16 v[64:67], v[248:251], v[148:151], v[64:67]\n"
"buffer_store_dwordx4 v[80:83], v5, %5, s46 offen offset:0 sc0 sc1\n"
"v_mul_f32 v84, v84, v9\n"
"s_waitcnt lgkmcnt(4)\n"
"v_mfma_f32_16x16x32_bf16 v[68:71], v[252:255], v[140:143], v[68:71]\n"
"v_mul_f32 v85, v85, v9\n"
"v_mfma_f32_16x16x32_bf16 v[72:75], v[252:255], v[144:147], v[72:75]\n"
"ds_read_b64_tr_b16 v[240:241], v0 offset:12288\n"
"ds_read_b64_tr_b16 v[242:243], v0 offset:14336\n"
"v_mfma_f32_16x16x32_bf16 v[76:79], v[252:255], v[148:151], v[76:79]\n"
"v_mul_f32 v86, v86, v9\n"
"v_mul_f32 v87, v87, v9\n"
"v_mfma_f32_16x16x32_bf16 v[32:35], v[224:227], v[152:155], v[32:35]\n"
"v_mul_f32 v96, v96, v9\n"
"v_mfma_f32_16x16x32_bf16 v[36:39], v[224:227], v[156:159], v[36:39]\n"
"ds_read_b64_tr_b16 v[244:245], v1 offset:12288\n"
"ds_read_b64_tr_b16 v[246:247], v1 offset:14336\n"
"v_mfma_f32_16x16x32_bf16 v[40:43], v[224:227], v[160:163], v[40:43]\n"
"v_mul_f32 v97, v97, v9\n"
"v_mul_f32 v98, v98, v9\n"
"v_mfma_f32_16x16x32_bf16 v[44:47], v[228:231], v[152:155], v[44:47]\n"
"v_mul_f32 v99, v99, v9\n"
"v_mfma_f32_16x16x32_bf16 v[48:51], v[228:231], v[156:159], v[48:51]\n"
"ds_read_b64_tr_b16 v[248:249], v2 offset:12288\n"
"ds_read_b64_tr_b16 v[250:251], v2 offset:14336\n"
"v_mfma_f32_16x16x32_bf16 v[52:55], v[228:231], v[160:163], v[52:55]\n"
"v_cvt_pk_bf16_f32 v84, v84, v85\n"
"v_cvt_pk_bf16_f32 v85, v86, v87\n"
"s_waitcnt lgkmcnt(4)\n"
"v_mfma_f32_16x16x32_bf16 v[56:59], v[232:235], v[152:155], v[56:59]\n"
"v_cvt_pk_bf16_f32 v86, v96, v97\n"
"v_mfma_f32_16x16x32_bf16 v[60:63], v[232:235], v[156:159], v[60:63]\n"
"ds_read_b64_tr_b16 v[252:253], v3 offset:12288\n"
"ds_read_b64_tr_b16 v[254:255], v3 offset:14336\n"
"v_mfma_f32_16x16x32_bf16 v[64:67], v[232:235], v[160:163], v[64:67]\n"
"v_cvt_pk_bf16_f32 v87, v98, v99\n"
"buffer_store_dwordx4 v[84:87], v6, %5, s46 offen offset:0 sc0 sc1\n"
"v_mfma_f32_16x16x32_bf16 v[68:71], v[236:239], v[152:155], v[68:71]\n"
"v_mul_f32 v88, v88, v10\n"
"v_mfma_f32_16x16x32_bf16 v[72:75], v[236:239], v[156:159], v[72:75]\n"
"ds_read_b64_tr_b16 v[224:225], v0 offset:16384\n"
"ds_read_b64_tr_b16 v[226:227], v0 offset:18432\n"
"v_mfma_f32_16x16x32_bf16 v[76:79], v[236:239], v[160:163], v[76:79]\n"
"v_mul_f32 v89, v89, v10\n"
"v_mul_f32 v90, v90, v10\n"
"v_mfma_f32_16x16x32_bf16 v[32:35], v[240:243], v[164:167], v[32:35]\n"
"v_mul_f32 v91, v91, v10\n"
"v_mfma_f32_16x16x32_bf16 v[36:39], v[240:243], v[168:171], v[36:39]\n"
"ds_read_b64_tr_b16 v[228:229], v1 offset:16384\n"
"ds_read_b64_tr_b16 v[230:231], v1 offset:18432\n"
"v_mfma_f32_16x16x32_bf16 v[40:43], v[240:243], v[172:175], v[40:43]\n"
"v_mul_f32 v100, v100, v10\n"
"v_mul_f32 v101, v101, v10\n"
"s_waitcnt lgkmcnt(4)\n"
"v_mfma_f32_16x16x32_bf16 v[44:47], v[244:247], v[164:167], v[44:47]\n"
"v_mul_f32 v102, v102, v10\n"
"v_mfma_f32_16x16x32_bf16 v[48:51], v[244:247], v[168:171], v[48:51]\n"
"ds_read_b64_tr_b16 v[232:233], v2 offset:16384\n"
"ds_read_b64_tr_b16 v[234:235], v2 offset:18432\n"
"v_mfma_f32_16x16x32_bf16 v[52:55], v[244:247], v[172:175], v[52:55]\n"
"v_mul_f32 v103, v103, v10\n"
"v_cvt_pk_bf16_f32 v88, v88, v89\n"
"v_mfma_f32_16x16x32_bf16 v[56:59], v[248:251], v[164:167], v[56:59]\n"
"v_cvt_pk_bf16_f32 v89, v90, v91\n"
"v_mfma_f32_16x16x32_bf16 v[60:63], v[248:251], v[168:171], v[60:63]\n"
"ds_read_b64_tr_b16 v[236:237], v3 offset:16384\n"
"ds_read_b64_tr_b16 v[238:239], v3 offset:18432\n"
"v_mfma_f32_16x16x32_bf16 v[64:67], v[248:251], v[172:175], v[64:67]\n"
"v_cvt_pk_bf16_f32 v90, v100, v101\n"
"v_cvt_pk_bf16_f32 v91, v102, v103\n"
"v_mfma_f32_16x16x32_bf16 v[68:71], v[252:255], v[164:167], v[68:71]\n"
"buffer_store_dwordx4 v[88:91], v7, %5, s46 offen offset:0 sc0 sc1\n"
"v_mfma_f32_16x16x32_bf16 v[72:75], v[252:255], v[168:171], v[72:75]\n"
"ds_read_b64_tr_b16 v[240:241], v0 offset:20480\n"
"ds_read_b64_tr_b16 v[242:243], v0 offset:22528\n"
"v_mfma_f32_16x16x32_bf16 v[76:79], v[252:255], v[172:175], v[76:79]\n"
"v_mul_f32 v104, v104, v8\n"
"v_mul_f32 v105, v105, v8\n"
"s_waitcnt lgkmcnt(4)\n"
"v_mfma_f32_16x16x32_bf16 v[32:35], v[224:227], v[176:179], v[32:35]\n"
"v_mul_f32 v106, v106, v8\n"
"v_mfma_f32_16x16x32_bf16 v[36:39], v[224:227], v[180:183], v[36:39]\n"
"ds_read_b64_tr_b16 v[244:245], v1 offset:20480\n"
"ds_read_b64_tr_b16 v[246:247], v1 offset:22528\n"
"v_mfma_f32_16x16x32_bf16 v[40:43], v[224:227], v[184:187], v[40:43]\n"
"v_mul_f32 v107, v107, v8\n"
"v_mul_f32 v116, v116, v8\n"
"v_mfma_f32_16x16x32_bf16 v[44:47], v[228:231], v[176:179], v[44:47]\n"
"v_mul_f32 v117, v117, v8\n"
"v_mfma_f32_16x16x32_bf16 v[48:51], v[228:231], v[180:183], v[48:51]\n"
"ds_read_b64_tr_b16 v[248:249], v2 offset:20480\n"
"ds_read_b64_tr_b16 v[250:251], v2 offset:22528\n"
"v_mfma_f32_16x16x32_bf16 v[52:55], v[228:231], v[184:187], v[52:55]\n"
"v_mul_f32 v118, v118, v8\n"
"v_mul_f32 v119, v119, v8\n"
"v_mfma_f32_16x16x32_bf16 v[56:59], v[232:235], v[176:179], v[56:59]\n"
"v_cvt_pk_bf16_f32 v104, v104, v105\n"
"v_mfma_f32_16x16x32_bf16 v[60:63], v[232:235], v[180:183], v[60:63]\n"
"ds_read_b64_tr_b16 v[252:253], v3 offset:20480\n"
"ds_read_b64_tr_b16 v[254:255], v3 offset:22528\n"
"v_mfma_f32_16x16x32_bf16 v[64:67], v[232:235], v[184:187], v[64:67]\n"
"v_cvt_pk_bf16_f32 v105, v106, v107\n"
"v_cvt_pk_bf16_f32 v106, v116, v117\n"
"s_waitcnt lgkmcnt(4)\n"
"v_mfma_f32_16x16x32_bf16 v[68:71], v[236:239], v[176:179], v[68:71]\n"
"v_cvt_pk_bf16_f32 v107, v118, v119\n"
"v_mfma_f32_16x16x32_bf16 v[72:75], v[236:239], v[180:183], v[72:75]\n"
"ds_read_b64_tr_b16 v[224:225], v0 offset:24576\n"
"ds_read_b64_tr_b16 v[226:227], v0 offset:26624\n"
"v_mfma_f32_16x16x32_bf16 v[76:79], v[236:239], v[184:187], v[76:79]\n"
"buffer_store_dwordx4 v[104:107], v5, %5, s46 offen offset:1024 sc0 sc1\n"
"v_mul_f32 v108, v108, v9\n"
"v_mfma_f32_16x16x32_bf16 v[32:35], v[240:243], v[188:191], v[32:35]\n"
"v_mul_f32 v109, v109, v9\n"
"v_mfma_f32_16x16x32_bf16 v[36:39], v[240:243], v[192:195], v[36:39]\n"
"ds_read_b64_tr_b16 v[228:229], v1 offset:24576\n"
"ds_read_b64_tr_b16 v[230:231], v1 offset:26624\n"
"v_mfma_f32_16x16x32_bf16 v[40:43], v[240:243], v[196:199], v[40:43]\n"
"v_mul_f32 v110, v110, v9\n"
"v_mul_f32 v111, v111, v9\n"
"v_mfma_f32_16x16x32_bf16 v[44:47], v[244:247], v[188:191], v[44:47]\n"
"v_mul_f32 v120, v120, v9\n"
"v_mfma_f32_16x16x32_bf16 v[48:51], v[244:247], v[192:195], v[48:51]\n"
"ds_read_b64_tr_b16 v[232:233], v2 offset:24576\n"
"ds_read_b64_tr_b16 v[234:235], v2 offset:26624\n"
"v_mfma_f32_16x16x32_bf16 v[52:55], v[244:247], v[196:199], v[52:55]\n"
"v_mul_f32 v121, v121, v9\n"
"v_mul_f32 v122, v122, v9\n"
"s_waitcnt lgkmcnt(4)\n"
"v_mfma_f32_16x16x32_bf16 v[56:59], v[248:251], v[188:191], v[56:59]\n"
"v_mul_f32 v123, v123, v9\n"
"v_mfma_f32_16x16x32_bf16 v[60:63], v[248:251], v[192:195], v[60:63]\n"
"ds_read_b64_tr_b16 v[236:237], v3 offset:24576\n"
"ds_read_b64_tr_b16 v[238:239], v3 offset:26624\n"
"v_mfma_f32_16x16x32_bf16 v[64:67], v[248:251], v[196:199], v[64:67]\n"
"v_cvt_pk_bf16_f32 v108, v108, v109\n"
"v_cvt_pk_bf16_f32 v109, v110, v111\n"
"v_mfma_f32_16x16x32_bf16 v[68:71], v[252:255], v[188:191], v[68:71]\n"
"v_cvt_pk_bf16_f32 v110, v120, v121\n"
"v_mfma_f32_16x16x32_bf16 v[72:75], v[252:255], v[192:195], v[72:75]\n"
"ds_read_b64_tr_b16 v[240:241], v0 offset:28672\n"
"ds_read_b64_tr_b16 v[242:243], v0 offset:30720\n"
"v_mfma_f32_16x16x32_bf16 v[76:79], v[252:255], v[196:199], v[76:79]\n"
"v_cvt_pk_bf16_f32 v111, v122, v123\n"
"buffer_store_dwordx4 v[108:111], v6, %5, s46 offen offset:1024 sc0 sc1\n"
"v_mfma_f32_16x16x32_bf16 v[32:35], v[224:227], v[200:203], v[32:35]\n"
"v_mul_f32 v112, v112, v10\n"
"v_mfma_f32_16x16x32_bf16 v[36:39], v[224:227], v[204:207], v[36:39]\n"
"ds_read_b64_tr_b16 v[244:245], v1 offset:28672\n"
"ds_read_b64_tr_b16 v[246:247], v1 offset:30720\n"
"v_mfma_f32_16x16x32_bf16 v[40:43], v[224:227], v[208:211], v[40:43]\n"
"v_mul_f32 v113, v113, v10\n"
"v_mul_f32 v114, v114, v10\n"
"s_waitcnt lgkmcnt(4)\n"
"v_mfma_f32_16x16x32_bf16 v[44:47], v[228:231], v[200:203], v[44:47]\n"
"v_mul_f32 v115, v115, v10\n"
"v_mfma_f32_16x16x32_bf16 v[48:51], v[228:231], v[204:207], v[48:51]\n"
"ds_read_b64_tr_b16 v[248:249], v2 offset:28672\n"
"ds_read_b64_tr_b16 v[250:251], v2 offset:30720\n"
"v_mfma_f32_16x16x32_bf16 v[52:55], v[228:231], v[208:211], v[52:55]\n"
"ds_read_b64_tr_b16 v[252:253], v3 offset:28672\n"
"ds_read_b64_tr_b16 v[254:255], v3 offset:30720\n"
"v_mfma_f32_16x16x32_bf16 v[56:59], v[232:235], v[200:203], v[56:59]\n"
"v_mul_f32 v124, v124, v10\n"
"v_mul_f32 v125, v125, v10\n"
"v_mfma_f32_16x16x32_bf16 v[60:63], v[232:235], v[204:207], v[60:63]\n"
"v_mul_f32 v126, v126, v10\n"
"v_mul_f32 v127, v127, v10\n"
"v_mfma_f32_16x16x32_bf16 v[64:67], v[232:235], v[208:211], v[64:67]\n"
"v_cvt_pk_bf16_f32 v112, v112, v113\n"
"v_mfma_f32_16x16x32_bf16 v[68:71], v[236:239], v[200:203], v[68:71]\n"
"v_cvt_pk_bf16_f32 v113, v114, v115\n"
"v_mfma_f32_16x16x32_bf16 v[72:75], v[236:239], v[204:207], v[72:75]\n"
"v_cvt_pk_bf16_f32 v114, v124, v125\n"
"v_mfma_f32_16x16x32_bf16 v[76:79], v[236:239], v[208:211], v[76:79]\n"
"v_cvt_pk_bf16_f32 v115, v126, v127\n"
"s_waitcnt lgkmcnt(1)\n"
"v_mfma_f32_16x16x32_bf16 v[32:35], v[240:243], v[212:215], v[32:35]\n"
"buffer_store_dwordx4 v[112:115], v7, %5, s46 offen offset:1024 sc0 sc1\n"
"v_mfma_f32_16x16x32_bf16 v[36:39], v[240:243], v[216:219], v[36:39]\n"
"v_mfma_f32_16x16x32_bf16 v[40:43], v[240:243], v[220:223], v[40:43]\n"
"v_mfma_f32_16x16x32_bf16 v[44:47], v[244:247], v[212:215], v[44:47]\n"
"v_mfma_f32_16x16x32_bf16 v[48:51], v[244:247], v[216:219], v[48:51]\n"
"v_mfma_f32_16x16x32_bf16 v[52:55], v[244:247], v[220:223], v[52:55]\n"
"v_mfma_f32_16x16x32_bf16 v[56:59], v[248:251], v[212:215], v[56:59]\n"
"v_mfma_f32_16x16x32_bf16 v[60:63], v[248:251], v[216:219], v[60:63]\n"
"v_mfma_f32_16x16x32_bf16 v[64:67], v[248:251], v[220:223], v[64:67]\n"
"s_waitcnt lgkmcnt(0)\n"
"v_mfma_f32_16x16x32_bf16 v[68:71], v[252:255], v[212:215], v[68:71]\n"
"v_mfma_f32_16x16x32_bf16 v[72:75], v[252:255], v[216:219], v[72:75]\n"
"v_mfma_f32_16x16x32_bf16 v[76:79], v[252:255], v[220:223], v[76:79]\n"
"s_nop 7\n"
"s_mov_b32 s46, 12288\n"
"v_mul_f32 v32, v32, v8\n"
"v_mul_f32 v33, v33, v8\n"
"v_mul_f32 v34, v34, v8\n"
"v_mul_f32 v35, v35, v8\n"
"v_mul_f32 v44, v44, v8\n"
"v_mul_f32 v45, v45, v8\n"
"v_mul_f32 v46, v46, v8\n"
"v_mul_f32 v47, v47, v8\n"
"v_cvt_pk_bf16_f32 v32, v32, v33\n"
"v_cvt_pk_bf16_f32 v33, v34, v35\n"
"v_cvt_pk_bf16_f32 v34, v44, v45\n"
"v_cvt_pk_bf16_f32 v35, v46, v47\n"
"buffer_store_dwordx4 v[32:35], v5, %5, s46 offen offset:0 sc0 sc1\n"
"v_mul_f32 v36, v36, v9\n"
"v_mul_f32 v37, v37, v9\n"
"v_mul_f32 v38, v38, v9\n"
"v_mul_f32 v39, v39, v9\n"
"v_mul_f32 v48, v48, v9\n"
"v_mul_f32 v49, v49, v9\n"
"v_mul_f32 v50, v50, v9\n"
"v_mul_f32 v51, v51, v9\n"
"v_cvt_pk_bf16_f32 v36, v36, v37\n"
"v_cvt_pk_bf16_f32 v37, v38, v39\n"
"v_cvt_pk_bf16_f32 v38, v48, v49\n"
"v_cvt_pk_bf16_f32 v39, v50, v51\n"
"buffer_store_dwordx4 v[36:39], v6, %5, s46 offen offset:0 sc0 sc1\n"
"v_mul_f32 v40, v40, v10\n"
"v_mul_f32 v41, v41, v10\n"
"v_mul_f32 v42, v42, v10\n"
"v_mul_f32 v43, v43, v10\n"
"v_mul_f32 v52, v52, v10\n"
"v_mul_f32 v53, v53, v10\n"
"v_mul_f32 v54, v54, v10\n"
"v_mul_f32 v55, v55, v10\n"
"v_cvt_pk_bf16_f32 v40, v40, v41\n"
"v_cvt_pk_bf16_f32 v41, v42, v43\n"
"v_cvt_pk_bf16_f32 v42, v52, v53\n"
"v_cvt_pk_bf16_f32 v43, v54, v55\n"
"buffer_store_dwordx4 v[40:43], v7, %5, s46 offen offset:0 sc0 sc1\n"
"v_mul_f32 v56, v56, v8\n"
"v_mul_f32 v57, v57, v8\n"
"v_mul_f32 v58, v58, v8\n"
"v_mul_f32 v59, v59, v8\n"
"v_mul_f32 v68, v68, v8\n"
"v_mul_f32 v69, v69, v8\n"
"v_mul_f32 v70, v70, v8\n"
"v_mul_f32 v71, v71, v8\n"
"v_cvt_pk_bf16_f32 v56, v56, v57\n"
"v_cvt_pk_bf16_f32 v57, v58, v59\n"
"v_cvt_pk_bf16_f32 v58, v68, v69\n"
"v_cvt_pk_bf16_f32 v59, v70, v71\n"
"buffer_store_dwordx4 v[56:59], v5, %5, s46 offen offset:1024 sc0 sc1\n"
"v_mul_f32 v60, v60, v9\n"
"v_mul_f32 v61, v61, v9\n"
"v_mul_f32 v62, v62, v9\n"
"v_mul_f32 v63, v63, v9\n"
"v_mul_f32 v72, v72, v9\n"
"v_mul_f32 v73, v73, v9\n"
"v_mul_f32 v74, v74, v9\n"
"v_mul_f32 v75, v75, v9\n"
"v_cvt_pk_bf16_f32 v60, v60, v61\n"
"v_cvt_pk_bf16_f32 v61, v62, v63\n"
"v_cvt_pk_bf16_f32 v62, v72, v73\n"
"v_cvt_pk_bf16_f32 v63, v74, v75\n"
"buffer_store_dwordx4 v[60:63], v6, %5, s46 offen offset:1024 sc0 sc1\n"
"v_mul_f32 v64, v64, v10\n"
"v_mul_f32 v65, v65, v10\n"
"v_mul_f32 v66, v66, v10\n"
"v_mul_f32 v67, v67, v10\n"
"v_mul_f32 v76, v76, v10\n"
"v_mul_f32 v77, v77, v10\n"
"v_mul_f32 v78, v78, v10\n"
"v_mul_f32 v79, v79, v10\n"
"v_cvt_pk_bf16_f32 v64, v64, v65\n"
"v_cvt_pk_bf16_f32 v65, v66, v67\n"
"v_cvt_pk_bf16_f32 v66, v76, v77\n"
"v_cvt_pk_bf16_f32 v67, v78, v79\n"
"buffer_store_dwordx4 v[64:67], v7, %5, s46 offen offset:1024 sc0 sc1\n"
"s_branch LEND_%=\n"
"LRARE3_%=:\n"
"s_nop 7\n"
"s_nop 7\n"
"v_mbcnt_lo_u32_b32 v240, -1, 0\n"
"v_mbcnt_hi_u32_b32 v240, -1, v240\n"
"v_xor_b32 v241, 16, v240\n"
"v_lshlrev_b32 v241, 2, v241\n"
"v_xor_b32 v242, 32, v240\n"
"v_lshlrev_b32 v242, 2, v242\n"
"v_max3_f32 v243, v32, v33, v34\n"
"v_max3_f32 v243, v243, v35, v44\n"
"v_max3_f32 v243, v243, v45, v46\n"
"v_max_f32 v243, v243, v47\n"
"v_max3_f32 v244, v36, v37, v38\n"
"v_max3_f32 v244, v244, v39, v48\n"
"v_max3_f32 v244, v244, v49, v50\n"
"v_max_f32 v244, v244, v51\n"
"v_max3_f32 v245, v40, v41, v42\n"
"v_max3_f32 v245, v245, v43, v52\n"
"v_max3_f32 v245, v245, v53, v54\n"
"v_max_f32 v245, v245, v55\n"
"ds_bpermute_b32 v248, v241, v243\n"
"ds_bpermute_b32 v249, v241, v244\n"
"ds_bpermute_b32 v250, v241, v245\n"
"s_waitcnt lgkmcnt(0)\n"
"v_max_f32 v243, v243, v248\n"
"v_max_f32 v244, v244, v249\n"
"v_max_f32 v245, v245, v250\n"
"ds_bpermute_b32 v248, v242, v243\n"
"ds_bpermute_b32 v249, v242, v244\n"
"ds_bpermute_b32 v250, v242, v245\n"
"s_waitcnt lgkmcnt(0)\n"
"v_max_f32 v243, v243, v248\n"
"v_max_f32 v244, v244, v249\n"
"v_max_f32 v245, v245, v250\n"
"v_ceil_f32 v243, v243\n"
"v_max_f32 v243, s44, v243\n"
"v_sub_f32 v32, v32, v243\n"
"v_sub_f32 v33, v33, v243\n"
"v_sub_f32 v34, v34, v243\n"
"v_sub_f32 v35, v35, v243\n"
"v_sub_f32 v44, v44, v243\n"
"v_sub_f32 v45, v45, v243\n"
"v_sub_f32 v46, v46, v243\n"
"v_sub_f32 v47, v47, v243\n"
"v_sub_f32 v20, v20, v243\n"
"v_sub_f32 v21, v21, v243\n"
"v_sub_f32 v22, v22, v243\n"
"v_sub_f32 v23, v23, v243\n"
"v_cvt_i32_f32 v248, v243\n"
"v_sub_u32 v248, 0, v248\n"
"v_ceil_f32 v244, v244\n"
"v_max_f32 v244, s44, v244\n"
"v_sub_f32 v36, v36, v244\n"
"v_sub_f32 v37, v37, v244\n"
"v_sub_f32 v38, v38, v244\n"
"v_sub_f32 v39, v39, v244\n"
"v_sub_f32 v48, v48, v244\n"
"v_sub_f32 v49, v49, v244\n"
"v_sub_f32 v50, v50, v244\n"
"v_sub_f32 v51, v51, v244\n"
"v_sub_f32 v24, v24, v244\n"
"v_sub_f32 v25, v25, v244\n"
"v_sub_f32 v26, v26, v244\n"
"v_sub_f32 v27, v27, v244\n"
"v_cvt_i32_f32 v249, v244\n"
"v_sub_u32 v249, 0, v249\n"
"v_ceil_f32 v245, v245\n"
"v_max_f32 v245, s44, v245\n"
"v_sub_f32 v40, v40, v245\n"
"v_sub_f32 v41, v41, v245\n"
"v_sub_f32 v42, v42, v245\n"
"v_sub_f32 v43, v43, v245\n"
"v_sub_f32 v52, v52, v245\n"
"v_sub_f32 v53, v53, v245\n"
"v_sub_f32 v54, v54, v245\n"
"v_sub_f32 v55, v55, v245\n"
"v_sub_f32 v28, v28, v245\n"
"v_sub_f32 v29, v29, v245\n"
"v_sub_f32 v30, v30, v245\n"
"v_sub_f32 v31, v31, v245\n"
"v_cvt_i32_f32 v250, v245\n"
"v_sub_u32 v250, 0, v250\n"
"s_cmp_lg_u32 s44, 0\n"
"s_cbranch_scc1 LRDONE3_%=\n"
"v_accvgpr_read_b32 v246, a0\n"
"v_ldexp_f32 v246, v246, v248\n"
"v_accvgpr_write_b32 a0, v246\n"
"v_accvgpr_read_b32 v246, a1\n"
"v_ldexp_f32 v246, v246, v248\n"
"v_accvgpr_write_b32 a1, v246\n"
"v_accvgpr_read_b32 v246, a2\n"
"v_ldexp_f32 v246, v246, v248\n"
"v_accvgpr_write_b32 a2, v246\n"
"v_accvgpr_read_b32 v246, a3\n"
"v_ldexp_f32 v246, v246, v248\n"
"v_accvgpr_write_b32 a3, v246\n"
"v_accvgpr_read_b32 v246, a12\n"
"v_ldexp_f32 v246, v246, v248\n"
"v_accvgpr_write_b32 a12, v246\n"
"v_accvgpr_read_b32 v246, a13\n"
"v_ldexp_f32 v246, v246, v248\n"
"v_accvgpr_write_b32 a13, v246\n"
"v_accvgpr_read_b32 v246, a14\n"
"v_ldexp_f32 v246, v246, v248\n"
"v_accvgpr_write_b32 a14, v246\n"
"v_accvgpr_read_b32 v246, a15\n"
"v_ldexp_f32 v246, v246, v248\n"
"v_accvgpr_write_b32 a15, v246\n"
"v_accvgpr_read_b32 v246, a24\n"
"v_ldexp_f32 v246, v246, v248\n"
"v_accvgpr_write_b32 a24, v246\n"
"v_accvgpr_read_b32 v246, a25\n"
"v_ldexp_f32 v246, v246, v248\n"
"v_accvgpr_write_b32 a25, v246\n"
"v_accvgpr_read_b32 v246, a26\n"
"v_ldexp_f32 v246, v246, v248\n"
"v_accvgpr_write_b32 a26, v246\n"
"v_accvgpr_read_b32 v246, a27\n"
"v_ldexp_f32 v246, v246, v248\n"
"v_accvgpr_write_b32 a27, v246\n"
"v_accvgpr_read_b32 v246, a36\n"
"v_ldexp_f32 v246, v246, v248\n"
"v_accvgpr_write_b32 a36, v246\n"
"v_accvgpr_read_b32 v246, a37\n"
"v_ldexp_f32 v246, v246, v248\n"
"v_accvgpr_write_b32 a37, v246\n"
"v_accvgpr_read_b32 v246, a38\n"
"v_ldexp_f32 v246, v246, v248\n"
"v_accvgpr_write_b32 a38, v246\n"
"v_accvgpr_read_b32 v246, a39\n"
"v_ldexp_f32 v246, v246, v248\n"
"v_accvgpr_write_b32 a39, v246\n"
"v_accvgpr_read_b32 v246, a48\n"
"v_ldexp_f32 v246, v246, v248\n"
"v_accvgpr_write_b32 a48, v246\n"
"v_accvgpr_read_b32 v246, a49\n"
"v_ldexp_f32 v246, v246, v248\n"
"v_accvgpr_write_b32 a49, v246\n"
"v_accvgpr_read_b32 v246, a50\n"
"v_ldexp_f32 v246, v246, v248\n"
"v_accvgpr_write_b32 a50, v246\n"
"v_accvgpr_read_b32 v246, a51\n"
"v_ldexp_f32 v246, v246, v248\n"
"v_accvgpr_write_b32 a51, v246\n"
"v_accvgpr_read_b32 v246, a60\n"
"v_ldexp_f32 v246, v246, v248\n"
"v_accvgpr_write_b32 a60, v246\n"
"v_accvgpr_read_b32 v246, a61\n"
"v_ldexp_f32 v246, v246, v248\n"
"v_accvgpr_write_b32 a61, v246\n"
"v_accvgpr_read_b32 v246, a62\n"
"v_ldexp_f32 v246, v246, v248\n"
"v_accvgpr_write_b32 a62, v246\n"
"v_accvgpr_read_b32 v246, a63\n"
"v_ldexp_f32 v246, v246, v248\n"
"v_accvgpr_write_b32 a63, v246\n"
"v_accvgpr_read_b32 v246, a72\n"
"v_ldexp_f32 v246, v246, v248\n"
"v_accvgpr_write_b32 a72, v246\n"
"v_accvgpr_read_b32 v246, a73\n"
"v_ldexp_f32 v246, v246, v248\n"
"v_accvgpr_write_b32 a73, v246\n"
"v_accvgpr_read_b32 v246, a74\n"
"v_ldexp_f32 v246, v246, v248\n"
"v_accvgpr_write_b32 a74, v246\n"
"v_accvgpr_read_b32 v246, a75\n"
"v_ldexp_f32 v246, v246, v248\n"
"v_accvgpr_write_b32 a75, v246\n"
"v_accvgpr_read_b32 v246, a84\n"
"v_ldexp_f32 v246, v246, v248\n"
"v_accvgpr_write_b32 a84, v246\n"
"v_accvgpr_read_b32 v246, a85\n"
"v_ldexp_f32 v246, v246, v248\n"
"v_accvgpr_write_b32 a85, v246\n"
"v_accvgpr_read_b32 v246, a86\n"
"v_ldexp_f32 v246, v246, v248\n"
"v_accvgpr_write_b32 a86, v246\n"
"v_accvgpr_read_b32 v246, a87\n"
"v_ldexp_f32 v246, v246, v248\n"
"v_accvgpr_write_b32 a87, v246\n"
"v_accvgpr_read_b32 v246, a96\n"
"v_ldexp_f32 v246, v246, v248\n"
"v_accvgpr_write_b32 a96, v246\n"
"v_accvgpr_read_b32 v246, a97\n"
"v_ldexp_f32 v246, v246, v248\n"
"v_accvgpr_write_b32 a97, v246\n"
"v_accvgpr_read_b32 v246, a98\n"
"v_ldexp_f32 v246, v246, v248\n"
"v_accvgpr_write_b32 a98, v246\n"
"v_accvgpr_read_b32 v246, a99\n"
"v_ldexp_f32 v246, v246, v248\n"
"v_accvgpr_write_b32 a99, v246\n"
"v_accvgpr_read_b32 v246, a108\n"
"v_ldexp_f32 v246, v246, v248\n"
"v_accvgpr_write_b32 a108, v246\n"
"v_accvgpr_read_b32 v246, a109\n"
"v_ldexp_f32 v246, v246, v248\n"
"v_accvgpr_write_b32 a109, v246\n"
"v_accvgpr_read_b32 v246, a110\n"
"v_ldexp_f32 v246, v246, v248\n"
"v_accvgpr_write_b32 a110, v246\n"
"v_accvgpr_read_b32 v246, a111\n"
"v_ldexp_f32 v246, v246, v248\n"
"v_accvgpr_write_b32 a111, v246\n"
"v_accvgpr_read_b32 v246, a120\n"
"v_ldexp_f32 v246, v246, v248\n"
"v_accvgpr_write_b32 a120, v246\n"
"v_accvgpr_read_b32 v246, a121\n"
"v_ldexp_f32 v246, v246, v248\n"
"v_accvgpr_write_b32 a121, v246\n"
"v_accvgpr_read_b32 v246, a122\n"
"v_ldexp_f32 v246, v246, v248\n"
"v_accvgpr_write_b32 a122, v246\n"
"v_accvgpr_read_b32 v246, a123\n"
"v_ldexp_f32 v246, v246, v248\n"
"v_accvgpr_write_b32 a123, v246\n"
"v_accvgpr_read_b32 v246, a132\n"
"v_ldexp_f32 v246, v246, v248\n"
"v_accvgpr_write_b32 a132, v246\n"
"v_accvgpr_read_b32 v246, a133\n"
"v_ldexp_f32 v246, v246, v248\n"
"v_accvgpr_write_b32 a133, v246\n"
"v_accvgpr_read_b32 v246, a134\n"
"v_ldexp_f32 v246, v246, v248\n"
"v_accvgpr_write_b32 a134, v246\n"
"v_accvgpr_read_b32 v246, a135\n"
"v_ldexp_f32 v246, v246, v248\n"
"v_accvgpr_write_b32 a135, v246\n"
"v_accvgpr_read_b32 v246, a144\n"
"v_ldexp_f32 v246, v246, v248\n"
"v_accvgpr_write_b32 a144, v246\n"
"v_accvgpr_read_b32 v246, a145\n"
"v_ldexp_f32 v246, v246, v248\n"
"v_accvgpr_write_b32 a145, v246\n"
"v_accvgpr_read_b32 v246, a146\n"
"v_ldexp_f32 v246, v246, v248\n"
"v_accvgpr_write_b32 a146, v246\n"
"v_accvgpr_read_b32 v246, a147\n"
"v_ldexp_f32 v246, v246, v248\n"
"v_accvgpr_write_b32 a147, v246\n"
"v_accvgpr_read_b32 v246, a156\n"
"v_ldexp_f32 v246, v246, v248\n"
"v_accvgpr_write_b32 a156, v246\n"
"v_accvgpr_read_b32 v246, a157\n"
"v_ldexp_f32 v246, v246, v248\n"
"v_accvgpr_write_b32 a157, v246\n"
"v_accvgpr_read_b32 v246, a158\n"
"v_ldexp_f32 v246, v246, v248\n"
"v_accvgpr_write_b32 a158, v246\n"
"v_accvgpr_read_b32 v246, a159\n"
"v_ldexp_f32 v246, v246, v248\n"
"v_accvgpr_write_b32 a159, v246\n"
"v_accvgpr_read_b32 v246, a168\n"
"v_ldexp_f32 v246, v246, v248\n"
"v_accvgpr_write_b32 a168, v246\n"
"v_accvgpr_read_b32 v246, a169\n"
"v_ldexp_f32 v246, v246, v248\n"
"v_accvgpr_write_b32 a169, v246\n"
"v_accvgpr_read_b32 v246, a170\n"
"v_ldexp_f32 v246, v246, v248\n"
"v_accvgpr_write_b32 a170, v246\n"
"v_accvgpr_read_b32 v246, a171\n"
"v_ldexp_f32 v246, v246, v248\n"
"v_accvgpr_write_b32 a171, v246\n"
"v_accvgpr_read_b32 v246, a180\n"
"v_ldexp_f32 v246, v246, v248\n"
"v_accvgpr_write_b32 a180, v246\n"
"v_accvgpr_read_b32 v246, a181\n"
"v_ldexp_f32 v246, v246, v248\n"
"v_accvgpr_write_b32 a181, v246\n"
"v_accvgpr_read_b32 v246, a182\n"
"v_ldexp_f32 v246, v246, v248\n"
"v_accvgpr_write_b32 a182, v246\n"
"v_accvgpr_read_b32 v246, a183\n"
"v_ldexp_f32 v246, v246, v248\n"
"v_accvgpr_write_b32 a183, v246\n"
"v_ldexp_f32 v205, v205, v248\n"
"v_lshlrev_b32 v246, 16, v56\n"
"v_and_b32 v247, 0xffff0000, v56\n"
"v_ldexp_f32 v246, v246, v248\n"
"v_ldexp_f32 v247, v247, v248\n"
"v_cvt_pk_bf16_f32 v56, v246, v247\n"
"v_lshlrev_b32 v246, 16, v57\n"
"v_and_b32 v247, 0xffff0000, v57\n"
"v_ldexp_f32 v246, v246, v248\n"
"v_ldexp_f32 v247, v247, v248\n"
"v_cvt_pk_bf16_f32 v57, v246, v247\n"
"v_lshlrev_b32 v246, 16, v58\n"
"v_and_b32 v247, 0xffff0000, v58\n"
"v_ldexp_f32 v246, v246, v248\n"
"v_ldexp_f32 v247, v247, v248\n"
"v_cvt_pk_bf16_f32 v58, v246, v247\n"
"v_lshlrev_b32 v246, 16, v59\n"
"v_and_b32 v247, 0xffff0000, v59\n"
"v_ldexp_f32 v246, v246, v248\n"
"v_ldexp_f32 v247, v247, v248\n"
"v_cvt_pk_bf16_f32 v59, v246, v247\n"
"v_lshlrev_b32 v246, 16, v68\n"
"v_and_b32 v247, 0xffff0000, v68\n"
"v_ldexp_f32 v246, v246, v248\n"
"v_ldexp_f32 v247, v247, v248\n"
"v_cvt_pk_bf16_f32 v68, v246, v247\n"
"v_lshlrev_b32 v246, 16, v69\n"
"v_and_b32 v247, 0xffff0000, v69\n"
"v_ldexp_f32 v246, v246, v248\n"
"v_ldexp_f32 v247, v247, v248\n"
"v_cvt_pk_bf16_f32 v69, v246, v247\n"
"v_lshlrev_b32 v246, 16, v70\n"
"v_and_b32 v247, 0xffff0000, v70\n"
"v_ldexp_f32 v246, v246, v248\n"
"v_ldexp_f32 v247, v247, v248\n"
"v_cvt_pk_bf16_f32 v70, v246, v247\n"
"v_lshlrev_b32 v246, 16, v71\n"
"v_and_b32 v247, 0xffff0000, v71\n"
"v_ldexp_f32 v246, v246, v248\n"
"v_ldexp_f32 v247, v247, v248\n"
"v_cvt_pk_bf16_f32 v71, v246, v247\n"
"v_accvgpr_read_b32 v246, a4\n"
"v_ldexp_f32 v246, v246, v249\n"
"v_accvgpr_write_b32 a4, v246\n"
"v_accvgpr_read_b32 v246, a5\n"
"v_ldexp_f32 v246, v246, v249\n"
"v_accvgpr_write_b32 a5, v246\n"
"v_accvgpr_read_b32 v246, a6\n"
"v_ldexp_f32 v246, v246, v249\n"
"v_accvgpr_write_b32 a6, v246\n"
"v_accvgpr_read_b32 v246, a7\n"
"v_ldexp_f32 v246, v246, v249\n"
"v_accvgpr_write_b32 a7, v246\n"
"v_accvgpr_read_b32 v246, a16\n"
"v_ldexp_f32 v246, v246, v249\n"
"v_accvgpr_write_b32 a16, v246\n"
"v_accvgpr_read_b32 v246, a17\n"
"v_ldexp_f32 v246, v246, v249\n"
"v_accvgpr_write_b32 a17, v246\n"
"v_accvgpr_read_b32 v246, a18\n"
"v_ldexp_f32 v246, v246, v249\n"
"v_accvgpr_write_b32 a18, v246\n"
"v_accvgpr_read_b32 v246, a19\n"
"v_ldexp_f32 v246, v246, v249\n"
"v_accvgpr_write_b32 a19, v246\n"
"v_accvgpr_read_b32 v246, a28\n"
"v_ldexp_f32 v246, v246, v249\n"
"v_accvgpr_write_b32 a28, v246\n"
"v_accvgpr_read_b32 v246, a29\n"
"v_ldexp_f32 v246, v246, v249\n"
"v_accvgpr_write_b32 a29, v246\n"
"v_accvgpr_read_b32 v246, a30\n"
"v_ldexp_f32 v246, v246, v249\n"
"v_accvgpr_write_b32 a30, v246\n"
"v_accvgpr_read_b32 v246, a31\n"
"v_ldexp_f32 v246, v246, v249\n"
"v_accvgpr_write_b32 a31, v246\n"
"v_accvgpr_read_b32 v246, a40\n"
"v_ldexp_f32 v246, v246, v249\n"
"v_accvgpr_write_b32 a40, v246\n"
"v_accvgpr_read_b32 v246, a41\n"
"v_ldexp_f32 v246, v246, v249\n"
"v_accvgpr_write_b32 a41, v246\n"
"v_accvgpr_read_b32 v246, a42\n"
"v_ldexp_f32 v246, v246, v249\n"
"v_accvgpr_write_b32 a42, v246\n"
"v_accvgpr_read_b32 v246, a43\n"
"v_ldexp_f32 v246, v246, v249\n"
"v_accvgpr_write_b32 a43, v246\n"
"v_accvgpr_read_b32 v246, a52\n"
"v_ldexp_f32 v246, v246, v249\n"
"v_accvgpr_write_b32 a52, v246\n"
"v_accvgpr_read_b32 v246, a53\n"
"v_ldexp_f32 v246, v246, v249\n"
"v_accvgpr_write_b32 a53, v246\n"
"v_accvgpr_read_b32 v246, a54\n"
"v_ldexp_f32 v246, v246, v249\n"
"v_accvgpr_write_b32 a54, v246\n"
"v_accvgpr_read_b32 v246, a55\n"
"v_ldexp_f32 v246, v246, v249\n"
"v_accvgpr_write_b32 a55, v246\n"
"v_accvgpr_read_b32 v246, a64\n"
"v_ldexp_f32 v246, v246, v249\n"
"v_accvgpr_write_b32 a64, v246\n"
"v_accvgpr_read_b32 v246, a65\n"
"v_ldexp_f32 v246, v246, v249\n"
"v_accvgpr_write_b32 a65, v246\n"
"v_accvgpr_read_b32 v246, a66\n"
"v_ldexp_f32 v246, v246, v249\n"
"v_accvgpr_write_b32 a66, v246\n"
"v_accvgpr_read_b32 v246, a67\n"
"v_ldexp_f32 v246, v246, v249\n"
"v_accvgpr_write_b32 a67, v246\n"
"v_accvgpr_read_b32 v246, a76\n"
"v_ldexp_f32 v246, v246, v249\n"
"v_accvgpr_write_b32 a76, v246\n"
"v_accvgpr_read_b32 v246, a77\n"
"v_ldexp_f32 v246, v246, v249\n"
"v_accvgpr_write_b32 a77, v246\n"
"v_accvgpr_read_b32 v246, a78\n"
"v_ldexp_f32 v246, v246, v249\n"
"v_accvgpr_write_b32 a78, v246\n"
"v_accvgpr_read_b32 v246, a79\n"
"v_ldexp_f32 v246, v246, v249\n"
"v_accvgpr_write_b32 a79, v246\n"
"v_accvgpr_read_b32 v246, a88\n"
"v_ldexp_f32 v246, v246, v249\n"
"v_accvgpr_write_b32 a88, v246\n"
"v_accvgpr_read_b32 v246, a89\n"
"v_ldexp_f32 v246, v246, v249\n"
"v_accvgpr_write_b32 a89, v246\n"
"v_accvgpr_read_b32 v246, a90\n"
"v_ldexp_f32 v246, v246, v249\n"
"v_accvgpr_write_b32 a90, v246\n"
"v_accvgpr_read_b32 v246, a91\n"
"v_ldexp_f32 v246, v246, v249\n"
"v_accvgpr_write_b32 a91, v246\n"
"v_accvgpr_read_b32 v246, a100\n"
"v_ldexp_f32 v246, v246, v249\n"
"v_accvgpr_write_b32 a100, v246\n"
"v_accvgpr_read_b32 v246, a101\n"
"v_ldexp_f32 v246, v246, v249\n"
"v_accvgpr_write_b32 a101, v246\n"
"v_accvgpr_read_b32 v246, a102\n"
"v_ldexp_f32 v246, v246, v249\n"
"v_accvgpr_write_b32 a102, v246\n"
"v_accvgpr_read_b32 v246, a103\n"
"v_ldexp_f32 v246, v246, v249\n"
"v_accvgpr_write_b32 a103, v246\n"
"v_accvgpr_read_b32 v246, a112\n"
"v_ldexp_f32 v246, v246, v249\n"
"v_accvgpr_write_b32 a112, v246\n"
"v_accvgpr_read_b32 v246, a113\n"
"v_ldexp_f32 v246, v246, v249\n"
"v_accvgpr_write_b32 a113, v246\n"
"v_accvgpr_read_b32 v246, a114\n"
"v_ldexp_f32 v246, v246, v249\n"
"v_accvgpr_write_b32 a114, v246\n"
"v_accvgpr_read_b32 v246, a115\n"
"v_ldexp_f32 v246, v246, v249\n"
"v_accvgpr_write_b32 a115, v246\n"
"v_accvgpr_read_b32 v246, a124\n"
"v_ldexp_f32 v246, v246, v249\n"
"v_accvgpr_write_b32 a124, v246\n"
"v_accvgpr_read_b32 v246, a125\n"
"v_ldexp_f32 v246, v246, v249\n"
"v_accvgpr_write_b32 a125, v246\n"
"v_accvgpr_read_b32 v246, a126\n"
"v_ldexp_f32 v246, v246, v249\n"
"v_accvgpr_write_b32 a126, v246\n"
"v_accvgpr_read_b32 v246, a127\n"
"v_ldexp_f32 v246, v246, v249\n"
"v_accvgpr_write_b32 a127, v246\n"
"v_accvgpr_read_b32 v246, a136\n"
"v_ldexp_f32 v246, v246, v249\n"
"v_accvgpr_write_b32 a136, v246\n"
"v_accvgpr_read_b32 v246, a137\n"
"v_ldexp_f32 v246, v246, v249\n"
"v_accvgpr_write_b32 a137, v246\n"
"v_accvgpr_read_b32 v246, a138\n"
"v_ldexp_f32 v246, v246, v249\n"
"v_accvgpr_write_b32 a138, v246\n"
"v_accvgpr_read_b32 v246, a139\n"
"v_ldexp_f32 v246, v246, v249\n"
"v_accvgpr_write_b32 a139, v246\n"
"v_accvgpr_read_b32 v246, a148\n"
"v_ldexp_f32 v246, v246, v249\n"
"v_accvgpr_write_b32 a148, v246\n"
"v_accvgpr_read_b32 v246, a149\n"
"v_ldexp_f32 v246, v246, v249\n"
"v_accvgpr_write_b32 a149, v246\n"
"v_accvgpr_read_b32 v246, a150\n"
"v_ldexp_f32 v246, v246, v249\n"
"v_accvgpr_write_b32 a150, v246\n"
"v_accvgpr_read_b32 v246, a151\n"
"v_ldexp_f32 v246, v246, v249\n"
"v_accvgpr_write_b32 a151, v246\n"
"v_accvgpr_read_b32 v246, a160\n"
"v_ldexp_f32 v246, v246, v249\n"
"v_accvgpr_write_b32 a160, v246\n"
"v_accvgpr_read_b32 v246, a161\n"
"v_ldexp_f32 v246, v246, v249\n"
"v_accvgpr_write_b32 a161, v246\n"
"v_accvgpr_read_b32 v246, a162\n"
"v_ldexp_f32 v246, v246, v249\n"
"v_accvgpr_write_b32 a162, v246\n"
"v_accvgpr_read_b32 v246, a163\n"
"v_ldexp_f32 v246, v246, v249\n"
"v_accvgpr_write_b32 a163, v246\n"
"v_accvgpr_read_b32 v246, a172\n"
"v_ldexp_f32 v246, v246, v249\n"
"v_accvgpr_write_b32 a172, v246\n"
"v_accvgpr_read_b32 v246, a173\n"
"v_ldexp_f32 v246, v246, v249\n"
"v_accvgpr_write_b32 a173, v246\n"
"v_accvgpr_read_b32 v246, a174\n"
"v_ldexp_f32 v246, v246, v249\n"
"v_accvgpr_write_b32 a174, v246\n"
"v_accvgpr_read_b32 v246, a175\n"
"v_ldexp_f32 v246, v246, v249\n"
"v_accvgpr_write_b32 a175, v246\n"
"v_accvgpr_read_b32 v246, a184\n"
"v_ldexp_f32 v246, v246, v249\n"
"v_accvgpr_write_b32 a184, v246\n"
"v_accvgpr_read_b32 v246, a185\n"
"v_ldexp_f32 v246, v246, v249\n"
"v_accvgpr_write_b32 a185, v246\n"
"v_accvgpr_read_b32 v246, a186\n"
"v_ldexp_f32 v246, v246, v249\n"
"v_accvgpr_write_b32 a186, v246\n"
"v_accvgpr_read_b32 v246, a187\n"
"v_ldexp_f32 v246, v246, v249\n"
"v_accvgpr_write_b32 a187, v246\n"
"v_ldexp_f32 v206, v206, v249\n"
"v_lshlrev_b32 v246, 16, v60\n"
"v_and_b32 v247, 0xffff0000, v60\n"
"v_ldexp_f32 v246, v246, v249\n"
"v_ldexp_f32 v247, v247, v249\n"
"v_cvt_pk_bf16_f32 v60, v246, v247\n"
"v_lshlrev_b32 v246, 16, v61\n"
"v_and_b32 v247, 0xffff0000, v61\n"
"v_ldexp_f32 v246, v246, v249\n"
"v_ldexp_f32 v247, v247, v249\n"
"v_cvt_pk_bf16_f32 v61, v246, v247\n"
"v_lshlrev_b32 v246, 16, v62\n"
"v_and_b32 v247, 0xffff0000, v62\n"
"v_ldexp_f32 v246, v246, v249\n"
"v_ldexp_f32 v247, v247, v249\n"
"v_cvt_pk_bf16_f32 v62, v246, v247\n"
"v_lshlrev_b32 v246, 16, v63\n"
"v_and_b32 v247, 0xffff0000, v63\n"
"v_ldexp_f32 v246, v246, v249\n"
"v_ldexp_f32 v247, v247, v249\n"
"v_cvt_pk_bf16_f32 v63, v246, v247\n"
"v_lshlrev_b32 v246, 16, v72\n"
"v_and_b32 v247, 0xffff0000, v72\n"
"v_ldexp_f32 v246, v246, v249\n"
"v_ldexp_f32 v247, v247, v249\n"
"v_cvt_pk_bf16_f32 v72, v246, v247\n"
"v_lshlrev_b32 v246, 16, v73\n"
"v_and_b32 v247, 0xffff0000, v73\n"
"v_ldexp_f32 v246, v246, v249\n"
"v_ldexp_f32 v247, v247, v249\n"
"v_cvt_pk_bf16_f32 v73, v246, v247\n"
"v_lshlrev_b32 v246, 16, v74\n"
"v_and_b32 v247, 0xffff0000, v74\n"
"v_ldexp_f32 v246, v246, v249\n"
"v_ldexp_f32 v247, v247, v249\n"
"v_cvt_pk_bf16_f32 v74, v246, v247\n"
"v_lshlrev_b32 v246, 16, v75\n"
"v_and_b32 v247, 0xffff0000, v75\n"
"v_ldexp_f32 v246, v246, v249\n"
"v_ldexp_f32 v247, v247, v249\n"
"v_cvt_pk_bf16_f32 v75, v246, v247\n"
"v_accvgpr_read_b32 v246, a8\n"
"v_ldexp_f32 v246, v246, v250\n"
"v_accvgpr_write_b32 a8, v246\n"
"v_accvgpr_read_b32 v246, a9\n"
"v_ldexp_f32 v246, v246, v250\n"
"v_accvgpr_write_b32 a9, v246\n"
"v_accvgpr_read_b32 v246, a10\n"
"v_ldexp_f32 v246, v246, v250\n"
"v_accvgpr_write_b32 a10, v246\n"
"v_accvgpr_read_b32 v246, a11\n"
"v_ldexp_f32 v246, v246, v250\n"
"v_accvgpr_write_b32 a11, v246\n"
"v_accvgpr_read_b32 v246, a20\n"
"v_ldexp_f32 v246, v246, v250\n"
"v_accvgpr_write_b32 a20, v246\n"
"v_accvgpr_read_b32 v246, a21\n"
"v_ldexp_f32 v246, v246, v250\n"
"v_accvgpr_write_b32 a21, v246\n"
"v_accvgpr_read_b32 v246, a22\n"
"v_ldexp_f32 v246, v246, v250\n"
"v_accvgpr_write_b32 a22, v246\n"
"v_accvgpr_read_b32 v246, a23\n"
"v_ldexp_f32 v246, v246, v250\n"
"v_accvgpr_write_b32 a23, v246\n"
"v_accvgpr_read_b32 v246, a32\n"
"v_ldexp_f32 v246, v246, v250\n"
"v_accvgpr_write_b32 a32, v246\n"
"v_accvgpr_read_b32 v246, a33\n"
"v_ldexp_f32 v246, v246, v250\n"
"v_accvgpr_write_b32 a33, v246\n"
"v_accvgpr_read_b32 v246, a34\n"
"v_ldexp_f32 v246, v246, v250\n"
"v_accvgpr_write_b32 a34, v246\n"
"v_accvgpr_read_b32 v246, a35\n"
"v_ldexp_f32 v246, v246, v250\n"
"v_accvgpr_write_b32 a35, v246\n"
"v_accvgpr_read_b32 v246, a44\n"
"v_ldexp_f32 v246, v246, v250\n"
"v_accvgpr_write_b32 a44, v246\n"
"v_accvgpr_read_b32 v246, a45\n"
"v_ldexp_f32 v246, v246, v250\n"
"v_accvgpr_write_b32 a45, v246\n"
"v_accvgpr_read_b32 v246, a46\n"
"v_ldexp_f32 v246, v246, v250\n"
"v_accvgpr_write_b32 a46, v246\n"
"v_accvgpr_read_b32 v246, a47\n"
"v_ldexp_f32 v246, v246, v250\n"
"v_accvgpr_write_b32 a47, v246\n"
"v_accvgpr_read_b32 v246, a56\n"
"v_ldexp_f32 v246, v246, v250\n"
"v_accvgpr_write_b32 a56, v246\n"
"v_accvgpr_read_b32 v246, a57\n"
"v_ldexp_f32 v246, v246, v250\n"
"v_accvgpr_write_b32 a57, v246\n"
"v_accvgpr_read_b32 v246, a58\n"
"v_ldexp_f32 v246, v246, v250\n"
"v_accvgpr_write_b32 a58, v246\n"
"v_accvgpr_read_b32 v246, a59\n"
"v_ldexp_f32 v246, v246, v250\n"
"v_accvgpr_write_b32 a59, v246\n"
"v_accvgpr_read_b32 v246, a68\n"
"v_ldexp_f32 v246, v246, v250\n"
"v_accvgpr_write_b32 a68, v246\n"
"v_accvgpr_read_b32 v246, a69\n"
"v_ldexp_f32 v246, v246, v250\n"
"v_accvgpr_write_b32 a69, v246\n"
"v_accvgpr_read_b32 v246, a70\n"
"v_ldexp_f32 v246, v246, v250\n"
"v_accvgpr_write_b32 a70, v246\n"
"v_accvgpr_read_b32 v246, a71\n"
"v_ldexp_f32 v246, v246, v250\n"
"v_accvgpr_write_b32 a71, v246\n"
"v_accvgpr_read_b32 v246, a80\n"
"v_ldexp_f32 v246, v246, v250\n"
"v_accvgpr_write_b32 a80, v246\n"
"v_accvgpr_read_b32 v246, a81\n"
"v_ldexp_f32 v246, v246, v250\n"
"v_accvgpr_write_b32 a81, v246\n"
"v_accvgpr_read_b32 v246, a82\n"
"v_ldexp_f32 v246, v246, v250\n"
"v_accvgpr_write_b32 a82, v246\n"
"v_accvgpr_read_b32 v246, a83\n"
"v_ldexp_f32 v246, v246, v250\n"
"v_accvgpr_write_b32 a83, v246\n"
"v_accvgpr_read_b32 v246, a92\n"
"v_ldexp_f32 v246, v246, v250\n"
"v_accvgpr_write_b32 a92, v246\n"
"v_accvgpr_read_b32 v246, a93\n"
"v_ldexp_f32 v246, v246, v250\n"
"v_accvgpr_write_b32 a93, v246\n"
"v_accvgpr_read_b32 v246, a94\n"
"v_ldexp_f32 v246, v246, v250\n"
"v_accvgpr_write_b32 a94, v246\n"
"v_accvgpr_read_b32 v246, a95\n"
"v_ldexp_f32 v246, v246, v250\n"
"v_accvgpr_write_b32 a95, v246\n"
"v_accvgpr_read_b32 v246, a104\n"
"v_ldexp_f32 v246, v246, v250\n"
"v_accvgpr_write_b32 a104, v246\n"
"v_accvgpr_read_b32 v246, a105\n"
"v_ldexp_f32 v246, v246, v250\n"
"v_accvgpr_write_b32 a105, v246\n"
"v_accvgpr_read_b32 v246, a106\n"
"v_ldexp_f32 v246, v246, v250\n"
"v_accvgpr_write_b32 a106, v246\n"
"v_accvgpr_read_b32 v246, a107\n"
"v_ldexp_f32 v246, v246, v250\n"
"v_accvgpr_write_b32 a107, v246\n"
"v_accvgpr_read_b32 v246, a116\n"
"v_ldexp_f32 v246, v246, v250\n"
"v_accvgpr_write_b32 a116, v246\n"
"v_accvgpr_read_b32 v246, a117\n"
"v_ldexp_f32 v246, v246, v250\n"
"v_accvgpr_write_b32 a117, v246\n"
"v_accvgpr_read_b32 v246, a118\n"
"v_ldexp_f32 v246, v246, v250\n"
"v_accvgpr_write_b32 a118, v246\n"
"v_accvgpr_read_b32 v246, a119\n"
"v_ldexp_f32 v246, v246, v250\n"
"v_accvgpr_write_b32 a119, v246\n"
"v_accvgpr_read_b32 v246, a128\n"
"v_ldexp_f32 v246, v246, v250\n"
"v_accvgpr_write_b32 a128, v246\n"
"v_accvgpr_read_b32 v246, a129\n"
"v_ldexp_f32 v246, v246, v250\n"
"v_accvgpr_write_b32 a129, v246\n"
"v_accvgpr_read_b32 v246, a130\n"
"v_ldexp_f32 v246, v246, v250\n"
"v_accvgpr_write_b32 a130, v246\n"
"v_accvgpr_read_b32 v246, a131\n"
"v_ldexp_f32 v246, v246, v250\n"
"v_accvgpr_write_b32 a131, v246\n"
"v_accvgpr_read_b32 v246, a140\n"
"v_ldexp_f32 v246, v246, v250\n"
"v_accvgpr_write_b32 a140, v246\n"
"v_accvgpr_read_b32 v246, a141\n"
"v_ldexp_f32 v246, v246, v250\n"
"v_accvgpr_write_b32 a141, v246\n"
"v_accvgpr_read_b32 v246, a142\n"
"v_ldexp_f32 v246, v246, v250\n"
"v_accvgpr_write_b32 a142, v246\n"
"v_accvgpr_read_b32 v246, a143\n"
"v_ldexp_f32 v246, v246, v250\n"
"v_accvgpr_write_b32 a143, v246\n"
"v_accvgpr_read_b32 v246, a152\n"
"v_ldexp_f32 v246, v246, v250\n"
"v_accvgpr_write_b32 a152, v246\n"
"v_accvgpr_read_b32 v246, a153\n"
"v_ldexp_f32 v246, v246, v250\n"
"v_accvgpr_write_b32 a153, v246\n"
"v_accvgpr_read_b32 v246, a154\n"
"v_ldexp_f32 v246, v246, v250\n"
"v_accvgpr_write_b32 a154, v246\n"
"v_accvgpr_read_b32 v246, a155\n"
"v_ldexp_f32 v246, v246, v250\n"
"v_accvgpr_write_b32 a155, v246\n"
"v_accvgpr_read_b32 v246, a164\n"
"v_ldexp_f32 v246, v246, v250\n"
"v_accvgpr_write_b32 a164, v246\n"
"v_accvgpr_read_b32 v246, a165\n"
"v_ldexp_f32 v246, v246, v250\n"
"v_accvgpr_write_b32 a165, v246\n"
"v_accvgpr_read_b32 v246, a166\n"
"v_ldexp_f32 v246, v246, v250\n"
"v_accvgpr_write_b32 a166, v246\n"
"v_accvgpr_read_b32 v246, a167\n"
"v_ldexp_f32 v246, v246, v250\n"
"v_accvgpr_write_b32 a167, v246\n"
"v_accvgpr_read_b32 v246, a176\n"
"v_ldexp_f32 v246, v246, v250\n"
"v_accvgpr_write_b32 a176, v246\n"
"v_accvgpr_read_b32 v246, a177\n"
"v_ldexp_f32 v246, v246, v250\n"
"v_accvgpr_write_b32 a177, v246\n"
"v_accvgpr_read_b32 v246, a178\n"
"v_ldexp_f32 v246, v246, v250\n"
"v_accvgpr_write_b32 a178, v246\n"
"v_accvgpr_read_b32 v246, a179\n"
"v_ldexp_f32 v246, v246, v250\n"
"v_accvgpr_write_b32 a179, v246\n"
"v_accvgpr_read_b32 v246, a188\n"
"v_ldexp_f32 v246, v246, v250\n"
"v_accvgpr_write_b32 a188, v246\n"
"v_accvgpr_read_b32 v246, a189\n"
"v_ldexp_f32 v246, v246, v250\n"
"v_accvgpr_write_b32 a189, v246\n"
"v_accvgpr_read_b32 v246, a190\n"
"v_ldexp_f32 v246, v246, v250\n"
"v_accvgpr_write_b32 a190, v246\n"
"v_accvgpr_read_b32 v246, a191\n"
"v_ldexp_f32 v246, v246, v250\n"
"v_accvgpr_write_b32 a191, v246\n"
"v_ldexp_f32 v207, v207, v250\n"
"v_lshlrev_b32 v246, 16, v64\n"
"v_and_b32 v247, 0xffff0000, v64\n"
"v_ldexp_f32 v246, v246, v250\n"
"v_ldexp_f32 v247, v247, v250\n"
"v_cvt_pk_bf16_f32 v64, v246, v247\n"
"v_lshlrev_b32 v246, 16, v65\n"
"v_and_b32 v247, 0xffff0000, v65\n"
"v_ldexp_f32 v246, v246, v250\n"
"v_ldexp_f32 v247, v247, v250\n"
"v_cvt_pk_bf16_f32 v65, v246, v247\n"
"v_lshlrev_b32 v246, 16, v66\n"
"v_and_b32 v247, 0xffff0000, v66\n"
"v_ldexp_f32 v246, v246, v250\n"
"v_ldexp_f32 v247, v247, v250\n"
"v_cvt_pk_bf16_f32 v66, v246, v247\n"
"v_lshlrev_b32 v246, 16, v67\n"
"v_and_b32 v247, 0xffff0000, v67\n"
"v_ldexp_f32 v246, v246, v250\n"
"v_ldexp_f32 v247, v247, v250\n"
"v_cvt_pk_bf16_f32 v67, v246, v247\n"
"v_lshlrev_b32 v246, 16, v76\n"
"v_and_b32 v247, 0xffff0000, v76\n"
"v_ldexp_f32 v246, v246, v250\n"
"v_ldexp_f32 v247, v247, v250\n"
"v_cvt_pk_bf16_f32 v76, v246, v247\n"
"v_lshlrev_b32 v246, 16, v77\n"
"v_and_b32 v247, 0xffff0000, v77\n"
"v_ldexp_f32 v246, v246, v250\n"
"v_ldexp_f32 v247, v247, v250\n"
"v_cvt_pk_bf16_f32 v77, v246, v247\n"
"v_lshlrev_b32 v246, 16, v78\n"
"v_and_b32 v247, 0xffff0000, v78\n"
"v_ldexp_f32 v246, v246, v250\n"
"v_ldexp_f32 v247, v247, v250\n"
"v_cvt_pk_bf16_f32 v78, v246, v247\n"
"v_lshlrev_b32 v246, 16, v79\n"
"v_and_b32 v247, 0xffff0000, v79\n"
"v_ldexp_f32 v246, v246, v250\n"
"v_ldexp_f32 v247, v247, v250\n"
"v_cvt_pk_bf16_f32 v79, v246, v247\n"
"LRDONE3_%=:\n"
"s_mov_b32 s44, 0\n"
"s_nop 4\n"
"s_cmp_eq_u32 s45, 0\n"
"s_cbranch_scc1 LRET0_%=\n"
"s_cmp_eq_u32 s45, 1\n"
"s_cbranch_scc1 LRET1_%=\n"
"s_cmp_eq_u32 s45, 2\n"
"s_cbranch_scc1 LRET2_%=\n"
"s_cmp_eq_u32 s45, 3\n"
"s_cbranch_scc1 LRET3_%=\n"
"s_cmp_eq_u32 s45, 4\n"
"s_cbranch_scc1 LRET4_%=\n"
"s_cmp_eq_u32 s45, 5\n"
"s_cbranch_scc1 LRET5_%=\n"
"s_cmp_eq_u32 s45, 6\n"
"s_cbranch_scc1 LRET6_%=\n"
"s_cmp_eq_u32 s45, 7\n"
"s_cbranch_scc1 LRET7_%=\n"
"s_cmp_eq_u32 s45, 8\n"
"s_cbranch_scc1 LRET8_%=\n"
"s_cmp_eq_u32 s45, 9\n"
"s_cbranch_scc1 LRET9_%=\n"
"s_cmp_eq_u32 s45, 10\n"
"s_cbranch_scc1 LRET10_%=\n"
"s_cmp_eq_u32 s45, 11\n"
"s_cbranch_scc1 LRET11_%=\n"
"s_endpgm\n"
"LSTUB0_%=:\n"
"s_mov_b32 s45, 0\n"
"s_branch LRARE3_%=\n"
"LSTUB1_%=:\n"
"s_mov_b32 s45, 1\n"
"s_branch LRARE3_%=\n"
"LSTUB2_%=:\n"
"s_mov_b32 s45, 2\n"
"s_branch LRARE3_%=\n"
"LSTUB3_%=:\n"
"s_mov_b32 s45, 3\n"
"s_branch LRARE3_%=\n"
"LSTUB4_%=:\n"
"s_mov_b32 s45, 4\n"
"s_branch LRARE3_%=\n"
"LSTUB5_%=:\n"
"s_mov_b32 s45, 5\n"
"s_branch LRARE3_%=\n"
"LSTUB6_%=:\n"
"s_mov_b32 s45, 6\n"
"s_branch LRARE3_%=\n"
"LSTUB7_%=:\n"
"s_mov_b32 s45, 7\n"
"s_branch LRARE3_%=\n"
"LSTUB8_%=:\n"
"s_mov_b32 s45, 8\n"
"s_branch LRARE3_%=\n"
"LSTUB9_%=:\n"
"s_mov_b32 s45, 9\n"
"s_branch LRARE3_%=\n"
"LSTUB10_%=:\n"
"s_mov_b32 s45, 10\n"
"s_branch LRARE3_%=\n"
"LSTUB11_%=:\n"
"s_mov_b32 s45, 11\n"
"s_branch LRARE3_%=\n"
"LY5_%=:\n"
"s_sleep 1\n"
"s_sub_u32 s56, s56, 1\n"
"s_cmp_lg_u32 s56, 0\n"
"s_cbranch_scc1 LY5_%=\n"
"s_branch LZ5_%=\n"
"LY6_%=:\n"
"s_sleep 1\n"
"s_sub_u32 s56, s56, 1\n"
"s_cmp_lg_u32 s56, 0\n"
"s_cbranch_scc1 LY6_%=\n"
"s_branch LZ6_%=\n"
"LY7_%=:\n"
"s_sleep 1\n"
"s_sub_u32 s56, s56, 1\n"
"s_cmp_lg_u32 s56, 0\n"
"s_cbranch_scc1 LY7_%=\n"
"s_branch LZ7_%=\n"
"LY8_%=:\n"
"s_sleep 1\n"
"s_sub_u32 s56, s56, 1\n"
"s_cmp_lg_u32 s56, 0\n"
"s_cbranch_scc1 LY8_%=\n"
"s_branch LZ8_%=\n"
"LY9_%=:\n"
"s_sleep 1\n"
"s_sub_u32 s56, s56, 1\n"
"s_cmp_lg_u32 s56, 0\n"
"s_cbranch_scc1 LY9_%=\n"
"s_branch LZ9_%=\n"
"LY10_%=:\n"
"s_sleep 1\n"
"s_sub_u32 s56, s56, 1\n"
"s_cmp_lg_u32 s56, 0\n"
"s_cbranch_scc1 LY10_%=\n"
"s_branch LZ10_%=\n"
"LY11_%=:\n"
"s_sleep 1\n"
"s_sub_u32 s56, s56, 1\n"
"s_cmp_lg_u32 s56, 0\n"
"s_cbranch_scc1 LY11_%=\n"
"s_branch LZ11_%=\n"
"LY12_%=:\n"
"s_sleep 1\n"
"s_sub_u32 s56, s56, 1\n"
"s_cmp_lg_u32 s56, 0\n"
"s_cbranch_scc1 LY12_%=\n"
"s_branch LZ12_%=\n"
"LW3_%=:\n"
"s_lshl_b32 s40, %6, 12\n"
"s_add_u32 s40, s40, %7\n"
"s_lshl_b32 s48, %6, 13\n"
"s_add_u32 s48, s48, %7\n"
"s_mov_b32 s50, 0x3db8aa3b\n"
"s_mov_b32 s51, 0x3db8aa3b\n"
"v_mbcnt_lo_u32_b32 v240, -1, 0\n"
"v_mbcnt_hi_u32_b32 v240, -1, v240\n"
"v_and_b32 v241, 15, v240\n"
"v_lshrrev_b32 v242, 4, v240\n"
"v_bfe_u32 v243, v240, 2, 2\n"
"v_and_b32 v244, 3, v240\n"
"v_lshrrev_b32 v245, 5, v240\n"
"v_and_b32 v246, 31, v240\n"
"s_mul_i32 s46, %6, 48\n"
"v_lshrrev_b32 v247, 3, v240\n"
"v_and_b32 v248, 7, v240\n"
"v_xor_b32 v248, v248, v247\n"
"v_add_u32 v247, s46, v247\n"
"v_mul_u32_u24 v247, 896, v247\n"
"v_lshl_add_u32 v23, v248, 4, v247\n"
"v_add_u32 v24, 6144, v23\n"
"v_add_u32 v25, 12288, v23\n"
"v_add_u32 v26, 18432, v23\n"
"s_mul_i32 s49, %6, 12288\n"
"s_add_u32 s49, s49, 16384\n"
"s_add_u32 s49, s49, %7\n"
"s_mov_b32 s46, 0\n"
"s_add_u32 m0, s49, 0\n"
"s_nop 0\n"
"buffer_load_dwordx4 v23, %2, s46 offen offset:0 lds\n"
"buffer_load_dwordx4 v24, %2, s46 offen offset:1024 lds\n"
"buffer_load_dwordx4 v25, %2, s46 offen offset:2048 lds\n"
"buffer_load_dwordx4 v26, %2, s46 offen offset:3072 lds\n"
"s_lshl_b32 s46, %6, 3\n"
"v_add_u32 v247, s46, v245\n"
"v_add_u32 v247, 0, v247\n"
"v_bfe_u32 v248, v247, 2, 2\n"
"v_lshlrev_b32 v248, 3, v248\n"
"v_lshrrev_b32 v249, 4, v247\n"
"v_lshl_add_u32 v248, v249, 2, v248\n"
"v_and_b32 v249, 3, v247\n"
"v_add_u32 v248, v248, v249\n"
"v_lshlrev_b32 v248, 13, v248\n"
"v_and_b32 v247, 7, v247\n"
"v_lshlrev_b32 v247, 1, v247\n"
"v_xor_b32 v247, v247, v246\n"
"v_lshl_add_u32 v248, v247, 4, v248\n"
"v_subrev_u32 v8, 0, v248\n"
"v_add_u32 v247, s46, v245\n"
"v_add_u32 v247, 2, v247\n"
"v_bfe_u32 v248, v247, 2, 2\n"
"v_lshlrev_b32 v248, 3, v248\n"
"v_lshrrev_b32 v249, 4, v247\n"
"v_lshl_add_u32 v248, v249, 2, v248\n"
"v_and_b32 v249, 3, v247\n"
"v_add_u32 v248, v248, v249\n"
"v_lshlrev_b32 v248, 13, v248\n"
"v_and_b32 v247, 7, v247\n"
"v_lshlrev_b32 v247, 1, v247\n"
"v_xor_b32 v247, v247, v246\n"
"v_lshl_add_u32 v248, v247, 4, v248\n"
"v_subrev_u32 v9, 1024, v248\n"
"v_add_u32 v247, s46, v245\n"
"v_add_u32 v247, 4, v247\n"
"v_bfe_u32 v248, v247, 2, 2\n"
"v_lshlrev_b32 v248, 3, v248\n"
"v_lshrrev_b32 v249, 4, v247\n"
"v_lshl_add_u32 v248, v249, 2, v248\n"
"v_and_b32 v249, 3, v247\n"
"v_add_u32 v248, v248, v249\n"
"v_lshlrev_b32 v248, 13, v248\n"
"v_and_b32 v247, 7, v247\n"
"v_lshlrev_b32 v247, 1, v247\n"
"v_xor_b32 v247, v247, v246\n"
"v_lshl_add_u32 v248, v247, 4, v248\n"
"v_subrev_u32 v10, 2048, v248\n"
"v_add_u32 v247, s46, v245\n"
"v_add_u32 v247, 6, v247\n"
"v_bfe_u32 v248, v247, 2, 2\n"
"v_lshlrev_b32 v248, 3, v248\n"
"v_lshrrev_b32 v249, 4, v247\n"
"v_lshl_add_u32 v248, v249, 2, v248\n"
"v_and_b32 v249, 3, v247\n"
"v_add_u32 v248, v248, v249\n"
"v_lshlrev_b32 v248, 13, v248\n"
"v_and_b32 v247, 7, v247\n"
"v_lshlrev_b32 v247, 1, v247\n"
"v_xor_b32 v247, v247, v246\n"
"v_lshl_add_u32 v248, v247, 4, v248\n"
"v_subrev_u32 v11, 3072, v248\n"
"s_mov_b32 s46, 0\n"
"s_add_u32 m0, s40, 65536\n"
"s_nop 0\n"
"buffer_load_dwordx4 v8, %3, s46 offen offset:0 lds\n"
"buffer_load_dwordx4 v9, %3, s46 offen offset:1024 lds\n"
"buffer_load_dwordx4 v10, %3, s46 offen offset:2048 lds\n"
"buffer_load_dwordx4 v11, %3, s46 offen offset:3072 lds\n"
"s_mov_b32 s46, 128\n"
"s_add_u32 m0, s49, 6144\n"
"s_nop 0\n"
"buffer_load_dwordx4 v23, %2, s46 offen offset:0 lds\n"
"buffer_load_dwordx4 v24, %2, s46 offen offset:1024 lds\n"
"buffer_load_dwordx4 v25, %2, s46 offen offset:2048 lds\n"
"buffer_load_dwordx4 v26, %2, s46 offen offset:3072 lds\n"
"s_mov_b32 s46, 262144\n"
"s_add_u32 m0, s40, 81920\n"
"s_nop 0\n"
"buffer_load_dwordx4 v8, %3, s46 offen offset:0 lds\n"
"buffer_load_dwordx4 v9, %3, s46 offen offset:1024 lds\n"
"buffer_load_dwordx4 v10, %3, s46 offen offset:2048 lds\n"
"buffer_load_dwordx4 v11, %3, s46 offen offset:3072 lds\n"
"v_lshl_add_u32 v249, v242, 2, v243\n"
"v_and_b32 v248, 7, v249\n"
"v_lshrrev_b32 v247, 1, v244\n"
"v_add_u32 v247, 0, v247\n"
"v_xor_b32 v247, v247, v248\n"
"v_lshlrev_b32 v247, 5, v247\n"
"v_lshl_add_u32 v247, v249, 9, v247\n"
"v_and_b32 v0, 1, v244\n"
"v_lshl_add_u32 v247, v0, 4, v247\n"
"v_add_u32 v247, %7, v247\n"
"v_add_u32 v0, 0x10000, v247\n"
"v_add_u32 v4, 0x20000, v247\n"
"v_lshrrev_b32 v247, 1, v244\n"
"v_add_u32 v247, 2, v247\n"
"v_xor_b32 v247, v247, v248\n"
"v_lshlrev_b32 v247, 5, v247\n"
"v_lshl_add_u32 v247, v249, 9, v247\n"
"v_and_b32 v1, 1, v244\n"
"v_lshl_add_u32 v247, v1, 4, v247\n"
"v_add_u32 v247, %7, v247\n"
"v_add_u32 v1, 0x10000, v247\n"
"v_add_u32 v5, 0x20000, v247\n"
"v_lshrrev_b32 v247, 1, v244\n"
"v_add_u32 v247, 4, v247\n"
"v_xor_b32 v247, v247, v248\n"
"v_lshlrev_b32 v247, 5, v247\n"
"v_lshl_add_u32 v247, v249, 9, v247\n"
"v_and_b32 v2, 1, v244\n"
"v_lshl_add_u32 v247, v2, 4, v247\n"
"v_add_u32 v247, %7, v247\n"
"v_add_u32 v2, 0x10000, v247\n"
"v_add_u32 v6, 0x20000, v247\n"
"v_lshrrev_b32 v247, 1, v244\n"
"v_add_u32 v247, 6, v247\n"
"v_xor_b32 v247, v247, v248\n"
"v_lshlrev_b32 v247, 5, v247\n"
"v_lshl_add_u32 v247, v249, 9, v247\n"
"v_and_b32 v3, 1, v244\n"
"v_lshl_add_u32 v247, v3, 4, v247\n"
"v_add_u32 v247, %7, v247\n"
"v_add_u32 v3, 0x10000, v247\n"
"v_add_u32 v7, 0x20000, v247\n"
"v_and_b32 v248, 7, v241\n"
"v_add_u32 v247, 0, v242\n"
"v_xor_b32 v247, v247, v248\n"
"v_lshlrev_b32 v247, 4, v247\n"
"v_lshl_add_u32 v247, v241, 7, v247\n"
"v_add_u32 v21, s49, v247\n"
"v_add_u32 v247, 4, v242\n"
"v_xor_b32 v247, v247, v248\n"
"v_lshlrev_b32 v247, 4, v247\n"
"v_lshl_add_u32 v247, v241, 7, v247\n"
"v_add_u32 v22, s49, v247\n"
"s_mov_b32 s46, 524288\n"
"s_add_u32 m0, s40, 98304\n"
"s_nop 0\n"
"buffer_load_dwordx4 v8, %3, s46 offen offset:0 lds\n"
"buffer_load_dwordx4 v9, %3, s46 offen offset:1024 lds\n"
"buffer_load_dwordx4 v10, %3, s46 offen offset:2048 lds\n"
"buffer_load_dwordx4 v11, %3, s46 offen offset:3072 lds\n"
"s_lshl_b32 s46, %6, 3\n"
"s_lshl_b32 s47, %6, 12\n"
"v_lshlrev_b32 v249, 9, v245\n"
"v_add_u32 v249, s47, v249\n"
"v_add_u32 v247, s46, v245\n"
"v_add_u32 v247, 0, v247\n"
"v_and_b32 v247, 15, v247\n"
"v_xor_b32 v247, v247, v246\n"
"v_lshl_add_u32 v12, v247, 4, v249\n"
"v_add_u32 v247, s46, v245\n"
"v_add_u32 v247, 2, v247\n"
"v_and_b32 v247, 15, v247\n"
"v_xor_b32 v247, v247, v246\n"
"v_lshl_add_u32 v13, v247, 4, v249\n"
"v_add_u32 v247, s46, v245\n"
"v_add_u32 v247, 4, v247\n"
"v_and_b32 v247, 15, v247\n"
"v_xor_b32 v247, v247, v246\n"
"v_lshl_add_u32 v14, v247, 4, v249\n"
"v_add_u32 v247, s46, v245\n"
"v_add_u32 v247, 6, v247\n"
"v_and_b32 v247, 15, v247\n"
"v_xor_b32 v247, v247, v246\n"
"v_lshl_add_u32 v15, v247, 4, v249\n"
"v_add_u32 v247, 0, v245\n"
"v_and_b32 v247, 7, v247\n"
"v_lshlrev_b32 v247, 1, v247\n"
"v_xor_b32 v247, v247, v246\n"
"v_lshl_add_u32 v16, v247, 4, v249\n"
"v_add_u32 v247, 2, v245\n"
"v_and_b32 v247, 7, v247\n"
"v_lshlrev_b32 v247, 1, v247\n"
"v_xor_b32 v247, v247, v246\n"
"v_lshl_add_u32 v17, v247, 4, v249\n"
"v_add_u32 v247, 4, v245\n"
"v_and_b32 v247, 7, v247\n"
"v_lshlrev_b32 v247, 1, v247\n"
"v_xor_b32 v247, v247, v246\n"
"v_lshl_add_u32 v18, v247, 4, v249\n"
"v_add_u32 v247, 6, v245\n"
"v_and_b32 v247, 7, v247\n"
"v_lshlrev_b32 v247, 1, v247\n"
"v_xor_b32 v247, v247, v246\n"
"v_lshl_add_u32 v19, v247, 4, v249\n"
"s_mov_b32 s46, 786432\n"
"s_add_u32 m0, s40, 114688\n"
"s_nop 0\n"
"buffer_load_dwordx4 v8, %3, s46 offen offset:0 lds\n"
"buffer_load_dwordx4 v9, %3, s46 offen offset:1024 lds\n"
"buffer_load_dwordx4 v10, %3, s46 offen offset:2048 lds\n"
"buffer_load_dwordx4 v11, %3, s46 offen offset:3072 lds\n"
"s_getpc_b64 s[88:89]\n"
"LPCA2_%=:\n"
"s_sub_u32 s88, s88, LPCA2_%=-LC0_%=\n"
"s_subb_u32 s89, s89, 0\n"
"s_and_b32 s89, s89, 0xffff\n"
"s_mov_b32 s90, LEND_%=-LC0_%=+2560\n"
"s_mov_b32 s91, 0x20000\n"
"v_lshlrev_b32 v27, 6, v240\n"
"v_add_u32 v27, %11, v27\n"
"v_lshlrev_b32 v29, 7, v240\n"
"v_add_u32 v29, %12, v29\n"
"s_waitcnt vmcnt(20)\n"
"ds_read_b128 a[0:3], v21 offset:0\n"
"ds_read_b128 a[4:7], v21 offset:2048\n"
"ds_read_b128 a[12:15], v22 offset:0\n"
"ds_read_b128 a[16:19], v22 offset:2048\n"
"s_waitcnt vmcnt(16) lgkmcnt(0)\n"
"s_barrier\n"
"s_waitcnt lgkmcnt(0)\n"
"ds_read_b64_tr_b16 v[32:33], v0 offset:0\n"
"ds_read_b64_tr_b16 v[34:35], v0 offset:8192\n"
"ds_read_b64_tr_b16 v[36:37], v0 offset:8\n"
"ds_read_b64_tr_b16 v[38:39], v0 offset:8200\n"
"ds_read_b64_tr_b16 v[40:41], v1 offset:0\n"
"ds_read_b64_tr_b16 v[42:43], v1 offset:8192\n"
"ds_read_b64_tr_b16 v[44:45], v1 offset:8\n"
"ds_read_b64_tr_b16 v[46:47], v1 offset:8200\n"
"s_waitcnt lgkmcnt(6)\n"
"v_mfma_f32_16x16x32_bf16 v[64:67], v[32:35], a[0:3], 0\n"
"ds_read_b64_tr_b16 v[48:49], v2 offset:0\n"
"ds_read_b64_tr_b16 v[50:51], v2 offset:8192\n"
"v_mfma_f32_16x16x32_bf16 v[68:71], v[32:35], a[4:7], 0\n"
"s_mov_b32 s46, 256\n"
"s_add_u32 m0, s49, 0\n"
"buffer_load_dwordx4 v23, %2, s46 offen offset:0 lds\n"
"ds_read_b64_tr_b16 v[52:53], v2 offset:8\n"
"s_waitcnt lgkmcnt(7)\n"
"v_mfma_f32_16x16x32_bf16 v[76:79], v[36:39], a[0:3], 0\n"
"ds_read_b64_tr_b16 v[54:55], v2 offset:8200\n"
"v_mfma_f32_16x16x32_bf16 v[80:83], v[36:39], a[4:7], 0\n"
"buffer_load_dwordx4 v24, %2, s46 offen offset:1024 lds\n"
"ds_read_b64_tr_b16 v[56:57], v3 offset:0\n"
"ds_read_b64_tr_b16 v[58:59], v3 offset:8192\n"
"s_waitcnt lgkmcnt(5)\n"
"v_mfma_f32_16x16x32_bf16 v[88:91], v[40:43], a[0:3], 0\n"
"v_mfma_f32_16x16x32_bf16 v[92:95], v[40:43], a[4:7], 0\n"
"ds_read_b64_tr_b16 v[60:61], v3 offset:8\n"
"ds_read_b64_tr_b16 v[62:63], v3 offset:8200\n"
"buffer_load_dwordx4 v25, %2, s46 offen offset:2048 lds\n"
"v_mfma_f32_16x16x32_bf16 v[100:103], v[44:47], a[0:3], 0\n"
"v_mfma_f32_16x16x32_bf16 v[104:107], v[44:47], a[4:7], 0\n"
"ds_read_b64_tr_b16 v[32:33], v0 offset:256\n"
"ds_read_b64_tr_b16 v[34:35], v0 offset:8448\n"
"buffer_load_dwordx4 v26, %2, s46 offen offset:3072 lds\n"
"v_accvgpr_write_b32 a168, 0\n"
"s_waitcnt lgkmcnt(5)\n"
"v_mfma_f32_16x16x32_bf16 v[112:115], v[48:51], a[0:3], 0\n"
"v_accvgpr_write_b32 a169, 0\n"
"v_accvgpr_write_b32 a170, 0\n"
"v_mfma_f32_16x16x32_bf16 v[116:119], v[48:51], a[4:7], 0\n"
"ds_read_b64_tr_b16 v[36:37], v0 offset:264\n"
"ds_read_b64_tr_b16 v[38:39], v0 offset:8456\n"
"s_mov_b32 s46, 1048576\n"
"s_add_u32 m0, s40, 131072\n"
"v_mfma_f32_16x16x32_bf16 v[124:127], v[52:55], a[0:3], 0\n"
"buffer_load_dwordx4 v8, %3, s46 offen offset:0 lds\n"
"v_mfma_f32_16x16x32_bf16 v[224:227], v[52:55], a[4:7], 0\n"
"ds_read_b64_tr_b16 v[40:41], v1 offset:256\n"
"ds_read_b64_tr_b16 v[42:43], v1 offset:8448\n"
"s_waitcnt lgkmcnt(4)\n"
"v_mfma_f32_16x16x32_bf16 v[232:235], v[56:59], a[0:3], 0\n"
"buffer_load_dwordx4 v9, %3, s46 offen offset:1024 lds\n"
"v_mfma_f32_16x16x32_bf16 v[236:239], v[56:59], a[4:7], 0\n"
"ds_read_b64_tr_b16 v[44:45], v1 offset:264\n"
"ds_read_b64_tr_b16 v[46:47], v1 offset:8456\n"
"v_mfma_f32_16x16x32_bf16 v[244:247], v[60:63], a[0:3], 0\n"
"v_mfma_f32_16x16x32_bf16 v[248:251], v[60:63], a[4:7], 0\n"
"buffer_load_dwordx4 v10, %3, s46 offen offset:2048 lds\n"
"ds_read_b64_tr_b16 v[48:49], v2 offset:256\n"
"ds_read_b64_tr_b16 v[50:51], v2 offset:8448\n"
"v_mfma_f32_16x16x32_bf16 v[128:131], v[32:35], a[0:3], 0\n"
"v_mfma_f32_16x16x32_bf16 v[132:135], v[32:35], a[4:7], 0\n"
"ds_read_b64_tr_b16 v[52:53], v2 offset:264\n"
"ds_read_b64_tr_b16 v[54:55], v2 offset:8456\n"
"buffer_load_dwordx4 v11, %3, s46 offen offset:3072 lds\n"
"s_waitcnt lgkmcnt(6)\n"
"v_mfma_f32_16x16x32_bf16 v[140:143], v[36:39], a[0:3], 0\n"
"v_mfma_f32_16x16x32_bf16 v[144:147], v[36:39], a[4:7], 0\n"
"ds_read_b64_tr_b16 v[56:57], v3 offset:256\n"
"ds_read_b64_tr_b16 v[58:59], v3 offset:8448\n"
"ds_read_b64_tr_b16 v[60:61], v3 offset:264\n"
"ds_read_b64_tr_b16 v[62:63], v3 offset:8456\n"
"v_mfma_f32_16x16x32_bf16 v[152:155], v[40:43], a[0:3], 0\n"
"s_mov_b32 s46, 1310720\n"
"s_add_u32 m0, s40, 147456\n"
"v_mfma_f32_16x16x32_bf16 v[156:159], v[40:43], a[4:7], 0\n"
"buffer_load_dwordx4 v8, %3, s46 offen offset:0 lds\n"
"s_waitcnt lgkmcnt(4)\n"
"v_mfma_f32_16x16x32_bf16 v[164:167], v[44:47], a[0:3], 0\n"
"buffer_load_dwordx4 v9, %3, s46 offen offset:1024 lds\n"
"v_mfma_f32_16x16x32_bf16 v[168:171], v[44:47], a[4:7], 0\n"
"v_mfma_f32_16x16x32_bf16 v[176:179], v[48:51], a[0:3], 0\n"
"buffer_load_dwordx4 v10, %3, s46 offen offset:2048 lds\n"
"v_mfma_f32_16x16x32_bf16 v[180:183], v[48:51], a[4:7], 0\n"
"s_waitcnt vmcnt(19) lgkmcnt(0)\n"
"s_barrier\n"
"v_mfma_f32_16x16x32_bf16 v[188:191], v[52:55], a[0:3], 0\n"
"ds_read_b128 a[24:27], v21 offset:6144\n"
"ds_read_b128 a[28:31], v21 offset:8192\n"
"v_mfma_f32_16x16x32_bf16 v[192:195], v[52:55], a[4:7], 0\n"
"buffer_load_dwordx4 v11, %3, s46 offen offset:3072 lds\n"
"ds_read_b128 a[36:39], v22 offset:6144\n"
"ds_read_b128 a[40:43], v22 offset:8192\n"
"ds_read_b64_tr_b16 v[32:33], v0 offset:16384\n"
"v_mfma_f32_16x16x32_bf16 v[200:203], v[56:59], a[0:3], 0\n"
"ds_read_b64_tr_b16 v[34:35], v0 offset:24576\n"
"ds_read_b64_tr_b16 v[36:37], v0 offset:16392\n"
"v_mfma_f32_16x16x32_bf16 v[204:207], v[56:59], a[4:7], 0\n"
"ds_read_b64_tr_b16 v[38:39], v0 offset:24584\n"
"ds_read_b64_tr_b16 v[40:41], v1 offset:16384\n"
"ds_read_b64_tr_b16 v[42:43], v1 offset:24576\n"
"ds_read_b64_tr_b16 v[44:45], v1 offset:16392\n"
"v_mfma_f32_16x16x32_bf16 v[212:215], v[60:63], a[0:3], 0\n"
"ds_read_b64_tr_b16 v[46:47], v1 offset:24584\n"
"v_mfma_f32_16x16x32_bf16 v[216:219], v[60:63], a[4:7], 0\n"
"s_waitcnt lgkmcnt(6)\n"
"v_mfma_f32_16x16x32_bf16 v[64:67], v[32:35], a[12:15], v[64:67]\n"
"ds_read_b64_tr_b16 v[48:49], v2 offset:16384\n"
"ds_read_b64_tr_b16 v[50:51], v2 offset:24576\n"
"v_mfma_f32_16x16x32_bf16 v[68:71], v[32:35], a[16:19], v[68:71]\n"
"s_mov_b32 s46, 1572864\n"
"s_add_u32 m0, s40, 65536\n"
"buffer_load_dwordx4 v8, %3, s46 offen offset:0 lds\n"
"ds_read_b64_tr_b16 v[52:53], v2 offset:16392\n"
"s_waitcnt lgkmcnt(5)\n"
"v_mfma_f32_16x16x32_bf16 v[76:79], v[36:39], a[12:15], v[76:79]\n"
"ds_read_b64_tr_b16 v[54:55], v2 offset:24584\n"
"v_mfma_f32_16x16x32_bf16 v[80:83], v[36:39], a[16:19], v[80:83]\n"
"ds_read_b64_tr_b16 v[56:57], v3 offset:16384\n"
"ds_read_b64_tr_b16 v[58:59], v3 offset:24576\n"
"v_mfma_f32_16x16x32_bf16 v[88:91], v[40:43], a[12:15], v[88:91]\n"
"v_mfma_f32_16x16x32_bf16 v[92:95], v[40:43], a[16:19], v[92:95]\n"
"ds_read_b64_tr_b16 v[60:61], v3 offset:16392\n"
"ds_read_b64_tr_b16 v[62:63], v3 offset:24584\n"
"s_waitcnt lgkmcnt(6)\n"
"v_mfma_f32_16x16x32_bf16 v[100:103], v[44:47], a[12:15], v[100:103]\n"
"v_mfma_f32_16x16x32_bf16 v[104:107], v[44:47], a[16:19], v[104:107]\n"
"ds_read_b64_tr_b16 v[32:33], v0 offset:16640\n"
"ds_read_b64_tr_b16 v[34:35], v0 offset:24832\n"
"buffer_load_dwordx4 v9, %3, s46 offen offset:1024 lds\n"
"v_accvgpr_write_b32 a0, 0\n"
"v_mfma_f32_16x16x32_bf16 v[112:115], v[48:51], a[12:15], v[112:115]\n"
"v_accvgpr_write_b32 a1, 0\n"
"v_accvgpr_write_b32 a2, 0\n"
"v_mfma_f32_16x16x32_bf16 v[116:119], v[48:51], a[16:19], v[116:119]\n"
"ds_read_b64_tr_b16 v[36:37], v0 offset:16648\n"
"ds_read_b64_tr_b16 v[38:39], v0 offset:24840\n"
"v_accvgpr_write_b32 a3, 0\n"
"v_accvgpr_write_b32 a4, 0\n"
"s_waitcnt lgkmcnt(4)\n"
"v_mfma_f32_16x16x32_bf16 v[124:127], v[52:55], a[12:15], v[124:127]\n"
"v_accvgpr_write_b32 a5, 0\n"
"v_accvgpr_write_b32 a6, 0\n"
"v_mfma_f32_16x16x32_bf16 v[224:227], v[52:55], a[16:19], v[224:227]\n"
"ds_read_b64_tr_b16 v[40:41], v1 offset:16640\n"
"ds_read_b64_tr_b16 v[42:43], v1 offset:24832\n"
"v_accvgpr_write_b32 a7, 0\n"
"v_accvgpr_write_b32 a8, 0\n"
"v_mfma_f32_16x16x32_bf16 v[232:235], v[56:59], a[12:15], v[232:235]\n"
"v_accvgpr_write_b32 a9, 0\n"
"v_accvgpr_write_b32 a10, 0\n"
"v_mfma_f32_16x16x32_bf16 v[236:239], v[56:59], a[16:19], v[236:239]\n"
"ds_read_b64_tr_b16 v[44:45], v1 offset:16648\n"
"ds_read_b64_tr_b16 v[46:47], v1 offset:24840\n"
"v_accvgpr_write_b32 a11, 0\n"
"v_accvgpr_write_b32 a171, 0\n"
"v_mfma_f32_16x16x32_bf16 v[244:247], v[60:63], a[12:15], v[244:247]\n"
"v_accvgpr_write_b32 a172, 0\n"
"v_accvgpr_write_b32 a173, 0\n"
"v_mfma_f32_16x16x32_bf16 v[248:251], v[60:63], a[16:19], v[248:251]\n"
"buffer_load_dwordx4 v10, %3, s46 offen offset:2048 lds\n"
"ds_read_b64_tr_b16 v[48:49], v2 offset:16640\n"
"ds_read_b64_tr_b16 v[50:51], v2 offset:24832\n"
"s_waitcnt lgkmcnt(4)\n"
"v_mfma_f32_16x16x32_bf16 v[128:131], v[32:35], a[12:15], v[128:131]\n"
"v_mfma_f32_16x16x32_bf16 v[132:135], v[32:35], a[16:19], v[132:135]\n"
"ds_read_b64_tr_b16 v[52:53], v2 offset:16648\n"
"ds_read_b64_tr_b16 v[54:55], v2 offset:24840\n"
"v_mfma_f32_16x16x32_bf16 v[140:143], v[36:39], a[12:15], v[140:143]\n"
"v_mfma_f32_16x16x32_bf16 v[144:147], v[36:39], a[16:19], v[144:147]\n"
"ds_read_b64_tr_b16 v[56:57], v3 offset:16640\n"
"ds_read_b64_tr_b16 v[58:59], v3 offset:24832\n"
"ds_read_b64_tr_b16 v[60:61], v3 offset:16648\n"
"ds_read_b64_tr_b16 v[62:63], v3 offset:24840\n"
"v_mfma_f32_16x16x32_bf16 v[152:155], v[40:43], a[12:15], v[152:155]\n"
"v_mfma_f32_16x16x32_bf16 v[156:159], v[40:43], a[16:19], v[156:159]\n"
"s_waitcnt lgkmcnt(6)\n"
"v_mfma_f32_16x16x32_bf16 v[164:167], v[44:47], a[12:15], v[164:167]\n"
"buffer_load_dwordx4 v11, %3, s46 offen offset:3072 lds\n"
"v_mfma_f32_16x16x32_bf16 v[168:171], v[44:47], a[16:19], v[168:171]\n"
"v_mfma_f32_16x16x32_bf16 v[176:179], v[48:51], a[12:15], v[176:179]\n"
"v_mfma_f32_16x16x32_bf16 v[180:183], v[48:51], a[16:19], v[180:183]\n"
"s_waitcnt vmcnt(20) lgkmcnt(0)\n"
"s_barrier\n"
"v_mfma_f32_16x16x32_bf16 v[188:191], v[52:55], a[12:15], v[188:191]\n"
"ds_read_b64_tr_b16 v[32:33], v0 offset:32768\n"
"ds_read_b64_tr_b16 v[34:35], v0 offset:40960\n"
"v_mfma_f32_16x16x32_bf16 v[192:195], v[52:55], a[16:19], v[192:195]\n"
"ds_read_b64_tr_b16 v[36:37], v0 offset:32776\n"
"ds_read_b64_tr_b16 v[38:39], v0 offset:40968\n"
"ds_read_b64_tr_b16 v[40:41], v1 offset:32768\n"
"ds_read_b64_tr_b16 v[42:43], v1 offset:40960\n"
"v_mfma_f32_16x16x32_bf16 v[200:203], v[56:59], a[12:15], v[200:203]\n"
"ds_read_b64_tr_b16 v[44:45], v1 offset:32776\n"
"ds_read_b64_tr_b16 v[46:47], v1 offset:40968\n"
"v_mfma_f32_16x16x32_bf16 v[204:207], v[56:59], a[16:19], v[204:207]\n"
"v_mfma_f32_16x16x32_bf16 v[212:215], v[60:63], a[12:15], v[212:215]\n"
"v_mfma_f32_16x16x32_bf16 v[216:219], v[60:63], a[16:19], v[216:219]\n"
"s_waitcnt lgkmcnt(6)\n"
"v_mfma_f32_16x16x32_bf16 v[64:67], v[32:35], a[24:27], v[64:67]\n"
"ds_read_b64_tr_b16 v[48:49], v2 offset:32768\n"
"ds_read_b64_tr_b16 v[50:51], v2 offset:40960\n"
"v_mfma_f32_16x16x32_bf16 v[68:71], v[32:35], a[28:31], v[68:71]\n"
"s_mov_b32 s46, 384\n"
"s_add_u32 m0, s49, 6144\n"
"buffer_load_dwordx4 v23, %2, s46 offen offset:0 lds\n"
"ds_read_b64_tr_b16 v[52:53], v2 offset:32776\n"
"s_waitcnt lgkmcnt(3)\n"
"v_mfma_f32_16x16x32_bf16 v[76:79], v[36:39], a[24:27], v[76:79]\n"
"ds_read_b64_tr_b16 v[54:55], v2 offset:40968\n"
"v_mfma_f32_16x16x32_bf16 v[80:83], v[36:39], a[28:31], v[80:83]\n"
"ds_read_b64_tr_b16 v[56:57], v3 offset:32768\n"
"ds_read_b64_tr_b16 v[58:59], v3 offset:40960\n"
"v_mfma_f32_16x16x32_bf16 v[88:91], v[40:43], a[24:27], v[88:91]\n"
"buffer_load_dwordx4 v24, %2, s46 offen offset:1024 lds\n"
"v_mfma_f32_16x16x32_bf16 v[92:95], v[40:43], a[28:31], v[92:95]\n"
"ds_read_b64_tr_b16 v[60:61], v3 offset:32776\n"
"ds_read_b64_tr_b16 v[62:63], v3 offset:40968\n"
"v_mfma_f32_16x16x32_bf16 v[100:103], v[44:47], a[24:27], v[100:103]\n"
"v_mfma_f32_16x16x32_bf16 v[104:107], v[44:47], a[28:31], v[104:107]\n"
"ds_read_b64_tr_b16 v[32:33], v0 offset:33024\n"
"ds_read_b64_tr_b16 v[34:35], v0 offset:41216\n"
"buffer_load_dwordx4 v25, %2, s46 offen offset:2048 lds\n"
"v_accvgpr_write_b32 a12, 0\n"
"s_waitcnt lgkmcnt(5)\n"
"v_mfma_f32_16x16x32_bf16 v[112:115], v[48:51], a[24:27], v[112:115]\n"
"v_accvgpr_write_b32 a13, 0\n"
"v_accvgpr_write_b32 a14, 0\n"
"v_mfma_f32_16x16x32_bf16 v[116:119], v[48:51], a[28:31], v[116:119]\n"
"ds_read_b64_tr_b16 v[36:37], v0 offset:33032\n"
"ds_read_b64_tr_b16 v[38:39], v0 offset:41224\n"
"v_accvgpr_write_b32 a15, 0\n"
"v_accvgpr_write_b32 a16, 0\n"
"v_mfma_f32_16x16x32_bf16 v[124:127], v[52:55], a[24:27], v[124:127]\n"
"v_accvgpr_write_b32 a17, 0\n"
"v_accvgpr_write_b32 a18, 0\n"
"v_mfma_f32_16x16x32_bf16 v[224:227], v[52:55], a[28:31], v[224:227]\n"
"ds_read_b64_tr_b16 v[40:41], v1 offset:33024\n"
"ds_read_b64_tr_b16 v[42:43], v1 offset:41216\n"
"buffer_load_dwordx4 v26, %2, s46 offen offset:3072 lds\n"
"v_accvgpr_write_b32 a19, 0\n"
"s_waitcnt lgkmcnt(4)\n"
"v_mfma_f32_16x16x32_bf16 v[232:235], v[56:59], a[24:27], v[232:235]\n"
"v_accvgpr_write_b32 a20, 0\n"
"v_accvgpr_write_b32 a21, 0\n"
"v_mfma_f32_16x16x32_bf16 v[236:239], v[56:59], a[28:31], v[236:239]\n"
"ds_read_b64_tr_b16 v[44:45], v1 offset:33032\n"
"ds_read_b64_tr_b16 v[46:47], v1 offset:41224\n"
"v_accvgpr_write_b32 a22, 0\n"
"v_accvgpr_write_b32 a23, 0\n"
"v_mfma_f32_16x16x32_bf16 v[244:247], v[60:63], a[24:27], v[244:247]\n"
"s_mov_b32 s46, 1835008\n"
"s_add_u32 m0, s40, 81920\n"
"v_mfma_f32_16x16x32_bf16 v[248:251], v[60:63], a[28:31], v[248:251]\n"
"buffer_load_dwordx4 v8, %3, s46 offen offset:0 lds\n"
"ds_read_b64_tr_b16 v[48:49], v2 offset:33024\n"
"ds_read_b64_tr_b16 v[50:51], v2 offset:41216\n"
"v_accvgpr_write_b32 a174, 0\n"
"v_mfma_f32_16x16x32_bf16 v[128:131], v[32:35], a[24:27], v[128:131]\n"
"v_accvgpr_write_b32 a175, 0\n"
"v_accvgpr_write_b32 a176, 0\n"
"v_mfma_f32_16x16x32_bf16 v[132:135], v[32:35], a[28:31], v[132:135]\n"
"ds_read_b64_tr_b16 v[52:53], v2 offset:33032\n"
"ds_read_b64_tr_b16 v[54:55], v2 offset:41224\n"
"s_waitcnt lgkmcnt(4)\n"
"v_mfma_f32_16x16x32_bf16 v[140:143], v[36:39], a[24:27], v[140:143]\n"
"buffer_load_dwordx4 v9, %3, s46 offen offset:1024 lds\n"
"v_mfma_f32_16x16x32_bf16 v[144:147], v[36:39], a[28:31], v[144:147]\n"
"ds_read_b64_tr_b16 v[56:57], v3 offset:33024\n"
"ds_read_b64_tr_b16 v[58:59], v3 offset:41216\n"
"ds_read_b64_tr_b16 v[60:61], v3 offset:33032\n"
"ds_read_b64_tr_b16 v[62:63], v3 offset:41224\n"
"v_mfma_f32_16x16x32_bf16 v[152:155], v[40:43], a[24:27], v[152:155]\n"
"v_mfma_f32_16x16x32_bf16 v[156:159], v[40:43], a[28:31], v[156:159]\n"
"v_mfma_f32_16x16x32_bf16 v[164:167], v[44:47], a[24:27], v[164:167]\n"
"buffer_load_dwordx4 v10, %3, s46 offen offset:2048 lds\n"
"v_mfma_f32_16x16x32_bf16 v[168:171], v[44:47], a[28:31], v[168:171]\n"
"s_waitcnt lgkmcnt(4)\n"
"v_mfma_f32_16x16x32_bf16 v[176:179], v[48:51], a[24:27], v[176:179]\n"
"v_mfma_f32_16x16x32_bf16 v[180:183], v[48:51], a[28:31], v[180:183]\n"
"s_waitcnt vmcnt(19) lgkmcnt(0)\n"
"s_barrier\n"
"v_mfma_f32_16x16x32_bf16 v[188:191], v[52:55], a[24:27], v[188:191]\n"
"buffer_load_dwordx4 v11, %3, s46 offen offset:3072 lds\n"
"ds_read_b128 a[48:51], v21 offset:0\n"
"v_mfma_f32_16x16x32_bf16 v[192:195], v[52:55], a[28:31], v[192:195]\n"
"ds_read_b128 a[52:55], v21 offset:2048\n"
"ds_read_b128 a[60:63], v22 offset:0\n"
"ds_read_b128 a[64:67], v22 offset:2048\n"
"ds_read_b64_tr_b16 v[32:33], v0 offset:49152\n"
"v_mfma_f32_16x16x32_bf16 v[200:203], v[56:59], a[24:27], v[200:203]\n"
"ds_read_b64_tr_b16 v[34:35], v0 offset:57344\n"
"ds_read_b64_tr_b16 v[36:37], v0 offset:49160\n"
"v_mfma_f32_16x16x32_bf16 v[204:207], v[56:59], a[28:31], v[204:207]\n"
"ds_read_b64_tr_b16 v[38:39], v0 offset:57352\n"
"ds_read_b64_tr_b16 v[40:41], v1 offset:49152\n"
"ds_read_b64_tr_b16 v[42:43], v1 offset:57344\n"
"ds_read_b64_tr_b16 v[44:45], v1 offset:49160\n"
"v_mfma_f32_16x16x32_bf16 v[212:215], v[60:63], a[24:27], v[212:215]\n"
"ds_read_b64_tr_b16 v[46:47], v1 offset:57352\n"
"v_mfma_f32_16x16x32_bf16 v[216:219], v[60:63], a[28:31], v[216:219]\n"
"s_waitcnt lgkmcnt(6)\n"
"v_mfma_f32_16x16x32_bf16 v[64:67], v[32:35], a[36:39], v[64:67]\n"
"ds_read_b64_tr_b16 v[48:49], v2 offset:49152\n"
"ds_read_b64_tr_b16 v[50:51], v2 offset:57344\n"
"v_mfma_f32_16x16x32_bf16 v[68:71], v[32:35], a[40:43], v[68:71]\n"
"s_mov_b32 s46, 2097152\n"
"s_add_u32 m0, s40, 98304\n"
"buffer_load_dwordx4 v8, %3, s46 offen offset:0 lds\n"
"ds_read_b64_tr_b16 v[52:53], v2 offset:49160\n"
"s_waitcnt lgkmcnt(5)\n"
"v_mfma_f32_16x16x32_bf16 v[76:79], v[36:39], a[36:39], v[76:79]\n"
"ds_read_b64_tr_b16 v[54:55], v2 offset:57352\n"
"v_mfma_f32_16x16x32_bf16 v[80:83], v[36:39], a[40:43], v[80:83]\n"
"ds_read_b64_tr_b16 v[56:57], v3 offset:49152\n"
"ds_read_b64_tr_b16 v[58:59], v3 offset:57344\n"
"v_mfma_f32_16x16x32_bf16 v[88:91], v[40:43], a[36:39], v[88:91]\n"
"v_mfma_f32_16x16x32_bf16 v[92:95], v[40:43], a[40:43], v[92:95]\n"
"ds_read_b64_tr_b16 v[60:61], v3 offset:49160\n"
"ds_read_b64_tr_b16 v[62:63], v3 offset:57352\n"
"s_waitcnt lgkmcnt(6)\n"
"v_mfma_f32_16x16x32_bf16 v[100:103], v[44:47], a[36:39], v[100:103]\n"
"v_mfma_f32_16x16x32_bf16 v[104:107], v[44:47], a[40:43], v[104:107]\n"
"ds_read_b64_tr_b16 v[32:33], v0 offset:49408\n"
"ds_read_b64_tr_b16 v[34:35], v0 offset:57600\n"
"buffer_load_dwordx4 v9, %3, s46 offen offset:1024 lds\n"
"v_accvgpr_write_b32 a24, 0\n"
"v_mfma_f32_16x16x32_bf16 v[112:115], v[48:51], a[36:39], v[112:115]\n"
"v_accvgpr_write_b32 a25, 0\n"
"v_accvgpr_write_b32 a26, 0\n"
"v_mfma_f32_16x16x32_bf16 v[116:119], v[48:51], a[40:43], v[116:119]\n"
"ds_read_b64_tr_b16 v[36:37], v0 offset:49416\n"
"ds_read_b64_tr_b16 v[38:39], v0 offset:57608\n"
"v_accvgpr_write_b32 a27, 0\n"
"v_accvgpr_write_b32 a28, 0\n"
"s_waitcnt lgkmcnt(4)\n"
"v_mfma_f32_16x16x32_bf16 v[124:127], v[52:55], a[36:39], v[124:127]\n"
"v_accvgpr_write_b32 a29, 0\n"
"v_accvgpr_write_b32 a30, 0\n"
"v_mfma_f32_16x16x32_bf16 v[224:227], v[52:55], a[40:43], v[224:227]\n"
"ds_read_b64_tr_b16 v[40:41], v1 offset:49408\n"
"ds_read_b64_tr_b16 v[42:43], v1 offset:57600\n"
"v_accvgpr_write_b32 a31, 0\n"
"v_accvgpr_write_b32 a32, 0\n"
"v_mfma_f32_16x16x32_bf16 v[232:235], v[56:59], a[36:39], v[232:235]\n"
"v_accvgpr_write_b32 a33, 0\n"
"v_accvgpr_write_b32 a34, 0\n"
"v_mfma_f32_16x16x32_bf16 v[236:239], v[56:59], a[40:43], v[236:239]\n"
"ds_read_b64_tr_b16 v[44:45], v1 offset:49416\n"
"ds_read_b64_tr_b16 v[46:47], v1 offset:57608\n"
"v_accvgpr_write_b32 a35, 0\n"
"v_accvgpr_write_b32 a177, 0\n"
"v_mfma_f32_16x16x32_bf16 v[244:247], v[60:63], a[36:39], v[244:247]\n"
"v_accvgpr_write_b32 a178, 0\n"
"v_accvgpr_write_b32 a179, 0\n"
"v_mfma_f32_16x16x32_bf16 v[248:251], v[60:63], a[40:43], v[248:251]\n"
"buffer_load_dwordx4 v10, %3, s46 offen offset:2048 lds\n"
"ds_read_b64_tr_b16 v[48:49], v2 offset:49408\n"
"ds_read_b64_tr_b16 v[50:51], v2 offset:57600\n"
"s_waitcnt lgkmcnt(4)\n"
"v_mfma_f32_16x16x32_bf16 v[128:131], v[32:35], a[36:39], v[128:131]\n"
"v_mfma_f32_16x16x32_bf16 v[132:135], v[32:35], a[40:43], v[132:135]\n"
"ds_read_b64_tr_b16 v[52:53], v2 offset:49416\n"
"ds_read_b64_tr_b16 v[54:55], v2 offset:57608\n"
"v_mfma_f32_16x16x32_bf16 v[140:143], v[36:39], a[36:39], v[140:143]\n"
"v_mfma_f32_16x16x32_bf16 v[144:147], v[36:39], a[40:43], v[144:147]\n"
"ds_read_b64_tr_b16 v[56:57], v3 offset:49408\n"
"ds_read_b64_tr_b16 v[58:59], v3 offset:57600\n"
"ds_read_b64_tr_b16 v[60:61], v3 offset:49416\n"
"ds_read_b64_tr_b16 v[62:63], v3 offset:57608\n"
"v_mfma_f32_16x16x32_bf16 v[152:155], v[40:43], a[36:39], v[152:155]\n"
"v_mfma_f32_16x16x32_bf16 v[156:159], v[40:43], a[40:43], v[156:159]\n"
"s_waitcnt lgkmcnt(6)\n"
"v_mfma_f32_16x16x32_bf16 v[164:167], v[44:47], a[36:39], v[164:167]\n"
"buffer_load_dwordx4 v11, %3, s46 offen offset:3072 lds\n"
"v_mfma_f32_16x16x32_bf16 v[168:171], v[44:47], a[40:43], v[168:171]\n"
"v_mfma_f32_16x16x32_bf16 v[176:179], v[48:51], a[36:39], v[176:179]\n"
"v_mfma_f32_16x16x32_bf16 v[180:183], v[48:51], a[40:43], v[180:183]\n"
"s_waitcnt vmcnt(20) lgkmcnt(0)\n"
"s_barrier\n"
"v_mfma_f32_16x16x32_bf16 v[188:191], v[52:55], a[36:39], v[188:191]\n"
"ds_read_b64_tr_b16 v[32:33], v4 offset:0\n"
"ds_read_b64_tr_b16 v[34:35], v4 offset:8192\n"
"v_mfma_f32_16x16x32_bf16 v[192:195], v[52:55], a[40:43], v[192:195]\n"
"ds_read_b64_tr_b16 v[36:37], v4 offset:8\n"
"ds_read_b64_tr_b16 v[38:39], v4 offset:8200\n"
"ds_read_b64_tr_b16 v[40:41], v5 offset:0\n"
"ds_read_b64_tr_b16 v[42:43], v5 offset:8192\n"
"v_mfma_f32_16x16x32_bf16 v[200:203], v[56:59], a[36:39], v[200:203]\n"
"ds_read_b64_tr_b16 v[44:45], v5 offset:8\n"
"ds_read_b64_tr_b16 v[46:47], v5 offset:8200\n"
"v_mfma_f32_16x16x32_bf16 v[204:207], v[56:59], a[40:43], v[204:207]\n"
"v_mfma_f32_16x16x32_bf16 v[212:215], v[60:63], a[36:39], v[212:215]\n"
"v_mfma_f32_16x16x32_bf16 v[216:219], v[60:63], a[40:43], v[216:219]\n"
"s_waitcnt lgkmcnt(6)\n"
"v_mfma_f32_16x16x32_bf16 v[64:67], v[32:35], a[48:51], v[64:67]\n"
"ds_read_b64_tr_b16 v[48:49], v6 offset:0\n"
"ds_read_b64_tr_b16 v[50:51], v6 offset:8192\n"
"v_mfma_f32_16x16x32_bf16 v[68:71], v[32:35], a[52:55], v[68:71]\n"
"s_mov_b32 s46, 512\n"
"s_add_u32 m0, s49, 0\n"
"buffer_load_dwordx4 v23, %2, s46 offen offset:0 lds\n"
"ds_read_b64_tr_b16 v[52:53], v6 offset:8\n"
"s_waitcnt lgkmcnt(3)\n"
"v_mfma_f32_16x16x32_bf16 v[76:79], v[36:39], a[48:51], v[76:79]\n"
"ds_read_b64_tr_b16 v[54:55], v6 offset:8200\n"
"v_mfma_f32_16x16x32_bf16 v[80:83], v[36:39], a[52:55], v[80:83]\n"
"ds_read_b64_tr_b16 v[56:57], v7 offset:0\n"
"ds_read_b64_tr_b16 v[58:59], v7 offset:8192\n"
"v_mfma_f32_16x16x32_bf16 v[88:91], v[40:43], a[48:51], v[88:91]\n"
"buffer_load_dwordx4 v24, %2, s46 offen offset:1024 lds\n"
"v_mfma_f32_16x16x32_bf16 v[92:95], v[40:43], a[52:55], v[92:95]\n"
"ds_read_b64_tr_b16 v[60:61], v7 offset:8\n"
"ds_read_b64_tr_b16 v[62:63], v7 offset:8200\n"
"v_mfma_f32_16x16x32_bf16 v[100:103], v[44:47], a[48:51], v[100:103]\n"
"v_mfma_f32_16x16x32_bf16 v[104:107], v[44:47], a[52:55], v[104:107]\n"
"ds_read_b64_tr_b16 v[32:33], v4 offset:256\n"
"ds_read_b64_tr_b16 v[34:35], v4 offset:8448\n"
"buffer_load_dwordx4 v25, %2, s46 offen offset:2048 lds\n"
"v_accvgpr_write_b32 a36, 0\n"
"s_waitcnt lgkmcnt(5)\n"
"v_mfma_f32_16x16x32_bf16 v[112:115], v[48:51], a[48:51], v[112:115]\n"
"v_accvgpr_write_b32 a37, 0\n"
"v_accvgpr_write_b32 a38, 0\n"
"v_mfma_f32_16x16x32_bf16 v[116:119], v[48:51], a[52:55], v[116:119]\n"
"ds_read_b64_tr_b16 v[36:37], v4 offset:264\n"
"ds_read_b64_tr_b16 v[38:39], v4 offset:8456\n"
"v_accvgpr_write_b32 a39, 0\n"
"v_accvgpr_write_b32 a40, 0\n"
"v_mfma_f32_16x16x32_bf16 v[124:127], v[52:55], a[48:51], v[124:127]\n"
"v_accvgpr_write_b32 a41, 0\n"
"v_accvgpr_write_b32 a42, 0\n"
"v_mfma_f32_16x16x32_bf16 v[224:227], v[52:55], a[52:55], v[224:227]\n"
"ds_read_b64_tr_b16 v[40:41], v5 offset:256\n"
"ds_read_b64_tr_b16 v[42:43], v5 offset:8448\n"
"buffer_load_dwordx4 v26, %2, s46 offen offset:3072 lds\n"
"v_accvgpr_write_b32 a43, 0\n"
"s_waitcnt lgkmcnt(4)\n"
"v_mfma_f32_16x16x32_bf16 v[232:235], v[56:59], a[48:51], v[232:235]\n"
"v_accvgpr_write_b32 a44, 0\n"
"v_accvgpr_write_b32 a45, 0\n"
"v_mfma_f32_16x16x32_bf16 v[236:239], v[56:59], a[52:55], v[236:239]\n"
"ds_read_b64_tr_b16 v[44:45], v5 offset:264\n"
"ds_read_b64_tr_b16 v[46:47], v5 offset:8456\n"
"v_accvgpr_write_b32 a46, 0\n"
"v_accvgpr_write_b32 a47, 0\n"
"v_mfma_f32_16x16x32_bf16 v[244:247], v[60:63], a[48:51], v[244:247]\n"
"s_mov_b32 s46, 2359296\n"
"s_add_u32 m0, s40, 114688\n"
"v_mfma_f32_16x16x32_bf16 v[248:251], v[60:63], a[52:55], v[248:251]\n"
"buffer_load_dwordx4 v8, %3, s46 offen offset:0 lds\n"
"ds_read_b64_tr_b16 v[48:49], v6 offset:256\n"
"ds_read_b64_tr_b16 v[50:51], v6 offset:8448\n"
"v_accvgpr_write_b32 a180, 0\n"
"v_mfma_f32_16x16x32_bf16 v[128:131], v[32:35], a[48:51], v[128:131]\n"
"v_accvgpr_write_b32 a181, 0\n"
"v_accvgpr_write_b32 a182, 0\n"
"v_mfma_f32_16x16x32_bf16 v[132:135], v[32:35], a[52:55], v[132:135]\n"
"ds_read_b64_tr_b16 v[52:53], v6 offset:264\n"
"ds_read_b64_tr_b16 v[54:55], v6 offset:8456\n"
"s_waitcnt lgkmcnt(4)\n"
"v_mfma_f32_16x16x32_bf16 v[140:143], v[36:39], a[48:51], v[140:143]\n"
"buffer_load_dwordx4 v9, %3, s46 offen offset:1024 lds\n"
"v_mfma_f32_16x16x32_bf16 v[144:147], v[36:39], a[52:55], v[144:147]\n"
"ds_read_b64_tr_b16 v[56:57], v7 offset:256\n"
"ds_read_b64_tr_b16 v[58:59], v7 offset:8448\n"
"ds_read_b64_tr_b16 v[60:61], v7 offset:264\n"
"ds_read_b64_tr_b16 v[62:63], v7 offset:8456\n"
"v_mfma_f32_16x16x32_bf16 v[152:155], v[40:43], a[48:51], v[152:155]\n"
"v_mfma_f32_16x16x32_bf16 v[156:159], v[40:43], a[52:55], v[156:159]\n"
"v_mfma_f32_16x16x32_bf16 v[164:167], v[44:47], a[48:51], v[164:167]\n"
"buffer_load_dwordx4 v10, %3, s46 offen offset:2048 lds\n"
"v_mfma_f32_16x16x32_bf16 v[168:171], v[44:47], a[52:55], v[168:171]\n"
"s_waitcnt lgkmcnt(4)\n"
"v_mfma_f32_16x16x32_bf16 v[176:179], v[48:51], a[48:51], v[176:179]\n"
"v_mfma_f32_16x16x32_bf16 v[180:183], v[48:51], a[52:55], v[180:183]\n"
"s_waitcnt vmcnt(15) lgkmcnt(0)\n"
"s_barrier\n"
"v_mfma_f32_16x16x32_bf16 v[188:191], v[52:55], a[48:51], v[188:191]\n"
"buffer_load_dwordx4 v11, %3, s46 offen offset:3072 lds\n"
"ds_read_b128 a[72:75], v21 offset:6144\n"
"v_mfma_f32_16x16x32_bf16 v[192:195], v[52:55], a[52:55], v[192:195]\n"
"ds_read_b128 a[76:79], v21 offset:8192\n"
"ds_read_b128 a[84:87], v22 offset:6144\n"
"ds_read_b128 a[88:91], v22 offset:8192\n"
"ds_read_b64_tr_b16 v[32:33], v4 offset:16384\n"
"v_mfma_f32_16x16x32_bf16 v[200:203], v[56:59], a[48:51], v[200:203]\n"
"ds_read_b64_tr_b16 v[34:35], v4 offset:24576\n"
"ds_read_b64_tr_b16 v[36:37], v4 offset:16392\n"
"v_mfma_f32_16x16x32_bf16 v[204:207], v[56:59], a[52:55], v[204:207]\n"
"ds_read_b64_tr_b16 v[38:39], v4 offset:24584\n"
"ds_read_b64_tr_b16 v[40:41], v5 offset:16384\n"
"ds_read_b64_tr_b16 v[42:43], v5 offset:24576\n"
"ds_read_b64_tr_b16 v[44:45], v5 offset:16392\n"
"v_mfma_f32_16x16x32_bf16 v[212:215], v[60:63], a[48:51], v[212:215]\n"
"ds_read_b64_tr_b16 v[46:47], v5 offset:24584\n"
"v_mfma_f32_16x16x32_bf16 v[216:219], v[60:63], a[52:55], v[216:219]\n"
"s_waitcnt lgkmcnt(6)\n"
"v_mfma_f32_16x16x32_bf16 v[64:67], v[32:35], a[60:63], v[64:67]\n"
"ds_read_b64_tr_b16 v[48:49], v6 offset:16384\n"
"ds_read_b64_tr_b16 v[50:51], v6 offset:24576\n"
"v_mfma_f32_16x16x32_bf16 v[68:71], v[32:35], a[64:67], v[68:71]\n"
"s_mov_b32 s46, 2621440\n"
"s_add_u32 m0, s40, 131072\n"
"buffer_load_dwordx4 v8, %3, s46 offen offset:0 lds\n"
"ds_read_b64_tr_b16 v[52:53], v6 offset:16392\n"
"s_waitcnt lgkmcnt(5)\n"
"v_mfma_f32_16x16x32_bf16 v[76:79], v[36:39], a[60:63], v[76:79]\n"
"ds_read_b64_tr_b16 v[54:55], v6 offset:24584\n"
"v_mfma_f32_16x16x32_bf16 v[80:83], v[36:39], a[64:67], v[80:83]\n"
"ds_read_b64_tr_b16 v[56:57], v7 offset:16384\n"
"ds_read_b64_tr_b16 v[58:59], v7 offset:24576\n"
"v_mfma_f32_16x16x32_bf16 v[88:91], v[40:43], a[60:63], v[88:91]\n"
"v_mfma_f32_16x16x32_bf16 v[92:95], v[40:43], a[64:67], v[92:95]\n"
"ds_read_b64_tr_b16 v[60:61], v7 offset:16392\n"
"ds_read_b64_tr_b16 v[62:63], v7 offset:24584\n"
"s_waitcnt lgkmcnt(6)\n"
"v_mfma_f32_16x16x32_bf16 v[100:103], v[44:47], a[60:63], v[100:103]\n"
"v_mfma_f32_16x16x32_bf16 v[104:107], v[44:47], a[64:67], v[104:107]\n"
"ds_read_b64_tr_b16 v[32:33], v4 offset:16640\n"
"ds_read_b64_tr_b16 v[34:35], v4 offset:24832\n"
"buffer_load_dwordx4 v9, %3, s46 offen offset:1024 lds\n"
"v_accvgpr_write_b32 a48, 0\n"
"v_mfma_f32_16x16x32_bf16 v[112:115], v[48:51], a[60:63], v[112:115]\n"
"v_accvgpr_write_b32 a49, 0\n"
"v_accvgpr_write_b32 a50, 0\n"
"v_mfma_f32_16x16x32_bf16 v[116:119], v[48:51], a[64:67], v[116:119]\n"
"ds_read_b64_tr_b16 v[36:37], v4 offset:16648\n"
"ds_read_b64_tr_b16 v[38:39], v4 offset:24840\n"
"v_accvgpr_write_b32 a51, 0\n"
"v_accvgpr_write_b32 a52, 0\n"
"s_waitcnt lgkmcnt(4)\n"
"v_mfma_f32_16x16x32_bf16 v[124:127], v[52:55], a[60:63], v[124:127]\n"
"v_accvgpr_write_b32 a53, 0\n"
"v_accvgpr_write_b32 a54, 0\n"
"v_mfma_f32_16x16x32_bf16 v[224:227], v[52:55], a[64:67], v[224:227]\n"
"ds_read_b64_tr_b16 v[40:41], v5 offset:16640\n"
"ds_read_b64_tr_b16 v[42:43], v5 offset:24832\n"
"v_accvgpr_write_b32 a55, 0\n"
"v_accvgpr_write_b32 a56, 0\n"
"v_mfma_f32_16x16x32_bf16 v[232:235], v[56:59], a[60:63], v[232:235]\n"
"v_accvgpr_write_b32 a57, 0\n"
"v_accvgpr_write_b32 a58, 0\n"
"v_mfma_f32_16x16x32_bf16 v[236:239], v[56:59], a[64:67], v[236:239]\n"
"ds_read_b64_tr_b16 v[44:45], v5 offset:16648\n"
"ds_read_b64_tr_b16 v[46:47], v5 offset:24840\n"
"v_accvgpr_write_b32 a59, 0\n"
"v_accvgpr_write_b32 a183, 0\n"
"v_mfma_f32_16x16x32_bf16 v[244:247], v[60:63], a[60:63], v[244:247]\n"
"v_accvgpr_write_b32 a184, 0\n"
"v_accvgpr_write_b32 a185, 0\n"
"v_mfma_f32_16x16x32_bf16 v[248:251], v[60:63], a[64:67], v[248:251]\n"
"buffer_load_dwordx4 v10, %3, s46 offen offset:2048 lds\n"
"ds_read_b64_tr_b16 v[48:49], v6 offset:16640\n"
"ds_read_b64_tr_b16 v[50:51], v6 offset:24832\n"
"s_waitcnt lgkmcnt(4)\n"
"v_mfma_f32_16x16x32_bf16 v[128:131], v[32:35], a[60:63], v[128:131]\n"
"v_mfma_f32_16x16x32_bf16 v[132:135], v[32:35], a[64:67], v[132:135]\n"
"ds_read_b64_tr_b16 v[52:53], v6 offset:16648\n"
"ds_read_b64_tr_b16 v[54:55], v6 offset:24840\n"
"v_mfma_f32_16x16x32_bf16 v[140:143], v[36:39], a[60:63], v[140:143]\n"
"v_mfma_f32_16x16x32_bf16 v[144:147], v[36:39], a[64:67], v[144:147]\n"
"ds_read_b64_tr_b16 v[56:57], v7 offset:16640\n"
"ds_read_b64_tr_b16 v[58:59], v7 offset:24832\n"
"ds_read_b64_tr_b16 v[60:61], v7 offset:16648\n"
"ds_read_b64_tr_b16 v[62:63], v7 offset:24840\n"
"v_mfma_f32_16x16x32_bf16 v[152:155], v[40:43], a[60:63], v[152:155]\n"
"v_mfma_f32_16x16x32_bf16 v[156:159], v[40:43], a[64:67], v[156:159]\n"
"s_waitcnt lgkmcnt(6)\n"
"v_mfma_f32_16x16x32_bf16 v[164:167], v[44:47], a[60:63], v[164:167]\n"
"buffer_load_dwordx4 v11, %3, s46 offen offset:3072 lds\n"
"v_mfma_f32_16x16x32_bf16 v[168:171], v[44:47], a[64:67], v[168:171]\n"
"v_mfma_f32_16x16x32_bf16 v[176:179], v[48:51], a[60:63], v[176:179]\n"
"v_mfma_f32_16x16x32_bf16 v[180:183], v[48:51], a[64:67], v[180:183]\n"
"s_waitcnt vmcnt(24) lgkmcnt(0)\n"
"s_barrier\n"
"v_mfma_f32_16x16x32_bf16 v[188:191], v[52:55], a[60:63], v[188:191]\n"
"ds_read_b64_tr_b16 v[32:33], v0 offset:0\n"
"ds_read_b64_tr_b16 v[34:35], v0 offset:8192\n"
"v_mfma_f32_16x16x32_bf16 v[192:195], v[52:55], a[64:67], v[192:195]\n"
"ds_read_b64_tr_b16 v[36:37], v0 offset:8\n"
"ds_read_b64_tr_b16 v[38:39], v0 offset:8200\n"
"ds_read_b64_tr_b16 v[40:41], v1 offset:0\n"
"ds_read_b64_tr_b16 v[42:43], v1 offset:8192\n"
"v_mfma_f32_16x16x32_bf16 v[200:203], v[56:59], a[60:63], v[200:203]\n"
"ds_read_b64_tr_b16 v[44:45], v1 offset:8\n"
"ds_read_b64_tr_b16 v[46:47], v1 offset:8200\n"
"v_mfma_f32_16x16x32_bf16 v[204:207], v[56:59], a[64:67], v[204:207]\n"
"v_mfma_f32_16x16x32_bf16 v[212:215], v[60:63], a[60:63], v[212:215]\n"
"v_mfma_f32_16x16x32_bf16 v[216:219], v[60:63], a[64:67], v[216:219]\n"
"s_waitcnt lgkmcnt(6)\n"
"v_mfma_f32_16x16x32_bf16 v[64:67], v[32:35], a[72:75], v[64:67]\n"
"ds_read_b64_tr_b16 v[48:49], v2 offset:0\n"
"ds_read_b64_tr_b16 v[50:51], v2 offset:8192\n"
"v_mfma_f32_16x16x32_bf16 v[68:71], v[32:35], a[76:79], v[68:71]\n"
"s_mov_b32 s46, 640\n"
"s_add_u32 m0, s49, 6144\n"
"buffer_load_dwordx4 v23, %2, s46 offen offset:0 lds\n"
"ds_read_b64_tr_b16 v[52:53], v2 offset:8\n"
"s_waitcnt lgkmcnt(3)\n"
"v_mfma_f32_16x16x32_bf16 v[76:79], v[36:39], a[72:75], v[76:79]\n"
"ds_read_b64_tr_b16 v[54:55], v2 offset:8200\n"
"v_mfma_f32_16x16x32_bf16 v[80:83], v[36:39], a[76:79], v[80:83]\n"
"ds_read_b64_tr_b16 v[56:57], v3 offset:0\n"
"ds_read_b64_tr_b16 v[58:59], v3 offset:8192\n"
"v_mfma_f32_16x16x32_bf16 v[88:91], v[40:43], a[72:75], v[88:91]\n"
"buffer_load_dwordx4 v24, %2, s46 offen offset:1024 lds\n"
"v_mfma_f32_16x16x32_bf16 v[92:95], v[40:43], a[76:79], v[92:95]\n"
"ds_read_b64_tr_b16 v[60:61], v3 offset:8\n"
"ds_read_b64_tr_b16 v[62:63], v3 offset:8200\n"
"v_mfma_f32_16x16x32_bf16 v[100:103], v[44:47], a[72:75], v[100:103]\n"
"v_mfma_f32_16x16x32_bf16 v[104:107], v[44:47], a[76:79], v[104:107]\n"
"ds_read_b64_tr_b16 v[32:33], v0 offset:256\n"
"ds_read_b64_tr_b16 v[34:35], v0 offset:8448\n"
"buffer_load_dwordx4 v25, %2, s46 offen offset:2048 lds\n"
"v_accvgpr_write_b32 a60, 0\n"
"s_waitcnt lgkmcnt(5)\n"
"v_mfma_f32_16x16x32_bf16 v[112:115], v[48:51], a[72:75], v[112:115]\n"
"v_accvgpr_write_b32 a61, 0\n"
"v_accvgpr_write_b32 a62, 0\n"
"v_mfma_f32_16x16x32_bf16 v[116:119], v[48:51], a[76:79], v[116:119]\n"
"ds_read_b64_tr_b16 v[36:37], v0 offset:264\n"
"ds_read_b64_tr_b16 v[38:39], v0 offset:8456\n"
"v_accvgpr_write_b32 a63, 0\n"
"v_accvgpr_write_b32 a64, 0\n"
"v_mfma_f32_16x16x32_bf16 v[124:127], v[52:55], a[72:75], v[124:127]\n"
"v_accvgpr_write_b32 a65, 0\n"
"v_accvgpr_write_b32 a66, 0\n"
"v_mfma_f32_16x16x32_bf16 v[224:227], v[52:55], a[76:79], v[224:227]\n"
"ds_read_b64_tr_b16 v[40:41], v1 offset:256\n"
"ds_read_b64_tr_b16 v[42:43], v1 offset:8448\n"
"buffer_load_dwordx4 v26, %2, s46 offen offset:3072 lds\n"
"v_accvgpr_write_b32 a67, 0\n"
"s_waitcnt lgkmcnt(4)\n"
"v_mfma_f32_16x16x32_bf16 v[232:235], v[56:59], a[72:75], v[232:235]\n"
"v_accvgpr_write_b32 a68, 0\n"
"v_accvgpr_write_b32 a69, 0\n"
"v_mfma_f32_16x16x32_bf16 v[236:239], v[56:59], a[76:79], v[236:239]\n"
"ds_read_b64_tr_b16 v[44:45], v1 offset:264\n"
"ds_read_b64_tr_b16 v[46:47], v1 offset:8456\n"
"v_accvgpr_write_b32 a70, 0\n"
"v_accvgpr_write_b32 a71, 0\n"
"v_mfma_f32_16x16x32_bf16 v[244:247], v[60:63], a[72:75], v[244:247]\n"
"s_mov_b32 s46, 2883584\n"
"s_add_u32 m0, s40, 147456\n"
"v_mfma_f32_16x16x32_bf16 v[248:251], v[60:63], a[76:79], v[248:251]\n"
"buffer_load_dwordx4 v8, %3, s46 offen offset:0 lds\n"
"ds_read_b64_tr_b16 v[48:49], v2 offset:256\n"
"ds_read_b64_tr_b16 v[50:51], v2 offset:8448\n"
"v_accvgpr_write_b32 a186, 0\n"
"v_mfma_f32_16x16x32_bf16 v[128:131], v[32:35], a[72:75], v[128:131]\n"
"v_accvgpr_write_b32 a187, 0\n"
"v_accvgpr_write_b32 a188, 0\n"
"v_mfma_f32_16x16x32_bf16 v[132:135], v[32:35], a[76:79], v[132:135]\n"
"ds_read_b64_tr_b16 v[52:53], v2 offset:264\n"
"ds_read_b64_tr_b16 v[54:55], v2 offset:8456\n"
"s_waitcnt lgkmcnt(4)\n"
"v_mfma_f32_16x16x32_bf16 v[140:143], v[36:39], a[72:75], v[140:143]\n"
"buffer_load_dwordx4 v9, %3, s46 offen offset:1024 lds\n"
"v_mfma_f32_16x16x32_bf16 v[144:147], v[36:39], a[76:79], v[144:147]\n"
"ds_read_b64_tr_b16 v[56:57], v3 offset:256\n"
"ds_read_b64_tr_b16 v[58:59], v3 offset:8448\n"
"ds_read_b64_tr_b16 v[60:61], v3 offset:264\n"
"ds_read_b64_tr_b16 v[62:63], v3 offset:8456\n"
"v_mfma_f32_16x16x32_bf16 v[152:155], v[40:43], a[72:75], v[152:155]\n"
"v_mfma_f32_16x16x32_bf16 v[156:159], v[40:43], a[76:79], v[156:159]\n"
"v_mfma_f32_16x16x32_bf16 v[164:167], v[44:47], a[72:75], v[164:167]\n"
"buffer_load_dwordx4 v10, %3, s46 offen offset:2048 lds\n"
"v_mfma_f32_16x16x32_bf16 v[168:171], v[44:47], a[76:79], v[168:171]\n"
"s_waitcnt lgkmcnt(4)\n"
"v_mfma_f32_16x16x32_bf16 v[176:179], v[48:51], a[72:75], v[176:179]\n"
"v_mfma_f32_16x16x32_bf16 v[180:183], v[48:51], a[76:79], v[180:183]\n"
"s_waitcnt vmcnt(15) lgkmcnt(0)\n"
"s_barrier\n"
"v_mfma_f32_16x16x32_bf16 v[188:191], v[52:55], a[72:75], v[188:191]\n"
"buffer_load_dwordx4 v11, %3, s46 offen offset:3072 lds\n"
"ds_read_b128 a[96:99], v21 offset:0\n"
"v_mfma_f32_16x16x32_bf16 v[192:195], v[52:55], a[76:79], v[192:195]\n"
"ds_read_b128 a[100:103], v21 offset:2048\n"
"ds_read_b128 a[108:111], v22 offset:0\n"
"ds_read_b128 a[112:115], v22 offset:2048\n"
"ds_read_b64_tr_b16 v[32:33], v0 offset:16384\n"
"v_mfma_f32_16x16x32_bf16 v[200:203], v[56:59], a[72:75], v[200:203]\n"
"ds_read_b64_tr_b16 v[34:35], v0 offset:24576\n"
"ds_read_b64_tr_b16 v[36:37], v0 offset:16392\n"
"v_mfma_f32_16x16x32_bf16 v[204:207], v[56:59], a[76:79], v[204:207]\n"
"ds_read_b64_tr_b16 v[38:39], v0 offset:24584\n"
"ds_read_b64_tr_b16 v[40:41], v1 offset:16384\n"
"ds_read_b64_tr_b16 v[42:43], v1 offset:24576\n"
"ds_read_b64_tr_b16 v[44:45], v1 offset:16392\n"
"v_mfma_f32_16x16x32_bf16 v[212:215], v[60:63], a[72:75], v[212:215]\n"
"ds_read_b64_tr_b16 v[46:47], v1 offset:24584\n"
"v_mfma_f32_16x16x32_bf16 v[216:219], v[60:63], a[76:79], v[216:219]\n"
"s_waitcnt lgkmcnt(6)\n"
"v_mfma_f32_16x16x32_bf16 v[64:67], v[32:35], a[84:87], v[64:67]\n"
"ds_read_b64_tr_b16 v[48:49], v2 offset:16384\n"
"ds_read_b64_tr_b16 v[50:51], v2 offset:24576\n"
"v_mfma_f32_16x16x32_bf16 v[68:71], v[32:35], a[88:91], v[68:71]\n"
"s_mov_b32 s46, 3145728\n"
"s_add_u32 m0, s40, 65536\n"
"buffer_load_dwordx4 v8, %3, s46 offen offset:0 lds\n"
"ds_read_b64_tr_b16 v[52:53], v2 offset:16392\n"
"s_waitcnt lgkmcnt(5)\n"
"v_mfma_f32_16x16x32_bf16 v[76:79], v[36:39], a[84:87], v[76:79]\n"
"ds_read_b64_tr_b16 v[54:55], v2 offset:24584\n"
"v_mfma_f32_16x16x32_bf16 v[80:83], v[36:39], a[88:91], v[80:83]\n"
"ds_read_b64_tr_b16 v[56:57], v3 offset:16384\n"
"ds_read_b64_tr_b16 v[58:59], v3 offset:24576\n"
"v_mfma_f32_16x16x32_bf16 v[88:91], v[40:43], a[84:87], v[88:91]\n"
"buffer_load_dwordx4 v9, %3, s46 offen offset:1024 lds\n"
"v_mfma_f32_16x16x32_bf16 v[92:95], v[40:43], a[88:91], v[92:95]\n"
"ds_read_b64_tr_b16 v[60:61], v3 offset:16392\n"
"ds_read_b64_tr_b16 v[62:63], v3 offset:24584\n"
"s_waitcnt lgkmcnt(6)\n"
"v_mfma_f32_16x16x32_bf16 v[100:103], v[44:47], a[84:87], v[100:103]\n"
"v_mfma_f32_16x16x32_bf16 v[104:107], v[44:47], a[88:91], v[104:107]\n"
"ds_read_b64_tr_b16 v[32:33], v0 offset:16640\n"
"ds_read_b64_tr_b16 v[34:35], v0 offset:24832\n"
"buffer_load_dwordx4 v10, %3, s46 offen offset:2048 lds\n"
"v_accvgpr_write_b32 a72, 0\n"
"v_mfma_f32_16x16x32_bf16 v[112:115], v[48:51], a[84:87], v[112:115]\n"
"v_accvgpr_write_b32 a73, 0\n"
"v_accvgpr_write_b32 a74, 0\n"
"v_mfma_f32_16x16x32_bf16 v[116:119], v[48:51], a[88:91], v[116:119]\n"
"ds_read_b64_tr_b16 v[36:37], v0 offset:16648\n"
"ds_read_b64_tr_b16 v[38:39], v0 offset:24840\n"
"v_accvgpr_write_b32 a75, 0\n"
"v_accvgpr_write_b32 a76, 0\n"
"s_waitcnt lgkmcnt(4)\n"
"v_mfma_f32_16x16x32_bf16 v[124:127], v[52:55], a[84:87], v[124:127]\n"
"v_accvgpr_write_b32 a77, 0\n"
"v_accvgpr_write_b32 a78, 0\n"
"v_mfma_f32_16x16x32_bf16 v[224:227], v[52:55], a[88:91], v[224:227]\n"
"ds_read_b64_tr_b16 v[40:41], v1 offset:16640\n"
"ds_read_b64_tr_b16 v[42:43], v1 offset:24832\n"
"buffer_load_dwordx4 v11, %3, s46 offen offset:3072 lds\n"
"v_accvgpr_write_b32 a79, 0\n"
"v_mfma_f32_16x16x32_bf16 v[232:235], v[56:59], a[84:87], v[232:235]\n"
"v_accvgpr_write_b32 a80, 0\n"
"v_accvgpr_write_b32 a81, 0\n"
"v_mfma_f32_16x16x32_bf16 v[236:239], v[56:59], a[88:91], v[236:239]\n"
"ds_read_b64_tr_b16 v[44:45], v1 offset:16648\n"
"ds_read_b64_tr_b16 v[46:47], v1 offset:24840\n"
"v_accvgpr_write_b32 a82, 0\n"
"v_accvgpr_write_b32 a83, 0\n"
"v_mfma_f32_16x16x32_bf16 v[244:247], v[60:63], a[84:87], v[244:247]\n"
"s_add_u32 s47, %9, 0\n"
"s_and_b32 s47, s47, 0xfffff\n"
"v_mfma_f32_16x16x32_bf16 v[248:251], v[60:63], a[88:91], v[248:251]\n"
"s_add_u32 m0, s40, 0\n"
"ds_read_b64_tr_b16 v[48:49], v2 offset:16640\n"
"buffer_load_dwordx4 v12, %0, s47 offen offset:0 lds\n"
"ds_read_b64_tr_b16 v[50:51], v2 offset:24832\n"
"s_waitcnt lgkmcnt(4)\n"
"v_mfma_f32_16x16x32_bf16 v[128:131], v[32:35], a[84:87], v[128:131]\n"
"v_accvgpr_write_b32 a189, 0\n"
"v_accvgpr_write_b32 a190, 0\n"
"v_mfma_f32_16x16x32_bf16 v[132:135], v[32:35], a[88:91], v[132:135]\n"
"ds_read_b64_tr_b16 v[52:53], v2 offset:16648\n"
"ds_read_b64_tr_b16 v[54:55], v2 offset:24840\n"
"v_accvgpr_write_b32 a191, 0\n"
"v_mfma_f32_16x16x32_bf16 v[140:143], v[36:39], a[84:87], v[140:143]\n"
"buffer_load_dwordx4 v13, %0, s47 offen offset:1024 lds\n"
"v_mfma_f32_16x16x32_bf16 v[144:147], v[36:39], a[88:91], v[144:147]\n"
"ds_read_b64_tr_b16 v[56:57], v3 offset:16640\n"
"ds_read_b64_tr_b16 v[58:59], v3 offset:24832\n"
"ds_read_b64_tr_b16 v[60:61], v3 offset:16648\n"
"ds_read_b64_tr_b16 v[62:63], v3 offset:24840\n"
"v_mfma_f32_16x16x32_bf16 v[152:155], v[40:43], a[84:87], v[152:155]\n"
"v_mfma_f32_16x16x32_bf16 v[156:159], v[40:43], a[88:91], v[156:159]\n"
"s_waitcnt lgkmcnt(6)\n"
"v_mfma_f32_16x16x32_bf16 v[164:167], v[44:47], a[84:87], v[164:167]\n"
"buffer_load_dwordx4 v14, %0, s47 offen offset:2048 lds\n"
"v_mfma_f32_16x16x32_bf16 v[168:171], v[44:47], a[88:91], v[168:171]\n"
"v_mfma_f32_16x16x32_bf16 v[176:179], v[48:51], a[84:87], v[176:179]\n"
"v_mfma_f32_16x16x32_bf16 v[180:183], v[48:51], a[88:91], v[180:183]\n"
"s_waitcnt vmcnt(27) lgkmcnt(0)\n"
"s_barrier\n"
"v_mfma_f32_16x16x32_bf16 v[188:191], v[52:55], a[84:87], v[188:191]\n"
"buffer_load_dwordx4 v15, %0, s47 offen offset:3072 lds\n"
"ds_read_b64_tr_b16 v[32:33], v0 offset:32768\n"
"v_mfma_f32_16x16x32_bf16 v[192:195], v[52:55], a[88:91], v[192:195]\n"
"ds_read_b64_tr_b16 v[34:35], v0 offset:40960\n"
"ds_read_b64_tr_b16 v[36:37], v0 offset:32776\n"
"ds_read_b64_tr_b16 v[38:39], v0 offset:40968\n"
"ds_read_b64_tr_b16 v[40:41], v1 offset:32768\n"
"v_mfma_f32_16x16x32_bf16 v[200:203], v[56:59], a[84:87], v[200:203]\n"
"ds_read_b64_tr_b16 v[42:43], v1 offset:40960\n"
"ds_read_b64_tr_b16 v[44:45], v1 offset:32776\n"
"v_mfma_f32_16x16x32_bf16 v[204:207], v[56:59], a[88:91], v[204:207]\n"
"ds_read_b64_tr_b16 v[46:47], v1 offset:40968\n"
"v_mfma_f32_16x16x32_bf16 v[212:215], v[60:63], a[84:87], v[212:215]\n"
"v_mfma_f32_16x16x32_bf16 v[216:219], v[60:63], a[88:91], v[216:219]\n"
"s_waitcnt lgkmcnt(6)\n"
"v_mfma_f32_16x16x32_bf16 v[64:67], v[32:35], a[96:99], v[64:67]\n"
"ds_read_b64_tr_b16 v[48:49], v2 offset:32768\n"
"ds_read_b64_tr_b16 v[50:51], v2 offset:40960\n"
"v_mfma_f32_16x16x32_bf16 v[68:71], v[32:35], a[100:103], v[68:71]\n"
"s_mov_b32 s46, 768\n"
"s_add_u32 m0, s49, 0\n"
"buffer_load_dwordx4 v23, %2, s46 offen offset:0 lds\n"
"ds_read_b64_tr_b16 v[52:53], v2 offset:32776\n"
"s_waitcnt lgkmcnt(4)\n"
"v_mfma_f32_16x16x32_bf16 v[76:79], v[36:39], a[96:99], v[76:79]\n"
"ds_read_b64_tr_b16 v[54:55], v2 offset:40968\n"
"v_mfma_f32_16x16x32_bf16 v[80:83], v[36:39], a[100:103], v[80:83]\n"
"ds_read_b64_tr_b16 v[56:57], v3 offset:32768\n"
"ds_read_b64_tr_b16 v[58:59], v3 offset:40960\n"
"buffer_load_dwordx4 v24, %2, s46 offen offset:1024 lds\n"
"v_mfma_f32_16x16x32_bf16 v[88:91], v[40:43], a[96:99], v[88:91]\n"
"v_mfma_f32_16x16x32_bf16 v[92:95], v[40:43], a[100:103], v[92:95]\n"
"ds_read_b64_tr_b16 v[60:61], v3 offset:32776\n"
"ds_read_b64_tr_b16 v[62:63], v3 offset:40968\n"
"s_waitcnt lgkmcnt(6)\n"
"v_mfma_f32_16x16x32_bf16 v[100:103], v[44:47], a[96:99], v[100:103]\n"
"buffer_load_dwordx4 v25, %2, s46 offen offset:2048 lds\n"
"v_mfma_f32_16x16x32_bf16 v[104:107], v[44:47], a[100:103], v[104:107]\n"
"ds_read_b64_tr_b16 v[32:33], v0 offset:33024\n"
"ds_read_b64_tr_b16 v[34:35], v0 offset:41216\n"
"v_accvgpr_write_b32 a84, 0\n"
"v_accvgpr_write_b32 a85, 0\n"
"v_mfma_f32_16x16x32_bf16 v[112:115], v[48:51], a[96:99], v[112:115]\n"
"v_accvgpr_write_b32 a86, 0\n"
"v_accvgpr_write_b32 a87, 0\n"
"v_mfma_f32_16x16x32_bf16 v[116:119], v[48:51], a[100:103], v[116:119]\n"
"buffer_load_dwordx4 v26, %2, s46 offen offset:3072 lds\n"
"ds_read_b64_tr_b16 v[36:37], v0 offset:33032\n"
"ds_read_b64_tr_b16 v[38:39], v0 offset:41224\n"
"v_accvgpr_write_b32 a88, 0\n"
"s_waitcnt lgkmcnt(4)\n"
"v_mfma_f32_16x16x32_bf16 v[124:127], v[52:55], a[96:99], v[124:127]\n"
"v_accvgpr_write_b32 a89, 0\n"
"v_accvgpr_write_b32 a90, 0\n"
"v_mfma_f32_16x16x32_bf16 v[224:227], v[52:55], a[100:103], v[224:227]\n"
"ds_read_b64_tr_b16 v[40:41], v1 offset:33024\n"
"ds_read_b64_tr_b16 v[42:43], v1 offset:41216\n"
"s_mov_b32 s46, 3407872\n"
"s_add_u32 m0, s40, 81920\n"
"v_mfma_f32_16x16x32_bf16 v[232:235], v[56:59], a[96:99], v[232:235]\n"
"buffer_load_dwordx4 v8, %3, s46 offen offset:0 lds\n"
"v_accvgpr_write_b32 a91, 0\n"
"v_mfma_f32_16x16x32_bf16 v[236:239], v[56:59], a[100:103], v[236:239]\n"
"ds_read_b64_tr_b16 v[44:45], v1 offset:33032\n"
"ds_read_b64_tr_b16 v[46:47], v1 offset:41224\n"
"v_accvgpr_write_b32 a92, 0\n"
"v_accvgpr_write_b32 a93, 0\n"
"v_mfma_f32_16x16x32_bf16 v[244:247], v[60:63], a[96:99], v[244:247]\n"
"v_accvgpr_write_b32 a94, 0\n"
"v_accvgpr_write_b32 a95, 0\n"
"v_mfma_f32_16x16x32_bf16 v[248:251], v[60:63], a[100:103], v[248:251]\n"
"buffer_load_dwordx4 v9, %3, s46 offen offset:1024 lds\n"
"ds_read_b64_tr_b16 v[48:49], v2 offset:33024\n"
"ds_read_b64_tr_b16 v[50:51], v2 offset:41216\n"
"v_accvgpr_write_b32 a192, 0\n"
"s_waitcnt lgkmcnt(4)\n"
"v_mfma_f32_16x16x32_bf16 v[128:131], v[32:35], a[96:99], v[128:131]\n"
"v_accvgpr_write_b32 a193, 0\n"
"v_accvgpr_write_b32 a194, 0\n"
"v_mfma_f32_16x16x32_bf16 v[132:135], v[32:35], a[100:103], v[132:135]\n"
"ds_read_b64_tr_b16 v[52:53], v2 offset:33032\n"
"ds_read_b64_tr_b16 v[54:55], v2 offset:41224\n"
"buffer_load_dwordx4 v10, %3, s46 offen offset:2048 lds\n"
"v_mfma_f32_16x16x32_bf16 v[140:143], v[36:39], a[96:99], v[140:143]\n"
"v_mfma_f32_16x16x32_bf16 v[144:147], v[36:39], a[100:103], v[144:147]\n"
"ds_read_b64_tr_b16 v[56:57], v3 offset:33024\n"
"ds_read_b64_tr_b16 v[58:59], v3 offset:41216\n"
"ds_read_b64_tr_b16 v[60:61], v3 offset:33032\n"
"ds_read_b64_tr_b16 v[62:63], v3 offset:41224\n"
"v_mfma_f32_16x16x32_bf16 v[152:155], v[40:43], a[96:99], v[152:155]\n"
"v_mfma_f32_16x16x32_bf16 v[156:159], v[40:43], a[100:103], v[156:159]\n"
"buffer_load_dwordx4 v11, %3, s46 offen offset:3072 lds\n"
"s_waitcnt lgkmcnt(6)\n"
"v_mfma_f32_16x16x32_bf16 v[164:167], v[44:47], a[96:99], v[164:167]\n"
"v_mfma_f32_16x16x32_bf16 v[168:171], v[44:47], a[100:103], v[168:171]\n"
"buffer_load_dword a200, v27, s[88:91], 0 offen offset:0\n"
"v_mfma_f32_16x16x32_bf16 v[176:179], v[48:51], a[96:99], v[176:179]\n"
"v_mfma_f32_16x16x32_bf16 v[180:183], v[48:51], a[100:103], v[180:183]\n"
"s_waitcnt vmcnt(21) lgkmcnt(0)\n"
"s_barrier\n"
"v_mfma_f32_16x16x32_bf16 v[188:191], v[52:55], a[96:99], v[188:191]\n"
"buffer_load_dword a200, v29, %4, 0 offen offset:0\n"
"ds_read_b128 a[120:123], v21 offset:6144\n"
"v_mfma_f32_16x16x32_bf16 v[192:195], v[52:55], a[100:103], v[192:195]\n"
"ds_read_b128 a[124:127], v21 offset:8192\n"
"ds_read_b128 a[132:135], v22 offset:6144\n"
"ds_read_b128 a[136:139], v22 offset:8192\n"
"ds_read_b64_tr_b16 v[32:33], v0 offset:49152\n"
"v_mfma_f32_16x16x32_bf16 v[200:203], v[56:59], a[96:99], v[200:203]\n"
"ds_read_b64_tr_b16 v[34:35], v0 offset:57344\n"
"ds_read_b64_tr_b16 v[36:37], v0 offset:49160\n"
"v_mfma_f32_16x16x32_bf16 v[204:207], v[56:59], a[100:103], v[204:207]\n"
"ds_read_b64_tr_b16 v[38:39], v0 offset:57352\n"
"ds_read_b64_tr_b16 v[40:41], v1 offset:49152\n"
"ds_read_b64_tr_b16 v[42:43], v1 offset:57344\n"
"ds_read_b64_tr_b16 v[44:45], v1 offset:49160\n"
"v_mfma_f32_16x16x32_bf16 v[212:215], v[60:63], a[96:99], v[212:215]\n"
"ds_read_b64_tr_b16 v[46:47], v1 offset:57352\n"
"v_mfma_f32_16x16x32_bf16 v[216:219], v[60:63], a[100:103], v[216:219]\n"
"s_waitcnt lgkmcnt(6)\n"
"v_mfma_f32_16x16x32_bf16 v[64:67], v[32:35], a[108:111], v[64:67]\n"
"ds_read_b64_tr_b16 v[48:49], v2 offset:49152\n"
"ds_read_b64_tr_b16 v[50:51], v2 offset:57344\n"
"v_mfma_f32_16x16x32_bf16 v[68:71], v[32:35], a[112:115], v[68:71]\n"
"s_add_u32 s47, %9, 0\n"
"s_and_b32 s47, s47, 0xfffff\n"
"s_add_u32 m0, s40, 98304\n"
"ds_read_b64_tr_b16 v[52:53], v2 offset:49160\n"
"s_waitcnt lgkmcnt(5)\n"
"v_mfma_f32_16x16x32_bf16 v[76:79], v[36:39], a[108:111], v[76:79]\n"
"buffer_load_dwordx4 v16, %1, s47 offen offset:0 lds\n"
"ds_read_b64_tr_b16 v[54:55], v2 offset:57352\n"
"v_mfma_f32_16x16x32_bf16 v[80:83], v[36:39], a[112:115], v[80:83]\n"
"ds_read_b64_tr_b16 v[56:57], v3 offset:49152\n"
"ds_read_b64_tr_b16 v[58:59], v3 offset:57344\n"
"v_mfma_f32_16x16x32_bf16 v[88:91], v[40:43], a[108:111], v[88:91]\n"
"v_mfma_f32_16x16x32_bf16 v[92:95], v[40:43], a[112:115], v[92:95]\n"
"ds_read_b64_tr_b16 v[60:61], v3 offset:49160\n"
"ds_read_b64_tr_b16 v[62:63], v3 offset:57352\n"
"s_waitcnt lgkmcnt(6)\n"
"v_mfma_f32_16x16x32_bf16 v[100:103], v[44:47], a[108:111], v[100:103]\n"
"v_mfma_f32_16x16x32_bf16 v[104:107], v[44:47], a[112:115], v[104:107]\n"
"ds_read_b64_tr_b16 v[32:33], v0 offset:49408\n"
"ds_read_b64_tr_b16 v[34:35], v0 offset:57600\n"
"buffer_load_dwordx4 v17, %1, s47 offen offset:1024 lds\n"
"v_accvgpr_write_b32 a96, 0\n"
"v_mfma_f32_16x16x32_bf16 v[112:115], v[48:51], a[108:111], v[112:115]\n"
"v_accvgpr_write_b32 a97, 0\n"
"v_accvgpr_write_b32 a98, 0\n"
"v_mfma_f32_16x16x32_bf16 v[116:119], v[48:51], a[112:115], v[116:119]\n"
"ds_read_b64_tr_b16 v[36:37], v0 offset:49416\n"
"ds_read_b64_tr_b16 v[38:39], v0 offset:57608\n"
"v_accvgpr_write_b32 a99, 0\n"
"v_accvgpr_write_b32 a100, 0\n"
"s_waitcnt lgkmcnt(4)\n"
"v_mfma_f32_16x16x32_bf16 v[124:127], v[52:55], a[108:111], v[124:127]\n"
"v_accvgpr_write_b32 a101, 0\n"
"v_accvgpr_write_b32 a102, 0\n"
"v_mfma_f32_16x16x32_bf16 v[224:227], v[52:55], a[112:115], v[224:227]\n"
"ds_read_b64_tr_b16 v[40:41], v1 offset:49408\n"
"ds_read_b64_tr_b16 v[42:43], v1 offset:57600\n"
"v_accvgpr_write_b32 a103, 0\n"
"v_accvgpr_write_b32 a104, 0\n"
"v_mfma_f32_16x16x32_bf16 v[232:235], v[56:59], a[108:111], v[232:235]\n"
"v_accvgpr_write_b32 a105, 0\n"
"v_accvgpr_write_b32 a106, 0\n"
"v_mfma_f32_16x16x32_bf16 v[236:239], v[56:59], a[112:115], v[236:239]\n"
"ds_read_b64_tr_b16 v[44:45], v1 offset:49416\n"
"ds_read_b64_tr_b16 v[46:47], v1 offset:57608\n"
"v_accvgpr_write_b32 a107, 0\n"
"v_accvgpr_write_b32 a195, 0\n"
"v_mfma_f32_16x16x32_bf16 v[244:247], v[60:63], a[108:111], v[244:247]\n"
"v_accvgpr_write_b32 a196, 0\n"
"v_accvgpr_write_b32 a197, 0\n"
"v_mfma_f32_16x16x32_bf16 v[248:251], v[60:63], a[112:115], v[248:251]\n"
"buffer_load_dwordx4 v18, %1, s47 offen offset:2048 lds\n"
"ds_read_b64_tr_b16 v[48:49], v2 offset:49408\n"
"ds_read_b64_tr_b16 v[50:51], v2 offset:57600\n"
"s_waitcnt lgkmcnt(4)\n"
"v_mfma_f32_16x16x32_bf16 v[128:131], v[32:35], a[108:111], v[128:131]\n"
"v_mfma_f32_16x16x32_bf16 v[132:135], v[32:35], a[112:115], v[132:135]\n"
"ds_read_b64_tr_b16 v[52:53], v2 offset:49416\n"
"ds_read_b64_tr_b16 v[54:55], v2 offset:57608\n"
"v_mfma_f32_16x16x32_bf16 v[140:143], v[36:39], a[108:111], v[140:143]\n"
"v_mfma_f32_16x16x32_bf16 v[144:147], v[36:39], a[112:115], v[144:147]\n"
"ds_read_b64_tr_b16 v[56:57], v3 offset:49408\n"
"ds_read_b64_tr_b16 v[58:59], v3 offset:57600\n"
"ds_read_b64_tr_b16 v[60:61], v3 offset:49416\n"
"ds_read_b64_tr_b16 v[62:63], v3 offset:57608\n"
"v_mfma_f32_16x16x32_bf16 v[152:155], v[40:43], a[108:111], v[152:155]\n"
"v_mfma_f32_16x16x32_bf16 v[156:159], v[40:43], a[112:115], v[156:159]\n"
"s_waitcnt lgkmcnt(6)\n"
"v_mfma_f32_16x16x32_bf16 v[164:167], v[44:47], a[108:111], v[164:167]\n"
"buffer_load_dwordx4 v19, %1, s47 offen offset:3072 lds\n"
"v_mfma_f32_16x16x32_bf16 v[168:171], v[44:47], a[112:115], v[168:171]\n"
"v_mfma_f32_16x16x32_bf16 v[176:179], v[48:51], a[108:111], v[176:179]\n"
"v_mfma_f32_16x16x32_bf16 v[180:183], v[48:51], a[112:115], v[180:183]\n"
"s_waitcnt vmcnt(30) lgkmcnt(0)\n"
"s_barrier\n"
"v_mfma_f32_16x16x32_bf16 v[188:191], v[52:55], a[108:111], v[188:191]\n"
"ds_read_b64_tr_b16 v[32:33], v4 offset:0\n"
"ds_read_b64_tr_b16 v[34:35], v4 offset:8192\n"
"v_mfma_f32_16x16x32_bf16 v[192:195], v[52:55], a[112:115], v[192:195]\n"
"ds_read_b64_tr_b16 v[36:37], v4 offset:8\n"
"ds_read_b64_tr_b16 v[38:39], v4 offset:8200\n"
"ds_read_b64_tr_b16 v[40:41], v5 offset:0\n"
"ds_read_b64_tr_b16 v[42:43], v5 offset:8192\n"
"v_mfma_f32_16x16x32_bf16 v[200:203], v[56:59], a[108:111], v[200:203]\n"
"ds_read_b64_tr_b16 v[44:45], v5 offset:8\n"
"ds_read_b64_tr_b16 v[46:47], v5 offset:8200\n"
"v_mfma_f32_16x16x32_bf16 v[204:207], v[56:59], a[112:115], v[204:207]\n"
"v_mfma_f32_16x16x32_bf16 v[212:215], v[60:63], a[108:111], v[212:215]\n"
"v_mfma_f32_16x16x32_bf16 v[216:219], v[60:63], a[112:115], v[216:219]\n"
"s_waitcnt lgkmcnt(6)\n"
"v_mfma_f32_16x16x32_bf16 v[64:67], v[32:35], a[120:123], v[64:67]\n"
"ds_read_b64_tr_b16 v[48:49], v6 offset:0\n"
"ds_read_b64_tr_b16 v[50:51], v6 offset:8192\n"
"v_mfma_f32_16x16x32_bf16 v[68:71], v[32:35], a[124:127], v[68:71]\n"
"s_add_u32 s47, %9, 16384\n"
"s_and_b32 s47, s47, 0xfffff\n"
"s_add_u32 m0, s40, 114688\n"
"ds_read_b64_tr_b16 v[52:53], v6 offset:8\n"
"s_waitcnt lgkmcnt(3)\n"
"v_mfma_f32_16x16x32_bf16 v[76:79], v[36:39], a[120:123], v[76:79]\n"
"buffer_load_dwordx4 v16, %1, s47 offen offset:0 lds\n"
"ds_read_b64_tr_b16 v[54:55], v6 offset:8200\n"
"v_mfma_f32_16x16x32_bf16 v[80:83], v[36:39], a[124:127], v[80:83]\n"
"ds_read_b64_tr_b16 v[56:57], v7 offset:0\n"
"ds_read_b64_tr_b16 v[58:59], v7 offset:8192\n"
"v_mfma_f32_16x16x32_bf16 v[88:91], v[40:43], a[120:123], v[88:91]\n"
"v_mfma_f32_16x16x32_bf16 v[92:95], v[40:43], a[124:127], v[92:95]\n"
"ds_read_b64_tr_b16 v[60:61], v7 offset:8\n"
"ds_read_b64_tr_b16 v[62:63], v7 offset:8200\n"
"v_mfma_f32_16x16x32_bf16 v[100:103], v[44:47], a[120:123], v[100:103]\n"
"v_mfma_f32_16x16x32_bf16 v[104:107], v[44:47], a[124:127], v[104:107]\n"
"ds_read_b64_tr_b16 v[32:33], v4 offset:256\n"
"ds_read_b64_tr_b16 v[34:35], v4 offset:8448\n"
"buffer_load_dwordx4 v17, %1, s47 offen offset:1024 lds\n"
"v_accvgpr_write_b32 a108, 0\n"
"s_waitcnt lgkmcnt(6)\n"
"v_mfma_f32_16x16x32_bf16 v[112:115], v[48:51], a[120:123], v[112:115]\n"
"v_accvgpr_write_b32 a109, 0\n"
"v_accvgpr_write_b32 a110, 0\n"
"v_mfma_f32_16x16x32_bf16 v[116:119], v[48:51], a[124:127], v[116:119]\n"
"ds_read_b64_tr_b16 v[36:37], v4 offset:264\n"
"ds_read_b64_tr_b16 v[38:39], v4 offset:8456\n"
"v_accvgpr_write_b32 a111, 0\n"
"v_accvgpr_write_b32 a112, 0\n"
"v_mfma_f32_16x16x32_bf16 v[124:127], v[52:55], a[120:123], v[124:127]\n"
"v_accvgpr_write_b32 a113, 0\n"
"v_accvgpr_write_b32 a114, 0\n"
"v_mfma_f32_16x16x32_bf16 v[224:227], v[52:55], a[124:127], v[224:227]\n"
"ds_read_b64_tr_b16 v[40:41], v5 offset:256\n"
"ds_read_b64_tr_b16 v[42:43], v5 offset:8448\n"
"v_accvgpr_write_b32 a115, 0\n"
"v_accvgpr_write_b32 a116, 0\n"
"s_waitcnt lgkmcnt(4)\n"
"v_mfma_f32_16x16x32_bf16 v[232:235], v[56:59], a[120:123], v[232:235]\n"
"v_accvgpr_write_b32 a117, 0\n"
"v_accvgpr_write_b32 a118, 0\n"
"v_mfma_f32_16x16x32_bf16 v[236:239], v[56:59], a[124:127], v[236:239]\n"
"ds_read_b64_tr_b16 v[44:45], v5 offset:264\n"
"ds_read_b64_tr_b16 v[46:47], v5 offset:8456\n"
"v_accvgpr_write_b32 a119, 0\n"
"v_accvgpr_write_b32 a198, 0\n"
"v_mfma_f32_16x16x32_bf16 v[244:247], v[60:63], a[120:123], v[244:247]\n"
"v_accvgpr_write_b32 a199, 0\n"
"v_accvgpr_write_b32 a200, 0\n"
"v_mfma_f32_16x16x32_bf16 v[248:251], v[60:63], a[124:127], v[248:251]\n"
"buffer_load_dwordx4 v18, %1, s47 offen offset:2048 lds\n"
"ds_read_b64_tr_b16 v[48:49], v6 offset:256\n"
"ds_read_b64_tr_b16 v[50:51], v6 offset:8448\n"
"v_mfma_f32_16x16x32_bf16 v[128:131], v[32:35], a[120:123], v[128:131]\n"
"v_mfma_f32_16x16x32_bf16 v[132:135], v[32:35], a[124:127], v[132:135]\n"
"ds_read_b64_tr_b16 v[52:53], v6 offset:264\n"
"ds_read_b64_tr_b16 v[54:55], v6 offset:8456\n"
"s_waitcnt lgkmcnt(4)\n"
"v_mfma_f32_16x16x32_bf16 v[140:143], v[36:39], a[120:123], v[140:143]\n"
"v_mfma_f32_16x16x32_bf16 v[144:147], v[36:39], a[124:127], v[144:147]\n"
"ds_read_b64_tr_b16 v[56:57], v7 offset:256\n"
"ds_read_b64_tr_b16 v[58:59], v7 offset:8448\n"
"ds_read_b64_tr_b16 v[60:61], v7 offset:264\n"
"ds_read_b64_tr_b16 v[62:63], v7 offset:8456\n"
"v_mfma_f32_16x16x32_bf16 v[152:155], v[40:43], a[120:123], v[152:155]\n"
"v_mfma_f32_16x16x32_bf16 v[156:159], v[40:43], a[124:127], v[156:159]\n"
"v_mfma_f32_16x16x32_bf16 v[164:167], v[44:47], a[120:123], v[164:167]\n"
"buffer_load_dwordx4 v19, %1, s47 offen offset:3072 lds\n"
"v_mfma_f32_16x16x32_bf16 v[168:171], v[44:47], a[124:127], v[168:171]\n"
"s_waitcnt lgkmcnt(4)\n"
"v_mfma_f32_16x16x32_bf16 v[176:179], v[48:51], a[120:123], v[176:179]\n"
"v_mfma_f32_16x16x32_bf16 v[180:183], v[48:51], a[124:127], v[180:183]\n"
"s_waitcnt vmcnt(14) lgkmcnt(0)\n"
"s_barrier\n"
"v_mfma_f32_16x16x32_bf16 v[188:191], v[52:55], a[120:123], v[188:191]\n"
"ds_read_b128 a[144:147], v21 offset:0\n"
"ds_read_b128 a[148:151], v21 offset:2048\n"
"v_mfma_f32_16x16x32_bf16 v[192:195], v[52:55], a[124:127], v[192:195]\n"
"ds_read_b128 a[156:159], v22 offset:0\n"
"ds_read_b128 a[160:163], v22 offset:2048\n"
"ds_read_b64_tr_b16 v[32:33], v4 offset:16384\n"
"ds_read_b64_tr_b16 v[34:35], v4 offset:24576\n"
"v_mfma_f32_16x16x32_bf16 v[200:203], v[56:59], a[120:123], v[200:203]\n"
"ds_read_b64_tr_b16 v[36:37], v4 offset:16392\n"
"ds_read_b64_tr_b16 v[38:39], v4 offset:24584\n"
"v_mfma_f32_16x16x32_bf16 v[204:207], v[56:59], a[124:127], v[204:207]\n"
"ds_read_b64_tr_b16 v[40:41], v5 offset:16384\n"
"ds_read_b64_tr_b16 v[42:43], v5 offset:24576\n"
"ds_read_b64_tr_b16 v[44:45], v5 offset:16392\n"
"ds_read_b64_tr_b16 v[46:47], v5 offset:24584\n"
"v_mfma_f32_16x16x32_bf16 v[212:215], v[60:63], a[120:123], v[212:215]\n"
"v_mfma_f32_16x16x32_bf16 v[216:219], v[60:63], a[124:127], v[216:219]\n"
"s_waitcnt lgkmcnt(6)\n"
"v_mfma_f32_16x16x32_bf16 v[64:67], v[32:35], a[132:135], v[64:67]\n"
"ds_read_b64_tr_b16 v[48:49], v6 offset:16384\n"
"ds_read_b64_tr_b16 v[50:51], v6 offset:24576\n"
"v_mfma_f32_16x16x32_bf16 v[68:71], v[32:35], a[136:139], v[68:71]\n"
"ds_read_b64_tr_b16 v[52:53], v6 offset:16392\n"
"ds_read_b64_tr_b16 v[54:55], v6 offset:24584\n"
"s_waitcnt lgkmcnt(7)\n"
"v_mfma_f32_16x16x32_bf16 v[76:79], v[36:39], a[132:135], v[76:79]\n"
"v_mfma_f32_16x16x32_bf16 v[80:83], v[36:39], a[136:139], v[80:83]\n"
"ds_read_b64_tr_b16 v[56:57], v7 offset:16384\n"
"ds_read_b64_tr_b16 v[58:59], v7 offset:24576\n"
"s_waitcnt lgkmcnt(6)\n"
"v_mfma_f32_16x16x32_bf16 v[88:91], v[40:43], a[132:135], v[88:91]\n"
"v_mfma_f32_16x16x32_bf16 v[92:95], v[40:43], a[136:139], v[92:95]\n"
"ds_read_b64_tr_b16 v[60:61], v7 offset:16392\n"
"ds_read_b64_tr_b16 v[62:63], v7 offset:24584\n"
"v_mfma_f32_16x16x32_bf16 v[100:103], v[44:47], a[132:135], v[100:103]\n"
"v_mfma_f32_16x16x32_bf16 v[104:107], v[44:47], a[136:139], v[104:107]\n"
"ds_read_b64_tr_b16 v[32:33], v4 offset:16640\n"
"ds_read_b64_tr_b16 v[34:35], v4 offset:24832\n"
"v_accvgpr_write_b32 a120, 0\n"
"v_accvgpr_write_b32 a121, 0\n"
"s_waitcnt lgkmcnt(5)\n"
"v_mfma_f32_16x16x32_bf16 v[112:115], v[48:51], a[132:135], v[112:115]\n"
"v_accvgpr_write_b32 a122, 0\n"
"v_accvgpr_write_b32 a123, 0\n"
"v_mfma_f32_16x16x32_bf16 v[116:119], v[48:51], a[136:139], v[116:119]\n"
"ds_read_b64_tr_b16 v[36:37], v4 offset:16648\n"
"ds_read_b64_tr_b16 v[38:39], v4 offset:24840\n"
"v_accvgpr_write_b32 a124, 0\n"
"v_accvgpr_write_b32 a125, 0\n"
"v_mfma_f32_16x16x32_bf16 v[124:127], v[52:55], a[132:135], v[124:127]\n"
"v_accvgpr_write_b32 a126, 0\n"
"v_accvgpr_write_b32 a127, 0\n"
"v_mfma_f32_16x16x32_bf16 v[224:227], v[52:55], a[136:139], v[224:227]\n"
"ds_read_b64_tr_b16 v[40:41], v5 offset:16640\n"
"ds_read_b64_tr_b16 v[42:43], v5 offset:24832\n"
"v_accvgpr_write_b32 a128, 0\n"
"v_accvgpr_write_b32 a129, 0\n"
"s_waitcnt lgkmcnt(4)\n"
"v_mfma_f32_16x16x32_bf16 v[232:235], v[56:59], a[132:135], v[232:235]\n"
"v_accvgpr_write_b32 a130, 0\n"
"v_accvgpr_write_b32 a131, 0\n"
"v_mfma_f32_16x16x32_bf16 v[236:239], v[56:59], a[136:139], v[236:239]\n"
"ds_read_b64_tr_b16 v[44:45], v5 offset:16648\n"
"ds_read_b64_tr_b16 v[46:47], v5 offset:24840\n"
"v_accvgpr_write_b32 a201, 0\n"
"v_accvgpr_write_b32 a202, 0\n"
"v_mfma_f32_16x16x32_bf16 v[244:247], v[60:63], a[132:135], v[244:247]\n"
"v_accvgpr_write_b32 a203, 0\n"
"v_mfma_f32_16x16x32_bf16 v[248:251], v[60:63], a[136:139], v[248:251]\n"
"ds_read_b64_tr_b16 v[48:49], v6 offset:16640\n"
"ds_read_b64_tr_b16 v[50:51], v6 offset:24832\n"
"v_mfma_f32_16x16x32_bf16 v[128:131], v[32:35], a[132:135], v[128:131]\n"
"v_mfma_f32_16x16x32_bf16 v[132:135], v[32:35], a[136:139], v[132:135]\n"
"ds_read_b64_tr_b16 v[52:53], v6 offset:16648\n"
"ds_read_b64_tr_b16 v[54:55], v6 offset:24840\n"
"s_waitcnt lgkmcnt(5)\n"
"v_mfma_f32_16x16x32_bf16 v[140:143], v[36:39], a[132:135], v[140:143]\n"
"v_mfma_f32_16x16x32_bf16 v[144:147], v[36:39], a[136:139], v[144:147]\n"
"ds_read_b64_tr_b16 v[56:57], v7 offset:16640\n"
"ds_read_b64_tr_b16 v[58:59], v7 offset:24832\n"
"ds_read_b64_tr_b16 v[60:61], v7 offset:16648\n"
"ds_read_b64_tr_b16 v[62:63], v7 offset:24840\n"
"v_mfma_f32_16x16x32_bf16 v[152:155], v[40:43], a[132:135], v[152:155]\n"
"v_mfma_f32_16x16x32_bf16 v[156:159], v[40:43], a[136:139], v[156:159]\n"
"s_waitcnt lgkmcnt(6)\n"
"v_mfma_f32_16x16x32_bf16 v[164:167], v[44:47], a[132:135], v[164:167]\n"
"v_mfma_f32_16x16x32_bf16 v[168:171], v[44:47], a[136:139], v[168:171]\n"
"v_mfma_f32_16x16x32_bf16 v[176:179], v[48:51], a[132:135], v[176:179]\n"
"v_mfma_f32_16x16x32_bf16 v[180:183], v[48:51], a[136:139], v[180:183]\n"
"s_waitcnt vmcnt(22) lgkmcnt(0)\n"
"s_barrier\n"
"v_mfma_f32_16x16x32_bf16 v[188:191], v[52:55], a[132:135], v[188:191]\n"
"ds_read_b64_tr_b16 v[32:33], v0 offset:0\n"
"ds_read_b64_tr_b16 v[34:35], v0 offset:8192\n"
"v_mfma_f32_16x16x32_bf16 v[192:195], v[52:55], a[136:139], v[192:195]\n"
"ds_read_b64_tr_b16 v[36:37], v0 offset:8\n"
"ds_read_b64_tr_b16 v[38:39], v0 offset:8200\n"
"ds_read_b64_tr_b16 v[40:41], v1 offset:0\n"
"ds_read_b64_tr_b16 v[42:43], v1 offset:8192\n"
"v_mfma_f32_16x16x32_bf16 v[200:203], v[56:59], a[132:135], v[200:203]\n"
"ds_read_b64_tr_b16 v[44:45], v1 offset:8\n"
"ds_read_b64_tr_b16 v[46:47], v1 offset:8200\n"
"v_mfma_f32_16x16x32_bf16 v[204:207], v[56:59], a[136:139], v[204:207]\n"
"v_mfma_f32_16x16x32_bf16 v[212:215], v[60:63], a[132:135], v[212:215]\n"
"v_mfma_f32_16x16x32_bf16 v[216:219], v[60:63], a[136:139], v[216:219]\n"
"s_waitcnt lgkmcnt(6)\n"
"v_mfma_f32_16x16x32_bf16 v[64:67], v[32:35], a[144:147], v[64:67]\n"
"ds_read_b64_tr_b16 v[48:49], v2 offset:0\n"
"ds_read_b64_tr_b16 v[50:51], v2 offset:8192\n"
"v_mfma_f32_16x16x32_bf16 v[68:71], v[32:35], a[148:151], v[68:71]\n"
"s_add_u32 s47, %9, 16384\n"
"s_and_b32 s47, s47, 0xfffff\n"
"s_add_u32 m0, s40, 16384\n"
"ds_read_b64_tr_b16 v[52:53], v2 offset:8\n"
"s_waitcnt lgkmcnt(3)\n"
"v_mfma_f32_16x16x32_bf16 v[76:79], v[36:39], a[144:147], v[76:79]\n"
"buffer_load_dwordx4 v12, %0, s47 offen offset:0 lds\n"
"ds_read_b64_tr_b16 v[54:55], v2 offset:8200\n"
"v_mfma_f32_16x16x32_bf16 v[80:83], v[36:39], a[148:151], v[80:83]\n"
"ds_read_b64_tr_b16 v[56:57], v3 offset:0\n"
"ds_read_b64_tr_b16 v[58:59], v3 offset:8192\n"
"v_mfma_f32_16x16x32_bf16 v[88:91], v[40:43], a[144:147], v[88:91]\n"
"buffer_load_dwordx4 v13, %0, s47 offen offset:1024 lds\n"
"v_mfma_f32_16x16x32_bf16 v[92:95], v[40:43], a[148:151], v[92:95]\n"
"ds_read_b64_tr_b16 v[60:61], v3 offset:8\n"
"ds_read_b64_tr_b16 v[62:63], v3 offset:8200\n"
"v_mfma_f32_16x16x32_bf16 v[100:103], v[44:47], a[144:147], v[100:103]\n"
"v_mfma_f32_16x16x32_bf16 v[104:107], v[44:47], a[148:151], v[104:107]\n"
"ds_read_b64_tr_b16 v[32:33], v0 offset:256\n"
"ds_read_b64_tr_b16 v[34:35], v0 offset:8448\n"
"buffer_load_dwordx4 v14, %0, s47 offen offset:2048 lds\n"
"v_accvgpr_write_b32 a132, 0\n"
"s_waitcnt lgkmcnt(5)\n"
"v_mfma_f32_16x16x32_bf16 v[112:115], v[48:51], a[144:147], v[112:115]\n"
"v_accvgpr_write_b32 a133, 0\n"
"v_accvgpr_write_b32 a134, 0\n"
"v_mfma_f32_16x16x32_bf16 v[116:119], v[48:51], a[148:151], v[116:119]\n"
"ds_read_b64_tr_b16 v[36:37], v0 offset:264\n"
"ds_read_b64_tr_b16 v[38:39], v0 offset:8456\n"
"v_accvgpr_write_b32 a135, 0\n"
"v_accvgpr_write_b32 a136, 0\n"
"v_mfma_f32_16x16x32_bf16 v[124:127], v[52:55], a[144:147], v[124:127]\n"
"v_accvgpr_write_b32 a137, 0\n"
"v_accvgpr_write_b32 a138, 0\n"
"v_mfma_f32_16x16x32_bf16 v[224:227], v[52:55], a[148:151], v[224:227]\n"
"ds_read_b64_tr_b16 v[40:41], v1 offset:256\n"
"ds_read_b64_tr_b16 v[42:43], v1 offset:8448\n"
"buffer_load_dwordx4 v15, %0, s47 offen offset:3072 lds\n"
"v_accvgpr_write_b32 a139, 0\n"
"s_waitcnt lgkmcnt(4)\n"
"v_mfma_f32_16x16x32_bf16 v[232:235], v[56:59], a[144:147], v[232:235]\n"
"v_accvgpr_write_b32 a140, 0\n"
"v_accvgpr_write_b32 a141, 0\n"
"v_mfma_f32_16x16x32_bf16 v[236:239], v[56:59], a[148:151], v[236:239]\n"
"ds_read_b64_tr_b16 v[44:45], v1 offset:264\n"
"ds_read_b64_tr_b16 v[46:47], v1 offset:8456\n"
"v_accvgpr_write_b32 a142, 0\n"
"v_accvgpr_write_b32 a143, 0\n"
"v_mfma_f32_16x16x32_bf16 v[244:247], v[60:63], a[144:147], v[244:247]\n"
"s_add_u32 s47, %9, 32768\n"
"s_and_b32 s47, s47, 0xfffff\n"
"v_mfma_f32_16x16x32_bf16 v[248:251], v[60:63], a[148:151], v[248:251]\n"
"s_add_u32 m0, s40, 32768\n"
"ds_read_b64_tr_b16 v[48:49], v2 offset:256\n"
"buffer_load_dwordx4 v12, %0, s47 offen offset:0 lds\n"
"ds_read_b64_tr_b16 v[50:51], v2 offset:8448\n"
"v_mfma_f32_16x16x32_bf16 v[128:131], v[32:35], a[144:147], v[128:131]\n"
"v_mfma_f32_16x16x32_bf16 v[132:135], v[32:35], a[148:151], v[132:135]\n"
"ds_read_b64_tr_b16 v[52:53], v2 offset:264\n"
"ds_read_b64_tr_b16 v[54:55], v2 offset:8456\n"
"s_waitcnt lgkmcnt(4)\n"
"v_mfma_f32_16x16x32_bf16 v[140:143], v[36:39], a[144:147], v[140:143]\n"
"buffer_load_dwordx4 v13, %0, s47 offen offset:1024 lds\n"
"v_mfma_f32_16x16x32_bf16 v[144:147], v[36:39], a[148:151], v[144:147]\n"
"ds_read_b64_tr_b16 v[56:57], v3 offset:256\n"
"ds_read_b64_tr_b16 v[58:59], v3 offset:8448\n"
"ds_read_b64_tr_b16 v[60:61], v3 offset:264\n"
"ds_read_b64_tr_b16 v[62:63], v3 offset:8456\n"
"v_mfma_f32_16x16x32_bf16 v[152:155], v[40:43], a[144:147], v[152:155]\n"
"v_mfma_f32_16x16x32_bf16 v[156:159], v[40:43], a[148:151], v[156:159]\n"
"v_mfma_f32_16x16x32_bf16 v[164:167], v[44:47], a[144:147], v[164:167]\n"
"buffer_load_dwordx4 v14, %0, s47 offen offset:2048 lds\n"
"v_mfma_f32_16x16x32_bf16 v[168:171], v[44:47], a[148:151], v[168:171]\n"
"s_waitcnt lgkmcnt(4)\n"
"v_mfma_f32_16x16x32_bf16 v[176:179], v[48:51], a[144:147], v[176:179]\n"
"v_mfma_f32_16x16x32_bf16 v[180:183], v[48:51], a[148:151], v[180:183]\n"
"s_waitcnt vmcnt(17) lgkmcnt(0)\n"
"s_barrier\n"
"v_mfma_f32_16x16x32_bf16 v[188:191], v[52:55], a[144:147], v[188:191]\n"
"buffer_load_dwordx4 v15, %0, s47 offen offset:3072 lds\n"
"ds_read_b64_tr_b16 v[32:33], v0 offset:16384\n"
"v_mfma_f32_16x16x32_bf16 v[192:195], v[52:55], a[148:151], v[192:195]\n"
"ds_read_b64_tr_b16 v[34:35], v0 offset:24576\n"
"ds_read_b64_tr_b16 v[36:37], v0 offset:16392\n"
"ds_read_b64_tr_b16 v[38:39], v0 offset:24584\n"
"ds_read_b64_tr_b16 v[40:41], v1 offset:16384\n"
"v_mfma_f32_16x16x32_bf16 v[200:203], v[56:59], a[144:147], v[200:203]\n"
"ds_read_b64_tr_b16 v[42:43], v1 offset:24576\n"
"ds_read_b64_tr_b16 v[44:45], v1 offset:16392\n"
"v_mfma_f32_16x16x32_bf16 v[204:207], v[56:59], a[148:151], v[204:207]\n"
"ds_read_b64_tr_b16 v[46:47], v1 offset:24584\n"
"v_mfma_f32_16x16x32_bf16 v[212:215], v[60:63], a[144:147], v[212:215]\n"
"v_mfma_f32_16x16x32_bf16 v[216:219], v[60:63], a[148:151], v[216:219]\n"
"s_waitcnt lgkmcnt(6)\n"
"v_mfma_f32_16x16x32_bf16 v[64:67], v[32:35], a[156:159], v[64:67]\n"
"ds_read_b64_tr_b16 v[48:49], v2 offset:16384\n"
"ds_read_b64_tr_b16 v[50:51], v2 offset:24576\n"
"v_mfma_f32_16x16x32_bf16 v[68:71], v[32:35], a[160:163], v[68:71]\n"
"s_add_u32 s47, %9, 49152\n"
"s_and_b32 s47, s47, 0xfffff\n"
"s_add_u32 m0, s40, 49152\n"
"ds_read_b64_tr_b16 v[52:53], v2 offset:16392\n"
"s_waitcnt lgkmcnt(4)\n"
"v_mfma_f32_16x16x32_bf16 v[76:79], v[36:39], a[156:159], v[76:79]\n"
"buffer_load_dwordx4 v12, %0, s47 offen offset:0 lds\n"
"ds_read_b64_tr_b16 v[54:55], v2 offset:24584\n"
"v_mfma_f32_16x16x32_bf16 v[80:83], v[36:39], a[160:163], v[80:83]\n"
"ds_read_b64_tr_b16 v[56:57], v3 offset:16384\n"
"ds_read_b64_tr_b16 v[58:59], v3 offset:24576\n"
"v_mfma_f32_16x16x32_bf16 v[88:91], v[40:43], a[156:159], v[88:91]\n"
"buffer_load_dwordx4 v13, %0, s47 offen offset:1024 lds\n"
"v_mfma_f32_16x16x32_bf16 v[92:95], v[40:43], a[160:163], v[92:95]\n"
"ds_read_b64_tr_b16 v[60:61], v3 offset:16392\n"
"ds_read_b64_tr_b16 v[62:63], v3 offset:24584\n"
"s_waitcnt lgkmcnt(6)\n"
"v_mfma_f32_16x16x32_bf16 v[100:103], v[44:47], a[156:159], v[100:103]\n"
"v_mfma_f32_16x16x32_bf16 v[104:107], v[44:47], a[160:163], v[104:107]\n"
"ds_read_b64_tr_b16 v[32:33], v0 offset:16640\n"
"ds_read_b64_tr_b16 v[34:35], v0 offset:24832\n"
"buffer_load_dwordx4 v14, %0, s47 offen offset:2048 lds\n"
"v_accvgpr_write_b32 a144, 0\n"
"v_mfma_f32_16x16x32_bf16 v[112:115], v[48:51], a[156:159], v[112:115]\n"
"v_accvgpr_write_b32 a145, 0\n"
"v_accvgpr_write_b32 a146, 0\n"
"v_mfma_f32_16x16x32_bf16 v[116:119], v[48:51], a[160:163], v[116:119]\n"
"ds_read_b64_tr_b16 v[36:37], v0 offset:16648\n"
"ds_read_b64_tr_b16 v[38:39], v0 offset:24840\n"
"v_accvgpr_write_b32 a147, 0\n"
"v_accvgpr_write_b32 a148, 0\n"
"s_waitcnt lgkmcnt(4)\n"
"v_mfma_f32_16x16x32_bf16 v[124:127], v[52:55], a[156:159], v[124:127]\n"
"v_accvgpr_write_b32 a149, 0\n"
"v_accvgpr_write_b32 a150, 0\n"
"v_mfma_f32_16x16x32_bf16 v[224:227], v[52:55], a[160:163], v[224:227]\n"
"ds_read_b64_tr_b16 v[40:41], v1 offset:16640\n"
"ds_read_b64_tr_b16 v[42:43], v1 offset:24832\n"
"buffer_load_dwordx4 v15, %0, s47 offen offset:3072 lds\n"
"v_accvgpr_write_b32 a151, 0\n"
"v_mfma_f32_16x16x32_bf16 v[232:235], v[56:59], a[156:159], v[232:235]\n"
"v_accvgpr_write_b32 a152, 0\n"
"v_accvgpr_write_b32 a153, 0\n"
"v_mfma_f32_16x16x32_bf16 v[236:239], v[56:59], a[160:163], v[236:239]\n"
"ds_read_b64_tr_b16 v[44:45], v1 offset:16648\n"
"ds_read_b64_tr_b16 v[46:47], v1 offset:24840\n"
"v_accvgpr_write_b32 a154, 0\n"
"v_accvgpr_write_b32 a155, 0\n"
"v_mfma_f32_16x16x32_bf16 v[244:247], v[60:63], a[156:159], v[244:247]\n"
"s_add_u32 s47, %9, 32768\n"
"s_and_b32 s47, s47, 0xfffff\n"
"v_mfma_f32_16x16x32_bf16 v[248:251], v[60:63], a[160:163], v[248:251]\n"
"s_add_u32 m0, s40, 65536\n"
"ds_read_b64_tr_b16 v[48:49], v2 offset:16640\n"
"buffer_load_dwordx4 v16, %1, s47 offen offset:0 lds\n"
"ds_read_b64_tr_b16 v[50:51], v2 offset:24832\n"
"s_waitcnt lgkmcnt(4)\n"
"v_mfma_f32_16x16x32_bf16 v[128:131], v[32:35], a[156:159], v[128:131]\n"
"v_mfma_f32_16x16x32_bf16 v[132:135], v[32:35], a[160:163], v[132:135]\n"
"ds_read_b64_tr_b16 v[52:53], v2 offset:16648\n"
"ds_read_b64_tr_b16 v[54:55], v2 offset:24840\n"
"v_mfma_f32_16x16x32_bf16 v[140:143], v[36:39], a[156:159], v[140:143]\n"
"buffer_load_dwordx4 v17, %1, s47 offen offset:1024 lds\n"
"v_mfma_f32_16x16x32_bf16 v[144:147], v[36:39], a[160:163], v[144:147]\n"
"ds_read_b64_tr_b16 v[56:57], v3 offset:16640\n"
"ds_read_b64_tr_b16 v[58:59], v3 offset:24832\n"
"ds_read_b64_tr_b16 v[60:61], v3 offset:16648\n"
"ds_read_b64_tr_b16 v[62:63], v3 offset:24840\n"
"v_mfma_f32_16x16x32_bf16 v[152:155], v[40:43], a[156:159], v[152:155]\n"
"v_mfma_f32_16x16x32_bf16 v[156:159], v[40:43], a[160:163], v[156:159]\n"
"s_waitcnt lgkmcnt(6)\n"
"v_mfma_f32_16x16x32_bf16 v[164:167], v[44:47], a[156:159], v[164:167]\n"
"buffer_load_dwordx4 v18, %1, s47 offen offset:2048 lds\n"
"v_mfma_f32_16x16x32_bf16 v[168:171], v[44:47], a[160:163], v[168:171]\n"
"v_mfma_f32_16x16x32_bf16 v[176:179], v[48:51], a[156:159], v[176:179]\n"
"v_mfma_f32_16x16x32_bf16 v[180:183], v[48:51], a[160:163], v[180:183]\n"
"buffer_load_dwordx4 v19, %1, s47 offen offset:3072 lds\n"
"s_waitcnt lgkmcnt(2)\n"
"v_mfma_f32_16x16x32_bf16 v[188:191], v[52:55], a[156:159], v[188:191]\n"
"v_mfma_f32_16x16x32_bf16 v[192:195], v[52:55], a[160:163], v[192:195]\n"
"v_mfma_f32_16x16x32_bf16 v[200:203], v[56:59], a[156:159], v[200:203]\n"
"v_mfma_f32_16x16x32_bf16 v[204:207], v[56:59], a[160:163], v[204:207]\n"
"s_waitcnt lgkmcnt(0)\n"
"v_mfma_f32_16x16x32_bf16 v[212:215], v[60:63], a[156:159], v[212:215]\n"
"v_mfma_f32_16x16x32_bf16 v[216:219], v[60:63], a[160:163], v[216:219]\n"
"v_accvgpr_write_b32 a156, 0\n"
"v_accvgpr_write_b32 a157, 0\n"
"v_accvgpr_write_b32 a158, 0\n"
"v_accvgpr_write_b32 a159, 0\n"
"v_accvgpr_write_b32 a160, 0\n"
"v_accvgpr_write_b32 a161, 0\n"
"v_accvgpr_write_b32 a162, 0\n"
"v_accvgpr_write_b32 a163, 0\n"
"v_accvgpr_write_b32 a164, 0\n"
"v_accvgpr_write_b32 a165, 0\n"
"v_accvgpr_write_b32 a166, 0\n"
"v_accvgpr_write_b32 a167, 0\n"
"s_nop 4\n"
"v_pk_mul_f32 v[128:129], v[128:129], s[50:51]\n"
"v_pk_mul_f32 v[130:131], v[130:131], s[50:51]\n"
"v_pk_mul_f32 v[140:141], v[140:141], s[50:51]\n"
"v_pk_mul_f32 v[142:143], v[142:143], s[50:51]\n"
"v_cvt_pk_bf16_f32 v128, v128, v129\n"
"v_cvt_pk_bf16_f32 v130, v130, v131\n"
"v_cvt_pk_bf16_f32 v140, v140, v141\n"
"v_cvt_pk_bf16_f32 v142, v142, v143\n"
"v_accvgpr_write_b32 a204, v128\n"
"v_accvgpr_write_b32 a205, v130\n"
"v_accvgpr_write_b32 a206, v140\n"
"v_accvgpr_write_b32 a207, v142\n"
"v_pk_mul_f32 v[132:133], v[132:133], s[50:51]\n"
"v_pk_mul_f32 v[134:135], v[134:135], s[50:51]\n"
"v_pk_mul_f32 v[144:145], v[144:145], s[50:51]\n"
"v_pk_mul_f32 v[146:147], v[146:147], s[50:51]\n"
"v_cvt_pk_bf16_f32 v132, v132, v133\n"
"v_cvt_pk_bf16_f32 v134, v134, v135\n"
"v_cvt_pk_bf16_f32 v144, v144, v145\n"
"v_cvt_pk_bf16_f32 v146, v146, v147\n"
"v_accvgpr_write_b32 a220, v132\n"
"v_accvgpr_write_b32 a221, v134\n"
"v_accvgpr_write_b32 a222, v144\n"
"v_accvgpr_write_b32 a223, v146\n"
"v_pk_mul_f32 v[152:153], v[152:153], s[50:51]\n"
"v_pk_mul_f32 v[154:155], v[154:155], s[50:51]\n"
"v_pk_mul_f32 v[164:165], v[164:165], s[50:51]\n"
"v_pk_mul_f32 v[166:167], v[166:167], s[50:51]\n"
"v_cvt_pk_bf16_f32 v152, v152, v153\n"
"v_cvt_pk_bf16_f32 v154, v154, v155\n"
"v_cvt_pk_bf16_f32 v164, v164, v165\n"
"v_cvt_pk_bf16_f32 v166, v166, v167\n"
"v_accvgpr_write_b32 a208, v152\n"
"v_accvgpr_write_b32 a209, v154\n"
"v_accvgpr_write_b32 a210, v164\n"
"v_accvgpr_write_b32 a211, v166\n"
"v_pk_mul_f32 v[156:157], v[156:157], s[50:51]\n"
"v_pk_mul_f32 v[158:159], v[158:159], s[50:51]\n"
"v_pk_mul_f32 v[168:169], v[168:169], s[50:51]\n"
"v_pk_mul_f32 v[170:171], v[170:171], s[50:51]\n"
"v_cvt_pk_bf16_f32 v156, v156, v157\n"
"v_cvt_pk_bf16_f32 v158, v158, v159\n"
"v_cvt_pk_bf16_f32 v168, v168, v169\n"
"v_cvt_pk_bf16_f32 v170, v170, v171\n"
"v_accvgpr_write_b32 a224, v156\n"
"v_accvgpr_write_b32 a225, v158\n"
"v_accvgpr_write_b32 a226, v168\n"
"v_accvgpr_write_b32 a227, v170\n"
"v_pk_mul_f32 v[176:177], v[176:177], s[50:51]\n"
"v_pk_mul_f32 v[178:179], v[178:179], s[50:51]\n"
"v_pk_mul_f32 v[188:189], v[188:189], s[50:51]\n"
"v_pk_mul_f32 v[190:191], v[190:191], s[50:51]\n"
"v_cvt_pk_bf16_f32 v176, v176, v177\n"
"v_cvt_pk_bf16_f32 v178, v178, v179\n"
"v_cvt_pk_bf16_f32 v188, v188, v189\n"
"v_cvt_pk_bf16_f32 v190, v190, v191\n"
"v_accvgpr_write_b32 a212, v176\n"
"v_accvgpr_write_b32 a213, v178\n"
"v_accvgpr_write_b32 a214, v188\n"
"v_accvgpr_write_b32 a215, v190\n"
"v_pk_mul_f32 v[180:181], v[180:181], s[50:51]\n"
"v_pk_mul_f32 v[182:183], v[182:183], s[50:51]\n"
"v_pk_mul_f32 v[192:193], v[192:193], s[50:51]\n"
"v_pk_mul_f32 v[194:195], v[194:195], s[50:51]\n"
"v_cvt_pk_bf16_f32 v180, v180, v181\n"
"v_cvt_pk_bf16_f32 v182, v182, v183\n"
"v_cvt_pk_bf16_f32 v192, v192, v193\n"
"v_cvt_pk_bf16_f32 v194, v194, v195\n"
"v_accvgpr_write_b32 a228, v180\n"
"v_accvgpr_write_b32 a229, v182\n"
"v_accvgpr_write_b32 a230, v192\n"
"v_accvgpr_write_b32 a231, v194\n"
"v_pk_mul_f32 v[200:201], v[200:201], s[50:51]\n"
"v_pk_mul_f32 v[202:203], v[202:203], s[50:51]\n"
"v_pk_mul_f32 v[212:213], v[212:213], s[50:51]\n"
"v_pk_mul_f32 v[214:215], v[214:215], s[50:51]\n"
"v_cvt_pk_bf16_f32 v200, v200, v201\n"
"v_cvt_pk_bf16_f32 v202, v202, v203\n"
"v_cvt_pk_bf16_f32 v212, v212, v213\n"
"v_cvt_pk_bf16_f32 v214, v214, v215\n"
"v_accvgpr_write_b32 a216, v200\n"
"v_accvgpr_write_b32 a217, v202\n"
"v_accvgpr_write_b32 a218, v212\n"
"v_accvgpr_write_b32 a219, v214\n"
"v_pk_mul_f32 v[204:205], v[204:205], s[50:51]\n"
"v_pk_mul_f32 v[206:207], v[206:207], s[50:51]\n"
"v_pk_mul_f32 v[216:217], v[216:217], s[50:51]\n"
"v_pk_mul_f32 v[218:219], v[218:219], s[50:51]\n"
"v_cvt_pk_bf16_f32 v204, v204, v205\n"
"v_cvt_pk_bf16_f32 v206, v206, v207\n"
"v_cvt_pk_bf16_f32 v216, v216, v217\n"
"v_cvt_pk_bf16_f32 v218, v218, v219\n"
"v_accvgpr_write_b32 a232, v204\n"
"v_accvgpr_write_b32 a233, v206\n"
"v_accvgpr_write_b32 a234, v216\n"
"v_accvgpr_write_b32 a235, v218\n"
"v_pk_mul_f32 v[64:65], v[64:65], s[50:51]\n"
"v_pk_mul_f32 v[66:67], v[66:67], s[50:51]\n"
"v_pk_mul_f32 v[76:77], v[76:77], s[50:51]\n"
"v_pk_mul_f32 v[78:79], v[78:79], s[50:51]\n"
"v_cvt_pk_bf16_f32 v144, v64, v65\n"
"v_cvt_pk_bf16_f32 v145, v66, v67\n"
"v_cvt_pk_bf16_f32 v146, v76, v77\n"
"v_cvt_pk_bf16_f32 v147, v78, v79\n"
"v_pk_mul_f32 v[68:69], v[68:69], s[50:51]\n"
"v_pk_mul_f32 v[70:71], v[70:71], s[50:51]\n"
"v_pk_mul_f32 v[80:81], v[80:81], s[50:51]\n"
"v_pk_mul_f32 v[82:83], v[82:83], s[50:51]\n"
"v_cvt_pk_bf16_f32 v160, v68, v69\n"
"v_cvt_pk_bf16_f32 v161, v70, v71\n"
"v_cvt_pk_bf16_f32 v162, v80, v81\n"
"v_cvt_pk_bf16_f32 v163, v82, v83\n"
"v_pk_mul_f32 v[88:89], v[88:89], s[50:51]\n"
"v_pk_mul_f32 v[90:91], v[90:91], s[50:51]\n"
"v_pk_mul_f32 v[100:101], v[100:101], s[50:51]\n"
"v_pk_mul_f32 v[102:103], v[102:103], s[50:51]\n"
"v_cvt_pk_bf16_f32 v148, v88, v89\n"
"v_cvt_pk_bf16_f32 v149, v90, v91\n"
"v_cvt_pk_bf16_f32 v150, v100, v101\n"
"v_cvt_pk_bf16_f32 v151, v102, v103\n"
"v_pk_mul_f32 v[92:93], v[92:93], s[50:51]\n"
"v_pk_mul_f32 v[94:95], v[94:95], s[50:51]\n"
"v_pk_mul_f32 v[104:105], v[104:105], s[50:51]\n"
"v_pk_mul_f32 v[106:107], v[106:107], s[50:51]\n"
"v_cvt_pk_bf16_f32 v164, v92, v93\n"
"v_cvt_pk_bf16_f32 v165, v94, v95\n"
"v_cvt_pk_bf16_f32 v166, v104, v105\n"
"v_cvt_pk_bf16_f32 v167, v106, v107\n"
"v_pk_mul_f32 v[112:113], v[112:113], s[50:51]\n"
"v_pk_mul_f32 v[114:115], v[114:115], s[50:51]\n"
"v_pk_mul_f32 v[124:125], v[124:125], s[50:51]\n"
"v_pk_mul_f32 v[126:127], v[126:127], s[50:51]\n"
"v_cvt_pk_bf16_f32 v152, v112, v113\n"
"v_cvt_pk_bf16_f32 v153, v114, v115\n"
"v_cvt_pk_bf16_f32 v154, v124, v125\n"
"v_cvt_pk_bf16_f32 v155, v126, v127\n"
"v_pk_mul_f32 v[116:117], v[116:117], s[50:51]\n"
"v_pk_mul_f32 v[118:119], v[118:119], s[50:51]\n"
"v_pk_mul_f32 v[224:225], v[224:225], s[50:51]\n"
"v_pk_mul_f32 v[226:227], v[226:227], s[50:51]\n"
"v_cvt_pk_bf16_f32 v168, v116, v117\n"
"v_cvt_pk_bf16_f32 v169, v118, v119\n"
"v_cvt_pk_bf16_f32 v170, v224, v225\n"
"v_cvt_pk_bf16_f32 v171, v226, v227\n"
"v_pk_mul_f32 v[232:233], v[232:233], s[50:51]\n"
"v_pk_mul_f32 v[234:235], v[234:235], s[50:51]\n"
"v_pk_mul_f32 v[244:245], v[244:245], s[50:51]\n"
"v_pk_mul_f32 v[246:247], v[246:247], s[50:51]\n"
"v_cvt_pk_bf16_f32 v156, v232, v233\n"
"v_cvt_pk_bf16_f32 v157, v234, v235\n"
"v_cvt_pk_bf16_f32 v158, v244, v245\n"
"v_cvt_pk_bf16_f32 v159, v246, v247\n"
"v_pk_mul_f32 v[236:237], v[236:237], s[50:51]\n"
"v_pk_mul_f32 v[238:239], v[238:239], s[50:51]\n"
"v_pk_mul_f32 v[248:249], v[248:249], s[50:51]\n"
"v_pk_mul_f32 v[250:251], v[250:251], s[50:51]\n"
"v_cvt_pk_bf16_f32 v172, v236, v237\n"
"v_cvt_pk_bf16_f32 v173, v238, v239\n"
"v_cvt_pk_bf16_f32 v174, v248, v249\n"
"v_cvt_pk_bf16_f32 v175, v250, v251\n"
"v_mbcnt_lo_u32_b32 v240, -1, 0\n"
"v_mbcnt_hi_u32_b32 v240, -1, v240\n"
"v_and_b32 v241, 15, v240\n"
"v_lshrrev_b32 v242, 4, v240\n"
"v_bfe_u32 v243, v240, 2, 2\n"
"v_and_b32 v244, 3, v240\n"
"v_lshrrev_b32 v245, 5, v240\n"
"v_and_b32 v246, 31, v240\n"
"v_add_u32 v247, 0, v242\n"
"v_xor_b32 v247, v247, v241\n"
"v_lshlrev_b32 v247, 4, v247\n"
"v_lshl_add_u32 v247, v241, 9, v247\n"
"v_add_u32 v0, %7, v247\n"
"v_add_u32 v247, 4, v242\n"
"v_xor_b32 v247, v247, v241\n"
"v_lshlrev_b32 v247, 4, v247\n"
"v_lshl_add_u32 v247, v241, 9, v247\n"
"v_add_u32 v1, %7, v247\n"
"v_add_u32 v247, 8, v242\n"
"v_xor_b32 v247, v247, v241\n"
"v_lshlrev_b32 v247, 4, v247\n"
"v_lshl_add_u32 v247, v241, 9, v247\n"
"v_add_u32 v2, %7, v247\n"
"v_add_u32 v247, 12, v242\n"
"v_xor_b32 v247, v247, v241\n"
"v_lshlrev_b32 v247, 4, v247\n"
"v_lshl_add_u32 v247, v241, 9, v247\n"
"v_add_u32 v3, %7, v247\n"
"v_lshl_add_u32 v249, v242, 2, v243\n"
"v_and_b32 v248, 7, v249\n"
"v_lshlrev_b32 v249, 9, v249\n"
"v_lshl_add_u32 v249, v244, 3, v249\n"
"v_add_u32 v249, 0x10000, v249\n"
"v_add_u32 v249, %7, v249\n"
"v_xor_b32 v247, 0, v248\n"
"v_lshl_add_u32 v4, v247, 5, v249\n"
"v_xor_b32 v247, 1, v248\n"
"v_lshl_add_u32 v5, v247, 5, v249\n"
"v_xor_b32 v247, 2, v248\n"
"v_lshl_add_u32 v6, v247, 5, v249\n"
"v_xor_b32 v247, 3, v248\n"
"v_lshl_add_u32 v7, v247, 5, v249\n"
"v_xor_b32 v247, 4, v248\n"
"v_lshl_add_u32 v8, v247, 5, v249\n"
"v_xor_b32 v247, 5, v248\n"
"v_lshl_add_u32 v9, v247, 5, v249\n"
"v_xor_b32 v247, 6, v248\n"
"v_lshl_add_u32 v10, v247, 5, v249\n"
"v_xor_b32 v247, 7, v248\n"
"v_lshl_add_u32 v11, v247, 5, v249\n"
"v_mov_b32 v20, 0\n"
"v_mov_b32 v21, 0\n"
"v_mov_b32 v22, 0\n"
"v_mov_b32 v23, 0\n"
"v_mov_b32 v24, 0\n"
"v_mov_b32 v25, 0\n"
"v_mov_b32 v26, 0\n"
"v_mov_b32 v27, 0\n"
"v_mov_b32 v28, 0\n"
"v_mov_b32 v29, 0\n"
"v_mov_b32 v30, 0\n"
"v_mov_b32 v31, 0\n"
"v_mov_b32 v205, 0\n"
"v_mov_b32 v206, 0\n"
"v_mov_b32 v207, 0\n"
"s_lshl_b32 s46, %6, 6\n"
"v_lshrrev_b32 v247, 3, v240\n"
"v_add_u32 v247, s46, v247\n"
"v_mul_u32_u24 v247, 896, v247\n"
"v_bfe_u32 v248, v240, 4, 2\n"
"v_lshlrev_b32 v248, 1, v248\n"
"v_and_b32 v208, 7, v240\n"
"v_xor_b32 v248, v248, v208\n"
"v_lshl_add_u32 v208, v248, 4, v247\n"
"v_add_u32 v209, 6144, v208\n"
"v_add_u32 v210, 12288, v208\n"
"v_add_u32 v211, 18432, v208\n"
"s_mov_b32 s44, 0xff800000\n"
"s_waitcnt vmcnt(12)\n"
"s_barrier\n"
"ds_read_b128 v[80:83], v0 offset:0\n"
"ds_read_b128 v[84:87], v1 offset:0\n"
"ds_read_b128 v[88:91], v2 offset:0\n"
"ds_read_b128 v[92:95], v3 offset:0\n"
"s_waitcnt lgkmcnt(3)\n"
"v_mfma_f32_16x16x32_bf16 v[32:35], v[80:83], v[144:147], v[20:23]\n"
"ds_read_b128 v[96:99], v0 offset:256\n"
"v_mfma_f32_16x16x32_bf16 v[36:39], v[80:83], v[160:163], v[24:27]\n"
"ds_read_b128 v[100:103], v1 offset:256\n"
"s_waitcnt lgkmcnt(4)\n"
"v_mfma_f32_16x16x32_bf16 v[32:35], v[84:87], v[148:151], v[32:35]\n"
"v_mfma_f32_16x16x32_bf16 v[36:39], v[84:87], v[164:167], v[36:39]\n"
"ds_read_b128 v[104:107], v2 offset:256\n"
"s_waitcnt lgkmcnt(4)\n"
"v_mfma_f32_16x16x32_bf16 v[32:35], v[88:91], v[152:155], v[32:35]\n"
"v_mfma_f32_16x16x32_bf16 v[36:39], v[88:91], v[168:171], v[36:39]\n"
"ds_read_b128 v[108:111], v3 offset:256\n"
"s_waitcnt lgkmcnt(3)\n"
"v_mfma_f32_16x16x32_bf16 v[32:35], v[92:95], v[156:159], v[32:35]\n"
"v_mfma_f32_16x16x32_bf16 v[36:39], v[92:95], v[172:175], v[36:39]\n"
"ds_read_b128 v[112:115], v0 offset:8192\n"
"v_mfma_f32_16x16x32_bf16 v[32:35], v[96:99], a[204:207], v[32:35]\n"
"v_mfma_f32_16x16x32_bf16 v[36:39], v[96:99], a[220:223], v[36:39]\n"
"ds_read_b128 v[116:119], v1 offset:8192\n"
"s_waitcnt lgkmcnt(3)\n"
"v_mfma_f32_16x16x32_bf16 v[32:35], v[100:103], a[208:211], v[32:35]\n"
"v_mfma_f32_16x16x32_bf16 v[36:39], v[100:103], a[224:227], v[36:39]\n"
"ds_read_b128 v[120:123], v2 offset:8192\n"
"v_mfma_f32_16x16x32_bf16 v[32:35], v[104:107], a[212:215], v[32:35]\n"
"v_mfma_f32_16x16x32_bf16 v[36:39], v[104:107], a[228:231], v[36:39]\n"
"ds_read_b128 v[124:127], v3 offset:8192\n"
"s_waitcnt lgkmcnt(3)\n"
"v_mfma_f32_16x16x32_bf16 v[32:35], v[108:111], a[216:219], v[32:35]\n"
"v_mfma_f32_16x16x32_bf16 v[36:39], v[108:111], a[232:235], v[36:39]\n"
"ds_read_b128 v[128:131], v0 offset:8448\n"
"v_mfma_f32_16x16x32_bf16 v[44:47], v[112:115], v[144:147], v[20:23]\n"
"v_mfma_f32_16x16x32_bf16 v[48:51], v[112:115], v[160:163], v[24:27]\n"
"ds_read_b128 v[132:135], v1 offset:8448\n"
"s_waitcnt lgkmcnt(3)\n"
"v_mfma_f32_16x16x32_bf16 v[44:47], v[116:119], v[148:151], v[44:47]\n"
"v_mfma_f32_16x16x32_bf16 v[48:51], v[116:119], v[164:167], v[48:51]\n"
"ds_read_b128 v[136:139], v2 offset:8448\n"
"v_mfma_f32_16x16x32_bf16 v[44:47], v[120:123], v[152:155], v[44:47]\n"
"v_mfma_f32_16x16x32_bf16 v[48:51], v[120:123], v[168:171], v[48:51]\n"
"ds_read_b128 v[140:143], v3 offset:8448\n"
"s_waitcnt lgkmcnt(3)\n"
"v_mfma_f32_16x16x32_bf16 v[44:47], v[124:127], v[156:159], v[44:47]\n"
"v_mfma_f32_16x16x32_bf16 v[48:51], v[124:127], v[172:175], v[48:51]\n"
"v_mfma_f32_16x16x32_bf16 v[44:47], v[128:131], a[204:207], v[44:47]\n"
"v_mfma_f32_16x16x32_bf16 v[48:51], v[128:131], a[220:223], v[48:51]\n"
"s_waitcnt lgkmcnt(2)\n"
"v_mfma_f32_16x16x32_bf16 v[44:47], v[132:135], a[208:211], v[44:47]\n"
"v_mfma_f32_16x16x32_bf16 v[48:51], v[132:135], a[224:227], v[48:51]\n"
"s_waitcnt lgkmcnt(1)\n"
"v_mfma_f32_16x16x32_bf16 v[44:47], v[136:139], a[212:215], v[44:47]\n"
"v_mfma_f32_16x16x32_bf16 v[48:51], v[136:139], a[228:231], v[48:51]\n"
"s_waitcnt lgkmcnt(0)\n"
"v_mfma_f32_16x16x32_bf16 v[44:47], v[140:143], a[216:219], v[44:47]\n"
"v_mfma_f32_16x16x32_bf16 v[48:51], v[140:143], a[232:235], v[48:51]\n"
"s_nop 7\n"
"s_branch LSTUB12_%=\n"
"LRET12_%=:\n"
"v_exp_f32 v32, v32\n"
"v_exp_f32 v33, v33\n"
"v_exp_f32 v34, v34\n"
"v_exp_f32 v35, v35\n"
"v_exp_f32 v36, v36\n"
"v_exp_f32 v37, v37\n"
"v_exp_f32 v38, v38\n"
"v_exp_f32 v39, v39\n"
"ds_read_b128 v[80:83], v0 offset:16384\n"
"ds_read_b128 v[84:87], v1 offset:16384\n"
"ds_read_b128 v[88:91], v2 offset:16384\n"
"v_cvt_pk_bf16_f32 v56, v32, v33\n"
"v_cvt_pk_bf16_f32 v57, v34, v35\n"
"v_add_f32 v205, v205, v32\n"
"v_add_f32 v205, v205, v33\n"
"v_add_f32 v205, v205, v34\n"
"v_add_f32 v205, v205, v35\n"
"v_cvt_pk_bf16_f32 v60, v36, v37\n"
"v_cvt_pk_bf16_f32 v61, v38, v39\n"
"v_add_f32 v206, v206, v36\n"
"v_add_f32 v206, v206, v37\n"
"v_add_f32 v206, v206, v38\n"
"v_add_f32 v206, v206, v39\n"
"s_waitcnt lgkmcnt(0)\n"
"s_add_u32 s41, %9, 65536\n"
"s_and_b32 s41, s41, 0xfffff\n"
"s_add_u32 s42, %9, 49152\n"
"s_and_b32 s42, s42, 0xfffff\n"
"s_mov_b32 s43, 15\n"
"s_cmp_eq_u32 %10, 1\n"
"s_cbranch_scc1 LLOOPB2_%=\n"
"LLOOPA2_%=:\n"
"s_waitcnt lgkmcnt(2)\n"
"v_mfma_f32_16x16x32_bf16 v[32:35], v[80:83], v[144:147], v[20:23]\n"
"ds_read_b128 v[92:95], v3 offset:16384\n"
"ds_read_b128 v[96:99], v0 offset:16640\n"
"v_mfma_f32_16x16x32_bf16 v[36:39], v[80:83], v[160:163], v[24:27]\n"
"v_exp_f32 v44, v44\n"
"ds_read_b128 v[100:103], v1 offset:16640\n"
"v_add_f32 v205, v205, v44\n"
"s_waitcnt lgkmcnt(3)\n"
"v_mfma_f32_16x16x32_bf16 v[32:35], v[84:87], v[148:151], v[32:35]\n"
"v_exp_f32 v45, v45\n"
"v_mfma_f32_16x16x32_bf16 v[36:39], v[84:87], v[164:167], v[36:39]\n"
"v_exp_f32 v46, v46\n"
"ds_read_b128 v[104:107], v2 offset:16640\n"
"v_cvt_pk_bf16_f32 v58, v44, v45\n"
"v_mfma_f32_16x16x32_bf16 v[32:35], v[88:91], v[152:155], v[32:35]\n"
"v_exp_f32 v47, v47\n"
"v_mfma_f32_16x16x32_bf16 v[36:39], v[88:91], v[168:171], v[36:39]\n"
"v_exp_f32 v48, v48\n"
"ds_read_b128 v[108:111], v3 offset:16640\n"
"v_cvt_pk_bf16_f32 v59, v46, v47\n"
"s_waitcnt lgkmcnt(2)\n"
"v_mfma_f32_16x16x32_bf16 v[32:35], v[92:95], v[156:159], v[32:35]\n"
"v_exp_f32 v49, v49\n"
"v_mfma_f32_16x16x32_bf16 v[36:39], v[92:95], v[172:175], v[36:39]\n"
"v_exp_f32 v50, v50\n"
"ds_read_b128 v[112:115], v0 offset:24576\n"
"v_add_f32 v205, v205, v45\n"
"v_mfma_f32_16x16x32_bf16 v[32:35], v[96:99], a[204:207], v[32:35]\n"
"v_exp_f32 v51, v51\n"
"v_mfma_f32_16x16x32_bf16 v[36:39], v[96:99], a[220:223], v[36:39]\n"
"v_add_f32 v205, v205, v46\n"
"v_add_f32 v205, v205, v47\n"
"ds_read_b128 v[116:119], v1 offset:24576\n"
"v_cvt_pk_bf16_f32 v62, v48, v49\n"
"v_mfma_f32_16x16x32_bf16 v[32:35], v[100:103], a[208:211], v[32:35]\n"
"v_cvt_pk_bf16_f32 v63, v50, v51\n"
"v_add_f32 v206, v206, v48\n"
"v_mfma_f32_16x16x32_bf16 v[36:39], v[100:103], a[224:227], v[36:39]\n"
"v_add_f32 v206, v206, v49\n"
"v_add_f32 v206, v206, v50\n"
"ds_read_b128 v[120:123], v2 offset:24576\n"
"v_add_f32 v206, v206, v51\n"
"s_waitcnt lgkmcnt(2)\n"
"v_mfma_f32_16x16x32_bf16 v[32:35], v[104:107], a[212:215], v[32:35]\n"
"v_mfma_f32_16x16x32_bf16 v[36:39], v[104:107], a[228:231], v[36:39]\n"
"ds_read_b128 v[124:127], v3 offset:24576\n"
"v_mfma_f32_16x16x32_bf16 v[32:35], v[108:111], a[216:219], v[32:35]\n"
"v_mfma_f32_16x16x32_bf16 v[36:39], v[108:111], a[232:235], v[36:39]\n"
"ds_read_b128 v[128:131], v0 offset:24832\n"
"v_mfma_f32_16x16x32_bf16 v[44:47], v[112:115], v[144:147], v[20:23]\n"
"v_mfma_f32_16x16x32_bf16 v[48:51], v[112:115], v[160:163], v[24:27]\n"
"ds_read_b128 v[132:135], v1 offset:24832\n"
"s_waitcnt lgkmcnt(3)\n"
"v_mfma_f32_16x16x32_bf16 v[44:47], v[116:119], v[148:151], v[44:47]\n"
"s_add_u32 m0, s40, 0\n"
"v_max3_f32 v196, v32, v33, v34\n"
"v_mfma_f32_16x16x32_bf16 v[48:51], v[116:119], v[164:167], v[48:51]\n"
"v_max3_f32 v196, v196, v35, v36\n"
"v_max3_f32 v196, v196, v37, v38\n"
"ds_read_b128 v[136:139], v2 offset:24832\n"
"v_max3_f32 v196, v196, v39, v39\n"
"v_mfma_f32_16x16x32_bf16 v[44:47], v[120:123], v[152:155], v[44:47]\n"
"ds_read_b64_tr_b16 v[80:81], v4 offset:32768\n"
"ds_read_b64_tr_b16 v[82:83], v4 offset:40960\n"
"v_mfma_f32_16x16x32_bf16 v[48:51], v[120:123], v[168:171], v[48:51]\n"
"buffer_load_dwordx4 v12, %0, s41 offen offset:0 lds\n"
"ds_read_b128 v[140:143], v3 offset:24832\n"
"s_waitcnt lgkmcnt(4)\n"
"v_mfma_f32_16x16x32_bf16 v[44:47], v[124:127], v[156:159], v[44:47]\n"
"ds_read_b64_tr_b16 v[84:85], v5 offset:32768\n"
"ds_read_b64_tr_b16 v[86:87], v5 offset:40960\n"
"v_mfma_f32_16x16x32_bf16 v[48:51], v[124:127], v[172:175], v[48:51]\n"
"buffer_load_dwordx4 v13, %0, s41 offen offset:1024 lds\n"
"v_mfma_f32_16x16x32_bf16 v[44:47], v[128:131], a[204:207], v[44:47]\n"
"ds_read_b64_tr_b16 v[88:89], v6 offset:32768\n"
"ds_read_b64_tr_b16 v[90:91], v6 offset:40960\n"
"v_mfma_f32_16x16x32_bf16 v[48:51], v[128:131], a[220:223], v[48:51]\n"
"v_mfma_f32_16x16x32_bf16 v[44:47], v[132:135], a[208:211], v[44:47]\n"
"buffer_load_dwordx4 v14, %0, s41 offen offset:2048 lds\n"
"ds_read_b64_tr_b16 v[92:93], v7 offset:32768\n"
"v_mfma_f32_16x16x32_bf16 v[48:51], v[132:135], a[224:227], v[48:51]\n"
"ds_read_b64_tr_b16 v[94:95], v7 offset:40960\n"
"s_waitcnt lgkmcnt(4)\n"
"v_mfma_f32_16x16x32_bf16 v[44:47], v[136:139], a[212:215], v[44:47]\n"
"ds_read_b64_tr_b16 v[96:97], v8 offset:32768\n"
"ds_read_b64_tr_b16 v[98:99], v8 offset:40960\n"
"v_mfma_f32_16x16x32_bf16 v[48:51], v[136:139], a[228:231], v[48:51]\n"
"buffer_load_dwordx4 v15, %0, s41 offen offset:3072 lds\n"
"v_mfma_f32_16x16x32_bf16 v[44:47], v[140:143], a[216:219], v[44:47]\n"
"ds_read_b64_tr_b16 v[100:101], v9 offset:32768\n"
"ds_read_b64_tr_b16 v[102:103], v9 offset:40960\n"
"v_mfma_f32_16x16x32_bf16 v[48:51], v[140:143], a[232:235], v[48:51]\n"
"s_add_u32 s41, s41, 16384\n"
"v_mfma_f32_16x16x32_bf16 a[0:3], v[80:83], v[56:59], a[0:3]\n"
"ds_read_b64_tr_b16 v[104:105], v10 offset:32768\n"
"ds_read_b64_tr_b16 v[106:107], v10 offset:40960\n"
"v_mfma_f32_16x16x32_bf16 a[4:7], v[80:83], v[60:63], a[4:7]\n"
"v_mfma_f32_16x16x32_bf16 a[12:15], v[84:87], v[56:59], a[12:15]\n"
"s_and_b32 s41, s41, 0xfffff\n"
"ds_read_b64_tr_b16 v[108:109], v11 offset:32768\n"
"v_mfma_f32_16x16x32_bf16 a[16:19], v[84:87], v[60:63], a[16:19]\n"
"ds_read_b64_tr_b16 v[110:111], v11 offset:40960\n"
"v_max3_f32 v196, v196, v44, v45\n"
"v_max3_f32 v196, v196, v46, v47\n"
"v_max3_f32 v196, v196, v48, v49\n"
"s_waitcnt lgkmcnt(4)\n"
"v_mfma_f32_16x16x32_bf16 a[24:27], v[88:91], v[56:59], a[24:27]\n"
"v_max3_f32 v196, v196, v50, v51\n"
"v_cmp_lt_f32 vcc, 0x41000000, v196\n"
"v_mfma_f32_16x16x32_bf16 a[28:31], v[88:91], v[60:63], a[28:31]\n"
"s_cbranch_vccnz LSTUB13_%=\n"
"LRET13_%=:\n"
"ds_read_b64_tr_b16 v[112:113], v4 offset:33024\n"
"ds_read_b64_tr_b16 v[114:115], v4 offset:41216\n"
"v_mfma_f32_16x16x32_bf16 a[36:39], v[92:95], v[56:59], a[36:39]\n"
"ds_read_b64_tr_b16 v[116:117], v5 offset:33024\n"
"ds_read_b64_tr_b16 v[118:119], v5 offset:41216\n"
"v_mfma_f32_16x16x32_bf16 a[40:43], v[92:95], v[60:63], a[40:43]\n"
"v_exp_f32 v32, v32\n"
"v_exp_f32 v33, v33\n"
"v_mfma_f32_16x16x32_bf16 a[48:51], v[96:99], v[56:59], a[48:51]\n"
"s_add_u32 m0, s40, 81920\n"
"ds_read_b64_tr_b16 v[120:121], v6 offset:33024\n"
"v_mfma_f32_16x16x32_bf16 a[52:55], v[96:99], v[60:63], a[52:55]\n"
"ds_read_b64_tr_b16 v[122:123], v6 offset:41216\n"
"v_cvt_pk_bf16_f32 v68, v32, v33\n"
"v_exp_f32 v34, v34\n"
"v_mfma_f32_16x16x32_bf16 a[60:63], v[100:103], v[56:59], a[60:63]\n"
"ds_read_b64_tr_b16 v[124:125], v7 offset:33024\n"
"ds_read_b64_tr_b16 v[126:127], v7 offset:41216\n"
"v_mfma_f32_16x16x32_bf16 a[64:67], v[100:103], v[60:63], a[64:67]\n"
"v_exp_f32 v35, v35\n"
"buffer_load_dwordx4 v16, %1, s42 offen offset:0 lds\n"
"v_cvt_pk_bf16_f32 v69, v34, v35\n"
"s_waitcnt lgkmcnt(3)\n"
"v_mfma_f32_16x16x32_bf16 a[72:75], v[104:107], v[56:59], a[72:75]\n"
"ds_read_b64_tr_b16 v[128:129], v8 offset:33024\n"
"ds_read_b64_tr_b16 v[130:131], v8 offset:41216\n"
"v_mfma_f32_16x16x32_bf16 a[76:79], v[104:107], v[60:63], a[76:79]\n"
"v_exp_f32 v36, v36\n"
"v_exp_f32 v37, v37\n"
"v_mfma_f32_16x16x32_bf16 a[84:87], v[108:111], v[56:59], a[84:87]\n"
"ds_read_b64_tr_b16 v[132:133], v9 offset:33024\n"
"ds_read_b64_tr_b16 v[134:135], v9 offset:41216\n"
"v_mfma_f32_16x16x32_bf16 a[88:91], v[108:111], v[60:63], a[88:91]\n"
"buffer_load_dwordx4 v17, %1, s42 offen offset:1024 lds\n"
"v_add_f32 v205, v205, v32\n"
"v_exp_f32 v38, v38\n"
"v_mfma_f32_16x16x32_bf16 a[96:99], v[112:115], v[56:59], a[96:99]\n"
"ds_read_b64_tr_b16 v[136:137], v10 offset:33024\n"
"ds_read_b64_tr_b16 v[138:139], v10 offset:41216\n"
"v_mfma_f32_16x16x32_bf16 a[100:103], v[112:115], v[60:63], a[100:103]\n"
"v_exp_f32 v39, v39\n"
"v_add_f32 v205, v205, v33\n"
"v_add_f32 v205, v205, v34\n"
"v_mfma_f32_16x16x32_bf16 a[108:111], v[116:119], v[56:59], a[108:111]\n"
"ds_read_b64_tr_b16 v[140:141], v11 offset:33024\n"
"ds_read_b64_tr_b16 v[142:143], v11 offset:41216\n"
"v_mfma_f32_16x16x32_bf16 a[112:115], v[116:119], v[60:63], a[112:115]\n"
"buffer_load_dwordx4 v18, %1, s42 offen offset:2048 lds\n"
"v_add_f32 v205, v205, v35\n"
"v_cvt_pk_bf16_f32 v72, v36, v37\n"
"v_cvt_pk_bf16_f32 v73, v38, v39\n"
"s_waitcnt lgkmcnt(2)\n"
"v_mfma_f32_16x16x32_bf16 a[120:123], v[120:123], v[56:59], a[120:123]\n"
"v_add_f32 v206, v206, v36\n"
"v_add_f32 v206, v206, v37\n"
"v_mfma_f32_16x16x32_bf16 a[124:127], v[120:123], v[60:63], a[124:127]\n"
"v_add_f32 v206, v206, v38\n"
"v_add_f32 v206, v206, v39\n"
"buffer_load_dwordx4 v19, %1, s42 offen offset:3072 lds\n"
"v_mfma_f32_16x16x32_bf16 a[132:135], v[124:127], v[56:59], a[132:135]\n"
"v_mfma_f32_16x16x32_bf16 a[136:139], v[124:127], v[60:63], a[136:139]\n"
"v_mfma_f32_16x16x32_bf16 a[144:147], v[128:131], v[56:59], a[144:147]\n"
"v_mfma_f32_16x16x32_bf16 a[148:151], v[128:131], v[60:63], a[148:151]\n"
"s_add_u32 s42, s42, 16384\n"
"s_waitcnt vmcnt(16) lgkmcnt(0)\n"
"s_barrier\n"
"v_mfma_f32_16x16x32_bf16 a[156:159], v[132:135], v[56:59], a[156:159]\n"
"ds_read_b128 v[80:83], v0 offset:32768\n"
"ds_read_b128 v[84:87], v1 offset:32768\n"
"v_mfma_f32_16x16x32_bf16 a[160:163], v[132:135], v[60:63], a[160:163]\n"
"ds_read_b128 v[88:91], v2 offset:32768\n"
"v_mfma_f32_16x16x32_bf16 a[168:171], v[136:139], v[56:59], a[168:171]\n"
"s_and_b32 s42, s42, 0xfffff\n"
"v_mfma_f32_16x16x32_bf16 a[172:175], v[136:139], v[60:63], a[172:175]\n"
"v_mfma_f32_16x16x32_bf16 a[180:183], v[140:143], v[56:59], a[180:183]\n"
"v_mfma_f32_16x16x32_bf16 a[184:187], v[140:143], v[60:63], a[184:187]\n"
"s_waitcnt lgkmcnt(2)\n"
"v_mfma_f32_16x16x32_bf16 v[32:35], v[80:83], v[144:147], v[20:23]\n"
"ds_read_b128 v[92:95], v3 offset:32768\n"
"ds_read_b128 v[96:99], v0 offset:33024\n"
"v_mfma_f32_16x16x32_bf16 v[36:39], v[80:83], v[160:163], v[24:27]\n"
"v_exp_f32 v44, v44\n"
"ds_read_b128 v[100:103], v1 offset:33024\n"
"v_add_f32 v205, v205, v44\n"
"s_waitcnt lgkmcnt(3)\n"
"v_mfma_f32_16x16x32_bf16 v[32:35], v[84:87], v[148:151], v[32:35]\n"
"v_exp_f32 v45, v45\n"
"v_mfma_f32_16x16x32_bf16 v[36:39], v[84:87], v[164:167], v[36:39]\n"
"v_exp_f32 v46, v46\n"
"ds_read_b128 v[104:107], v2 offset:33024\n"
"v_cvt_pk_bf16_f32 v70, v44, v45\n"
"v_mfma_f32_16x16x32_bf16 v[32:35], v[88:91], v[152:155], v[32:35]\n"
"v_exp_f32 v47, v47\n"
"v_mfma_f32_16x16x32_bf16 v[36:39], v[88:91], v[168:171], v[36:39]\n"
"v_exp_f32 v48, v48\n"
"ds_read_b128 v[108:111], v3 offset:33024\n"
"v_cvt_pk_bf16_f32 v71, v46, v47\n"
"s_waitcnt lgkmcnt(2)\n"
"v_mfma_f32_16x16x32_bf16 v[32:35], v[92:95], v[156:159], v[32:35]\n"
"v_exp_f32 v49, v49\n"
"v_mfma_f32_16x16x32_bf16 v[36:39], v[92:95], v[172:175], v[36:39]\n"
"v_exp_f32 v50, v50\n"
"ds_read_b128 v[112:115], v0 offset:40960\n"
"v_add_f32 v205, v205, v45\n"
"v_mfma_f32_16x16x32_bf16 v[32:35], v[96:99], a[204:207], v[32:35]\n"
"v_exp_f32 v51, v51\n"
"v_mfma_f32_16x16x32_bf16 v[36:39], v[96:99], a[220:223], v[36:39]\n"
"v_add_f32 v205, v205, v46\n"
"v_add_f32 v205, v205, v47\n"
"ds_read_b128 v[116:119], v1 offset:40960\n"
"v_cvt_pk_bf16_f32 v74, v48, v49\n"
"v_mfma_f32_16x16x32_bf16 v[32:35], v[100:103], a[208:211], v[32:35]\n"
"v_cvt_pk_bf16_f32 v75, v50, v51\n"
"v_add_f32 v206, v206, v48\n"
"v_mfma_f32_16x16x32_bf16 v[36:39], v[100:103], a[224:227], v[36:39]\n"
"v_add_f32 v206, v206, v49\n"
"v_add_f32 v206, v206, v50\n"
"ds_read_b128 v[120:123], v2 offset:40960\n"
"v_add_f32 v206, v206, v51\n"
"s_waitcnt lgkmcnt(2)\n"
"v_mfma_f32_16x16x32_bf16 v[32:35], v[104:107], a[212:215], v[32:35]\n"
"v_mfma_f32_16x16x32_bf16 v[36:39], v[104:107], a[228:231], v[36:39]\n"
"ds_read_b128 v[124:127], v3 offset:40960\n"
"v_mfma_f32_16x16x32_bf16 v[32:35], v[108:111], a[216:219], v[32:35]\n"
"v_mfma_f32_16x16x32_bf16 v[36:39], v[108:111], a[232:235], v[36:39]\n"
"ds_read_b128 v[128:131], v0 offset:41216\n"
"v_mfma_f32_16x16x32_bf16 v[44:47], v[112:115], v[144:147], v[20:23]\n"
"v_mfma_f32_16x16x32_bf16 v[48:51], v[112:115], v[160:163], v[24:27]\n"
"ds_read_b128 v[132:135], v1 offset:41216\n"
"s_waitcnt lgkmcnt(3)\n"
"v_mfma_f32_16x16x32_bf16 v[44:47], v[116:119], v[148:151], v[44:47]\n"
"s_add_u32 m0, s40, 16384\n"
"v_max3_f32 v196, v32, v33, v34\n"
"v_mfma_f32_16x16x32_bf16 v[48:51], v[116:119], v[164:167], v[48:51]\n"
"v_max3_f32 v196, v196, v35, v36\n"
"v_max3_f32 v196, v196, v37, v38\n"
"ds_read_b128 v[136:139], v2 offset:41216\n"
"v_max3_f32 v196, v196, v39, v39\n"
"v_mfma_f32_16x16x32_bf16 v[44:47], v[120:123], v[152:155], v[44:47]\n"
"ds_read_b64_tr_b16 v[80:81], v4 offset:49152\n"
"ds_read_b64_tr_b16 v[82:83], v4 offset:57344\n"
"v_mfma_f32_16x16x32_bf16 v[48:51], v[120:123], v[168:171], v[48:51]\n"
"buffer_load_dwordx4 v12, %0, s41 offen offset:0 lds\n"
"ds_read_b128 v[140:143], v3 offset:41216\n"
"s_waitcnt lgkmcnt(4)\n"
"v_mfma_f32_16x16x32_bf16 v[44:47], v[124:127], v[156:159], v[44:47]\n"
"ds_read_b64_tr_b16 v[84:85], v5 offset:49152\n"
"ds_read_b64_tr_b16 v[86:87], v5 offset:57344\n"
"v_mfma_f32_16x16x32_bf16 v[48:51], v[124:127], v[172:175], v[48:51]\n"
"buffer_load_dwordx4 v13, %0, s41 offen offset:1024 lds\n"
"v_mfma_f32_16x16x32_bf16 v[44:47], v[128:131], a[204:207], v[44:47]\n"
"ds_read_b64_tr_b16 v[88:89], v6 offset:49152\n"
"ds_read_b64_tr_b16 v[90:91], v6 offset:57344\n"
"v_mfma_f32_16x16x32_bf16 v[48:51], v[128:131], a[220:223], v[48:51]\n"
"v_mfma_f32_16x16x32_bf16 v[44:47], v[132:135], a[208:211], v[44:47]\n"
"buffer_load_dwordx4 v14, %0, s41 offen offset:2048 lds\n"
"ds_read_b64_tr_b16 v[92:93], v7 offset:49152\n"
"v_mfma_f32_16x16x32_bf16 v[48:51], v[132:135], a[224:227], v[48:51]\n"
"ds_read_b64_tr_b16 v[94:95], v7 offset:57344\n"
"s_waitcnt lgkmcnt(4)\n"
"v_mfma_f32_16x16x32_bf16 v[44:47], v[136:139], a[212:215], v[44:47]\n"
"ds_read_b64_tr_b16 v[96:97], v8 offset:49152\n"
"ds_read_b64_tr_b16 v[98:99], v8 offset:57344\n"
"v_mfma_f32_16x16x32_bf16 v[48:51], v[136:139], a[228:231], v[48:51]\n"
"buffer_load_dwordx4 v15, %0, s41 offen offset:3072 lds\n"
"v_mfma_f32_16x16x32_bf16 v[44:47], v[140:143], a[216:219], v[44:47]\n"
"ds_read_b64_tr_b16 v[100:101], v9 offset:49152\n"
"ds_read_b64_tr_b16 v[102:103], v9 offset:57344\n"
"v_mfma_f32_16x16x32_bf16 v[48:51], v[140:143], a[232:235], v[48:51]\n"
"s_add_u32 s41, s41, 16384\n"
"v_mfma_f32_16x16x32_bf16 a[0:3], v[80:83], v[68:71], a[0:3]\n"
"ds_read_b64_tr_b16 v[104:105], v10 offset:49152\n"
"ds_read_b64_tr_b16 v[106:107], v10 offset:57344\n"
"v_mfma_f32_16x16x32_bf16 a[4:7], v[80:83], v[72:75], a[4:7]\n"
"v_mfma_f32_16x16x32_bf16 a[12:15], v[84:87], v[68:71], a[12:15]\n"
"s_and_b32 s41, s41, 0xfffff\n"
"ds_read_b64_tr_b16 v[108:109], v11 offset:49152\n"
"v_mfma_f32_16x16x32_bf16 a[16:19], v[84:87], v[72:75], a[16:19]\n"
"ds_read_b64_tr_b16 v[110:111], v11 offset:57344\n"
"v_max3_f32 v196, v196, v44, v45\n"
"v_max3_f32 v196, v196, v46, v47\n"
"v_max3_f32 v196, v196, v48, v49\n"
"s_waitcnt lgkmcnt(4)\n"
"v_mfma_f32_16x16x32_bf16 a[24:27], v[88:91], v[68:71], a[24:27]\n"
"v_max3_f32 v196, v196, v50, v51\n"
"v_cmp_lt_f32 vcc, 0x41000000, v196\n"
"v_mfma_f32_16x16x32_bf16 a[28:31], v[88:91], v[72:75], a[28:31]\n"
"s_cbranch_vccnz LSTUB14_%=\n"
"LRET14_%=:\n"
"ds_read_b64_tr_b16 v[112:113], v4 offset:49408\n"
"ds_read_b64_tr_b16 v[114:115], v4 offset:57600\n"
"v_mfma_f32_16x16x32_bf16 a[36:39], v[92:95], v[68:71], a[36:39]\n"
"ds_read_b64_tr_b16 v[116:117], v5 offset:49408\n"
"ds_read_b64_tr_b16 v[118:119], v5 offset:57600\n"
"v_mfma_f32_16x16x32_bf16 a[40:43], v[92:95], v[72:75], a[40:43]\n"
"v_exp_f32 v32, v32\n"
"v_exp_f32 v33, v33\n"
"v_mfma_f32_16x16x32_bf16 a[48:51], v[96:99], v[68:71], a[48:51]\n"
"s_add_u32 m0, s40, 98304\n"
"ds_read_b64_tr_b16 v[120:121], v6 offset:49408\n"
"v_mfma_f32_16x16x32_bf16 a[52:55], v[96:99], v[72:75], a[52:55]\n"
"ds_read_b64_tr_b16 v[122:123], v6 offset:57600\n"
"v_cvt_pk_bf16_f32 v56, v32, v33\n"
"v_exp_f32 v34, v34\n"
"v_mfma_f32_16x16x32_bf16 a[60:63], v[100:103], v[68:71], a[60:63]\n"
"ds_read_b64_tr_b16 v[124:125], v7 offset:49408\n"
"ds_read_b64_tr_b16 v[126:127], v7 offset:57600\n"
"v_mfma_f32_16x16x32_bf16 a[64:67], v[100:103], v[72:75], a[64:67]\n"
"v_exp_f32 v35, v35\n"
"buffer_load_dwordx4 v16, %1, s42 offen offset:0 lds\n"
"v_cvt_pk_bf16_f32 v57, v34, v35\n"
"s_waitcnt lgkmcnt(3)\n"
"v_mfma_f32_16x16x32_bf16 a[72:75], v[104:107], v[68:71], a[72:75]\n"
"ds_read_b64_tr_b16 v[128:129], v8 offset:49408\n"
"ds_read_b64_tr_b16 v[130:131], v8 offset:57600\n"
"v_mfma_f32_16x16x32_bf16 a[76:79], v[104:107], v[72:75], a[76:79]\n"
"v_exp_f32 v36, v36\n"
"v_exp_f32 v37, v37\n"
"v_mfma_f32_16x16x32_bf16 a[84:87], v[108:111], v[68:71], a[84:87]\n"
"ds_read_b64_tr_b16 v[132:133], v9 offset:49408\n"
"ds_read_b64_tr_b16 v[134:135], v9 offset:57600\n"
"v_mfma_f32_16x16x32_bf16 a[88:91], v[108:111], v[72:75], a[88:91]\n"
"buffer_load_dwordx4 v17, %1, s42 offen offset:1024 lds\n"
"v_add_f32 v205, v205, v32\n"
"v_exp_f32 v38, v38\n"
"v_mfma_f32_16x16x32_bf16 a[96:99], v[112:115], v[68:71], a[96:99]\n"
"ds_read_b64_tr_b16 v[136:137], v10 offset:49408\n"
"ds_read_b64_tr_b16 v[138:139], v10 offset:57600\n"
"v_mfma_f32_16x16x32_bf16 a[100:103], v[112:115], v[72:75], a[100:103]\n"
"v_exp_f32 v39, v39\n"
"v_add_f32 v205, v205, v33\n"
"v_add_f32 v205, v205, v34\n"
"v_mfma_f32_16x16x32_bf16 a[108:111], v[116:119], v[68:71], a[108:111]\n"
"ds_read_b64_tr_b16 v[140:141], v11 offset:49408\n"
"ds_read_b64_tr_b16 v[142:143], v11 offset:57600\n"
"v_mfma_f32_16x16x32_bf16 a[112:115], v[116:119], v[72:75], a[112:115]\n"
"buffer_load_dwordx4 v18, %1, s42 offen offset:2048 lds\n"
"v_add_f32 v205, v205, v35\n"
"v_cvt_pk_bf16_f32 v60, v36, v37\n"
"v_cvt_pk_bf16_f32 v61, v38, v39\n"
"s_waitcnt lgkmcnt(2)\n"
"v_mfma_f32_16x16x32_bf16 a[120:123], v[120:123], v[68:71], a[120:123]\n"
"v_add_f32 v206, v206, v36\n"
"v_add_f32 v206, v206, v37\n"
"v_mfma_f32_16x16x32_bf16 a[124:127], v[120:123], v[72:75], a[124:127]\n"
"v_add_f32 v206, v206, v38\n"
"v_add_f32 v206, v206, v39\n"
"buffer_load_dwordx4 v19, %1, s42 offen offset:3072 lds\n"
"v_mfma_f32_16x16x32_bf16 a[132:135], v[124:127], v[68:71], a[132:135]\n"
"v_mfma_f32_16x16x32_bf16 a[136:139], v[124:127], v[72:75], a[136:139]\n"
"v_mfma_f32_16x16x32_bf16 a[144:147], v[128:131], v[68:71], a[144:147]\n"
"v_mfma_f32_16x16x32_bf16 a[148:151], v[128:131], v[72:75], a[148:151]\n"
"s_add_u32 s42, s42, 16384\n"
"s_waitcnt vmcnt(16) lgkmcnt(0)\n"
"s_barrier\n"
"v_mfma_f32_16x16x32_bf16 a[156:159], v[132:135], v[68:71], a[156:159]\n"
"ds_read_b128 v[80:83], v0 offset:49152\n"
"ds_read_b128 v[84:87], v1 offset:49152\n"
"v_mfma_f32_16x16x32_bf16 a[160:163], v[132:135], v[72:75], a[160:163]\n"
"ds_read_b128 v[88:91], v2 offset:49152\n"
"v_mfma_f32_16x16x32_bf16 a[168:171], v[136:139], v[68:71], a[168:171]\n"
"s_and_b32 s42, s42, 0xfffff\n"
"v_mfma_f32_16x16x32_bf16 a[172:175], v[136:139], v[72:75], a[172:175]\n"
"v_mfma_f32_16x16x32_bf16 a[180:183], v[140:143], v[68:71], a[180:183]\n"
"v_mfma_f32_16x16x32_bf16 a[184:187], v[140:143], v[72:75], a[184:187]\n"
"s_waitcnt lgkmcnt(2)\n"
"v_mfma_f32_16x16x32_bf16 v[32:35], v[80:83], v[144:147], v[20:23]\n"
"ds_read_b128 v[92:95], v3 offset:49152\n"
"ds_read_b128 v[96:99], v0 offset:49408\n"
"v_mfma_f32_16x16x32_bf16 v[36:39], v[80:83], v[160:163], v[24:27]\n"
"v_exp_f32 v44, v44\n"
"ds_read_b128 v[100:103], v1 offset:49408\n"
"v_add_f32 v205, v205, v44\n"
"s_waitcnt lgkmcnt(3)\n"
"v_mfma_f32_16x16x32_bf16 v[32:35], v[84:87], v[148:151], v[32:35]\n"
"v_exp_f32 v45, v45\n"
"v_mfma_f32_16x16x32_bf16 v[36:39], v[84:87], v[164:167], v[36:39]\n"
"v_exp_f32 v46, v46\n"
"ds_read_b128 v[104:107], v2 offset:49408\n"
"v_cvt_pk_bf16_f32 v58, v44, v45\n"
"v_mfma_f32_16x16x32_bf16 v[32:35], v[88:91], v[152:155], v[32:35]\n"
"v_exp_f32 v47, v47\n"
"v_mfma_f32_16x16x32_bf16 v[36:39], v[88:91], v[168:171], v[36:39]\n"
"v_exp_f32 v48, v48\n"
"ds_read_b128 v[108:111], v3 offset:49408\n"
"v_cvt_pk_bf16_f32 v59, v46, v47\n"
"s_waitcnt lgkmcnt(2)\n"
"v_mfma_f32_16x16x32_bf16 v[32:35], v[92:95], v[156:159], v[32:35]\n"
"v_exp_f32 v49, v49\n"
"v_mfma_f32_16x16x32_bf16 v[36:39], v[92:95], v[172:175], v[36:39]\n"
"v_exp_f32 v50, v50\n"
"ds_read_b128 v[112:115], v0 offset:57344\n"
"v_add_f32 v205, v205, v45\n"
"v_mfma_f32_16x16x32_bf16 v[32:35], v[96:99], a[204:207], v[32:35]\n"
"v_exp_f32 v51, v51\n"
"v_mfma_f32_16x16x32_bf16 v[36:39], v[96:99], a[220:223], v[36:39]\n"
"v_add_f32 v205, v205, v46\n"
"v_add_f32 v205, v205, v47\n"
"ds_read_b128 v[116:119], v1 offset:57344\n"
"v_cvt_pk_bf16_f32 v62, v48, v49\n"
"v_mfma_f32_16x16x32_bf16 v[32:35], v[100:103], a[208:211], v[32:35]\n"
"v_cvt_pk_bf16_f32 v63, v50, v51\n"
"v_add_f32 v206, v206, v48\n"
"v_mfma_f32_16x16x32_bf16 v[36:39], v[100:103], a[224:227], v[36:39]\n"
"v_add_f32 v206, v206, v49\n"
"v_add_f32 v206, v206, v50\n"
"ds_read_b128 v[120:123], v2 offset:57344\n"
"v_add_f32 v206, v206, v51\n"
"s_waitcnt lgkmcnt(2)\n"
"v_mfma_f32_16x16x32_bf16 v[32:35], v[104:107], a[212:215], v[32:35]\n"
"v_mfma_f32_16x16x32_bf16 v[36:39], v[104:107], a[228:231], v[36:39]\n"
"ds_read_b128 v[124:127], v3 offset:57344\n"
"v_mfma_f32_16x16x32_bf16 v[32:35], v[108:111], a[216:219], v[32:35]\n"
"v_mfma_f32_16x16x32_bf16 v[36:39], v[108:111], a[232:235], v[36:39]\n"
"ds_read_b128 v[128:131], v0 offset:57600\n"
"v_mfma_f32_16x16x32_bf16 v[44:47], v[112:115], v[144:147], v[20:23]\n"
"v_mfma_f32_16x16x32_bf16 v[48:51], v[112:115], v[160:163], v[24:27]\n"
"ds_read_b128 v[132:135], v1 offset:57600\n"
"s_waitcnt lgkmcnt(3)\n"
"v_mfma_f32_16x16x32_bf16 v[44:47], v[116:119], v[148:151], v[44:47]\n"
"s_add_u32 m0, s40, 32768\n"
"v_max3_f32 v196, v32, v33, v34\n"
"v_mfma_f32_16x16x32_bf16 v[48:51], v[116:119], v[164:167], v[48:51]\n"
"v_max3_f32 v196, v196, v35, v36\n"
"v_max3_f32 v196, v196, v37, v38\n"
"ds_read_b128 v[136:139], v2 offset:57600\n"
"v_max3_f32 v196, v196, v39, v39\n"
"v_mfma_f32_16x16x32_bf16 v[44:47], v[120:123], v[152:155], v[44:47]\n"
"ds_read_b64_tr_b16 v[80:81], v4 offset:0\n"
"ds_read_b64_tr_b16 v[82:83], v4 offset:8192\n"
"v_mfma_f32_16x16x32_bf16 v[48:51], v[120:123], v[168:171], v[48:51]\n"
"buffer_load_dwordx4 v12, %0, s41 offen offset:0 lds\n"
"ds_read_b128 v[140:143], v3 offset:57600\n"
"s_waitcnt lgkmcnt(4)\n"
"v_mfma_f32_16x16x32_bf16 v[44:47], v[124:127], v[156:159], v[44:47]\n"
"ds_read_b64_tr_b16 v[84:85], v5 offset:0\n"
"ds_read_b64_tr_b16 v[86:87], v5 offset:8192\n"
"v_mfma_f32_16x16x32_bf16 v[48:51], v[124:127], v[172:175], v[48:51]\n"
"buffer_load_dwordx4 v13, %0, s41 offen offset:1024 lds\n"
"v_mfma_f32_16x16x32_bf16 v[44:47], v[128:131], a[204:207], v[44:47]\n"
"ds_read_b64_tr_b16 v[88:89], v6 offset:0\n"
"ds_read_b64_tr_b16 v[90:91], v6 offset:8192\n"
"v_mfma_f32_16x16x32_bf16 v[48:51], v[128:131], a[220:223], v[48:51]\n"
"v_mfma_f32_16x16x32_bf16 v[44:47], v[132:135], a[208:211], v[44:47]\n"
"buffer_load_dwordx4 v14, %0, s41 offen offset:2048 lds\n"
"ds_read_b64_tr_b16 v[92:93], v7 offset:0\n"
"v_mfma_f32_16x16x32_bf16 v[48:51], v[132:135], a[224:227], v[48:51]\n"
"ds_read_b64_tr_b16 v[94:95], v7 offset:8192\n"
"s_waitcnt lgkmcnt(4)\n"
"v_mfma_f32_16x16x32_bf16 v[44:47], v[136:139], a[212:215], v[44:47]\n"
"ds_read_b64_tr_b16 v[96:97], v8 offset:0\n"
"ds_read_b64_tr_b16 v[98:99], v8 offset:8192\n"
"v_mfma_f32_16x16x32_bf16 v[48:51], v[136:139], a[228:231], v[48:51]\n"
"buffer_load_dwordx4 v15, %0, s41 offen offset:3072 lds\n"
"v_mfma_f32_16x16x32_bf16 v[44:47], v[140:143], a[216:219], v[44:47]\n"
"ds_read_b64_tr_b16 v[100:101], v9 offset:0\n"
"ds_read_b64_tr_b16 v[102:103], v9 offset:8192\n"
"v_mfma_f32_16x16x32_bf16 v[48:51], v[140:143], a[232:235], v[48:51]\n"
"s_add_u32 s41, s41, 16384\n"
"v_mfma_f32_16x16x32_bf16 a[0:3], v[80:83], v[56:59], a[0:3]\n"
"ds_read_b64_tr_b16 v[104:105], v10 offset:0\n"
"ds_read_b64_tr_b16 v[106:107], v10 offset:8192\n"
"v_mfma_f32_16x16x32_bf16 a[4:7], v[80:83], v[60:63], a[4:7]\n"
"v_mfma_f32_16x16x32_bf16 a[12:15], v[84:87], v[56:59], a[12:15]\n"
"s_and_b32 s41, s41, 0xfffff\n"
"ds_read_b64_tr_b16 v[108:109], v11 offset:0\n"
"v_mfma_f32_16x16x32_bf16 a[16:19], v[84:87], v[60:63], a[16:19]\n"
"ds_read_b64_tr_b16 v[110:111], v11 offset:8192\n"
"v_max3_f32 v196, v196, v44, v45\n"
"v_max3_f32 v196, v196, v46, v47\n"
"v_max3_f32 v196, v196, v48, v49\n"
"s_waitcnt lgkmcnt(4)\n"
"v_mfma_f32_16x16x32_bf16 a[24:27], v[88:91], v[56:59], a[24:27]\n"
"v_max3_f32 v196, v196, v50, v51\n"
"v_cmp_lt_f32 vcc, 0x41000000, v196\n"
"v_mfma_f32_16x16x32_bf16 a[28:31], v[88:91], v[60:63], a[28:31]\n"
"s_cbranch_vccnz LSTUB15_%=\n"
"LRET15_%=:\n"
"ds_read_b64_tr_b16 v[112:113], v4 offset:256\n"
"ds_read_b64_tr_b16 v[114:115], v4 offset:8448\n"
"v_mfma_f32_16x16x32_bf16 a[36:39], v[92:95], v[56:59], a[36:39]\n"
"ds_read_b64_tr_b16 v[116:117], v5 offset:256\n"
"ds_read_b64_tr_b16 v[118:119], v5 offset:8448\n"
"v_mfma_f32_16x16x32_bf16 a[40:43], v[92:95], v[60:63], a[40:43]\n"
"v_exp_f32 v32, v32\n"
"v_exp_f32 v33, v33\n"
"v_mfma_f32_16x16x32_bf16 a[48:51], v[96:99], v[56:59], a[48:51]\n"
"s_add_u32 m0, s40, 114688\n"
"ds_read_b64_tr_b16 v[120:121], v6 offset:256\n"
"v_mfma_f32_16x16x32_bf16 a[52:55], v[96:99], v[60:63], a[52:55]\n"
"ds_read_b64_tr_b16 v[122:123], v6 offset:8448\n"
"v_cvt_pk_bf16_f32 v68, v32, v33\n"
"v_exp_f32 v34, v34\n"
"v_mfma_f32_16x16x32_bf16 a[60:63], v[100:103], v[56:59], a[60:63]\n"
"ds_read_b64_tr_b16 v[124:125], v7 offset:256\n"
"ds_read_b64_tr_b16 v[126:127], v7 offset:8448\n"
"v_mfma_f32_16x16x32_bf16 a[64:67], v[100:103], v[60:63], a[64:67]\n"
"v_exp_f32 v35, v35\n"
"buffer_load_dwordx4 v16, %1, s42 offen offset:0 lds\n"
"v_cvt_pk_bf16_f32 v69, v34, v35\n"
"s_waitcnt lgkmcnt(3)\n"
"v_mfma_f32_16x16x32_bf16 a[72:75], v[104:107], v[56:59], a[72:75]\n"
"ds_read_b64_tr_b16 v[128:129], v8 offset:256\n"
"ds_read_b64_tr_b16 v[130:131], v8 offset:8448\n"
"v_mfma_f32_16x16x32_bf16 a[76:79], v[104:107], v[60:63], a[76:79]\n"
"v_exp_f32 v36, v36\n"
"v_exp_f32 v37, v37\n"
"v_mfma_f32_16x16x32_bf16 a[84:87], v[108:111], v[56:59], a[84:87]\n"
"ds_read_b64_tr_b16 v[132:133], v9 offset:256\n"
"ds_read_b64_tr_b16 v[134:135], v9 offset:8448\n"
"v_mfma_f32_16x16x32_bf16 a[88:91], v[108:111], v[60:63], a[88:91]\n"
"buffer_load_dwordx4 v17, %1, s42 offen offset:1024 lds\n"
"v_add_f32 v205, v205, v32\n"
"v_exp_f32 v38, v38\n"
"v_mfma_f32_16x16x32_bf16 a[96:99], v[112:115], v[56:59], a[96:99]\n"
"ds_read_b64_tr_b16 v[136:137], v10 offset:256\n"
"ds_read_b64_tr_b16 v[138:139], v10 offset:8448\n"
"v_mfma_f32_16x16x32_bf16 a[100:103], v[112:115], v[60:63], a[100:103]\n"
"v_exp_f32 v39, v39\n"
"v_add_f32 v205, v205, v33\n"
"v_add_f32 v205, v205, v34\n"
"v_mfma_f32_16x16x32_bf16 a[108:111], v[116:119], v[56:59], a[108:111]\n"
"ds_read_b64_tr_b16 v[140:141], v11 offset:256\n"
"ds_read_b64_tr_b16 v[142:143], v11 offset:8448\n"
"v_mfma_f32_16x16x32_bf16 a[112:115], v[116:119], v[60:63], a[112:115]\n"
"buffer_load_dwordx4 v18, %1, s42 offen offset:2048 lds\n"
"v_add_f32 v205, v205, v35\n"
"v_cvt_pk_bf16_f32 v72, v36, v37\n"
"v_cvt_pk_bf16_f32 v73, v38, v39\n"
"s_waitcnt lgkmcnt(2)\n"
"v_mfma_f32_16x16x32_bf16 a[120:123], v[120:123], v[56:59], a[120:123]\n"
"v_add_f32 v206, v206, v36\n"
"v_add_f32 v206, v206, v37\n"
"v_mfma_f32_16x16x32_bf16 a[124:127], v[120:123], v[60:63], a[124:127]\n"
"v_add_f32 v206, v206, v38\n"
"v_add_f32 v206, v206, v39\n"
"buffer_load_dwordx4 v19, %1, s42 offen offset:3072 lds\n"
"v_mfma_f32_16x16x32_bf16 a[132:135], v[124:127], v[56:59], a[132:135]\n"
"v_mfma_f32_16x16x32_bf16 a[136:139], v[124:127], v[60:63], a[136:139]\n"
"v_mfma_f32_16x16x32_bf16 a[144:147], v[128:131], v[56:59], a[144:147]\n"
"v_mfma_f32_16x16x32_bf16 a[148:151], v[128:131], v[60:63], a[148:151]\n"
"s_add_u32 s42, s42, 16384\n"
"s_waitcnt vmcnt(16) lgkmcnt(0)\n"
"s_barrier\n"
"v_mfma_f32_16x16x32_bf16 a[156:159], v[132:135], v[56:59], a[156:159]\n"
"ds_read_b128 v[80:83], v0 offset:0\n"
"ds_read_b128 v[84:87], v1 offset:0\n"
"v_mfma_f32_16x16x32_bf16 a[160:163], v[132:135], v[60:63], a[160:163]\n"
"ds_read_b128 v[88:91], v2 offset:0\n"
"v_mfma_f32_16x16x32_bf16 a[168:171], v[136:139], v[56:59], a[168:171]\n"
"s_and_b32 s42, s42, 0xfffff\n"
"v_mfma_f32_16x16x32_bf16 a[172:175], v[136:139], v[60:63], a[172:175]\n"
"v_mfma_f32_16x16x32_bf16 a[180:183], v[140:143], v[56:59], a[180:183]\n"
"v_mfma_f32_16x16x32_bf16 a[184:187], v[140:143], v[60:63], a[184:187]\n"
"s_waitcnt lgkmcnt(2)\n"
"v_mfma_f32_16x16x32_bf16 v[32:35], v[80:83], v[144:147], v[20:23]\n"
"ds_read_b128 v[92:95], v3 offset:0\n"
"ds_read_b128 v[96:99], v0 offset:256\n"
"v_mfma_f32_16x16x32_bf16 v[36:39], v[80:83], v[160:163], v[24:27]\n"
"v_exp_f32 v44, v44\n"
"ds_read_b128 v[100:103], v1 offset:256\n"
"v_add_f32 v205, v205, v44\n"
"s_waitcnt lgkmcnt(3)\n"
"v_mfma_f32_16x16x32_bf16 v[32:35], v[84:87], v[148:151], v[32:35]\n"
"v_exp_f32 v45, v45\n"
"v_mfma_f32_16x16x32_bf16 v[36:39], v[84:87], v[164:167], v[36:39]\n"
"v_exp_f32 v46, v46\n"
"ds_read_b128 v[104:107], v2 offset:256\n"
"v_cvt_pk_bf16_f32 v70, v44, v45\n"
"v_mfma_f32_16x16x32_bf16 v[32:35], v[88:91], v[152:155], v[32:35]\n"
"v_exp_f32 v47, v47\n"
"v_mfma_f32_16x16x32_bf16 v[36:39], v[88:91], v[168:171], v[36:39]\n"
"v_exp_f32 v48, v48\n"
"ds_read_b128 v[108:111], v3 offset:256\n"
"v_cvt_pk_bf16_f32 v71, v46, v47\n"
"s_waitcnt lgkmcnt(2)\n"
"v_mfma_f32_16x16x32_bf16 v[32:35], v[92:95], v[156:159], v[32:35]\n"
"v_exp_f32 v49, v49\n"
"v_mfma_f32_16x16x32_bf16 v[36:39], v[92:95], v[172:175], v[36:39]\n"
"v_exp_f32 v50, v50\n"
"ds_read_b128 v[112:115], v0 offset:8192\n"
"v_add_f32 v205, v205, v45\n"
"v_mfma_f32_16x16x32_bf16 v[32:35], v[96:99], a[204:207], v[32:35]\n"
"v_exp_f32 v51, v51\n"
"v_mfma_f32_16x16x32_bf16 v[36:39], v[96:99], a[220:223], v[36:39]\n"
"v_add_f32 v205, v205, v46\n"
"v_add_f32 v205, v205, v47\n"
"ds_read_b128 v[116:119], v1 offset:8192\n"
"v_cvt_pk_bf16_f32 v74, v48, v49\n"
"v_mfma_f32_16x16x32_bf16 v[32:35], v[100:103], a[208:211], v[32:35]\n"
"v_cvt_pk_bf16_f32 v75, v50, v51\n"
"v_add_f32 v206, v206, v48\n"
"v_mfma_f32_16x16x32_bf16 v[36:39], v[100:103], a[224:227], v[36:39]\n"
"v_add_f32 v206, v206, v49\n"
"v_add_f32 v206, v206, v50\n"
"ds_read_b128 v[120:123], v2 offset:8192\n"
"v_add_f32 v206, v206, v51\n"
"s_waitcnt lgkmcnt(2)\n"
"v_mfma_f32_16x16x32_bf16 v[32:35], v[104:107], a[212:215], v[32:35]\n"
"v_mfma_f32_16x16x32_bf16 v[36:39], v[104:107], a[228:231], v[36:39]\n"
"ds_read_b128 v[124:127], v3 offset:8192\n"
"v_mfma_f32_16x16x32_bf16 v[32:35], v[108:111], a[216:219], v[32:35]\n"
"v_mfma_f32_16x16x32_bf16 v[36:39], v[108:111], a[232:235], v[36:39]\n"
"ds_read_b128 v[128:131], v0 offset:8448\n"
"v_mfma_f32_16x16x32_bf16 v[44:47], v[112:115], v[144:147], v[20:23]\n"
"v_mfma_f32_16x16x32_bf16 v[48:51], v[112:115], v[160:163], v[24:27]\n"
"ds_read_b128 v[132:135], v1 offset:8448\n"
"s_waitcnt lgkmcnt(3)\n"
"v_mfma_f32_16x16x32_bf16 v[44:47], v[116:119], v[148:151], v[44:47]\n"
"s_add_u32 m0, s40, 49152\n"
"v_max3_f32 v196, v32, v33, v34\n"
"v_mfma_f32_16x16x32_bf16 v[48:51], v[116:119], v[164:167], v[48:51]\n"
"v_max3_f32 v196, v196, v35, v36\n"
"v_max3_f32 v196, v196, v37, v38\n"
"ds_read_b128 v[136:139], v2 offset:8448\n"
"v_max3_f32 v196, v196, v39, v39\n"
"v_mfma_f32_16x16x32_bf16 v[44:47], v[120:123], v[152:155], v[44:47]\n"
"ds_read_b64_tr_b16 v[80:81], v4 offset:16384\n"
"ds_read_b64_tr_b16 v[82:83], v4 offset:24576\n"
"v_mfma_f32_16x16x32_bf16 v[48:51], v[120:123], v[168:171], v[48:51]\n"
"buffer_load_dwordx4 v12, %0, s41 offen offset:0 lds\n"
"ds_read_b128 v[140:143], v3 offset:8448\n"
"s_waitcnt lgkmcnt(4)\n"
"v_mfma_f32_16x16x32_bf16 v[44:47], v[124:127], v[156:159], v[44:47]\n"
"ds_read_b64_tr_b16 v[84:85], v5 offset:16384\n"
"ds_read_b64_tr_b16 v[86:87], v5 offset:24576\n"
"v_mfma_f32_16x16x32_bf16 v[48:51], v[124:127], v[172:175], v[48:51]\n"
"buffer_load_dwordx4 v13, %0, s41 offen offset:1024 lds\n"
"v_mfma_f32_16x16x32_bf16 v[44:47], v[128:131], a[204:207], v[44:47]\n"
"ds_read_b64_tr_b16 v[88:89], v6 offset:16384\n"
"ds_read_b64_tr_b16 v[90:91], v6 offset:24576\n"
"v_mfma_f32_16x16x32_bf16 v[48:51], v[128:131], a[220:223], v[48:51]\n"
"v_mfma_f32_16x16x32_bf16 v[44:47], v[132:135], a[208:211], v[44:47]\n"
"buffer_load_dwordx4 v14, %0, s41 offen offset:2048 lds\n"
"ds_read_b64_tr_b16 v[92:93], v7 offset:16384\n"
"v_mfma_f32_16x16x32_bf16 v[48:51], v[132:135], a[224:227], v[48:51]\n"
"ds_read_b64_tr_b16 v[94:95], v7 offset:24576\n"
"s_waitcnt lgkmcnt(4)\n"
"v_mfma_f32_16x16x32_bf16 v[44:47], v[136:139], a[212:215], v[44:47]\n"
"ds_read_b64_tr_b16 v[96:97], v8 offset:16384\n"
"ds_read_b64_tr_b16 v[98:99], v8 offset:24576\n"
"v_mfma_f32_16x16x32_bf16 v[48:51], v[136:139], a[228:231], v[48:51]\n"
"buffer_load_dwordx4 v15, %0, s41 offen offset:3072 lds\n"
"v_mfma_f32_16x16x32_bf16 v[44:47], v[140:143], a[216:219], v[44:47]\n"
"ds_read_b64_tr_b16 v[100:101], v9 offset:16384\n"
"ds_read_b64_tr_b16 v[102:103], v9 offset:24576\n"
"v_mfma_f32_16x16x32_bf16 v[48:51], v[140:143], a[232:235], v[48:51]\n"
"s_add_u32 s41, s41, 16384\n"
"v_mfma_f32_16x16x32_bf16 a[0:3], v[80:83], v[68:71], a[0:3]\n"
"ds_read_b64_tr_b16 v[104:105], v10 offset:16384\n"
"ds_read_b64_tr_b16 v[106:107], v10 offset:24576\n"
"v_mfma_f32_16x16x32_bf16 a[4:7], v[80:83], v[72:75], a[4:7]\n"
"v_mfma_f32_16x16x32_bf16 a[12:15], v[84:87], v[68:71], a[12:15]\n"
"s_and_b32 s41, s41, 0xfffff\n"
"ds_read_b64_tr_b16 v[108:109], v11 offset:16384\n"
"v_mfma_f32_16x16x32_bf16 a[16:19], v[84:87], v[72:75], a[16:19]\n"
"ds_read_b64_tr_b16 v[110:111], v11 offset:24576\n"
"v_max3_f32 v196, v196, v44, v45\n"
"v_max3_f32 v196, v196, v46, v47\n"
"v_max3_f32 v196, v196, v48, v49\n"
"s_waitcnt lgkmcnt(4)\n"
"v_mfma_f32_16x16x32_bf16 a[24:27], v[88:91], v[68:71], a[24:27]\n"
"v_max3_f32 v196, v196, v50, v51\n"
"v_cmp_lt_f32 vcc, 0x41000000, v196\n"
"v_mfma_f32_16x16x32_bf16 a[28:31], v[88:91], v[72:75], a[28:31]\n"
"s_cbranch_vccnz LSTUB16_%=\n"
"LRET16_%=:\n"
"ds_read_b64_tr_b16 v[112:113], v4 offset:16640\n"
"ds_read_b64_tr_b16 v[114:115], v4 offset:24832\n"
"v_mfma_f32_16x16x32_bf16 a[36:39], v[92:95], v[68:71], a[36:39]\n"
"ds_read_b64_tr_b16 v[116:117], v5 offset:16640\n"
"ds_read_b64_tr_b16 v[118:119], v5 offset:24832\n"
"v_mfma_f32_16x16x32_bf16 a[40:43], v[92:95], v[72:75], a[40:43]\n"
"v_exp_f32 v32, v32\n"
"v_exp_f32 v33, v33\n"
"v_mfma_f32_16x16x32_bf16 a[48:51], v[96:99], v[68:71], a[48:51]\n"
"s_add_u32 m0, s40, 65536\n"
"ds_read_b64_tr_b16 v[120:121], v6 offset:16640\n"
"v_mfma_f32_16x16x32_bf16 a[52:55], v[96:99], v[72:75], a[52:55]\n"
"ds_read_b64_tr_b16 v[122:123], v6 offset:24832\n"
"v_cvt_pk_bf16_f32 v56, v32, v33\n"
"v_exp_f32 v34, v34\n"
"v_mfma_f32_16x16x32_bf16 a[60:63], v[100:103], v[68:71], a[60:63]\n"
"ds_read_b64_tr_b16 v[124:125], v7 offset:16640\n"
"ds_read_b64_tr_b16 v[126:127], v7 offset:24832\n"
"v_mfma_f32_16x16x32_bf16 a[64:67], v[100:103], v[72:75], a[64:67]\n"
"v_exp_f32 v35, v35\n"
"buffer_load_dwordx4 v16, %1, s42 offen offset:0 lds\n"
"v_cvt_pk_bf16_f32 v57, v34, v35\n"
"s_waitcnt lgkmcnt(3)\n"
"v_mfma_f32_16x16x32_bf16 a[72:75], v[104:107], v[68:71], a[72:75]\n"
"ds_read_b64_tr_b16 v[128:129], v8 offset:16640\n"
"ds_read_b64_tr_b16 v[130:131], v8 offset:24832\n"
"v_mfma_f32_16x16x32_bf16 a[76:79], v[104:107], v[72:75], a[76:79]\n"
"v_exp_f32 v36, v36\n"
"v_exp_f32 v37, v37\n"
"v_mfma_f32_16x16x32_bf16 a[84:87], v[108:111], v[68:71], a[84:87]\n"
"ds_read_b64_tr_b16 v[132:133], v9 offset:16640\n"
"ds_read_b64_tr_b16 v[134:135], v9 offset:24832\n"
"v_mfma_f32_16x16x32_bf16 a[88:91], v[108:111], v[72:75], a[88:91]\n"
"buffer_load_dwordx4 v17, %1, s42 offen offset:1024 lds\n"
"v_add_f32 v205, v205, v32\n"
"v_exp_f32 v38, v38\n"
"v_mfma_f32_16x16x32_bf16 a[96:99], v[112:115], v[68:71], a[96:99]\n"
"ds_read_b64_tr_b16 v[136:137], v10 offset:16640\n"
"ds_read_b64_tr_b16 v[138:139], v10 offset:24832\n"
"v_mfma_f32_16x16x32_bf16 a[100:103], v[112:115], v[72:75], a[100:103]\n"
"v_exp_f32 v39, v39\n"
"v_add_f32 v205, v205, v33\n"
"v_add_f32 v205, v205, v34\n"
"v_mfma_f32_16x16x32_bf16 a[108:111], v[116:119], v[68:71], a[108:111]\n"
"ds_read_b64_tr_b16 v[140:141], v11 offset:16640\n"
"ds_read_b64_tr_b16 v[142:143], v11 offset:24832\n"
"v_mfma_f32_16x16x32_bf16 a[112:115], v[116:119], v[72:75], a[112:115]\n"
"buffer_load_dwordx4 v18, %1, s42 offen offset:2048 lds\n"
"v_add_f32 v205, v205, v35\n"
"v_cvt_pk_bf16_f32 v60, v36, v37\n"
"v_cvt_pk_bf16_f32 v61, v38, v39\n"
"s_waitcnt lgkmcnt(2)\n"
"v_mfma_f32_16x16x32_bf16 a[120:123], v[120:123], v[68:71], a[120:123]\n"
"v_add_f32 v206, v206, v36\n"
"v_add_f32 v206, v206, v37\n"
"v_mfma_f32_16x16x32_bf16 a[124:127], v[120:123], v[72:75], a[124:127]\n"
"v_add_f32 v206, v206, v38\n"
"v_add_f32 v206, v206, v39\n"
"buffer_load_dwordx4 v19, %1, s42 offen offset:3072 lds\n"
"v_mfma_f32_16x16x32_bf16 a[132:135], v[124:127], v[68:71], a[132:135]\n"
"v_mfma_f32_16x16x32_bf16 a[136:139], v[124:127], v[72:75], a[136:139]\n"
"v_mfma_f32_16x16x32_bf16 a[144:147], v[128:131], v[68:71], a[144:147]\n"
"v_mfma_f32_16x16x32_bf16 a[148:151], v[128:131], v[72:75], a[148:151]\n"
"s_add_u32 s42, s42, 16384\n"
"s_waitcnt vmcnt(16) lgkmcnt(0)\n"
"s_barrier\n"
"v_mfma_f32_16x16x32_bf16 a[156:159], v[132:135], v[68:71], a[156:159]\n"
"ds_read_b128 v[80:83], v0 offset:16384\n"
"ds_read_b128 v[84:87], v1 offset:16384\n"
"v_mfma_f32_16x16x32_bf16 a[160:163], v[132:135], v[72:75], a[160:163]\n"
"ds_read_b128 v[88:91], v2 offset:16384\n"
"v_mfma_f32_16x16x32_bf16 a[168:171], v[136:139], v[68:71], a[168:171]\n"
"s_and_b32 s42, s42, 0xfffff\n"
"v_mfma_f32_16x16x32_bf16 a[172:175], v[136:139], v[72:75], a[172:175]\n"
"v_mfma_f32_16x16x32_bf16 a[180:183], v[140:143], v[68:71], a[180:183]\n"
"v_mfma_f32_16x16x32_bf16 a[184:187], v[140:143], v[72:75], a[184:187]\n"
"s_sub_u32 s43, s43, 1\n"
"s_cmp_lg_u32 s43, 0\n"
"s_cbranch_scc1 LLOOPA2_%=\n"
"s_branch LTAIL2_%=\n"
"LLOOPB2_%=:\n"
"s_waitcnt lgkmcnt(2)\n"
"v_mfma_f32_16x16x32_bf16 v[32:35], v[80:83], v[144:147], v[20:23]\n"
"ds_read_b128 v[92:95], v3 offset:16384\n"
"ds_read_b128 v[96:99], v0 offset:16640\n"
"v_mfma_f32_16x16x32_bf16 v[36:39], v[80:83], v[160:163], v[24:27]\n"
"v_exp_f32 v44, v44\n"
"ds_read_b128 v[100:103], v1 offset:16640\n"
"v_add_f32 v205, v205, v44\n"
"s_waitcnt lgkmcnt(3)\n"
"v_mfma_f32_16x16x32_bf16 v[32:35], v[84:87], v[148:151], v[32:35]\n"
"v_exp_f32 v45, v45\n"
"v_mfma_f32_16x16x32_bf16 v[36:39], v[84:87], v[164:167], v[36:39]\n"
"v_exp_f32 v46, v46\n"
"ds_read_b128 v[104:107], v2 offset:16640\n"
"v_cvt_pk_bf16_f32 v58, v44, v45\n"
"v_mfma_f32_16x16x32_bf16 v[32:35], v[88:91], v[152:155], v[32:35]\n"
"v_exp_f32 v47, v47\n"
"v_mfma_f32_16x16x32_bf16 v[36:39], v[88:91], v[168:171], v[36:39]\n"
"v_exp_f32 v48, v48\n"
"ds_read_b128 v[108:111], v3 offset:16640\n"
"v_cvt_pk_bf16_f32 v59, v46, v47\n"
"s_waitcnt lgkmcnt(2)\n"
"v_mfma_f32_16x16x32_bf16 v[32:35], v[92:95], v[156:159], v[32:35]\n"
"v_exp_f32 v49, v49\n"
"v_mfma_f32_16x16x32_bf16 v[36:39], v[92:95], v[172:175], v[36:39]\n"
"v_exp_f32 v50, v50\n"
"ds_read_b128 v[112:115], v0 offset:24576\n"
"v_add_f32 v205, v205, v45\n"
"v_mfma_f32_16x16x32_bf16 v[32:35], v[96:99], a[204:207], v[32:35]\n"
"v_exp_f32 v51, v51\n"
"v_mfma_f32_16x16x32_bf16 v[36:39], v[96:99], a[220:223], v[36:39]\n"
"v_add_f32 v205, v205, v46\n"
"v_add_f32 v205, v205, v47\n"
"ds_read_b128 v[116:119], v1 offset:24576\n"
"v_cvt_pk_bf16_f32 v62, v48, v49\n"
"v_mfma_f32_16x16x32_bf16 v[32:35], v[100:103], a[208:211], v[32:35]\n"
"v_cvt_pk_bf16_f32 v63, v50, v51\n"
"v_add_f32 v206, v206, v48\n"
"v_mfma_f32_16x16x32_bf16 v[36:39], v[100:103], a[224:227], v[36:39]\n"
"v_add_f32 v206, v206, v49\n"
"v_add_f32 v206, v206, v50\n"
"ds_read_b128 v[120:123], v2 offset:24576\n"
"v_add_f32 v206, v206, v51\n"
"s_waitcnt lgkmcnt(2)\n"
"v_mfma_f32_16x16x32_bf16 v[32:35], v[104:107], a[212:215], v[32:35]\n"
"v_mfma_f32_16x16x32_bf16 v[36:39], v[104:107], a[228:231], v[36:39]\n"
"ds_read_b128 v[124:127], v3 offset:24576\n"
"v_mfma_f32_16x16x32_bf16 v[32:35], v[108:111], a[216:219], v[32:35]\n"
"v_mfma_f32_16x16x32_bf16 v[36:39], v[108:111], a[232:235], v[36:39]\n"
"ds_read_b128 v[128:131], v0 offset:24832\n"
"v_mfma_f32_16x16x32_bf16 v[44:47], v[112:115], v[144:147], v[20:23]\n"
"v_mfma_f32_16x16x32_bf16 v[48:51], v[112:115], v[160:163], v[24:27]\n"
"ds_read_b128 v[132:135], v1 offset:24832\n"
"s_waitcnt lgkmcnt(3)\n"
"v_mfma_f32_16x16x32_bf16 v[44:47], v[116:119], v[148:151], v[44:47]\n"
"s_add_u32 m0, s40, 0\n"
"v_max3_f32 v196, v32, v33, v34\n"
"v_mfma_f32_16x16x32_bf16 v[48:51], v[116:119], v[164:167], v[48:51]\n"
"v_max3_f32 v196, v196, v35, v36\n"
"v_max3_f32 v196, v196, v37, v38\n"
"ds_read_b128 v[136:139], v2 offset:24832\n"
"v_max3_f32 v196, v196, v39, v39\n"
"v_mfma_f32_16x16x32_bf16 v[44:47], v[120:123], v[152:155], v[44:47]\n"
"ds_read_b64_tr_b16 v[80:81], v4 offset:32768\n"
"ds_read_b64_tr_b16 v[82:83], v4 offset:40960\n"
"v_mfma_f32_16x16x32_bf16 v[48:51], v[120:123], v[168:171], v[48:51]\n"
"buffer_load_dwordx4 v12, %0, s41 offen offset:0 nt lds\n"
"ds_read_b128 v[140:143], v3 offset:24832\n"
"s_waitcnt lgkmcnt(4)\n"
"v_mfma_f32_16x16x32_bf16 v[44:47], v[124:127], v[156:159], v[44:47]\n"
"ds_read_b64_tr_b16 v[84:85], v5 offset:32768\n"
"ds_read_b64_tr_b16 v[86:87], v5 offset:40960\n"
"v_mfma_f32_16x16x32_bf16 v[48:51], v[124:127], v[172:175], v[48:51]\n"
"buffer_load_dwordx4 v13, %0, s41 offen offset:1024 nt lds\n"
"v_mfma_f32_16x16x32_bf16 v[44:47], v[128:131], a[204:207], v[44:47]\n"
"ds_read_b64_tr_b16 v[88:89], v6 offset:32768\n"
"ds_read_b64_tr_b16 v[90:91], v6 offset:40960\n"
"v_mfma_f32_16x16x32_bf16 v[48:51], v[128:131], a[220:223], v[48:51]\n"
"v_mfma_f32_16x16x32_bf16 v[44:47], v[132:135], a[208:211], v[44:47]\n"
"buffer_load_dwordx4 v14, %0, s41 offen offset:2048 nt lds\n"
"ds_read_b64_tr_b16 v[92:93], v7 offset:32768\n"
"v_mfma_f32_16x16x32_bf16 v[48:51], v[132:135], a[224:227], v[48:51]\n"
"ds_read_b64_tr_b16 v[94:95], v7 offset:40960\n"
"s_waitcnt lgkmcnt(4)\n"
"v_mfma_f32_16x16x32_bf16 v[44:47], v[136:139], a[212:215], v[44:47]\n"
"ds_read_b64_tr_b16 v[96:97], v8 offset:32768\n"
"ds_read_b64_tr_b16 v[98:99], v8 offset:40960\n"
"v_mfma_f32_16x16x32_bf16 v[48:51], v[136:139], a[228:231], v[48:51]\n"
"buffer_load_dwordx4 v15, %0, s41 offen offset:3072 nt lds\n"
"v_mfma_f32_16x16x32_bf16 v[44:47], v[140:143], a[216:219], v[44:47]\n"
"ds_read_b64_tr_b16 v[100:101], v9 offset:32768\n"
"ds_read_b64_tr_b16 v[102:103], v9 offset:40960\n"
"v_mfma_f32_16x16x32_bf16 v[48:51], v[140:143], a[232:235], v[48:51]\n"
"s_add_u32 s41, s41, 16384\n"
"v_mfma_f32_16x16x32_bf16 a[0:3], v[80:83], v[56:59], a[0:3]\n"
"ds_read_b64_tr_b16 v[104:105], v10 offset:32768\n"
"ds_read_b64_tr_b16 v[106:107], v10 offset:40960\n"
"v_mfma_f32_16x16x32_bf16 a[4:7], v[80:83], v[60:63], a[4:7]\n"
"v_mfma_f32_16x16x32_bf16 a[12:15], v[84:87], v[56:59], a[12:15]\n"
"s_and_b32 s41, s41, 0xfffff\n"
"ds_read_b64_tr_b16 v[108:109], v11 offset:32768\n"
"v_mfma_f32_16x16x32_bf16 a[16:19], v[84:87], v[60:63], a[16:19]\n"
"ds_read_b64_tr_b16 v[110:111], v11 offset:40960\n"
"v_max3_f32 v196, v196, v44, v45\n"
"v_max3_f32 v196, v196, v46, v47\n"
"v_max3_f32 v196, v196, v48, v49\n"
"s_waitcnt lgkmcnt(4)\n"
"v_mfma_f32_16x16x32_bf16 a[24:27], v[88:91], v[56:59], a[24:27]\n"
"v_max3_f32 v196, v196, v50, v51\n"
"v_cmp_lt_f32 vcc, 0x41000000, v196\n"
"v_mfma_f32_16x16x32_bf16 a[28:31], v[88:91], v[60:63], a[28:31]\n"
"s_cbranch_vccnz LSTUB17_%=\n"
"LRET17_%=:\n"
"ds_read_b64_tr_b16 v[112:113], v4 offset:33024\n"
"ds_read_b64_tr_b16 v[114:115], v4 offset:41216\n"
"v_mfma_f32_16x16x32_bf16 a[36:39], v[92:95], v[56:59], a[36:39]\n"
"ds_read_b64_tr_b16 v[116:117], v5 offset:33024\n"
"ds_read_b64_tr_b16 v[118:119], v5 offset:41216\n"
"v_mfma_f32_16x16x32_bf16 a[40:43], v[92:95], v[60:63], a[40:43]\n"
"v_exp_f32 v32, v32\n"
"v_exp_f32 v33, v33\n"
"v_mfma_f32_16x16x32_bf16 a[48:51], v[96:99], v[56:59], a[48:51]\n"
"s_add_u32 m0, s40, 81920\n"
"ds_read_b64_tr_b16 v[120:121], v6 offset:33024\n"
"v_mfma_f32_16x16x32_bf16 a[52:55], v[96:99], v[60:63], a[52:55]\n"
"ds_read_b64_tr_b16 v[122:123], v6 offset:41216\n"
"v_cvt_pk_bf16_f32 v68, v32, v33\n"
"v_exp_f32 v34, v34\n"
"v_mfma_f32_16x16x32_bf16 a[60:63], v[100:103], v[56:59], a[60:63]\n"
"ds_read_b64_tr_b16 v[124:125], v7 offset:33024\n"
"ds_read_b64_tr_b16 v[126:127], v7 offset:41216\n"
"v_mfma_f32_16x16x32_bf16 a[64:67], v[100:103], v[60:63], a[64:67]\n"
"v_exp_f32 v35, v35\n"
"buffer_load_dwordx4 v16, %1, s42 offen offset:0 nt lds\n"
"v_cvt_pk_bf16_f32 v69, v34, v35\n"
"s_waitcnt lgkmcnt(3)\n"
"v_mfma_f32_16x16x32_bf16 a[72:75], v[104:107], v[56:59], a[72:75]\n"
"ds_read_b64_tr_b16 v[128:129], v8 offset:33024\n"
"ds_read_b64_tr_b16 v[130:131], v8 offset:41216\n"
"v_mfma_f32_16x16x32_bf16 a[76:79], v[104:107], v[60:63], a[76:79]\n"
"v_exp_f32 v36, v36\n"
"v_exp_f32 v37, v37\n"
"v_mfma_f32_16x16x32_bf16 a[84:87], v[108:111], v[56:59], a[84:87]\n"
"ds_read_b64_tr_b16 v[132:133], v9 offset:33024\n"
"ds_read_b64_tr_b16 v[134:135], v9 offset:41216\n"
"v_mfma_f32_16x16x32_bf16 a[88:91], v[108:111], v[60:63], a[88:91]\n"
"buffer_load_dwordx4 v17, %1, s42 offen offset:1024 nt lds\n"
"v_add_f32 v205, v205, v32\n"
"v_exp_f32 v38, v38\n"
"v_mfma_f32_16x16x32_bf16 a[96:99], v[112:115], v[56:59], a[96:99]\n"
"ds_read_b64_tr_b16 v[136:137], v10 offset:33024\n"
"ds_read_b64_tr_b16 v[138:139], v10 offset:41216\n"
"v_mfma_f32_16x16x32_bf16 a[100:103], v[112:115], v[60:63], a[100:103]\n"
"v_exp_f32 v39, v39\n"
"v_add_f32 v205, v205, v33\n"
"v_add_f32 v205, v205, v34\n"
"v_mfma_f32_16x16x32_bf16 a[108:111], v[116:119], v[56:59], a[108:111]\n"
"ds_read_b64_tr_b16 v[140:141], v11 offset:33024\n"
"ds_read_b64_tr_b16 v[142:143], v11 offset:41216\n"
"v_mfma_f32_16x16x32_bf16 a[112:115], v[116:119], v[60:63], a[112:115]\n"
"buffer_load_dwordx4 v18, %1, s42 offen offset:2048 nt lds\n"
"v_add_f32 v205, v205, v35\n"
"v_cvt_pk_bf16_f32 v72, v36, v37\n"
"v_cvt_pk_bf16_f32 v73, v38, v39\n"
"s_waitcnt lgkmcnt(2)\n"
"v_mfma_f32_16x16x32_bf16 a[120:123], v[120:123], v[56:59], a[120:123]\n"
"v_add_f32 v206, v206, v36\n"
"v_add_f32 v206, v206, v37\n"
"v_mfma_f32_16x16x32_bf16 a[124:127], v[120:123], v[60:63], a[124:127]\n"
"v_add_f32 v206, v206, v38\n"
"v_add_f32 v206, v206, v39\n"
"buffer_load_dwordx4 v19, %1, s42 offen offset:3072 nt lds\n"
"v_mfma_f32_16x16x32_bf16 a[132:135], v[124:127], v[56:59], a[132:135]\n"
"v_mfma_f32_16x16x32_bf16 a[136:139], v[124:127], v[60:63], a[136:139]\n"
"v_mfma_f32_16x16x32_bf16 a[144:147], v[128:131], v[56:59], a[144:147]\n"
"v_mfma_f32_16x16x32_bf16 a[148:151], v[128:131], v[60:63], a[148:151]\n"
"s_add_u32 s42, s42, 16384\n"
"s_waitcnt vmcnt(16) lgkmcnt(0)\n"
"s_barrier\n"
"v_mfma_f32_16x16x32_bf16 a[156:159], v[132:135], v[56:59], a[156:159]\n"
"ds_read_b128 v[80:83], v0 offset:32768\n"
"ds_read_b128 v[84:87], v1 offset:32768\n"
"v_mfma_f32_16x16x32_bf16 a[160:163], v[132:135], v[60:63], a[160:163]\n"
"ds_read_b128 v[88:91], v2 offset:32768\n"
"v_mfma_f32_16x16x32_bf16 a[168:171], v[136:139], v[56:59], a[168:171]\n"
"s_and_b32 s42, s42, 0xfffff\n"
"v_mfma_f32_16x16x32_bf16 a[172:175], v[136:139], v[60:63], a[172:175]\n"
"v_mfma_f32_16x16x32_bf16 a[180:183], v[140:143], v[56:59], a[180:183]\n"
"v_mfma_f32_16x16x32_bf16 a[184:187], v[140:143], v[60:63], a[184:187]\n"
"s_waitcnt lgkmcnt(2)\n"
"v_mfma_f32_16x16x32_bf16 v[32:35], v[80:83], v[144:147], v[20:23]\n"
"ds_read_b128 v[92:95], v3 offset:32768\n"
"ds_read_b128 v[96:99], v0 offset:33024\n"
"v_mfma_f32_16x16x32_bf16 v[36:39], v[80:83], v[160:163], v[24:27]\n"
"v_exp_f32 v44, v44\n"
"ds_read_b128 v[100:103], v1 offset:33024\n"
"v_add_f32 v205, v205, v44\n"
"s_waitcnt lgkmcnt(3)\n"
"v_mfma_f32_16x16x32_bf16 v[32:35], v[84:87], v[148:151], v[32:35]\n"
"v_exp_f32 v45, v45\n"
"v_mfma_f32_16x16x32_bf16 v[36:39], v[84:87], v[164:167], v[36:39]\n"
"v_exp_f32 v46, v46\n"
"ds_read_b128 v[104:107], v2 offset:33024\n"
"v_cvt_pk_bf16_f32 v70, v44, v45\n"
"v_mfma_f32_16x16x32_bf16 v[32:35], v[88:91], v[152:155], v[32:35]\n"
"v_exp_f32 v47, v47\n"
"v_mfma_f32_16x16x32_bf16 v[36:39], v[88:91], v[168:171], v[36:39]\n"
"v_exp_f32 v48, v48\n"
"ds_read_b128 v[108:111], v3 offset:33024\n"
"v_cvt_pk_bf16_f32 v71, v46, v47\n"
"s_waitcnt lgkmcnt(2)\n"
"v_mfma_f32_16x16x32_bf16 v[32:35], v[92:95], v[156:159], v[32:35]\n"
"v_exp_f32 v49, v49\n"
"v_mfma_f32_16x16x32_bf16 v[36:39], v[92:95], v[172:175], v[36:39]\n"
"v_exp_f32 v50, v50\n"
"ds_read_b128 v[112:115], v0 offset:40960\n"
"v_add_f32 v205, v205, v45\n"
"v_mfma_f32_16x16x32_bf16 v[32:35], v[96:99], a[204:207], v[32:35]\n"
"v_exp_f32 v51, v51\n"
"v_mfma_f32_16x16x32_bf16 v[36:39], v[96:99], a[220:223], v[36:39]\n"
"v_add_f32 v205, v205, v46\n"
"v_add_f32 v205, v205, v47\n"
"ds_read_b128 v[116:119], v1 offset:40960\n"
"v_cvt_pk_bf16_f32 v74, v48, v49\n"
"v_mfma_f32_16x16x32_bf16 v[32:35], v[100:103], a[208:211], v[32:35]\n"
"v_cvt_pk_bf16_f32 v75, v50, v51\n"
"v_add_f32 v206, v206, v48\n"
"v_mfma_f32_16x16x32_bf16 v[36:39], v[100:103], a[224:227], v[36:39]\n"
"v_add_f32 v206, v206, v49\n"
"v_add_f32 v206, v206, v50\n"
"ds_read_b128 v[120:123], v2 offset:40960\n"
"v_add_f32 v206, v206, v51\n"
"s_waitcnt lgkmcnt(2)\n"
"v_mfma_f32_16x16x32_bf16 v[32:35], v[104:107], a[212:215], v[32:35]\n"
"v_mfma_f32_16x16x32_bf16 v[36:39], v[104:107], a[228:231], v[36:39]\n"
"ds_read_b128 v[124:127], v3 offset:40960\n"
"v_mfma_f32_16x16x32_bf16 v[32:35], v[108:111], a[216:219], v[32:35]\n"
"v_mfma_f32_16x16x32_bf16 v[36:39], v[108:111], a[232:235], v[36:39]\n"
"ds_read_b128 v[128:131], v0 offset:41216\n"
"v_mfma_f32_16x16x32_bf16 v[44:47], v[112:115], v[144:147], v[20:23]\n"
"v_mfma_f32_16x16x32_bf16 v[48:51], v[112:115], v[160:163], v[24:27]\n"
"ds_read_b128 v[132:135], v1 offset:41216\n"
"s_waitcnt lgkmcnt(3)\n"
"v_mfma_f32_16x16x32_bf16 v[44:47], v[116:119], v[148:151], v[44:47]\n"
"s_add_u32 m0, s40, 16384\n"
"v_max3_f32 v196, v32, v33, v34\n"
"v_mfma_f32_16x16x32_bf16 v[48:51], v[116:119], v[164:167], v[48:51]\n"
"v_max3_f32 v196, v196, v35, v36\n"
"v_max3_f32 v196, v196, v37, v38\n"
"ds_read_b128 v[136:139], v2 offset:41216\n"
"v_max3_f32 v196, v196, v39, v39\n"
"v_mfma_f32_16x16x32_bf16 v[44:47], v[120:123], v[152:155], v[44:47]\n"
"ds_read_b64_tr_b16 v[80:81], v4 offset:49152\n"
"ds_read_b64_tr_b16 v[82:83], v4 offset:57344\n"
"v_mfma_f32_16x16x32_bf16 v[48:51], v[120:123], v[168:171], v[48:51]\n"
"buffer_load_dwordx4 v12, %0, s41 offen offset:0 nt lds\n"
"ds_read_b128 v[140:143], v3 offset:41216\n"
"s_waitcnt lgkmcnt(4)\n"
"v_mfma_f32_16x16x32_bf16 v[44:47], v[124:127], v[156:159], v[44:47]\n"
"ds_read_b64_tr_b16 v[84:85], v5 offset:49152\n"
"ds_read_b64_tr_b16 v[86:87], v5 offset:57344\n"
"v_mfma_f32_16x16x32_bf16 v[48:51], v[124:127], v[172:175], v[48:51]\n"
"buffer_load_dwordx4 v13, %0, s41 offen offset:1024 nt lds\n"
"v_mfma_f32_16x16x32_bf16 v[44:47], v[128:131], a[204:207], v[44:47]\n"
"ds_read_b64_tr_b16 v[88:89], v6 offset:49152\n"
"ds_read_b64_tr_b16 v[90:91], v6 offset:57344\n"
"v_mfma_f32_16x16x32_bf16 v[48:51], v[128:131], a[220:223], v[48:51]\n"
"v_mfma_f32_16x16x32_bf16 v[44:47], v[132:135], a[208:211], v[44:47]\n"
"buffer_load_dwordx4 v14, %0, s41 offen offset:2048 nt lds\n"
"ds_read_b64_tr_b16 v[92:93], v7 offset:49152\n"
"v_mfma_f32_16x16x32_bf16 v[48:51], v[132:135], a[224:227], v[48:51]\n"
"ds_read_b64_tr_b16 v[94:95], v7 offset:57344\n"
"s_waitcnt lgkmcnt(4)\n"
"v_mfma_f32_16x16x32_bf16 v[44:47], v[136:139], a[212:215], v[44:47]\n"
"ds_read_b64_tr_b16 v[96:97], v8 offset:49152\n"
"ds_read_b64_tr_b16 v[98:99], v8 offset:57344\n"
"v_mfma_f32_16x16x32_bf16 v[48:51], v[136:139], a[228:231], v[48:51]\n"
"buffer_load_dwordx4 v15, %0, s41 offen offset:3072 nt lds\n"
"v_mfma_f32_16x16x32_bf16 v[44:47], v[140:143], a[216:219], v[44:47]\n"
"ds_read_b64_tr_b16 v[100:101], v9 offset:49152\n"
"ds_read_b64_tr_b16 v[102:103], v9 offset:57344\n"
"v_mfma_f32_16x16x32_bf16 v[48:51], v[140:143], a[232:235], v[48:51]\n"
"s_add_u32 s41, s41, 16384\n"
"v_mfma_f32_16x16x32_bf16 a[0:3], v[80:83], v[68:71], a[0:3]\n"
"ds_read_b64_tr_b16 v[104:105], v10 offset:49152\n"
"ds_read_b64_tr_b16 v[106:107], v10 offset:57344\n"
"v_mfma_f32_16x16x32_bf16 a[4:7], v[80:83], v[72:75], a[4:7]\n"
"v_mfma_f32_16x16x32_bf16 a[12:15], v[84:87], v[68:71], a[12:15]\n"
"s_and_b32 s41, s41, 0xfffff\n"
"ds_read_b64_tr_b16 v[108:109], v11 offset:49152\n"
"v_mfma_f32_16x16x32_bf16 a[16:19], v[84:87], v[72:75], a[16:19]\n"
"ds_read_b64_tr_b16 v[110:111], v11 offset:57344\n"
"v_max3_f32 v196, v196, v44, v45\n"
"v_max3_f32 v196, v196, v46, v47\n"
"v_max3_f32 v196, v196, v48, v49\n"
"s_waitcnt lgkmcnt(4)\n"
"v_mfma_f32_16x16x32_bf16 a[24:27], v[88:91], v[68:71], a[24:27]\n"
"v_max3_f32 v196, v196, v50, v51\n"
"v_cmp_lt_f32 vcc, 0x41000000, v196\n"
"v_mfma_f32_16x16x32_bf16 a[28:31], v[88:91], v[72:75], a[28:31]\n"
"s_cbranch_vccnz LSTUB18_%=\n"
"LRET18_%=:\n"
"ds_read_b64_tr_b16 v[112:113], v4 offset:49408\n"
"ds_read_b64_tr_b16 v[114:115], v4 offset:57600\n"
"v_mfma_f32_16x16x32_bf16 a[36:39], v[92:95], v[68:71], a[36:39]\n"
"ds_read_b64_tr_b16 v[116:117], v5 offset:49408\n"
"ds_read_b64_tr_b16 v[118:119], v5 offset:57600\n"
"v_mfma_f32_16x16x32_bf16 a[40:43], v[92:95], v[72:75], a[40:43]\n"
"v_exp_f32 v32, v32\n"
"v_exp_f32 v33, v33\n"
"v_mfma_f32_16x16x32_bf16 a[48:51], v[96:99], v[68:71], a[48:51]\n"
"s_add_u32 m0, s40, 98304\n"
"ds_read_b64_tr_b16 v[120:121], v6 offset:49408\n"
"v_mfma_f32_16x16x32_bf16 a[52:55], v[96:99], v[72:75], a[52:55]\n"
"ds_read_b64_tr_b16 v[122:123], v6 offset:57600\n"
"v_cvt_pk_bf16_f32 v56, v32, v33\n"
"v_exp_f32 v34, v34\n"
"v_mfma_f32_16x16x32_bf16 a[60:63], v[100:103], v[68:71], a[60:63]\n"
"ds_read_b64_tr_b16 v[124:125], v7 offset:49408\n"
"ds_read_b64_tr_b16 v[126:127], v7 offset:57600\n"
"v_mfma_f32_16x16x32_bf16 a[64:67], v[100:103], v[72:75], a[64:67]\n"
"v_exp_f32 v35, v35\n"
"buffer_load_dwordx4 v16, %1, s42 offen offset:0 nt lds\n"
"v_cvt_pk_bf16_f32 v57, v34, v35\n"
"s_waitcnt lgkmcnt(3)\n"
"v_mfma_f32_16x16x32_bf16 a[72:75], v[104:107], v[68:71], a[72:75]\n"
"ds_read_b64_tr_b16 v[128:129], v8 offset:49408\n"
"ds_read_b64_tr_b16 v[130:131], v8 offset:57600\n"
"v_mfma_f32_16x16x32_bf16 a[76:79], v[104:107], v[72:75], a[76:79]\n"
"v_exp_f32 v36, v36\n"
"v_exp_f32 v37, v37\n"
"v_mfma_f32_16x16x32_bf16 a[84:87], v[108:111], v[68:71], a[84:87]\n"
"ds_read_b64_tr_b16 v[132:133], v9 offset:49408\n"
"ds_read_b64_tr_b16 v[134:135], v9 offset:57600\n"
"v_mfma_f32_16x16x32_bf16 a[88:91], v[108:111], v[72:75], a[88:91]\n"
"buffer_load_dwordx4 v17, %1, s42 offen offset:1024 nt lds\n"
"v_add_f32 v205, v205, v32\n"
"v_exp_f32 v38, v38\n"
"v_mfma_f32_16x16x32_bf16 a[96:99], v[112:115], v[68:71], a[96:99]\n"
"ds_read_b64_tr_b16 v[136:137], v10 offset:49408\n"
"ds_read_b64_tr_b16 v[138:139], v10 offset:57600\n"
"v_mfma_f32_16x16x32_bf16 a[100:103], v[112:115], v[72:75], a[100:103]\n"
"v_exp_f32 v39, v39\n"
"v_add_f32 v205, v205, v33\n"
"v_add_f32 v205, v205, v34\n"
"v_mfma_f32_16x16x32_bf16 a[108:111], v[116:119], v[68:71], a[108:111]\n"
"ds_read_b64_tr_b16 v[140:141], v11 offset:49408\n"
"ds_read_b64_tr_b16 v[142:143], v11 offset:57600\n"
"v_mfma_f32_16x16x32_bf16 a[112:115], v[116:119], v[72:75], a[112:115]\n"
"buffer_load_dwordx4 v18, %1, s42 offen offset:2048 nt lds\n"
"v_add_f32 v205, v205, v35\n"
"v_cvt_pk_bf16_f32 v60, v36, v37\n"
"v_cvt_pk_bf16_f32 v61, v38, v39\n"
"s_waitcnt lgkmcnt(2)\n"
"v_mfma_f32_16x16x32_bf16 a[120:123], v[120:123], v[68:71], a[120:123]\n"
"v_add_f32 v206, v206, v36\n"
"v_add_f32 v206, v206, v37\n"
"v_mfma_f32_16x16x32_bf16 a[124:127], v[120:123], v[72:75], a[124:127]\n"
"v_add_f32 v206, v206, v38\n"
"v_add_f32 v206, v206, v39\n"
"buffer_load_dwordx4 v19, %1, s42 offen offset:3072 nt lds\n"
"v_mfma_f32_16x16x32_bf16 a[132:135], v[124:127], v[68:71], a[132:135]\n"
"v_mfma_f32_16x16x32_bf16 a[136:139], v[124:127], v[72:75], a[136:139]\n"
"v_mfma_f32_16x16x32_bf16 a[144:147], v[128:131], v[68:71], a[144:147]\n"
"v_mfma_f32_16x16x32_bf16 a[148:151], v[128:131], v[72:75], a[148:151]\n"
"s_add_u32 s42, s42, 16384\n"
"s_waitcnt vmcnt(16) lgkmcnt(0)\n"
"s_barrier\n"
"v_mfma_f32_16x16x32_bf16 a[156:159], v[132:135], v[68:71], a[156:159]\n"
"ds_read_b128 v[80:83], v0 offset:49152\n"
"ds_read_b128 v[84:87], v1 offset:49152\n"
"v_mfma_f32_16x16x32_bf16 a[160:163], v[132:135], v[72:75], a[160:163]\n"
"ds_read_b128 v[88:91], v2 offset:49152\n"
"v_mfma_f32_16x16x32_bf16 a[168:171], v[136:139], v[68:71], a[168:171]\n"
"s_and_b32 s42, s42, 0xfffff\n"
"v_mfma_f32_16x16x32_bf16 a[172:175], v[136:139], v[72:75], a[172:175]\n"
"v_mfma_f32_16x16x32_bf16 a[180:183], v[140:143], v[68:71], a[180:183]\n"
"v_mfma_f32_16x16x32_bf16 a[184:187], v[140:143], v[72:75], a[184:187]\n"
"s_waitcnt lgkmcnt(2)\n"
"v_mfma_f32_16x16x32_bf16 v[32:35], v[80:83], v[144:147], v[20:23]\n"
"ds_read_b128 v[92:95], v3 offset:49152\n"
"ds_read_b128 v[96:99], v0 offset:49408\n"
"v_mfma_f32_16x16x32_bf16 v[36:39], v[80:83], v[160:163], v[24:27]\n"
"v_exp_f32 v44, v44\n"
"ds_read_b128 v[100:103], v1 offset:49408\n"
"v_add_f32 v205, v205, v44\n"
"s_waitcnt lgkmcnt(3)\n"
"v_mfma_f32_16x16x32_bf16 v[32:35], v[84:87], v[148:151], v[32:35]\n"
"v_exp_f32 v45, v45\n"
"v_mfma_f32_16x16x32_bf16 v[36:39], v[84:87], v[164:167], v[36:39]\n"
"v_exp_f32 v46, v46\n"
"ds_read_b128 v[104:107], v2 offset:49408\n"
"v_cvt_pk_bf16_f32 v58, v44, v45\n"
"v_mfma_f32_16x16x32_bf16 v[32:35], v[88:91], v[152:155], v[32:35]\n"
"v_exp_f32 v47, v47\n"
"v_mfma_f32_16x16x32_bf16 v[36:39], v[88:91], v[168:171], v[36:39]\n"
"v_exp_f32 v48, v48\n"
"ds_read_b128 v[108:111], v3 offset:49408\n"
"v_cvt_pk_bf16_f32 v59, v46, v47\n"
"s_waitcnt lgkmcnt(2)\n"
"v_mfma_f32_16x16x32_bf16 v[32:35], v[92:95], v[156:159], v[32:35]\n"
"v_exp_f32 v49, v49\n"
"v_mfma_f32_16x16x32_bf16 v[36:39], v[92:95], v[172:175], v[36:39]\n"
"v_exp_f32 v50, v50\n"
"ds_read_b128 v[112:115], v0 offset:57344\n"
"v_add_f32 v205, v205, v45\n"
"v_mfma_f32_16x16x32_bf16 v[32:35], v[96:99], a[204:207], v[32:35]\n"
"v_exp_f32 v51, v51\n"
"v_mfma_f32_16x16x32_bf16 v[36:39], v[96:99], a[220:223], v[36:39]\n"
"v_add_f32 v205, v205, v46\n"
"v_add_f32 v205, v205, v47\n"
"ds_read_b128 v[116:119], v1 offset:57344\n"
"v_cvt_pk_bf16_f32 v62, v48, v49\n"
"v_mfma_f32_16x16x32_bf16 v[32:35], v[100:103], a[208:211], v[32:35]\n"
"v_cvt_pk_bf16_f32 v63, v50, v51\n"
"v_add_f32 v206, v206, v48\n"
"v_mfma_f32_16x16x32_bf16 v[36:39], v[100:103], a[224:227], v[36:39]\n"
"v_add_f32 v206, v206, v49\n"
"v_add_f32 v206, v206, v50\n"
"ds_read_b128 v[120:123], v2 offset:57344\n"
"v_add_f32 v206, v206, v51\n"
"s_waitcnt lgkmcnt(2)\n"
"v_mfma_f32_16x16x32_bf16 v[32:35], v[104:107], a[212:215], v[32:35]\n"
"v_mfma_f32_16x16x32_bf16 v[36:39], v[104:107], a[228:231], v[36:39]\n"
"ds_read_b128 v[124:127], v3 offset:57344\n"
"v_mfma_f32_16x16x32_bf16 v[32:35], v[108:111], a[216:219], v[32:35]\n"
"v_mfma_f32_16x16x32_bf16 v[36:39], v[108:111], a[232:235], v[36:39]\n"
"ds_read_b128 v[128:131], v0 offset:57600\n"
"v_mfma_f32_16x16x32_bf16 v[44:47], v[112:115], v[144:147], v[20:23]\n"
"v_mfma_f32_16x16x32_bf16 v[48:51], v[112:115], v[160:163], v[24:27]\n"
"ds_read_b128 v[132:135], v1 offset:57600\n"
"s_waitcnt lgkmcnt(3)\n"
"v_mfma_f32_16x16x32_bf16 v[44:47], v[116:119], v[148:151], v[44:47]\n"
"s_add_u32 m0, s40, 32768\n"
"v_max3_f32 v196, v32, v33, v34\n"
"v_mfma_f32_16x16x32_bf16 v[48:51], v[116:119], v[164:167], v[48:51]\n"
"v_max3_f32 v196, v196, v35, v36\n"
"v_max3_f32 v196, v196, v37, v38\n"
"ds_read_b128 v[136:139], v2 offset:57600\n"
"v_max3_f32 v196, v196, v39, v39\n"
"v_mfma_f32_16x16x32_bf16 v[44:47], v[120:123], v[152:155], v[44:47]\n"
"ds_read_b64_tr_b16 v[80:81], v4 offset:0\n"
"ds_read_b64_tr_b16 v[82:83], v4 offset:8192\n"
"v_mfma_f32_16x16x32_bf16 v[48:51], v[120:123], v[168:171], v[48:51]\n"
"buffer_load_dwordx4 v12, %0, s41 offen offset:0 nt lds\n"
"ds_read_b128 v[140:143], v3 offset:57600\n"
"s_waitcnt lgkmcnt(4)\n"
"v_mfma_f32_16x16x32_bf16 v[44:47], v[124:127], v[156:159], v[44:47]\n"
"ds_read_b64_tr_b16 v[84:85], v5 offset:0\n"
"ds_read_b64_tr_b16 v[86:87], v5 offset:8192\n"
"v_mfma_f32_16x16x32_bf16 v[48:51], v[124:127], v[172:175], v[48:51]\n"
"buffer_load_dwordx4 v13, %0, s41 offen offset:1024 nt lds\n"
"v_mfma_f32_16x16x32_bf16 v[44:47], v[128:131], a[204:207], v[44:47]\n"
"ds_read_b64_tr_b16 v[88:89], v6 offset:0\n"
"ds_read_b64_tr_b16 v[90:91], v6 offset:8192\n"
"v_mfma_f32_16x16x32_bf16 v[48:51], v[128:131], a[220:223], v[48:51]\n"
"v_mfma_f32_16x16x32_bf16 v[44:47], v[132:135], a[208:211], v[44:47]\n"
"buffer_load_dwordx4 v14, %0, s41 offen offset:2048 nt lds\n"
"ds_read_b64_tr_b16 v[92:93], v7 offset:0\n"
"v_mfma_f32_16x16x32_bf16 v[48:51], v[132:135], a[224:227], v[48:51]\n"
"ds_read_b64_tr_b16 v[94:95], v7 offset:8192\n"
"s_waitcnt lgkmcnt(4)\n"
"v_mfma_f32_16x16x32_bf16 v[44:47], v[136:139], a[212:215], v[44:47]\n"
"ds_read_b64_tr_b16 v[96:97], v8 offset:0\n"
"ds_read_b64_tr_b16 v[98:99], v8 offset:8192\n"
"v_mfma_f32_16x16x32_bf16 v[48:51], v[136:139], a[228:231], v[48:51]\n"
"buffer_load_dwordx4 v15, %0, s41 offen offset:3072 nt lds\n"
"v_mfma_f32_16x16x32_bf16 v[44:47], v[140:143], a[216:219], v[44:47]\n"
"ds_read_b64_tr_b16 v[100:101], v9 offset:0\n"
"ds_read_b64_tr_b16 v[102:103], v9 offset:8192\n"
"v_mfma_f32_16x16x32_bf16 v[48:51], v[140:143], a[232:235], v[48:51]\n"
"s_add_u32 s41, s41, 16384\n"
"v_mfma_f32_16x16x32_bf16 a[0:3], v[80:83], v[56:59], a[0:3]\n"
"ds_read_b64_tr_b16 v[104:105], v10 offset:0\n"
"ds_read_b64_tr_b16 v[106:107], v10 offset:8192\n"
"v_mfma_f32_16x16x32_bf16 a[4:7], v[80:83], v[60:63], a[4:7]\n"
"v_mfma_f32_16x16x32_bf16 a[12:15], v[84:87], v[56:59], a[12:15]\n"
"s_and_b32 s41, s41, 0xfffff\n"
"ds_read_b64_tr_b16 v[108:109], v11 offset:0\n"
"v_mfma_f32_16x16x32_bf16 a[16:19], v[84:87], v[60:63], a[16:19]\n"
"ds_read_b64_tr_b16 v[110:111], v11 offset:8192\n"
"v_max3_f32 v196, v196, v44, v45\n"
"v_max3_f32 v196, v196, v46, v47\n"
"v_max3_f32 v196, v196, v48, v49\n"
"s_waitcnt lgkmcnt(4)\n"
"v_mfma_f32_16x16x32_bf16 a[24:27], v[88:91], v[56:59], a[24:27]\n"
"v_max3_f32 v196, v196, v50, v51\n"
"v_cmp_lt_f32 vcc, 0x41000000, v196\n"
"v_mfma_f32_16x16x32_bf16 a[28:31], v[88:91], v[60:63], a[28:31]\n"
"s_cbranch_vccnz LSTUB19_%=\n"
"LRET19_%=:\n"
"ds_read_b64_tr_b16 v[112:113], v4 offset:256\n"
"ds_read_b64_tr_b16 v[114:115], v4 offset:8448\n"
"v_mfma_f32_16x16x32_bf16 a[36:39], v[92:95], v[56:59], a[36:39]\n"
"ds_read_b64_tr_b16 v[116:117], v5 offset:256\n"
"ds_read_b64_tr_b16 v[118:119], v5 offset:8448\n"
"v_mfma_f32_16x16x32_bf16 a[40:43], v[92:95], v[60:63], a[40:43]\n"
"v_exp_f32 v32, v32\n"
"v_exp_f32 v33, v33\n"
"v_mfma_f32_16x16x32_bf16 a[48:51], v[96:99], v[56:59], a[48:51]\n"
"s_add_u32 m0, s40, 114688\n"
"ds_read_b64_tr_b16 v[120:121], v6 offset:256\n"
"v_mfma_f32_16x16x32_bf16 a[52:55], v[96:99], v[60:63], a[52:55]\n"
"ds_read_b64_tr_b16 v[122:123], v6 offset:8448\n"
"v_cvt_pk_bf16_f32 v68, v32, v33\n"
"v_exp_f32 v34, v34\n"
"v_mfma_f32_16x16x32_bf16 a[60:63], v[100:103], v[56:59], a[60:63]\n"
"ds_read_b64_tr_b16 v[124:125], v7 offset:256\n"
"ds_read_b64_tr_b16 v[126:127], v7 offset:8448\n"
"v_mfma_f32_16x16x32_bf16 a[64:67], v[100:103], v[60:63], a[64:67]\n"
"v_exp_f32 v35, v35\n"
"buffer_load_dwordx4 v16, %1, s42 offen offset:0 nt lds\n"
"v_cvt_pk_bf16_f32 v69, v34, v35\n"
"s_waitcnt lgkmcnt(3)\n"
"v_mfma_f32_16x16x32_bf16 a[72:75], v[104:107], v[56:59], a[72:75]\n"
"ds_read_b64_tr_b16 v[128:129], v8 offset:256\n"
"ds_read_b64_tr_b16 v[130:131], v8 offset:8448\n"
"v_mfma_f32_16x16x32_bf16 a[76:79], v[104:107], v[60:63], a[76:79]\n"
"v_exp_f32 v36, v36\n"
"v_exp_f32 v37, v37\n"
"v_mfma_f32_16x16x32_bf16 a[84:87], v[108:111], v[56:59], a[84:87]\n"
"ds_read_b64_tr_b16 v[132:133], v9 offset:256\n"
"ds_read_b64_tr_b16 v[134:135], v9 offset:8448\n"
"v_mfma_f32_16x16x32_bf16 a[88:91], v[108:111], v[60:63], a[88:91]\n"
"buffer_load_dwordx4 v17, %1, s42 offen offset:1024 nt lds\n"
"v_add_f32 v205, v205, v32\n"
"v_exp_f32 v38, v38\n"
"v_mfma_f32_16x16x32_bf16 a[96:99], v[112:115], v[56:59], a[96:99]\n"
"ds_read_b64_tr_b16 v[136:137], v10 offset:256\n"
"ds_read_b64_tr_b16 v[138:139], v10 offset:8448\n"
"v_mfma_f32_16x16x32_bf16 a[100:103], v[112:115], v[60:63], a[100:103]\n"
"v_exp_f32 v39, v39\n"
"v_add_f32 v205, v205, v33\n"
"v_add_f32 v205, v205, v34\n"
"v_mfma_f32_16x16x32_bf16 a[108:111], v[116:119], v[56:59], a[108:111]\n"
"ds_read_b64_tr_b16 v[140:141], v11 offset:256\n"
"ds_read_b64_tr_b16 v[142:143], v11 offset:8448\n"
"v_mfma_f32_16x16x32_bf16 a[112:115], v[116:119], v[60:63], a[112:115]\n"
"buffer_load_dwordx4 v18, %1, s42 offen offset:2048 nt lds\n"
"v_add_f32 v205, v205, v35\n"
"v_cvt_pk_bf16_f32 v72, v36, v37\n"
"v_cvt_pk_bf16_f32 v73, v38, v39\n"
"s_waitcnt lgkmcnt(2)\n"
"v_mfma_f32_16x16x32_bf16 a[120:123], v[120:123], v[56:59], a[120:123]\n"
"v_add_f32 v206, v206, v36\n"
"v_add_f32 v206, v206, v37\n"
"v_mfma_f32_16x16x32_bf16 a[124:127], v[120:123], v[60:63], a[124:127]\n"
"v_add_f32 v206, v206, v38\n"
"v_add_f32 v206, v206, v39\n"
"buffer_load_dwordx4 v19, %1, s42 offen offset:3072 nt lds\n"
"v_mfma_f32_16x16x32_bf16 a[132:135], v[124:127], v[56:59], a[132:135]\n"
"v_mfma_f32_16x16x32_bf16 a[136:139], v[124:127], v[60:63], a[136:139]\n"
"v_mfma_f32_16x16x32_bf16 a[144:147], v[128:131], v[56:59], a[144:147]\n"
"v_mfma_f32_16x16x32_bf16 a[148:151], v[128:131], v[60:63], a[148:151]\n"
"s_add_u32 s42, s42, 16384\n"
"s_waitcnt vmcnt(16) lgkmcnt(0)\n"
"s_barrier\n"
"v_mfma_f32_16x16x32_bf16 a[156:159], v[132:135], v[56:59], a[156:159]\n"
"ds_read_b128 v[80:83], v0 offset:0\n"
"ds_read_b128 v[84:87], v1 offset:0\n"
"v_mfma_f32_16x16x32_bf16 a[160:163], v[132:135], v[60:63], a[160:163]\n"
"ds_read_b128 v[88:91], v2 offset:0\n"
"v_mfma_f32_16x16x32_bf16 a[168:171], v[136:139], v[56:59], a[168:171]\n"
"s_and_b32 s42, s42, 0xfffff\n"
"v_mfma_f32_16x16x32_bf16 a[172:175], v[136:139], v[60:63], a[172:175]\n"
"v_mfma_f32_16x16x32_bf16 a[180:183], v[140:143], v[56:59], a[180:183]\n"
"v_mfma_f32_16x16x32_bf16 a[184:187], v[140:143], v[60:63], a[184:187]\n"
"s_waitcnt lgkmcnt(2)\n"
"v_mfma_f32_16x16x32_bf16 v[32:35], v[80:83], v[144:147], v[20:23]\n"
"ds_read_b128 v[92:95], v3 offset:0\n"
"ds_read_b128 v[96:99], v0 offset:256\n"
"v_mfma_f32_16x16x32_bf16 v[36:39], v[80:83], v[160:163], v[24:27]\n"
"v_exp_f32 v44, v44\n"
"ds_read_b128 v[100:103], v1 offset:256\n"
"v_add_f32 v205, v205, v44\n"
"s_waitcnt lgkmcnt(3)\n"
"v_mfma_f32_16x16x32_bf16 v[32:35], v[84:87], v[148:151], v[32:35]\n"
"v_exp_f32 v45, v45\n"
"v_mfma_f32_16x16x32_bf16 v[36:39], v[84:87], v[164:167], v[36:39]\n"
"v_exp_f32 v46, v46\n"
"ds_read_b128 v[104:107], v2 offset:256\n"
"v_cvt_pk_bf16_f32 v70, v44, v45\n"
"v_mfma_f32_16x16x32_bf16 v[32:35], v[88:91], v[152:155], v[32:35]\n"
"v_exp_f32 v47, v47\n"
"v_mfma_f32_16x16x32_bf16 v[36:39], v[88:91], v[168:171], v[36:39]\n"
"v_exp_f32 v48, v48\n"
"ds_read_b128 v[108:111], v3 offset:256\n"
"v_cvt_pk_bf16_f32 v71, v46, v47\n"
"s_waitcnt lgkmcnt(2)\n"
"v_mfma_f32_16x16x32_bf16 v[32:35], v[92:95], v[156:159], v[32:35]\n"
"v_exp_f32 v49, v49\n"
"v_mfma_f32_16x16x32_bf16 v[36:39], v[92:95], v[172:175], v[36:39]\n"
"v_exp_f32 v50, v50\n"
"ds_read_b128 v[112:115], v0 offset:8192\n"
"v_add_f32 v205, v205, v45\n"
"v_mfma_f32_16x16x32_bf16 v[32:35], v[96:99], a[204:207], v[32:35]\n"
"v_exp_f32 v51, v51\n"
"v_mfma_f32_16x16x32_bf16 v[36:39], v[96:99], a[220:223], v[36:39]\n"
"v_add_f32 v205, v205, v46\n"
"v_add_f32 v205, v205, v47\n"
"ds_read_b128 v[116:119], v1 offset:8192\n"
"v_cvt_pk_bf16_f32 v74, v48, v49\n"
"v_mfma_f32_16x16x32_bf16 v[32:35], v[100:103], a[208:211], v[32:35]\n"
"v_cvt_pk_bf16_f32 v75, v50, v51\n"
"v_add_f32 v206, v206, v48\n"
"v_mfma_f32_16x16x32_bf16 v[36:39], v[100:103], a[224:227], v[36:39]\n"
"v_add_f32 v206, v206, v49\n"
"v_add_f32 v206, v206, v50\n"
"ds_read_b128 v[120:123], v2 offset:8192\n"
"v_add_f32 v206, v206, v51\n"
"s_waitcnt lgkmcnt(2)\n"
"v_mfma_f32_16x16x32_bf16 v[32:35], v[104:107], a[212:215], v[32:35]\n"
"v_mfma_f32_16x16x32_bf16 v[36:39], v[104:107], a[228:231], v[36:39]\n"
"ds_read_b128 v[124:127], v3 offset:8192\n"
"v_mfma_f32_16x16x32_bf16 v[32:35], v[108:111], a[216:219], v[32:35]\n"
"v_mfma_f32_16x16x32_bf16 v[36:39], v[108:111], a[232:235], v[36:39]\n"
"ds_read_b128 v[128:131], v0 offset:8448\n"
"v_mfma_f32_16x16x32_bf16 v[44:47], v[112:115], v[144:147], v[20:23]\n"
"v_mfma_f32_16x16x32_bf16 v[48:51], v[112:115], v[160:163], v[24:27]\n"
"ds_read_b128 v[132:135], v1 offset:8448\n"
"s_waitcnt lgkmcnt(3)\n"
"v_mfma_f32_16x16x32_bf16 v[44:47], v[116:119], v[148:151], v[44:47]\n"
"s_add_u32 m0, s40, 49152\n"
"v_max3_f32 v196, v32, v33, v34\n"
"v_mfma_f32_16x16x32_bf16 v[48:51], v[116:119], v[164:167], v[48:51]\n"
"v_max3_f32 v196, v196, v35, v36\n"
"v_max3_f32 v196, v196, v37, v38\n"
"ds_read_b128 v[136:139], v2 offset:8448\n"
"v_max3_f32 v196, v196, v39, v39\n"
"v_mfma_f32_16x16x32_bf16 v[44:47], v[120:123], v[152:155], v[44:47]\n"
"ds_read_b64_tr_b16 v[80:81], v4 offset:16384\n"
"ds_read_b64_tr_b16 v[82:83], v4 offset:24576\n"
"v_mfma_f32_16x16x32_bf16 v[48:51], v[120:123], v[168:171], v[48:51]\n"
"buffer_load_dwordx4 v12, %0, s41 offen offset:0 nt lds\n"
"ds_read_b128 v[140:143], v3 offset:8448\n"
"s_waitcnt lgkmcnt(4)\n"
"v_mfma_f32_16x16x32_bf16 v[44:47], v[124:127], v[156:159], v[44:47]\n"
"ds_read_b64_tr_b16 v[84:85], v5 offset:16384\n"
"ds_read_b64_tr_b16 v[86:87], v5 offset:24576\n"
"v_mfma_f32_16x16x32_bf16 v[48:51], v[124:127], v[172:175], v[48:51]\n"
"buffer_load_dwordx4 v13, %0, s41 offen offset:1024 nt lds\n"
"v_mfma_f32_16x16x32_bf16 v[44:47], v[128:131], a[204:207], v[44:47]\n"
"ds_read_b64_tr_b16 v[88:89], v6 offset:16384\n"
"ds_read_b64_tr_b16 v[90:91], v6 offset:24576\n"
"v_mfma_f32_16x16x32_bf16 v[48:51], v[128:131], a[220:223], v[48:51]\n"
"v_mfma_f32_16x16x32_bf16 v[44:47], v[132:135], a[208:211], v[44:47]\n"
"buffer_load_dwordx4 v14, %0, s41 offen offset:2048 nt lds\n"
"ds_read_b64_tr_b16 v[92:93], v7 offset:16384\n"
"v_mfma_f32_16x16x32_bf16 v[48:51], v[132:135], a[224:227], v[48:51]\n"
"ds_read_b64_tr_b16 v[94:95], v7 offset:24576\n"
"s_waitcnt lgkmcnt(4)\n"
"v_mfma_f32_16x16x32_bf16 v[44:47], v[136:139], a[212:215], v[44:47]\n"
"ds_read_b64_tr_b16 v[96:97], v8 offset:16384\n"
"ds_read_b64_tr_b16 v[98:99], v8 offset:24576\n"
"v_mfma_f32_16x16x32_bf16 v[48:51], v[136:139], a[228:231], v[48:51]\n"
"buffer_load_dwordx4 v15, %0, s41 offen offset:3072 nt lds\n"
"v_mfma_f32_16x16x32_bf16 v[44:47], v[140:143], a[216:219], v[44:47]\n"
"ds_read_b64_tr_b16 v[100:101], v9 offset:16384\n"
"ds_read_b64_tr_b16 v[102:103], v9 offset:24576\n"
"v_mfma_f32_16x16x32_bf16 v[48:51], v[140:143], a[232:235], v[48:51]\n"
"s_add_u32 s41, s41, 16384\n"
"v_mfma_f32_16x16x32_bf16 a[0:3], v[80:83], v[68:71], a[0:3]\n"
"ds_read_b64_tr_b16 v[104:105], v10 offset:16384\n"
"ds_read_b64_tr_b16 v[106:107], v10 offset:24576\n"
"v_mfma_f32_16x16x32_bf16 a[4:7], v[80:83], v[72:75], a[4:7]\n"
"v_mfma_f32_16x16x32_bf16 a[12:15], v[84:87], v[68:71], a[12:15]\n"
"s_and_b32 s41, s41, 0xfffff\n"
"ds_read_b64_tr_b16 v[108:109], v11 offset:16384\n"
"v_mfma_f32_16x16x32_bf16 a[16:19], v[84:87], v[72:75], a[16:19]\n"
"ds_read_b64_tr_b16 v[110:111], v11 offset:24576\n"
"v_max3_f32 v196, v196, v44, v45\n"
"v_max3_f32 v196, v196, v46, v47\n"
"v_max3_f32 v196, v196, v48, v49\n"
"s_waitcnt lgkmcnt(4)\n"
"v_mfma_f32_16x16x32_bf16 a[24:27], v[88:91], v[68:71], a[24:27]\n"
"v_max3_f32 v196, v196, v50, v51\n"
"v_cmp_lt_f32 vcc, 0x41000000, v196\n"
"v_mfma_f32_16x16x32_bf16 a[28:31], v[88:91], v[72:75], a[28:31]\n"
"s_cbranch_vccnz LSTUB20_%=\n"
"LRET20_%=:\n"
"ds_read_b64_tr_b16 v[112:113], v4 offset:16640\n"
"ds_read_b64_tr_b16 v[114:115], v4 offset:24832\n"
"v_mfma_f32_16x16x32_bf16 a[36:39], v[92:95], v[68:71], a[36:39]\n"
"ds_read_b64_tr_b16 v[116:117], v5 offset:16640\n"
"ds_read_b64_tr_b16 v[118:119], v5 offset:24832\n"
"v_mfma_f32_16x16x32_bf16 a[40:43], v[92:95], v[72:75], a[40:43]\n"
"v_exp_f32 v32, v32\n"
"v_exp_f32 v33, v33\n"
"v_mfma_f32_16x16x32_bf16 a[48:51], v[96:99], v[68:71], a[48:51]\n"
"s_add_u32 m0, s40, 65536\n"
"ds_read_b64_tr_b16 v[120:121], v6 offset:16640\n"
"v_mfma_f32_16x16x32_bf16 a[52:55], v[96:99], v[72:75], a[52:55]\n"
"ds_read_b64_tr_b16 v[122:123], v6 offset:24832\n"
"v_cvt_pk_bf16_f32 v56, v32, v33\n"
"v_exp_f32 v34, v34\n"
"v_mfma_f32_16x16x32_bf16 a[60:63], v[100:103], v[68:71], a[60:63]\n"
"ds_read_b64_tr_b16 v[124:125], v7 offset:16640\n"
"ds_read_b64_tr_b16 v[126:127], v7 offset:24832\n"
"v_mfma_f32_16x16x32_bf16 a[64:67], v[100:103], v[72:75], a[64:67]\n"
"v_exp_f32 v35, v35\n"
"buffer_load_dwordx4 v16, %1, s42 offen offset:0 nt lds\n"
"v_cvt_pk_bf16_f32 v57, v34, v35\n"
"s_waitcnt lgkmcnt(3)\n"
"v_mfma_f32_16x16x32_bf16 a[72:75], v[104:107], v[68:71], a[72:75]\n"
"ds_read_b64_tr_b16 v[128:129], v8 offset:16640\n"
"ds_read_b64_tr_b16 v[130:131], v8 offset:24832\n"
"v_mfma_f32_16x16x32_bf16 a[76:79], v[104:107], v[72:75], a[76:79]\n"
"v_exp_f32 v36, v36\n"
"v_exp_f32 v37, v37\n"
"v_mfma_f32_16x16x32_bf16 a[84:87], v[108:111], v[68:71], a[84:87]\n"
"ds_read_b64_tr_b16 v[132:133], v9 offset:16640\n"
"ds_read_b64_tr_b16 v[134:135], v9 offset:24832\n"
"v_mfma_f32_16x16x32_bf16 a[88:91], v[108:111], v[72:75], a[88:91]\n"
"buffer_load_dwordx4 v17, %1, s42 offen offset:1024 nt lds\n"
"v_add_f32 v205, v205, v32\n"
"v_exp_f32 v38, v38\n"
"v_mfma_f32_16x16x32_bf16 a[96:99], v[112:115], v[68:71], a[96:99]\n"
"ds_read_b64_tr_b16 v[136:137], v10 offset:16640\n"
"ds_read_b64_tr_b16 v[138:139], v10 offset:24832\n"
"v_mfma_f32_16x16x32_bf16 a[100:103], v[112:115], v[72:75], a[100:103]\n"
"v_exp_f32 v39, v39\n"
"v_add_f32 v205, v205, v33\n"
"v_add_f32 v205, v205, v34\n"
"v_mfma_f32_16x16x32_bf16 a[108:111], v[116:119], v[68:71], a[108:111]\n"
"ds_read_b64_tr_b16 v[140:141], v11 offset:16640\n"
"ds_read_b64_tr_b16 v[142:143], v11 offset:24832\n"
"v_mfma_f32_16x16x32_bf16 a[112:115], v[116:119], v[72:75], a[112:115]\n"
"buffer_load_dwordx4 v18, %1, s42 offen offset:2048 nt lds\n"
"v_add_f32 v205, v205, v35\n"
"v_cvt_pk_bf16_f32 v60, v36, v37\n"
"v_cvt_pk_bf16_f32 v61, v38, v39\n"
"s_waitcnt lgkmcnt(2)\n"
"v_mfma_f32_16x16x32_bf16 a[120:123], v[120:123], v[68:71], a[120:123]\n"
"v_add_f32 v206, v206, v36\n"
"v_add_f32 v206, v206, v37\n"
"v_mfma_f32_16x16x32_bf16 a[124:127], v[120:123], v[72:75], a[124:127]\n"
"v_add_f32 v206, v206, v38\n"
"v_add_f32 v206, v206, v39\n"
"buffer_load_dwordx4 v19, %1, s42 offen offset:3072 nt lds\n"
"v_mfma_f32_16x16x32_bf16 a[132:135], v[124:127], v[68:71], a[132:135]\n"
"v_mfma_f32_16x16x32_bf16 a[136:139], v[124:127], v[72:75], a[136:139]\n"
"v_mfma_f32_16x16x32_bf16 a[144:147], v[128:131], v[68:71], a[144:147]\n"
"v_mfma_f32_16x16x32_bf16 a[148:151], v[128:131], v[72:75], a[148:151]\n"
"s_add_u32 s42, s42, 16384\n"
"s_waitcnt vmcnt(16) lgkmcnt(0)\n"
"s_barrier\n"
"v_mfma_f32_16x16x32_bf16 a[156:159], v[132:135], v[68:71], a[156:159]\n"
"ds_read_b128 v[80:83], v0 offset:16384\n"
"ds_read_b128 v[84:87], v1 offset:16384\n"
"v_mfma_f32_16x16x32_bf16 a[160:163], v[132:135], v[72:75], a[160:163]\n"
"ds_read_b128 v[88:91], v2 offset:16384\n"
"v_mfma_f32_16x16x32_bf16 a[168:171], v[136:139], v[68:71], a[168:171]\n"
"s_and_b32 s42, s42, 0xfffff\n"
"v_mfma_f32_16x16x32_bf16 a[172:175], v[136:139], v[72:75], a[172:175]\n"
"v_mfma_f32_16x16x32_bf16 a[180:183], v[140:143], v[68:71], a[180:183]\n"
"v_mfma_f32_16x16x32_bf16 a[184:187], v[140:143], v[72:75], a[184:187]\n"
"s_sub_u32 s43, s43, 1\n"
"s_cmp_lg_u32 s43, 0\n"
"s_cbranch_scc1 LLOOPB2_%=\n"
"LTAIL2_%=:\n"
"s_waitcnt lgkmcnt(2)\n"
"v_mfma_f32_16x16x32_bf16 v[32:35], v[80:83], v[144:147], v[20:23]\n"
"ds_read_b128 v[92:95], v3 offset:16384\n"
"ds_read_b128 v[96:99], v0 offset:16640\n"
"v_mfma_f32_16x16x32_bf16 v[36:39], v[80:83], v[160:163], v[24:27]\n"
"v_exp_f32 v44, v44\n"
"ds_read_b128 v[100:103], v1 offset:16640\n"
"v_add_f32 v205, v205, v44\n"
"s_waitcnt lgkmcnt(3)\n"
"v_mfma_f32_16x16x32_bf16 v[32:35], v[84:87], v[148:151], v[32:35]\n"
"v_exp_f32 v45, v45\n"
"v_mfma_f32_16x16x32_bf16 v[36:39], v[84:87], v[164:167], v[36:39]\n"
"v_exp_f32 v46, v46\n"
"ds_read_b128 v[104:107], v2 offset:16640\n"
"v_cvt_pk_bf16_f32 v58, v44, v45\n"
"v_mfma_f32_16x16x32_bf16 v[32:35], v[88:91], v[152:155], v[32:35]\n"
"v_exp_f32 v47, v47\n"
"v_mfma_f32_16x16x32_bf16 v[36:39], v[88:91], v[168:171], v[36:39]\n"
"v_exp_f32 v48, v48\n"
"ds_read_b128 v[108:111], v3 offset:16640\n"
"v_cvt_pk_bf16_f32 v59, v46, v47\n"
"s_waitcnt lgkmcnt(2)\n"
"v_mfma_f32_16x16x32_bf16 v[32:35], v[92:95], v[156:159], v[32:35]\n"
"v_exp_f32 v49, v49\n"
"v_mfma_f32_16x16x32_bf16 v[36:39], v[92:95], v[172:175], v[36:39]\n"
"v_exp_f32 v50, v50\n"
"ds_read_b128 v[112:115], v0 offset:24576\n"
"v_add_f32 v205, v205, v45\n"
"v_mfma_f32_16x16x32_bf16 v[32:35], v[96:99], a[204:207], v[32:35]\n"
"v_exp_f32 v51, v51\n"
"v_mfma_f32_16x16x32_bf16 v[36:39], v[96:99], a[220:223], v[36:39]\n"
"v_add_f32 v205, v205, v46\n"
"v_add_f32 v205, v205, v47\n"
"ds_read_b128 v[116:119], v1 offset:24576\n"
"v_cvt_pk_bf16_f32 v62, v48, v49\n"
"v_mfma_f32_16x16x32_bf16 v[32:35], v[100:103], a[208:211], v[32:35]\n"
"v_cvt_pk_bf16_f32 v63, v50, v51\n"
"v_add_f32 v206, v206, v48\n"
"v_mfma_f32_16x16x32_bf16 v[36:39], v[100:103], a[224:227], v[36:39]\n"
"v_add_f32 v206, v206, v49\n"
"v_add_f32 v206, v206, v50\n"
"ds_read_b128 v[120:123], v2 offset:24576\n"
"v_add_f32 v206, v206, v51\n"
"s_waitcnt lgkmcnt(2)\n"
"v_mfma_f32_16x16x32_bf16 v[32:35], v[104:107], a[212:215], v[32:35]\n"
"s_add_u32 m0, s40, 81920\n"
"v_mfma_f32_16x16x32_bf16 v[36:39], v[104:107], a[228:231], v[36:39]\n"
"buffer_load_dwordx4 v16, %1, s42 offen offset:0 lds\n"
"ds_read_b128 v[124:127], v3 offset:24576\n"
"v_mfma_f32_16x16x32_bf16 v[32:35], v[108:111], a[216:219], v[32:35]\n"
"v_mfma_f32_16x16x32_bf16 v[36:39], v[108:111], a[232:235], v[36:39]\n"
"buffer_load_dwordx4 v17, %1, s42 offen offset:1024 lds\n"
"ds_read_b128 v[128:131], v0 offset:24832\n"
"v_mfma_f32_16x16x32_bf16 v[44:47], v[112:115], v[144:147], v[20:23]\n"
"v_mfma_f32_16x16x32_bf16 v[48:51], v[112:115], v[160:163], v[24:27]\n"
"buffer_load_dwordx4 v18, %1, s42 offen offset:2048 lds\n"
"ds_read_b128 v[132:135], v1 offset:24832\n"
"s_waitcnt lgkmcnt(3)\n"
"v_mfma_f32_16x16x32_bf16 v[44:47], v[116:119], v[148:151], v[44:47]\n"
"v_max3_f32 v196, v32, v33, v34\n"
"v_max3_f32 v196, v196, v35, v36\n"
"v_mfma_f32_16x16x32_bf16 v[48:51], v[116:119], v[164:167], v[48:51]\n"
"buffer_load_dwordx4 v19, %1, s42 offen offset:3072 lds\n"
"v_max3_f32 v196, v196, v37, v38\n"
"ds_read_b128 v[136:139], v2 offset:24832\n"
"v_max3_f32 v196, v196, v39, v39\n"
"v_mfma_f32_16x16x32_bf16 v[44:47], v[120:123], v[152:155], v[44:47]\n"
"ds_read_b64_tr_b16 v[80:81], v4 offset:32768\n"
"ds_read_b64_tr_b16 v[82:83], v4 offset:40960\n"
"v_mfma_f32_16x16x32_bf16 v[48:51], v[120:123], v[168:171], v[48:51]\n"
"s_add_u32 s42, s42, 16384\n"
"ds_read_b128 v[140:143], v3 offset:24832\n"
"s_waitcnt lgkmcnt(4)\n"
"v_mfma_f32_16x16x32_bf16 v[44:47], v[124:127], v[156:159], v[44:47]\n"
"ds_read_b64_tr_b16 v[84:85], v5 offset:32768\n"
"ds_read_b64_tr_b16 v[86:87], v5 offset:40960\n"
"v_mfma_f32_16x16x32_bf16 v[48:51], v[124:127], v[172:175], v[48:51]\n"
"s_and_b32 s42, s42, 0xfffff\n"
"v_mfma_f32_16x16x32_bf16 v[44:47], v[128:131], a[204:207], v[44:47]\n"
"ds_read_b64_tr_b16 v[88:89], v6 offset:32768\n"
"ds_read_b64_tr_b16 v[90:91], v6 offset:40960\n"
"v_mfma_f32_16x16x32_bf16 v[48:51], v[128:131], a[220:223], v[48:51]\n"
"v_mfma_f32_16x16x32_bf16 v[44:47], v[132:135], a[208:211], v[44:47]\n"
"s_mov_b32 s47, 0\n"
"s_add_u32 m0, s48, 131072\n"
"v_mfma_f32_16x16x32_bf16 v[48:51], v[132:135], a[224:227], v[48:51]\n"
"buffer_load_dwordx4 v208, %4, s47 offen offset:0 lds\n"
"ds_read_b64_tr_b16 v[92:93], v7 offset:32768\n"
"ds_read_b64_tr_b16 v[94:95], v7 offset:40960\n"
"s_waitcnt lgkmcnt(4)\n"
"v_mfma_f32_16x16x32_bf16 v[44:47], v[136:139], a[212:215], v[44:47]\n"
"ds_read_b64_tr_b16 v[96:97], v8 offset:32768\n"
"ds_read_b64_tr_b16 v[98:99], v8 offset:40960\n"
"v_mfma_f32_16x16x32_bf16 v[48:51], v[136:139], a[228:231], v[48:51]\n"
"v_mfma_f32_16x16x32_bf16 v[44:47], v[140:143], a[216:219], v[44:47]\n"
"buffer_load_dwordx4 v209, %4, s47 offen offset:1024 lds\n"
"ds_read_b64_tr_b16 v[100:101], v9 offset:32768\n"
"v_mfma_f32_16x16x32_bf16 v[48:51], v[140:143], a[232:235], v[48:51]\n"
"ds_read_b64_tr_b16 v[102:103], v9 offset:40960\n"
"v_mfma_f32_16x16x32_bf16 a[0:3], v[80:83], v[56:59], a[0:3]\n"
"ds_read_b64_tr_b16 v[104:105], v10 offset:32768\n"
"ds_read_b64_tr_b16 v[106:107], v10 offset:40960\n"
"v_mfma_f32_16x16x32_bf16 a[4:7], v[80:83], v[60:63], a[4:7]\n"
"v_mfma_f32_16x16x32_bf16 a[12:15], v[84:87], v[56:59], a[12:15]\n"
"buffer_load_dwordx4 v210, %4, s47 offen offset:2048 lds\n"
"ds_read_b64_tr_b16 v[108:109], v11 offset:32768\n"
"v_mfma_f32_16x16x32_bf16 a[16:19], v[84:87], v[60:63], a[16:19]\n"
"ds_read_b64_tr_b16 v[110:111], v11 offset:40960\n"
"v_max3_f32 v196, v196, v44, v45\n"
"v_max3_f32 v196, v196, v46, v47\n"
"v_max3_f32 v196, v196, v48, v49\n"
"s_waitcnt lgkmcnt(4)\n"
"v_mfma_f32_16x16x32_bf16 a[24:27], v[88:91], v[56:59], a[24:27]\n"
"v_max3_f32 v196, v196, v50, v51\n"
"v_cmp_lt_f32 vcc, 0x41000000, v196\n"
"v_mfma_f32_16x16x32_bf16 a[28:31], v[88:91], v[60:63], a[28:31]\n"
"s_cbranch_vccnz LSTUB21_%=\n"
"LRET21_%=:\n"
"ds_read_b64_tr_b16 v[112:113], v4 offset:33024\n"
"buffer_load_dwordx4 v211, %4, s47 offen offset:3072 lds\n"
"ds_read_b64_tr_b16 v[114:115], v4 offset:41216\n"
"v_mfma_f32_16x16x32_bf16 a[36:39], v[92:95], v[56:59], a[36:39]\n"
"ds_read_b64_tr_b16 v[116:117], v5 offset:33024\n"
"ds_read_b64_tr_b16 v[118:119], v5 offset:41216\n"
"v_mfma_f32_16x16x32_bf16 a[40:43], v[92:95], v[60:63], a[40:43]\n"
"v_exp_f32 v32, v32\n"
"v_exp_f32 v33, v33\n"
"v_mfma_f32_16x16x32_bf16 a[48:51], v[96:99], v[56:59], a[48:51]\n"
"ds_read_b64_tr_b16 v[120:121], v6 offset:33024\n"
"ds_read_b64_tr_b16 v[122:123], v6 offset:41216\n"
"v_mfma_f32_16x16x32_bf16 a[52:55], v[96:99], v[60:63], a[52:55]\n"
"s_mov_b32 s47, 28672\n"
"s_add_u32 m0, s48, 135168\n"
"buffer_load_dwordx4 v208, %4, s47 offen offset:0 lds\n"
"v_cvt_pk_bf16_f32 v68, v32, v33\n"
"v_mfma_f32_16x16x32_bf16 a[60:63], v[100:103], v[56:59], a[60:63]\n"
"ds_read_b64_tr_b16 v[124:125], v7 offset:33024\n"
"ds_read_b64_tr_b16 v[126:127], v7 offset:41216\n"
"v_mfma_f32_16x16x32_bf16 a[64:67], v[100:103], v[60:63], a[64:67]\n"
"v_exp_f32 v34, v34\n"
"v_exp_f32 v35, v35\n"
"s_waitcnt lgkmcnt(2)\n"
"v_mfma_f32_16x16x32_bf16 a[72:75], v[104:107], v[56:59], a[72:75]\n"
"ds_read_b64_tr_b16 v[128:129], v8 offset:33024\n"
"ds_read_b64_tr_b16 v[130:131], v8 offset:41216\n"
"v_mfma_f32_16x16x32_bf16 a[76:79], v[104:107], v[60:63], a[76:79]\n"
"v_exp_f32 v36, v36\n"
"buffer_load_dwordx4 v209, %4, s47 offen offset:1024 lds\n"
"v_cvt_pk_bf16_f32 v69, v34, v35\n"
"v_mfma_f32_16x16x32_bf16 a[84:87], v[108:111], v[56:59], a[84:87]\n"
"ds_read_b64_tr_b16 v[132:133], v9 offset:33024\n"
"ds_read_b64_tr_b16 v[134:135], v9 offset:41216\n"
"v_mfma_f32_16x16x32_bf16 a[88:91], v[108:111], v[60:63], a[88:91]\n"
"v_exp_f32 v37, v37\n"
"v_exp_f32 v38, v38\n"
"v_mfma_f32_16x16x32_bf16 a[96:99], v[112:115], v[56:59], a[96:99]\n"
"ds_read_b64_tr_b16 v[136:137], v10 offset:33024\n"
"ds_read_b64_tr_b16 v[138:139], v10 offset:41216\n"
"v_mfma_f32_16x16x32_bf16 a[100:103], v[112:115], v[60:63], a[100:103]\n"
"buffer_load_dwordx4 v210, %4, s47 offen offset:2048 lds\n"
"v_add_f32 v205, v205, v32\n"
"v_exp_f32 v39, v39\n"
"v_mfma_f32_16x16x32_bf16 a[108:111], v[116:119], v[56:59], a[108:111]\n"
"ds_read_b64_tr_b16 v[140:141], v11 offset:33024\n"
"ds_read_b64_tr_b16 v[142:143], v11 offset:41216\n"
"v_mfma_f32_16x16x32_bf16 a[112:115], v[116:119], v[60:63], a[112:115]\n"
"v_add_f32 v205, v205, v33\n"
"v_add_f32 v205, v205, v34\n"
"v_add_f32 v205, v205, v35\n"
"v_cvt_pk_bf16_f32 v72, v36, v37\n"
"v_mfma_f32_16x16x32_bf16 a[120:123], v[120:123], v[56:59], a[120:123]\n"
"v_cvt_pk_bf16_f32 v73, v38, v39\n"
"v_add_f32 v206, v206, v36\n"
"v_mfma_f32_16x16x32_bf16 a[124:127], v[120:123], v[60:63], a[124:127]\n"
"buffer_load_dwordx4 v211, %4, s47 offen offset:3072 lds\n"
"v_add_f32 v206, v206, v37\n"
"v_add_f32 v206, v206, v38\n"
"v_add_f32 v206, v206, v39\n"
"s_waitcnt lgkmcnt(0)\n"
"v_mfma_f32_16x16x32_bf16 a[132:135], v[124:127], v[56:59], a[132:135]\n"
"v_mfma_f32_16x16x32_bf16 a[136:139], v[124:127], v[60:63], a[136:139]\n"
"v_mfma_f32_16x16x32_bf16 a[144:147], v[128:131], v[56:59], a[144:147]\n"
"v_mfma_f32_16x16x32_bf16 a[148:151], v[128:131], v[60:63], a[148:151]\n"
"s_waitcnt vmcnt(20) lgkmcnt(0)\n"
"s_barrier\n"
"v_mfma_f32_16x16x32_bf16 a[156:159], v[132:135], v[56:59], a[156:159]\n"
"ds_read_b128 v[80:83], v0 offset:32768\n"
"ds_read_b128 v[84:87], v1 offset:32768\n"
"v_mfma_f32_16x16x32_bf16 a[160:163], v[132:135], v[60:63], a[160:163]\n"
"ds_read_b128 v[88:91], v2 offset:32768\n"
"v_mfma_f32_16x16x32_bf16 a[168:171], v[136:139], v[56:59], a[168:171]\n"
"v_mfma_f32_16x16x32_bf16 a[172:175], v[136:139], v[60:63], a[172:175]\n"
"v_mfma_f32_16x16x32_bf16 a[180:183], v[140:143], v[56:59], a[180:183]\n"
"v_mfma_f32_16x16x32_bf16 a[184:187], v[140:143], v[60:63], a[184:187]\n"
"s_waitcnt lgkmcnt(2)\n"
"v_mfma_f32_16x16x32_bf16 v[32:35], v[80:83], v[144:147], v[20:23]\n"
"ds_read_b128 v[92:95], v3 offset:32768\n"
"ds_read_b128 v[96:99], v0 offset:33024\n"
"v_mfma_f32_16x16x32_bf16 v[36:39], v[80:83], v[160:163], v[24:27]\n"
"v_exp_f32 v44, v44\n"
"ds_read_b128 v[100:103], v1 offset:33024\n"
"v_add_f32 v205, v205, v44\n"
"s_waitcnt lgkmcnt(3)\n"
"v_mfma_f32_16x16x32_bf16 v[32:35], v[84:87], v[148:151], v[32:35]\n"
"s_mov_b32 s47, 128\n"
"s_add_u32 m0, s48, 0\n"
"v_mfma_f32_16x16x32_bf16 v[36:39], v[84:87], v[164:167], v[36:39]\n"
"buffer_load_dwordx4 v208, %4, s47 offen offset:0 lds\n"
"ds_read_b128 v[104:107], v2 offset:33024\n"
"v_mfma_f32_16x16x32_bf16 v[32:35], v[88:91], v[152:155], v[32:35]\n"
"v_exp_f32 v45, v45\n"
"v_mfma_f32_16x16x32_bf16 v[36:39], v[88:91], v[168:171], v[36:39]\n"
"v_exp_f32 v46, v46\n"
"ds_read_b128 v[108:111], v3 offset:33024\n"
"v_cvt_pk_bf16_f32 v70, v44, v45\n"
"s_waitcnt lgkmcnt(2)\n"
"v_mfma_f32_16x16x32_bf16 v[32:35], v[92:95], v[156:159], v[32:35]\n"
"v_exp_f32 v47, v47\n"
"v_mfma_f32_16x16x32_bf16 v[36:39], v[92:95], v[172:175], v[36:39]\n"
"v_exp_f32 v48, v48\n"
"ds_read_b128 v[112:115], v0 offset:40960\n"
"v_cvt_pk_bf16_f32 v71, v46, v47\n"
"v_mfma_f32_16x16x32_bf16 v[32:35], v[96:99], a[204:207], v[32:35]\n"
"v_exp_f32 v49, v49\n"
"v_mfma_f32_16x16x32_bf16 v[36:39], v[96:99], a[220:223], v[36:39]\n"
"v_exp_f32 v50, v50\n"
"buffer_load_dwordx4 v209, %4, s47 offen offset:1024 lds\n"
"ds_read_b128 v[116:119], v1 offset:40960\n"
"v_mfma_f32_16x16x32_bf16 v[32:35], v[100:103], a[208:211], v[32:35]\n"
"v_exp_f32 v51, v51\n"
"v_mfma_f32_16x16x32_bf16 v[36:39], v[100:103], a[224:227], v[36:39]\n"
"v_add_f32 v205, v205, v45\n"
"v_add_f32 v205, v205, v46\n"
"ds_read_b128 v[120:123], v2 offset:40960\n"
"v_add_f32 v205, v205, v47\n"
"s_waitcnt lgkmcnt(2)\n"
"v_mfma_f32_16x16x32_bf16 v[32:35], v[104:107], a[212:215], v[32:35]\n"
"v_cvt_pk_bf16_f32 v74, v48, v49\n"
"v_cvt_pk_bf16_f32 v75, v50, v51\n"
"v_mfma_f32_16x16x32_bf16 v[36:39], v[104:107], a[228:231], v[36:39]\n"
"v_add_f32 v206, v206, v48\n"
"v_add_f32 v206, v206, v49\n"
"v_add_f32 v206, v206, v50\n"
"v_add_f32 v206, v206, v51\n"
"v_mfma_f32_16x16x32_bf16 v[32:35], v[108:111], a[216:219], v[32:35]\n"
"ds_read_b128 v[124:127], v3 offset:40960\n"
"v_mfma_f32_16x16x32_bf16 v[36:39], v[108:111], a[232:235], v[36:39]\n"
"ds_read_b128 v[128:131], v0 offset:41216\n"
"v_mfma_f32_16x16x32_bf16 v[44:47], v[112:115], v[144:147], v[20:23]\n"
"buffer_load_dwordx4 v210, %4, s47 offen offset:2048 lds\n"
"v_mfma_f32_16x16x32_bf16 v[48:51], v[112:115], v[160:163], v[24:27]\n"
"ds_read_b128 v[132:135], v1 offset:41216\n"
"s_waitcnt lgkmcnt(3)\n"
"v_mfma_f32_16x16x32_bf16 v[44:47], v[116:119], v[148:151], v[44:47]\n"
"v_max3_f32 v196, v32, v33, v34\n"
"v_max3_f32 v196, v196, v35, v36\n"
"v_mfma_f32_16x16x32_bf16 v[48:51], v[116:119], v[164:167], v[48:51]\n"
"v_max3_f32 v196, v196, v37, v38\n"
"v_max3_f32 v196, v196, v39, v39\n"
"ds_read_b128 v[136:139], v2 offset:41216\n"
"v_mfma_f32_16x16x32_bf16 v[44:47], v[120:123], v[152:155], v[44:47]\n"
"ds_read_b64_tr_b16 v[80:81], v4 offset:49152\n"
"ds_read_b64_tr_b16 v[82:83], v4 offset:57344\n"
"v_mfma_f32_16x16x32_bf16 v[48:51], v[120:123], v[168:171], v[48:51]\n"
"ds_read_b128 v[140:143], v3 offset:41216\n"
"s_waitcnt lgkmcnt(4)\n"
"v_mfma_f32_16x16x32_bf16 v[44:47], v[124:127], v[156:159], v[44:47]\n"
"ds_read_b64_tr_b16 v[84:85], v5 offset:49152\n"
"ds_read_b64_tr_b16 v[86:87], v5 offset:57344\n"
"v_mfma_f32_16x16x32_bf16 v[48:51], v[124:127], v[172:175], v[48:51]\n"
"buffer_load_dwordx4 v211, %4, s47 offen offset:3072 lds\n"
"v_mfma_f32_16x16x32_bf16 v[44:47], v[128:131], a[204:207], v[44:47]\n"
"ds_read_b64_tr_b16 v[88:89], v6 offset:49152\n"
"ds_read_b64_tr_b16 v[90:91], v6 offset:57344\n"
"v_mfma_f32_16x16x32_bf16 v[48:51], v[128:131], a[220:223], v[48:51]\n"
"v_mfma_f32_16x16x32_bf16 v[44:47], v[132:135], a[208:211], v[44:47]\n"
"ds_read_b64_tr_b16 v[92:93], v7 offset:49152\n"
"ds_read_b64_tr_b16 v[94:95], v7 offset:57344\n"
"v_mfma_f32_16x16x32_bf16 v[48:51], v[132:135], a[224:227], v[48:51]\n"
"s_waitcnt lgkmcnt(4)\n"
"v_mfma_f32_16x16x32_bf16 v[44:47], v[136:139], a[212:215], v[44:47]\n"
"ds_read_b64_tr_b16 v[96:97], v8 offset:49152\n"
"ds_read_b64_tr_b16 v[98:99], v8 offset:57344\n"
"v_mfma_f32_16x16x32_bf16 v[48:51], v[136:139], a[228:231], v[48:51]\n"
"s_mov_b32 s47, 28800\n"
"s_add_u32 m0, s48, 4096\n"
"buffer_load_dwordx4 v208, %4, s47 offen offset:0 lds\n"
"v_mfma_f32_16x16x32_bf16 v[44:47], v[140:143], a[216:219], v[44:47]\n"
"ds_read_b64_tr_b16 v[100:101], v9 offset:49152\n"
"ds_read_b64_tr_b16 v[102:103], v9 offset:57344\n"
"v_mfma_f32_16x16x32_bf16 v[48:51], v[140:143], a[232:235], v[48:51]\n"
"v_mfma_f32_16x16x32_bf16 a[0:3], v[80:83], v[68:71], a[0:3]\n"
"ds_read_b64_tr_b16 v[104:105], v10 offset:49152\n"
"ds_read_b64_tr_b16 v[106:107], v10 offset:57344\n"
"v_mfma_f32_16x16x32_bf16 a[4:7], v[80:83], v[72:75], a[4:7]\n"
"v_mfma_f32_16x16x32_bf16 a[12:15], v[84:87], v[68:71], a[12:15]\n"
"ds_read_b64_tr_b16 v[108:109], v11 offset:49152\n"
"ds_read_b64_tr_b16 v[110:111], v11 offset:57344\n"
"v_mfma_f32_16x16x32_bf16 a[16:19], v[84:87], v[72:75], a[16:19]\n"
"v_max3_f32 v196, v196, v44, v45\n"
"v_max3_f32 v196, v196, v46, v47\n"
"v_max3_f32 v196, v196, v48, v49\n"
"v_max3_f32 v196, v196, v50, v51\n"
"s_waitcnt lgkmcnt(4)\n"
"v_mfma_f32_16x16x32_bf16 a[24:27], v[88:91], v[68:71], a[24:27]\n"
"buffer_load_dwordx4 v209, %4, s47 offen offset:1024 lds\n"
"v_cmp_lt_f32 vcc, 0x41000000, v196\n"
"v_mfma_f32_16x16x32_bf16 a[28:31], v[88:91], v[72:75], a[28:31]\n"
"s_cbranch_vccnz LSTUB22_%=\n"
"LRET22_%=:\n"
"ds_read_b64_tr_b16 v[112:113], v4 offset:49408\n"
"ds_read_b64_tr_b16 v[114:115], v4 offset:57600\n"
"v_mfma_f32_16x16x32_bf16 a[36:39], v[92:95], v[68:71], a[36:39]\n"
"ds_read_b64_tr_b16 v[116:117], v5 offset:49408\n"
"ds_read_b64_tr_b16 v[118:119], v5 offset:57600\n"
"v_mfma_f32_16x16x32_bf16 a[40:43], v[92:95], v[72:75], a[40:43]\n"
"v_exp_f32 v32, v32\n"
"v_exp_f32 v33, v33\n"
"v_mfma_f32_16x16x32_bf16 a[48:51], v[96:99], v[68:71], a[48:51]\n"
"ds_read_b64_tr_b16 v[120:121], v6 offset:49408\n"
"ds_read_b64_tr_b16 v[122:123], v6 offset:57600\n"
"v_mfma_f32_16x16x32_bf16 a[52:55], v[96:99], v[72:75], a[52:55]\n"
"v_exp_f32 v34, v34\n"
"v_exp_f32 v35, v35\n"
"v_mfma_f32_16x16x32_bf16 a[60:63], v[100:103], v[68:71], a[60:63]\n"
"ds_read_b64_tr_b16 v[124:125], v7 offset:49408\n"
"ds_read_b64_tr_b16 v[126:127], v7 offset:57600\n"
"v_mfma_f32_16x16x32_bf16 a[64:67], v[100:103], v[72:75], a[64:67]\n"
"buffer_load_dwordx4 v210, %4, s47 offen offset:2048 lds\n"
"v_cvt_pk_bf16_f32 v56, v32, v33\n"
"v_exp_f32 v36, v36\n"
"s_waitcnt lgkmcnt(3)\n"
"v_mfma_f32_16x16x32_bf16 a[72:75], v[104:107], v[68:71], a[72:75]\n"
"ds_read_b64_tr_b16 v[128:129], v8 offset:49408\n"
"ds_read_b64_tr_b16 v[130:131], v8 offset:57600\n"
"v_mfma_f32_16x16x32_bf16 a[76:79], v[104:107], v[72:75], a[76:79]\n"
"v_exp_f32 v37, v37\n"
"v_exp_f32 v38, v38\n"
"v_mfma_f32_16x16x32_bf16 a[84:87], v[108:111], v[68:71], a[84:87]\n"
"ds_read_b64_tr_b16 v[132:133], v9 offset:49408\n"
"ds_read_b64_tr_b16 v[134:135], v9 offset:57600\n"
"v_mfma_f32_16x16x32_bf16 a[88:91], v[108:111], v[72:75], a[88:91]\n"
"v_exp_f32 v39, v39\n"
"v_cvt_pk_bf16_f32 v57, v34, v35\n"
"v_add_f32 v205, v205, v32\n"
"v_mfma_f32_16x16x32_bf16 a[96:99], v[112:115], v[68:71], a[96:99]\n"
"ds_read_b64_tr_b16 v[136:137], v10 offset:49408\n"
"ds_read_b64_tr_b16 v[138:139], v10 offset:57600\n"
"v_mfma_f32_16x16x32_bf16 a[100:103], v[112:115], v[72:75], a[100:103]\n"
"v_add_f32 v205, v205, v33\n"
"v_add_f32 v205, v205, v34\n"
"buffer_load_dwordx4 v211, %4, s47 offen offset:3072 lds\n"
"v_add_f32 v205, v205, v35\n"
"v_mfma_f32_16x16x32_bf16 a[108:111], v[116:119], v[68:71], a[108:111]\n"
"ds_read_b64_tr_b16 v[140:141], v11 offset:49408\n"
"ds_read_b64_tr_b16 v[142:143], v11 offset:57600\n"
"v_mfma_f32_16x16x32_bf16 a[112:115], v[116:119], v[72:75], a[112:115]\n"
"v_cvt_pk_bf16_f32 v60, v36, v37\n"
"v_cvt_pk_bf16_f32 v61, v38, v39\n"
"v_add_f32 v206, v206, v36\n"
"v_add_f32 v206, v206, v37\n"
"s_waitcnt lgkmcnt(2)\n"
"v_mfma_f32_16x16x32_bf16 a[120:123], v[120:123], v[68:71], a[120:123]\n"
"v_add_f32 v206, v206, v38\n"
"v_add_f32 v206, v206, v39\n"
"v_mfma_f32_16x16x32_bf16 a[124:127], v[120:123], v[72:75], a[124:127]\n"
"v_mfma_f32_16x16x32_bf16 a[132:135], v[124:127], v[68:71], a[132:135]\n"
"v_mfma_f32_16x16x32_bf16 a[136:139], v[124:127], v[72:75], a[136:139]\n"
"v_mfma_f32_16x16x32_bf16 a[144:147], v[128:131], v[68:71], a[144:147]\n"
"v_mfma_f32_16x16x32_bf16 a[148:151], v[128:131], v[72:75], a[148:151]\n"
"s_waitcnt vmcnt(20) lgkmcnt(0)\n"
"s_barrier\n"
"v_mfma_f32_16x16x32_bf16 a[156:159], v[132:135], v[68:71], a[156:159]\n"
"ds_read_b128 v[80:83], v0 offset:49152\n"
"ds_read_b128 v[84:87], v1 offset:49152\n"
"v_mfma_f32_16x16x32_bf16 a[160:163], v[132:135], v[72:75], a[160:163]\n"
"ds_read_b128 v[88:91], v2 offset:49152\n"
"v_mfma_f32_16x16x32_bf16 a[168:171], v[136:139], v[68:71], a[168:171]\n"
"v_mfma_f32_16x16x32_bf16 a[172:175], v[136:139], v[72:75], a[172:175]\n"
"v_mfma_f32_16x16x32_bf16 a[180:183], v[140:143], v[68:71], a[180:183]\n"
"v_mfma_f32_16x16x32_bf16 a[184:187], v[140:143], v[72:75], a[184:187]\n"
"s_waitcnt lgkmcnt(2)\n"
"v_mfma_f32_16x16x32_bf16 v[32:35], v[80:83], v[144:147], v[20:23]\n"
"ds_read_b128 v[92:95], v3 offset:49152\n"
"ds_read_b128 v[96:99], v0 offset:49408\n"
"v_mfma_f32_16x16x32_bf16 v[36:39], v[80:83], v[160:163], v[24:27]\n"
"v_exp_f32 v44, v44\n"
"ds_read_b128 v[100:103], v1 offset:49408\n"
"v_add_f32 v205, v205, v44\n"
"s_waitcnt lgkmcnt(3)\n"
"v_mfma_f32_16x16x32_bf16 v[32:35], v[84:87], v[148:151], v[32:35]\n"
"s_mov_b32 s47, 256\n"
"s_add_u32 m0, s48, 98304\n"
"v_mfma_f32_16x16x32_bf16 v[36:39], v[84:87], v[164:167], v[36:39]\n"
"buffer_load_dwordx4 v208, %4, s47 offen offset:0 lds\n"
"ds_read_b128 v[104:107], v2 offset:49408\n"
"v_mfma_f32_16x16x32_bf16 v[32:35], v[88:91], v[152:155], v[32:35]\n"
"v_exp_f32 v45, v45\n"
"v_mfma_f32_16x16x32_bf16 v[36:39], v[88:91], v[168:171], v[36:39]\n"
"v_exp_f32 v46, v46\n"
"ds_read_b128 v[108:111], v3 offset:49408\n"
"v_cvt_pk_bf16_f32 v58, v44, v45\n"
"s_waitcnt lgkmcnt(2)\n"
"v_mfma_f32_16x16x32_bf16 v[32:35], v[92:95], v[156:159], v[32:35]\n"
"v_exp_f32 v47, v47\n"
"v_mfma_f32_16x16x32_bf16 v[36:39], v[92:95], v[172:175], v[36:39]\n"
"v_exp_f32 v48, v48\n"
"ds_read_b128 v[112:115], v0 offset:57344\n"
"v_cvt_pk_bf16_f32 v59, v46, v47\n"
"v_mfma_f32_16x16x32_bf16 v[32:35], v[96:99], a[204:207], v[32:35]\n"
"v_exp_f32 v49, v49\n"
"v_mfma_f32_16x16x32_bf16 v[36:39], v[96:99], a[220:223], v[36:39]\n"
"v_exp_f32 v50, v50\n"
"buffer_load_dwordx4 v209, %4, s47 offen offset:1024 lds\n"
"ds_read_b128 v[116:119], v1 offset:57344\n"
"v_mfma_f32_16x16x32_bf16 v[32:35], v[100:103], a[208:211], v[32:35]\n"
"v_exp_f32 v51, v51\n"
"v_mfma_f32_16x16x32_bf16 v[36:39], v[100:103], a[224:227], v[36:39]\n"
"v_add_f32 v205, v205, v45\n"
"v_add_f32 v205, v205, v46\n"
"ds_read_b128 v[120:123], v2 offset:57344\n"
"v_add_f32 v205, v205, v47\n"
"s_waitcnt lgkmcnt(2)\n"
"v_mfma_f32_16x16x32_bf16 v[32:35], v[104:107], a[212:215], v[32:35]\n"
"v_cvt_pk_bf16_f32 v62, v48, v49\n"
"v_cvt_pk_bf16_f32 v63, v50, v51\n"
"v_mfma_f32_16x16x32_bf16 v[36:39], v[104:107], a[228:231], v[36:39]\n"
"v_add_f32 v206, v206, v48\n"
"v_add_f32 v206, v206, v49\n"
"v_add_f32 v206, v206, v50\n"
"v_add_f32 v206, v206, v51\n"
"v_mfma_f32_16x16x32_bf16 v[32:35], v[108:111], a[216:219], v[32:35]\n"
"ds_read_b128 v[124:127], v3 offset:57344\n"
"v_mfma_f32_16x16x32_bf16 v[36:39], v[108:111], a[232:235], v[36:39]\n"
"ds_read_b128 v[128:131], v0 offset:57600\n"
"v_mfma_f32_16x16x32_bf16 v[44:47], v[112:115], v[144:147], v[20:23]\n"
"buffer_load_dwordx4 v210, %4, s47 offen offset:2048 lds\n"
"v_mfma_f32_16x16x32_bf16 v[48:51], v[112:115], v[160:163], v[24:27]\n"
"ds_read_b128 v[132:135], v1 offset:57600\n"
"s_waitcnt lgkmcnt(3)\n"
"v_mfma_f32_16x16x32_bf16 v[44:47], v[116:119], v[148:151], v[44:47]\n"
"v_max3_f32 v196, v32, v33, v34\n"
"v_max3_f32 v196, v196, v35, v36\n"
"v_mfma_f32_16x16x32_bf16 v[48:51], v[116:119], v[164:167], v[48:51]\n"
"v_max3_f32 v196, v196, v37, v38\n"
"v_max3_f32 v196, v196, v39, v39\n"
"ds_read_b128 v[136:139], v2 offset:57600\n"
"v_mfma_f32_16x16x32_bf16 v[44:47], v[120:123], v[152:155], v[44:47]\n"
"ds_read_b64_tr_b16 v[80:81], v4 offset:0\n"
"ds_read_b64_tr_b16 v[82:83], v4 offset:8192\n"
"v_mfma_f32_16x16x32_bf16 v[48:51], v[120:123], v[168:171], v[48:51]\n"
"ds_read_b128 v[140:143], v3 offset:57600\n"
"s_waitcnt lgkmcnt(4)\n"
"v_mfma_f32_16x16x32_bf16 v[44:47], v[124:127], v[156:159], v[44:47]\n"
"ds_read_b64_tr_b16 v[84:85], v5 offset:0\n"
"ds_read_b64_tr_b16 v[86:87], v5 offset:8192\n"
"v_mfma_f32_16x16x32_bf16 v[48:51], v[124:127], v[172:175], v[48:51]\n"
"buffer_load_dwordx4 v211, %4, s47 offen offset:3072 lds\n"
"v_mfma_f32_16x16x32_bf16 v[44:47], v[128:131], a[204:207], v[44:47]\n"
"ds_read_b64_tr_b16 v[88:89], v6 offset:0\n"
"ds_read_b64_tr_b16 v[90:91], v6 offset:8192\n"
"v_mfma_f32_16x16x32_bf16 v[48:51], v[128:131], a[220:223], v[48:51]\n"
"v_mfma_f32_16x16x32_bf16 v[44:47], v[132:135], a[208:211], v[44:47]\n"
"ds_read_b64_tr_b16 v[92:93], v7 offset:0\n"
"ds_read_b64_tr_b16 v[94:95], v7 offset:8192\n"
"v_mfma_f32_16x16x32_bf16 v[48:51], v[132:135], a[224:227], v[48:51]\n"
"s_waitcnt lgkmcnt(4)\n"
"v_mfma_f32_16x16x32_bf16 v[44:47], v[136:139], a[212:215], v[44:47]\n"
"ds_read_b64_tr_b16 v[96:97], v8 offset:0\n"
"ds_read_b64_tr_b16 v[98:99], v8 offset:8192\n"
"v_mfma_f32_16x16x32_bf16 v[48:51], v[136:139], a[228:231], v[48:51]\n"
"s_mov_b32 s47, 28928\n"
"s_add_u32 m0, s48, 102400\n"
"buffer_load_dwordx4 v208, %4, s47 offen offset:0 lds\n"
"v_mfma_f32_16x16x32_bf16 v[44:47], v[140:143], a[216:219], v[44:47]\n"
"ds_read_b64_tr_b16 v[100:101], v9 offset:0\n"
"ds_read_b64_tr_b16 v[102:103], v9 offset:8192\n"
"v_mfma_f32_16x16x32_bf16 v[48:51], v[140:143], a[232:235], v[48:51]\n"
"v_mfma_f32_16x16x32_bf16 a[0:3], v[80:83], v[56:59], a[0:3]\n"
"ds_read_b64_tr_b16 v[104:105], v10 offset:0\n"
"ds_read_b64_tr_b16 v[106:107], v10 offset:8192\n"
"v_mfma_f32_16x16x32_bf16 a[4:7], v[80:83], v[60:63], a[4:7]\n"
"v_mfma_f32_16x16x32_bf16 a[12:15], v[84:87], v[56:59], a[12:15]\n"
"ds_read_b64_tr_b16 v[108:109], v11 offset:0\n"
"ds_read_b64_tr_b16 v[110:111], v11 offset:8192\n"
"v_mfma_f32_16x16x32_bf16 a[16:19], v[84:87], v[60:63], a[16:19]\n"
"v_max3_f32 v196, v196, v44, v45\n"
"v_max3_f32 v196, v196, v46, v47\n"
"v_max3_f32 v196, v196, v48, v49\n"
"v_max3_f32 v196, v196, v50, v51\n"
"s_waitcnt lgkmcnt(4)\n"
"v_mfma_f32_16x16x32_bf16 a[24:27], v[88:91], v[56:59], a[24:27]\n"
"buffer_load_dwordx4 v209, %4, s47 offen offset:1024 lds\n"
"v_cmp_lt_f32 vcc, 0x41000000, v196\n"
"v_mfma_f32_16x16x32_bf16 a[28:31], v[88:91], v[60:63], a[28:31]\n"
"s_cbranch_vccnz LSTUB23_%=\n"
"LRET23_%=:\n"
"ds_read_b64_tr_b16 v[112:113], v4 offset:256\n"
"ds_read_b64_tr_b16 v[114:115], v4 offset:8448\n"
"v_mfma_f32_16x16x32_bf16 a[36:39], v[92:95], v[56:59], a[36:39]\n"
"ds_read_b64_tr_b16 v[116:117], v5 offset:256\n"
"ds_read_b64_tr_b16 v[118:119], v5 offset:8448\n"
"v_mfma_f32_16x16x32_bf16 a[40:43], v[92:95], v[60:63], a[40:43]\n"
"v_exp_f32 v32, v32\n"
"v_exp_f32 v33, v33\n"
"v_mfma_f32_16x16x32_bf16 a[48:51], v[96:99], v[56:59], a[48:51]\n"
"ds_read_b64_tr_b16 v[120:121], v6 offset:256\n"
"ds_read_b64_tr_b16 v[122:123], v6 offset:8448\n"
"v_mfma_f32_16x16x32_bf16 a[52:55], v[96:99], v[60:63], a[52:55]\n"
"v_exp_f32 v34, v34\n"
"v_exp_f32 v35, v35\n"
"v_mfma_f32_16x16x32_bf16 a[60:63], v[100:103], v[56:59], a[60:63]\n"
"ds_read_b64_tr_b16 v[124:125], v7 offset:256\n"
"ds_read_b64_tr_b16 v[126:127], v7 offset:8448\n"
"v_mfma_f32_16x16x32_bf16 a[64:67], v[100:103], v[60:63], a[64:67]\n"
"buffer_load_dwordx4 v210, %4, s47 offen offset:2048 lds\n"
"v_cvt_pk_bf16_f32 v68, v32, v33\n"
"v_exp_f32 v36, v36\n"
"s_waitcnt lgkmcnt(3)\n"
"v_mfma_f32_16x16x32_bf16 a[72:75], v[104:107], v[56:59], a[72:75]\n"
"ds_read_b64_tr_b16 v[128:129], v8 offset:256\n"
"ds_read_b64_tr_b16 v[130:131], v8 offset:8448\n"
"v_mfma_f32_16x16x32_bf16 a[76:79], v[104:107], v[60:63], a[76:79]\n"
"v_exp_f32 v37, v37\n"
"v_exp_f32 v38, v38\n"
"v_mfma_f32_16x16x32_bf16 a[84:87], v[108:111], v[56:59], a[84:87]\n"
"ds_read_b64_tr_b16 v[132:133], v9 offset:256\n"
"ds_read_b64_tr_b16 v[134:135], v9 offset:8448\n"
"v_mfma_f32_16x16x32_bf16 a[88:91], v[108:111], v[60:63], a[88:91]\n"
"v_exp_f32 v39, v39\n"
"v_cvt_pk_bf16_f32 v69, v34, v35\n"
"v_add_f32 v205, v205, v32\n"
"v_mfma_f32_16x16x32_bf16 a[96:99], v[112:115], v[56:59], a[96:99]\n"
"ds_read_b64_tr_b16 v[136:137], v10 offset:256\n"
"ds_read_b64_tr_b16 v[138:139], v10 offset:8448\n"
"v_mfma_f32_16x16x32_bf16 a[100:103], v[112:115], v[60:63], a[100:103]\n"
"v_add_f32 v205, v205, v33\n"
"v_add_f32 v205, v205, v34\n"
"buffer_load_dwordx4 v211, %4, s47 offen offset:3072 lds\n"
"v_add_f32 v205, v205, v35\n"
"v_mfma_f32_16x16x32_bf16 a[108:111], v[116:119], v[56:59], a[108:111]\n"
"ds_read_b64_tr_b16 v[140:141], v11 offset:256\n"
"ds_read_b64_tr_b16 v[142:143], v11 offset:8448\n"
"v_mfma_f32_16x16x32_bf16 a[112:115], v[116:119], v[60:63], a[112:115]\n"
"v_cvt_pk_bf16_f32 v72, v36, v37\n"
"v_cvt_pk_bf16_f32 v73, v38, v39\n"
"v_add_f32 v206, v206, v36\n"
"v_add_f32 v206, v206, v37\n"
"s_waitcnt lgkmcnt(2)\n"
"v_mfma_f32_16x16x32_bf16 a[120:123], v[120:123], v[56:59], a[120:123]\n"
"v_add_f32 v206, v206, v38\n"
"v_add_f32 v206, v206, v39\n"
"v_mfma_f32_16x16x32_bf16 a[124:127], v[120:123], v[60:63], a[124:127]\n"
"v_mfma_f32_16x16x32_bf16 a[132:135], v[124:127], v[56:59], a[132:135]\n"
"v_mfma_f32_16x16x32_bf16 a[136:139], v[124:127], v[60:63], a[136:139]\n"
"v_mfma_f32_16x16x32_bf16 a[144:147], v[128:131], v[56:59], a[144:147]\n"
"v_mfma_f32_16x16x32_bf16 a[148:151], v[128:131], v[60:63], a[148:151]\n"
"s_waitcnt vmcnt(24) lgkmcnt(0)\n"
"s_barrier\n"
"v_mfma_f32_16x16x32_bf16 a[156:159], v[132:135], v[56:59], a[156:159]\n"
"v_mfma_f32_16x16x32_bf16 a[160:163], v[132:135], v[60:63], a[160:163]\n"
"v_mfma_f32_16x16x32_bf16 a[168:171], v[136:139], v[56:59], a[168:171]\n"
"v_mfma_f32_16x16x32_bf16 a[172:175], v[136:139], v[60:63], a[172:175]\n"
"v_mfma_f32_16x16x32_bf16 a[180:183], v[140:143], v[56:59], a[180:183]\n"
"v_mfma_f32_16x16x32_bf16 a[184:187], v[140:143], v[60:63], a[184:187]\n"
"s_mov_b32 s47, 384\n"
"s_add_u32 m0, s48, 32768\n"
"buffer_load_dwordx4 v208, %4, s47 offen offset:0 lds\n"
"v_exp_f32 v44, v44\n"
"v_exp_f32 v45, v45\n"
"v_exp_f32 v46, v46\n"
"v_exp_f32 v47, v47\n"
"v_exp_f32 v48, v48\n"
"v_exp_f32 v49, v49\n"
"v_exp_f32 v50, v50\n"
"v_exp_f32 v51, v51\n"
"v_cvt_pk_bf16_f32 v70, v44, v45\n"
"v_cvt_pk_bf16_f32 v71, v46, v47\n"
"buffer_load_dwordx4 v209, %4, s47 offen offset:1024 lds\n"
"v_add_f32 v205, v205, v44\n"
"v_add_f32 v205, v205, v45\n"
"v_add_f32 v205, v205, v46\n"
"v_add_f32 v205, v205, v47\n"
"v_cvt_pk_bf16_f32 v74, v48, v49\n"
"v_cvt_pk_bf16_f32 v75, v50, v51\n"
"v_add_f32 v206, v206, v48\n"
"v_add_f32 v206, v206, v49\n"
"v_add_f32 v206, v206, v50\n"
"v_add_f32 v206, v206, v51\n"
"buffer_load_dwordx4 v210, %4, s47 offen offset:2048 lds\n"
"ds_read_b64_tr_b16 v[80:81], v4 offset:16384\n"
"ds_read_b64_tr_b16 v[82:83], v4 offset:24576\n"
"buffer_load_dwordx4 v211, %4, s47 offen offset:3072 lds\n"
"ds_read_b64_tr_b16 v[84:85], v5 offset:16384\n"
"ds_read_b64_tr_b16 v[86:87], v5 offset:24576\n"
"ds_read_b64_tr_b16 v[88:89], v6 offset:16384\n"
"ds_read_b64_tr_b16 v[90:91], v6 offset:24576\n"
"ds_read_b64_tr_b16 v[92:93], v7 offset:16384\n"
"ds_read_b64_tr_b16 v[94:95], v7 offset:24576\n"
"ds_read_b64_tr_b16 v[96:97], v8 offset:16384\n"
"ds_read_b64_tr_b16 v[98:99], v8 offset:24576\n"
"s_mov_b32 s47, 29056\n"
"s_add_u32 m0, s48, 36864\n"
"buffer_load_dwordx4 v208, %4, s47 offen offset:0 lds\n"
"ds_read_b64_tr_b16 v[100:101], v9 offset:16384\n"
"ds_read_b64_tr_b16 v[102:103], v9 offset:24576\n"
"s_waitcnt lgkmcnt(10)\n"
"v_mfma_f32_16x16x32_bf16 a[0:3], v[80:83], v[68:71], a[0:3]\n"
"ds_read_b64_tr_b16 v[104:105], v10 offset:16384\n"
"ds_read_b64_tr_b16 v[106:107], v10 offset:24576\n"
"v_mfma_f32_16x16x32_bf16 a[4:7], v[80:83], v[72:75], a[4:7]\n"
"s_waitcnt lgkmcnt(6)\n"
"v_mfma_f32_16x16x32_bf16 a[12:15], v[84:87], v[68:71], a[12:15]\n"
"ds_read_b64_tr_b16 v[108:109], v11 offset:16384\n"
"ds_read_b64_tr_b16 v[110:111], v11 offset:24576\n"
"v_mfma_f32_16x16x32_bf16 a[16:19], v[84:87], v[72:75], a[16:19]\n"
"v_mfma_f32_16x16x32_bf16 a[24:27], v[88:91], v[68:71], a[24:27]\n"
"ds_read_b64_tr_b16 v[112:113], v4 offset:16640\n"
"ds_read_b64_tr_b16 v[114:115], v4 offset:24832\n"
"v_mfma_f32_16x16x32_bf16 a[28:31], v[88:91], v[72:75], a[28:31]\n"
"buffer_load_dwordx4 v209, %4, s47 offen offset:1024 lds\n"
"v_mfma_f32_16x16x32_bf16 a[36:39], v[92:95], v[68:71], a[36:39]\n"
"ds_read_b64_tr_b16 v[116:117], v5 offset:16640\n"
"ds_read_b64_tr_b16 v[118:119], v5 offset:24832\n"
"v_mfma_f32_16x16x32_bf16 a[40:43], v[92:95], v[72:75], a[40:43]\n"
"s_waitcnt lgkmcnt(4)\n"
"v_mfma_f32_16x16x32_bf16 a[48:51], v[96:99], v[68:71], a[48:51]\n"
"ds_read_b64_tr_b16 v[120:121], v6 offset:16640\n"
"ds_read_b64_tr_b16 v[122:123], v6 offset:24832\n"
"v_mfma_f32_16x16x32_bf16 a[52:55], v[96:99], v[72:75], a[52:55]\n"
"v_mfma_f32_16x16x32_bf16 a[60:63], v[100:103], v[68:71], a[60:63]\n"
"ds_read_b64_tr_b16 v[124:125], v7 offset:16640\n"
"ds_read_b64_tr_b16 v[126:127], v7 offset:24832\n"
"v_mfma_f32_16x16x32_bf16 a[64:67], v[100:103], v[72:75], a[64:67]\n"
"v_mfma_f32_16x16x32_bf16 a[72:75], v[104:107], v[68:71], a[72:75]\n"
"ds_read_b64_tr_b16 v[128:129], v8 offset:16640\n"
"ds_read_b64_tr_b16 v[130:131], v8 offset:24832\n"
"v_mfma_f32_16x16x32_bf16 a[76:79], v[104:107], v[72:75], a[76:79]\n"
"buffer_load_dwordx4 v210, %4, s47 offen offset:2048 lds\n"
"v_mfma_f32_16x16x32_bf16 a[84:87], v[108:111], v[68:71], a[84:87]\n"
"ds_read_b64_tr_b16 v[132:133], v9 offset:16640\n"
"ds_read_b64_tr_b16 v[134:135], v9 offset:24832\n"
"v_mfma_f32_16x16x32_bf16 a[88:91], v[108:111], v[72:75], a[88:91]\n"
"s_waitcnt lgkmcnt(4)\n"
"v_mfma_f32_16x16x32_bf16 a[96:99], v[112:115], v[68:71], a[96:99]\n"
"ds_read_b64_tr_b16 v[136:137], v10 offset:16640\n"
"ds_read_b64_tr_b16 v[138:139], v10 offset:24832\n"
"v_mfma_f32_16x16x32_bf16 a[100:103], v[112:115], v[72:75], a[100:103]\n"
"v_mfma_f32_16x16x32_bf16 a[108:111], v[116:119], v[68:71], a[108:111]\n"
"ds_read_b64_tr_b16 v[140:141], v11 offset:16640\n"
"ds_read_b64_tr_b16 v[142:143], v11 offset:24832\n"
"v_mfma_f32_16x16x32_bf16 a[112:115], v[116:119], v[72:75], a[112:115]\n"
"v_mfma_f32_16x16x32_bf16 a[120:123], v[120:123], v[68:71], a[120:123]\n"
"buffer_load_dwordx4 v211, %4, s47 offen offset:3072 lds\n"
"v_mfma_f32_16x16x32_bf16 a[124:127], v[120:123], v[72:75], a[124:127]\n"
"v_mfma_f32_16x16x32_bf16 a[132:135], v[124:127], v[68:71], a[132:135]\n"
"v_mfma_f32_16x16x32_bf16 a[136:139], v[124:127], v[72:75], a[136:139]\n"
"s_waitcnt lgkmcnt(2)\n"
"v_mfma_f32_16x16x32_bf16 a[144:147], v[128:131], v[68:71], a[144:147]\n"
"v_mfma_f32_16x16x32_bf16 a[148:151], v[128:131], v[72:75], a[148:151]\n"
"v_mfma_f32_16x16x32_bf16 a[156:159], v[132:135], v[68:71], a[156:159]\n"
"v_mfma_f32_16x16x32_bf16 a[160:163], v[132:135], v[72:75], a[160:163]\n"
"v_mfma_f32_16x16x32_bf16 a[168:171], v[136:139], v[68:71], a[168:171]\n"
"v_mfma_f32_16x16x32_bf16 a[172:175], v[136:139], v[72:75], a[172:175]\n"
"s_waitcnt lgkmcnt(0)\n"
"v_mfma_f32_16x16x32_bf16 a[180:183], v[140:143], v[68:71], a[180:183]\n"
"v_mfma_f32_16x16x32_bf16 a[184:187], v[140:143], v[72:75], a[184:187]\n"
"s_waitcnt lgkmcnt(0)\n"
"s_barrier\n"
"s_mov_b32 s47, 512\n"
"s_add_u32 m0, s48, 65536\n"
"s_nop 0\n"
"buffer_load_dwordx4 v208, %4, s47 offen offset:0 lds\n"
"buffer_load_dwordx4 v209, %4, s47 offen offset:1024 lds\n"
"buffer_load_dwordx4 v210, %4, s47 offen offset:2048 lds\n"
"buffer_load_dwordx4 v211, %4, s47 offen offset:3072 lds\n"
"s_mov_b32 s47, 29184\n"
"s_add_u32 m0, s48, 69632\n"
"s_nop 0\n"
"buffer_load_dwordx4 v208, %4, s47 offen offset:0 lds\n"
"buffer_load_dwordx4 v209, %4, s47 offen offset:1024 lds\n"
"buffer_load_dwordx4 v210, %4, s47 offen offset:2048 lds\n"
"buffer_load_dwordx4 v211, %4, s47 offen offset:3072 lds\n"
"v_mov_b32 v4, v208\n"
"v_mov_b32 v11, v209\n"
"v_mov_b32 v30, v210\n"
"v_mov_b32 v31, v211\n"
"v_mbcnt_lo_u32_b32 v20, -1, 0\n"
"v_mbcnt_hi_u32_b32 v20, -1, v20\n"
"v_and_b32 v21, 15, v20\n"
"v_lshrrev_b32 v22, 4, v20\n"
"v_bfe_u32 v23, v20, 2, 2\n"
"v_and_b32 v24, 3, v20\n"
"v_lshrrev_b32 v25, 5, v20\n"
"v_and_b32 v26, 31, v20\n"
"v_lshl_add_u32 v29, v22, 2, v23\n"
"v_bfe_u32 v28, v29, 1, 2\n"
"v_lshlrev_b32 v29, 7, v29\n"
"v_lshl_add_u32 v29, v24, 3, v29\n"
"v_add_u32 v29, %7, v29\n"
"v_add_u32 v29, 131072, v29\n"
"v_xor_b32 v27, 0, v28\n"
"v_lshl_add_u32 v0, v27, 5, v29\n"
"v_xor_b32 v27, 1, v28\n"
"v_lshl_add_u32 v1, v27, 5, v29\n"
"v_xor_b32 v27, 2, v28\n"
"v_lshl_add_u32 v2, v27, 5, v29\n"
"v_xor_b32 v27, 3, v28\n"
"v_lshl_add_u32 v3, v27, 5, v29\n"
"s_mul_i32 s46, %6, 43008\n"
"v_lshlrev_b32 v27, 4, v20\n"
"v_add_u32 v27, s46, v27\n"
"v_add_u32 v5, 0, v27\n"
"v_add_u32 v6, 14336, v27\n"
"v_add_u32 v7, 28672, v27\n"
"s_nop 4\n"
"v_xor_b32 v27, 16, v20\n"
"v_lshlrev_b32 v27, 2, v27\n"
"v_xor_b32 v28, 32, v20\n"
"v_lshlrev_b32 v28, 2, v28\n"
"v_mov_b32 v8, v205\n"
"v_mov_b32 v9, v206\n"
"v_mov_b32 v10, v207\n"
"ds_bpermute_b32 v12, v27, v8\n"
"ds_bpermute_b32 v13, v27, v9\n"
"ds_bpermute_b32 v14, v27, v10\n"
"s_waitcnt lgkmcnt(0)\n"
"v_add_f32 v8, v8, v12\n"
"v_add_f32 v9, v9, v13\n"
"v_add_f32 v10, v10, v14\n"
"ds_bpermute_b32 v12, v28, v8\n"
"ds_bpermute_b32 v13, v28, v9\n"
"ds_bpermute_b32 v14, v28, v10\n"
"s_waitcnt lgkmcnt(0)\n"
"v_add_f32 v8, v8, v12\n"
"v_add_f32 v9, v9, v13\n"
"v_add_f32 v10, v10, v14\n"
"s_nop 0\n"
"v_rcp_f32 v8, v8\n"
"v_rcp_f32 v9, v9\n"
"v_rcp_f32 v10, v10\n"
"v_accvgpr_read_b32 v12, a0\n"
"v_accvgpr_read_b32 v13, a1\n"
"v_accvgpr_read_b32 v14, a2\n"
"v_accvgpr_read_b32 v15, a3\n"
"v_accvgpr_read_b32 v16, a12\n"
"v_accvgpr_read_b32 v17, a13\n"
"v_accvgpr_read_b32 v18, a14\n"
"v_accvgpr_read_b32 v19, a15\n"
"v_cvt_pk_bf16_f32 v128, v12, v13\n"
"v_cvt_pk_bf16_f32 v129, v14, v15\n"
"v_cvt_pk_bf16_f32 v130, v16, v17\n"
"v_cvt_pk_bf16_f32 v131, v18, v19\n"
"v_accvgpr_read_b32 v20, a4\n"
"v_accvgpr_read_b32 v21, a5\n"
"v_accvgpr_read_b32 v22, a6\n"
"v_accvgpr_read_b32 v23, a7\n"
"v_accvgpr_read_b32 v24, a16\n"
"v_accvgpr_read_b32 v25, a17\n"
"v_accvgpr_read_b32 v26, a18\n"
"v_accvgpr_read_b32 v27, a19\n"
"v_cvt_pk_bf16_f32 v132, v20, v21\n"
"v_cvt_pk_bf16_f32 v133, v22, v23\n"
"v_cvt_pk_bf16_f32 v134, v24, v25\n"
"v_cvt_pk_bf16_f32 v135, v26, v27\n"
"s_waitcnt vmcnt(32)\n"
"s_barrier\n"
"s_waitcnt lgkmcnt(0)\n"
"ds_read_b64_tr_b16 v[224:225], v0 offset:0\n"
"ds_read_b64_tr_b16 v[226:227], v0 offset:2048\n"
"ds_read_b64_tr_b16 v[228:229], v1 offset:0\n"
"ds_read_b64_tr_b16 v[230:231], v1 offset:2048\n"
"ds_read_b64_tr_b16 v[232:233], v2 offset:0\n"
"ds_read_b64_tr_b16 v[234:235], v2 offset:2048\n"
"ds_read_b64_tr_b16 v[236:237], v3 offset:0\n"
"ds_read_b64_tr_b16 v[238:239], v3 offset:2048\n"
"s_waitcnt lgkmcnt(6)\n"
"v_mfma_f32_16x16x32_bf16 v[32:35], v[224:227], v[128:131], 0\n"
"ds_read_b64_tr_b16 v[240:241], v0 offset:4096\n"
"ds_read_b64_tr_b16 v[242:243], v0 offset:6144\n"
"v_accvgpr_read_b32 v20, a24\n"
"v_accvgpr_read_b32 v21, a25\n"
"v_mfma_f32_16x16x32_bf16 v[36:39], v[224:227], v[132:135], 0\n"
"ds_read_b64_tr_b16 v[244:245], v1 offset:4096\n"
"ds_read_b64_tr_b16 v[246:247], v1 offset:6144\n"
"v_accvgpr_read_b32 v22, a26\n"
"v_accvgpr_read_b32 v23, a27\n"
"v_accvgpr_read_b32 v24, a36\n"
"v_accvgpr_read_b32 v25, a37\n"
"v_accvgpr_read_b32 v26, a38\n"
"s_waitcnt lgkmcnt(4)\n"
"v_mfma_f32_16x16x32_bf16 v[44:47], v[228:231], v[128:131], 0\n"
"v_accvgpr_read_b32 v27, a39\n"
"v_cvt_pk_bf16_f32 v140, v20, v21\n"
"v_mfma_f32_16x16x32_bf16 v[48:51], v[228:231], v[132:135], 0\n"
"v_cvt_pk_bf16_f32 v141, v22, v23\n"
"v_cvt_pk_bf16_f32 v142, v24, v25\n"
"ds_read_b64_tr_b16 v[248:249], v2 offset:4096\n"
"ds_read_b64_tr_b16 v[250:251], v2 offset:6144\n"
"v_cvt_pk_bf16_f32 v143, v26, v27\n"
"v_accvgpr_read_b32 v12, a28\n"
"v_accvgpr_read_b32 v13, a29\n"
"v_mfma_f32_16x16x32_bf16 v[56:59], v[232:235], v[128:131], 0\n"
"v_accvgpr_read_b32 v14, a30\n"
"v_accvgpr_read_b32 v15, a31\n"
"v_mfma_f32_16x16x32_bf16 v[60:63], v[232:235], v[132:135], 0\n"
"v_accvgpr_read_b32 v16, a40\n"
"v_accvgpr_read_b32 v17, a41\n"
"ds_read_b64_tr_b16 v[252:253], v3 offset:4096\n"
"ds_read_b64_tr_b16 v[254:255], v3 offset:6144\n"
"v_accvgpr_read_b32 v18, a42\n"
"v_accvgpr_read_b32 v19, a43\n"
"v_mfma_f32_16x16x32_bf16 v[68:71], v[236:239], v[128:131], 0\n"
"v_cvt_pk_bf16_f32 v144, v12, v13\n"
"v_cvt_pk_bf16_f32 v145, v14, v15\n"
"v_mfma_f32_16x16x32_bf16 v[72:75], v[236:239], v[132:135], 0\n"
"v_cvt_pk_bf16_f32 v146, v16, v17\n"
"v_cvt_pk_bf16_f32 v147, v18, v19\n"
"ds_read_b64_tr_b16 v[224:225], v0 offset:8192\n"
"ds_read_b64_tr_b16 v[226:227], v0 offset:10240\n"
"s_waitcnt lgkmcnt(4)\n"
"v_mfma_f32_16x16x32_bf16 v[32:35], v[240:243], v[140:143], v[32:35]\n"
"v_accvgpr_read_b32 v12, a48\n"
"v_accvgpr_read_b32 v13, a49\n"
"v_accvgpr_read_b32 v14, a50\n"
"v_mfma_f32_16x16x32_bf16 v[36:39], v[240:243], v[144:147], v[36:39]\n"
"ds_read_b64_tr_b16 v[228:229], v1 offset:8192\n"
"ds_read_b64_tr_b16 v[230:231], v1 offset:10240\n"
"v_accvgpr_read_b32 v15, a51\n"
"v_accvgpr_read_b32 v16, a60\n"
"v_accvgpr_read_b32 v17, a61\n"
"v_accvgpr_read_b32 v18, a62\n"
"v_mfma_f32_16x16x32_bf16 v[44:47], v[244:247], v[140:143], v[44:47]\n"
"v_accvgpr_read_b32 v19, a63\n"
"v_cvt_pk_bf16_f32 v152, v12, v13\n"
"v_mfma_f32_16x16x32_bf16 v[48:51], v[244:247], v[144:147], v[48:51]\n"
"v_cvt_pk_bf16_f32 v153, v14, v15\n"
"v_cvt_pk_bf16_f32 v154, v16, v17\n"
"ds_read_b64_tr_b16 v[232:233], v2 offset:8192\n"
"ds_read_b64_tr_b16 v[234:235], v2 offset:10240\n"
"v_cvt_pk_bf16_f32 v155, v18, v19\n"
"v_accvgpr_read_b32 v20, a52\n"
"v_accvgpr_read_b32 v21, a53\n"
"v_mfma_f32_16x16x32_bf16 v[56:59], v[248:251], v[140:143], v[56:59]\n"
"v_accvgpr_read_b32 v22, a54\n"
"v_accvgpr_read_b32 v23, a55\n"
"v_mfma_f32_16x16x32_bf16 v[60:63], v[248:251], v[144:147], v[60:63]\n"
"v_accvgpr_read_b32 v24, a64\n"
"v_accvgpr_read_b32 v25, a65\n"
"ds_read_b64_tr_b16 v[236:237], v3 offset:8192\n"
"ds_read_b64_tr_b16 v[238:239], v3 offset:10240\n"
"v_accvgpr_read_b32 v26, a66\n"
"v_accvgpr_read_b32 v27, a67\n"
"s_waitcnt lgkmcnt(2)\n"
"v_mfma_f32_16x16x32_bf16 v[68:71], v[252:255], v[140:143], v[68:71]\n"
"v_cvt_pk_bf16_f32 v156, v20, v21\n"
"v_cvt_pk_bf16_f32 v157, v22, v23\n"
"v_mfma_f32_16x16x32_bf16 v[72:75], v[252:255], v[144:147], v[72:75]\n"
"v_cvt_pk_bf16_f32 v158, v24, v25\n"
"v_cvt_pk_bf16_f32 v159, v26, v27\n"
"ds_read_b64_tr_b16 v[240:241], v0 offset:12288\n"
"ds_read_b64_tr_b16 v[242:243], v0 offset:14336\n"
"v_mfma_f32_16x16x32_bf16 v[32:35], v[224:227], v[152:155], v[32:35]\n"
"v_accvgpr_read_b32 v20, a72\n"
"v_accvgpr_read_b32 v21, a73\n"
"v_accvgpr_read_b32 v22, a74\n"
"v_mfma_f32_16x16x32_bf16 v[36:39], v[224:227], v[156:159], v[36:39]\n"
"ds_read_b64_tr_b16 v[244:245], v1 offset:12288\n"
"ds_read_b64_tr_b16 v[246:247], v1 offset:14336\n"
"v_accvgpr_read_b32 v23, a75\n"
"v_accvgpr_read_b32 v24, a84\n"
"v_accvgpr_read_b32 v25, a85\n"
"v_accvgpr_read_b32 v26, a86\n"
"v_mfma_f32_16x16x32_bf16 v[44:47], v[228:231], v[152:155], v[44:47]\n"
"v_accvgpr_read_b32 v27, a87\n"
"v_cvt_pk_bf16_f32 v164, v20, v21\n"
"v_mfma_f32_16x16x32_bf16 v[48:51], v[228:231], v[156:159], v[48:51]\n"
"v_cvt_pk_bf16_f32 v165, v22, v23\n"
"v_cvt_pk_bf16_f32 v166, v24, v25\n"
"ds_read_b64_tr_b16 v[248:249], v2 offset:12288\n"
"ds_read_b64_tr_b16 v[250:251], v2 offset:14336\n"
"v_cvt_pk_bf16_f32 v167, v26, v27\n"
"v_accvgpr_read_b32 v12, a76\n"
"v_accvgpr_read_b32 v13, a77\n"
"v_mfma_f32_16x16x32_bf16 v[56:59], v[232:235], v[152:155], v[56:59]\n"
"v_accvgpr_read_b32 v14, a78\n"
"v_accvgpr_read_b32 v15, a79\n"
"v_mfma_f32_16x16x32_bf16 v[60:63], v[232:235], v[156:159], v[60:63]\n"
"v_accvgpr_read_b32 v16, a88\n"
"v_accvgpr_read_b32 v17, a89\n"
"ds_read_b64_tr_b16 v[252:253], v3 offset:12288\n"
"ds_read_b64_tr_b16 v[254:255], v3 offset:14336\n"
"v_accvgpr_read_b32 v18, a90\n"
"v_accvgpr_read_b32 v19, a91\n"
"s_waitcnt lgkmcnt(2)\n"
"v_mfma_f32_16x16x32_bf16 v[68:71], v[236:239], v[152:155], v[68:71]\n"
"v_cvt_pk_bf16_f32 v168, v12, v13\n"
"v_cvt_pk_bf16_f32 v169, v14, v15\n"
"v_mfma_f32_16x16x32_bf16 v[72:75], v[236:239], v[156:159], v[72:75]\n"
"v_cvt_pk_bf16_f32 v170, v16, v17\n"
"v_cvt_pk_bf16_f32 v171, v18, v19\n"
"ds_read_b64_tr_b16 v[224:225], v0 offset:16384\n"
"ds_read_b64_tr_b16 v[226:227], v0 offset:18432\n"
"v_mfma_f32_16x16x32_bf16 v[32:35], v[240:243], v[164:167], v[32:35]\n"
"v_accvgpr_read_b32 v12, a96\n"
"v_accvgpr_read_b32 v13, a97\n"
"v_accvgpr_read_b32 v14, a98\n"
"v_mfma_f32_16x16x32_bf16 v[36:39], v[240:243], v[168:171], v[36:39]\n"
"ds_read_b64_tr_b16 v[228:229], v1 offset:16384\n"
"ds_read_b64_tr_b16 v[230:231], v1 offset:18432\n"
"v_accvgpr_read_b32 v15, a99\n"
"v_accvgpr_read_b32 v16, a108\n"
"v_accvgpr_read_b32 v17, a109\n"
"v_accvgpr_read_b32 v18, a110\n"
"v_mfma_f32_16x16x32_bf16 v[44:47], v[244:247], v[164:167], v[44:47]\n"
"v_accvgpr_read_b32 v19, a111\n"
"v_cvt_pk_bf16_f32 v176, v12, v13\n"
"v_mfma_f32_16x16x32_bf16 v[48:51], v[244:247], v[168:171], v[48:51]\n"
"v_cvt_pk_bf16_f32 v177, v14, v15\n"
"v_cvt_pk_bf16_f32 v178, v16, v17\n"
"ds_read_b64_tr_b16 v[232:233], v2 offset:16384\n"
"ds_read_b64_tr_b16 v[234:235], v2 offset:18432\n"
"v_cvt_pk_bf16_f32 v179, v18, v19\n"
"v_accvgpr_read_b32 v20, a100\n"
"v_accvgpr_read_b32 v21, a101\n"
"v_mfma_f32_16x16x32_bf16 v[56:59], v[248:251], v[164:167], v[56:59]\n"
"v_accvgpr_read_b32 v22, a102\n"
"v_accvgpr_read_b32 v23, a103\n"
"v_mfma_f32_16x16x32_bf16 v[60:63], v[248:251], v[168:171], v[60:63]\n"
"v_accvgpr_read_b32 v24, a112\n"
"v_accvgpr_read_b32 v25, a113\n"
"ds_read_b64_tr_b16 v[236:237], v3 offset:16384\n"
"ds_read_b64_tr_b16 v[238:239], v3 offset:18432\n"
"v_accvgpr_read_b32 v26, a114\n"
"v_accvgpr_read_b32 v27, a115\n"
"s_waitcnt lgkmcnt(2)\n"
"v_mfma_f32_16x16x32_bf16 v[68:71], v[252:255], v[164:167], v[68:71]\n"
"v_cvt_pk_bf16_f32 v180, v20, v21\n"
"v_cvt_pk_bf16_f32 v181, v22, v23\n"
"v_mfma_f32_16x16x32_bf16 v[72:75], v[252:255], v[168:171], v[72:75]\n"
"v_cvt_pk_bf16_f32 v182, v24, v25\n"
"v_cvt_pk_bf16_f32 v183, v26, v27\n"
"ds_read_b64_tr_b16 v[240:241], v0 offset:20480\n"
"ds_read_b64_tr_b16 v[242:243], v0 offset:22528\n"
"v_mfma_f32_16x16x32_bf16 v[32:35], v[224:227], v[176:179], v[32:35]\n"
"v_accvgpr_read_b32 v20, a120\n"
"v_accvgpr_read_b32 v21, a121\n"
"v_accvgpr_read_b32 v22, a122\n"
"v_mfma_f32_16x16x32_bf16 v[36:39], v[224:227], v[180:183], v[36:39]\n"
"ds_read_b64_tr_b16 v[244:245], v1 offset:20480\n"
"ds_read_b64_tr_b16 v[246:247], v1 offset:22528\n"
"v_accvgpr_read_b32 v23, a123\n"
"v_accvgpr_read_b32 v24, a132\n"
"v_accvgpr_read_b32 v25, a133\n"
"v_accvgpr_read_b32 v26, a134\n"
"v_mfma_f32_16x16x32_bf16 v[44:47], v[228:231], v[176:179], v[44:47]\n"
"v_accvgpr_read_b32 v27, a135\n"
"v_cvt_pk_bf16_f32 v188, v20, v21\n"
"v_mfma_f32_16x16x32_bf16 v[48:51], v[228:231], v[180:183], v[48:51]\n"
"v_cvt_pk_bf16_f32 v189, v22, v23\n"
"v_cvt_pk_bf16_f32 v190, v24, v25\n"
"ds_read_b64_tr_b16 v[248:249], v2 offset:20480\n"
"ds_read_b64_tr_b16 v[250:251], v2 offset:22528\n"
"v_cvt_pk_bf16_f32 v191, v26, v27\n"
"v_accvgpr_read_b32 v12, a124\n"
"v_accvgpr_read_b32 v13, a125\n"
"v_mfma_f32_16x16x32_bf16 v[56:59], v[232:235], v[176:179], v[56:59]\n"
"v_accvgpr_read_b32 v14, a126\n"
"v_accvgpr_read_b32 v15, a127\n"
"v_mfma_f32_16x16x32_bf16 v[60:63], v[232:235], v[180:183], v[60:63]\n"
"v_accvgpr_read_b32 v16, a136\n"
"v_accvgpr_read_b32 v17, a137\n"
"ds_read_b64_tr_b16 v[252:253], v3 offset:20480\n"
"ds_read_b64_tr_b16 v[254:255], v3 offset:22528\n"
"v_accvgpr_read_b32 v18, a138\n"
"v_accvgpr_read_b32 v19, a139\n"
"s_waitcnt lgkmcnt(2)\n"
"v_mfma_f32_16x16x32_bf16 v[68:71], v[236:239], v[176:179], v[68:71]\n"
"v_cvt_pk_bf16_f32 v192, v12, v13\n"
"v_cvt_pk_bf16_f32 v193, v14, v15\n"
"v_mfma_f32_16x16x32_bf16 v[72:75], v[236:239], v[180:183], v[72:75]\n"
"v_cvt_pk_bf16_f32 v194, v16, v17\n"
"v_cvt_pk_bf16_f32 v195, v18, v19\n"
"ds_read_b64_tr_b16 v[224:225], v0 offset:24576\n"
"ds_read_b64_tr_b16 v[226:227], v0 offset:26624\n"
"v_mfma_f32_16x16x32_bf16 v[32:35], v[240:243], v[188:191], v[32:35]\n"
"v_accvgpr_read_b32 v12, a144\n"
"v_accvgpr_read_b32 v13, a145\n"
"v_accvgpr_read_b32 v14, a146\n"
"v_mfma_f32_16x16x32_bf16 v[36:39], v[240:243], v[192:195], v[36:39]\n"
"ds_read_b64_tr_b16 v[228:229], v1 offset:24576\n"
"ds_read_b64_tr_b16 v[230:231], v1 offset:26624\n"
"v_accvgpr_read_b32 v15, a147\n"
"v_accvgpr_read_b32 v16, a156\n"
"v_accvgpr_read_b32 v17, a157\n"
"v_accvgpr_read_b32 v18, a158\n"
"v_mfma_f32_16x16x32_bf16 v[44:47], v[244:247], v[188:191], v[44:47]\n"
"v_accvgpr_read_b32 v19, a159\n"
"v_cvt_pk_bf16_f32 v200, v12, v13\n"
"v_mfma_f32_16x16x32_bf16 v[48:51], v[244:247], v[192:195], v[48:51]\n"
"v_cvt_pk_bf16_f32 v201, v14, v15\n"
"v_cvt_pk_bf16_f32 v202, v16, v17\n"
"ds_read_b64_tr_b16 v[232:233], v2 offset:24576\n"
"ds_read_b64_tr_b16 v[234:235], v2 offset:26624\n"
"v_cvt_pk_bf16_f32 v203, v18, v19\n"
"v_accvgpr_read_b32 v20, a148\n"
"v_accvgpr_read_b32 v21, a149\n"
"v_mfma_f32_16x16x32_bf16 v[56:59], v[248:251], v[188:191], v[56:59]\n"
"v_accvgpr_read_b32 v22, a150\n"
"v_accvgpr_read_b32 v23, a151\n"
"v_mfma_f32_16x16x32_bf16 v[60:63], v[248:251], v[192:195], v[60:63]\n"
"v_accvgpr_read_b32 v24, a160\n"
"v_accvgpr_read_b32 v25, a161\n"
"ds_read_b64_tr_b16 v[236:237], v3 offset:24576\n"
"ds_read_b64_tr_b16 v[238:239], v3 offset:26624\n"
"v_accvgpr_read_b32 v26, a162\n"
"v_accvgpr_read_b32 v27, a163\n"
"s_waitcnt lgkmcnt(2)\n"
"v_mfma_f32_16x16x32_bf16 v[68:71], v[252:255], v[188:191], v[68:71]\n"
"v_cvt_pk_bf16_f32 v204, v20, v21\n"
"v_cvt_pk_bf16_f32 v205, v22, v23\n"
"v_mfma_f32_16x16x32_bf16 v[72:75], v[252:255], v[192:195], v[72:75]\n"
"v_cvt_pk_bf16_f32 v206, v24, v25\n"
"v_cvt_pk_bf16_f32 v207, v26, v27\n"
"ds_read_b64_tr_b16 v[240:241], v0 offset:28672\n"
"ds_read_b64_tr_b16 v[242:243], v0 offset:30720\n"
"v_mfma_f32_16x16x32_bf16 v[32:35], v[224:227], v[200:203], v[32:35]\n"
"v_accvgpr_read_b32 v20, a168\n"
"v_accvgpr_read_b32 v21, a169\n"
"v_accvgpr_read_b32 v22, a170\n"
"v_mfma_f32_16x16x32_bf16 v[36:39], v[224:227], v[204:207], v[36:39]\n"
"ds_read_b64_tr_b16 v[244:245], v1 offset:28672\n"
"ds_read_b64_tr_b16 v[246:247], v1 offset:30720\n"
"v_accvgpr_read_b32 v23, a171\n"
"v_accvgpr_read_b32 v24, a180\n"
"v_accvgpr_read_b32 v25, a181\n"
"v_accvgpr_read_b32 v26, a182\n"
"v_mfma_f32_16x16x32_bf16 v[44:47], v[228:231], v[200:203], v[44:47]\n"
"v_accvgpr_read_b32 v27, a183\n"
"v_cvt_pk_bf16_f32 v212, v20, v21\n"
"v_mfma_f32_16x16x32_bf16 v[48:51], v[228:231], v[204:207], v[48:51]\n"
"ds_read_b64_tr_b16 v[248:249], v2 offset:28672\n"
"ds_read_b64_tr_b16 v[250:251], v2 offset:30720\n"
"v_cvt_pk_bf16_f32 v213, v22, v23\n"
"v_cvt_pk_bf16_f32 v214, v24, v25\n"
"ds_read_b64_tr_b16 v[252:253], v3 offset:28672\n"
"ds_read_b64_tr_b16 v[254:255], v3 offset:30720\n"
"v_cvt_pk_bf16_f32 v215, v26, v27\n"
"v_accvgpr_read_b32 v12, a172\n"
"v_mfma_f32_16x16x32_bf16 v[56:59], v[232:235], v[200:203], v[56:59]\n"
"v_accvgpr_read_b32 v13, a173\n"
"v_accvgpr_read_b32 v14, a174\n"
"v_accvgpr_read_b32 v15, a175\n"
"v_mfma_f32_16x16x32_bf16 v[60:63], v[232:235], v[204:207], v[60:63]\n"
"v_accvgpr_read_b32 v16, a184\n"
"v_accvgpr_read_b32 v17, a185\n"
"v_accvgpr_read_b32 v18, a186\n"
"v_accvgpr_read_b32 v19, a187\n"
"s_waitcnt lgkmcnt(2)\n"
"v_mfma_f32_16x16x32_bf16 v[68:71], v[236:239], v[200:203], v[68:71]\n"
"v_cvt_pk_bf16_f32 v216, v12, v13\n"
"v_cvt_pk_bf16_f32 v217, v14, v15\n"
"v_mfma_f32_16x16x32_bf16 v[72:75], v[236:239], v[204:207], v[72:75]\n"
"v_cvt_pk_bf16_f32 v218, v16, v17\n"
"v_cvt_pk_bf16_f32 v219, v18, v19\n"
"v_mfma_f32_16x16x32_bf16 v[32:35], v[240:243], v[212:215], v[32:35]\n"
"s_nop 0\n"
"v_mfma_f32_16x16x32_bf16 v[36:39], v[240:243], v[216:219], v[36:39]\n"
"s_waitcnt vmcnt(24) lgkmcnt(0)\n"
"s_barrier\n"
"v_subrev_u32 v0, 131072, v0\n"
"v_subrev_u32 v1, 131072, v1\n"
"v_mfma_f32_16x16x32_bf16 v[44:47], v[244:247], v[212:215], v[44:47]\n"
"v_subrev_u32 v2, 131072, v2\n"
"v_subrev_u32 v3, 131072, v3\n"
"ds_read_b64_tr_b16 v[224:225], v0 offset:0\n"
"ds_read_b64_tr_b16 v[226:227], v0 offset:2048\n"
"v_mfma_f32_16x16x32_bf16 v[48:51], v[244:247], v[216:219], v[48:51]\n"
"ds_read_b64_tr_b16 v[228:229], v1 offset:0\n"
"ds_read_b64_tr_b16 v[230:231], v1 offset:2048\n"
"ds_read_b64_tr_b16 v[232:233], v2 offset:0\n"
"ds_read_b64_tr_b16 v[234:235], v2 offset:2048\n"
"ds_read_b64_tr_b16 v[236:237], v3 offset:0\n"
"ds_read_b64_tr_b16 v[238:239], v3 offset:2048\n"
"v_mfma_f32_16x16x32_bf16 v[56:59], v[248:251], v[212:215], v[56:59]\n"
"v_mfma_f32_16x16x32_bf16 v[60:63], v[248:251], v[216:219], v[60:63]\n"
"v_mfma_f32_16x16x32_bf16 v[68:71], v[252:255], v[212:215], v[68:71]\n"
"v_mfma_f32_16x16x32_bf16 v[72:75], v[252:255], v[216:219], v[72:75]\n"
"s_waitcnt lgkmcnt(6)\n"
"v_mfma_f32_16x16x32_bf16 v[80:83], v[224:227], v[128:131], 0\n"
"ds_read_b64_tr_b16 v[240:241], v0 offset:4096\n"
"ds_read_b64_tr_b16 v[242:243], v0 offset:6144\n"
"v_mfma_f32_16x16x32_bf16 v[84:87], v[224:227], v[132:135], 0\n"
"s_mov_b32 s47, 640\n"
"s_add_u32 m0, s48, 131072\n"
"buffer_load_dwordx4 v4, %4, s47 offen offset:0 lds\n"
"ds_read_b64_tr_b16 v[244:245], v1 offset:4096\n"
"s_waitcnt lgkmcnt(3)\n"
"v_mfma_f32_16x16x32_bf16 v[92:95], v[228:231], v[128:131], 0\n"
"ds_read_b64_tr_b16 v[246:247], v1 offset:6144\n"
"v_mfma_f32_16x16x32_bf16 v[96:99], v[228:231], v[132:135], 0\n"
"ds_read_b64_tr_b16 v[248:249], v2 offset:4096\n"
"ds_read_b64_tr_b16 v[250:251], v2 offset:6144\n"
"v_mfma_f32_16x16x32_bf16 v[104:107], v[232:235], v[128:131], 0\n"
"s_mov_b32 s46, 0\n"
"v_mfma_f32_16x16x32_bf16 v[108:111], v[232:235], v[132:135], 0\n"
"ds_read_b64_tr_b16 v[252:253], v3 offset:4096\n"
"ds_read_b64_tr_b16 v[254:255], v3 offset:6144\n"
"v_mul_f32 v32, v32, v8\n"
"v_mul_f32 v33, v33, v8\n"
"v_mfma_f32_16x16x32_bf16 v[116:119], v[236:239], v[128:131], 0\n"
"v_mul_f32 v34, v34, v8\n"
"v_mfma_f32_16x16x32_bf16 v[120:123], v[236:239], v[132:135], 0\n"
"ds_read_b64_tr_b16 v[224:225], v0 offset:8192\n"
"ds_read_b64_tr_b16 v[226:227], v0 offset:10240\n"
"buffer_load_dwordx4 v11, %4, s47 offen offset:1024 lds\n"
"v_mul_f32 v35, v35, v8\n"
"s_waitcnt lgkmcnt(4)\n"
"v_mfma_f32_16x16x32_bf16 v[80:83], v[240:243], v[140:143], v[80:83]\n"
"v_mul_f32 v44, v44, v8\n"
"v_mul_f32 v45, v45, v8\n"
"v_mfma_f32_16x16x32_bf16 v[84:87], v[240:243], v[144:147], v[84:87]\n"
"ds_read_b64_tr_b16 v[228:229], v1 offset:8192\n"
"ds_read_b64_tr_b16 v[230:231], v1 offset:10240\n"
"v_mul_f32 v46, v46, v8\n"
"v_mul_f32 v47, v47, v8\n"
"v_mfma_f32_16x16x32_bf16 v[92:95], v[244:247], v[140:143], v[92:95]\n"
"v_cvt_pk_bf16_f32 v32, v32, v33\n"
"v_mfma_f32_16x16x32_bf16 v[96:99], v[244:247], v[144:147], v[96:99]\n"
"ds_read_b64_tr_b16 v[232:233], v2 offset:8192\n"
"ds_read_b64_tr_b16 v[234:235], v2 offset:10240\n"
"v_cvt_pk_bf16_f32 v33, v34, v35\n"
"v_cvt_pk_bf16_f32 v34, v44, v45\n"
"v_mfma_f32_16x16x32_bf16 v[104:107], v[248:251], v[140:143], v[104:107]\n"
"v_cvt_pk_bf16_f32 v35, v46, v47\n"
"v_mfma_f32_16x16x32_bf16 v[108:111], v[248:251], v[144:147], v[108:111]\n"
"ds_read_b64_tr_b16 v[236:237], v3 offset:8192\n"
"ds_read_b64_tr_b16 v[238:239], v3 offset:10240\n"
"buffer_store_dwordx4 v[32:35], v5, %5, s46 offen offset:0 sc0 sc1\n"
"v_mul_f32 v36, v36, v9\n"
"s_waitcnt lgkmcnt(4)\n"
"v_mfma_f32_16x16x32_bf16 v[116:119], v[252:255], v[140:143], v[116:119]\n"
"buffer_load_dwordx4 v30, %4, s47 offen offset:2048 lds\n"
"v_mul_f32 v37, v37, v9\n"
"v_mfma_f32_16x16x32_bf16 v[120:123], v[252:255], v[144:147], v[120:123]\n"
"ds_read_b64_tr_b16 v[240:241], v0 offset:12288\n"
"ds_read_b64_tr_b16 v[242:243], v0 offset:14336\n"
"v_mul_f32 v38, v38, v9\n"
"v_mul_f32 v39, v39, v9\n"
"v_mfma_f32_16x16x32_bf16 v[80:83], v[224:227], v[152:155], v[80:83]\n"
"v_mul_f32 v48, v48, v9\n"
"v_mfma_f32_16x16x32_bf16 v[84:87], v[224:227], v[156:159], v[84:87]\n"
"ds_read_b64_tr_b16 v[244:245], v1 offset:12288\n"
"ds_read_b64_tr_b16 v[246:247], v1 offset:14336\n"
"v_mul_f32 v49, v49, v9\n"
"v_mul_f32 v50, v50, v9\n"
"v_mfma_f32_16x16x32_bf16 v[92:95], v[228:231], v[152:155], v[92:95]\n"
"v_mul_f32 v51, v51, v9\n"
"v_mfma_f32_16x16x32_bf16 v[96:99], v[228:231], v[156:159], v[96:99]\n"
"ds_read_b64_tr_b16 v[248:249], v2 offset:12288\n"
"ds_read_b64_tr_b16 v[250:251], v2 offset:14336\n"
"v_cvt_pk_bf16_f32 v36, v36, v37\n"
"v_cvt_pk_bf16_f32 v37, v38, v39\n"
"s_waitcnt lgkmcnt(4)\n"
"v_mfma_f32_16x16x32_bf16 v[104:107], v[232:235], v[152:155], v[104:107]\n"
"v_cvt_pk_bf16_f32 v38, v48, v49\n"
"v_mfma_f32_16x16x32_bf16 v[108:111], v[232:235], v[156:159], v[108:111]\n"
"buffer_load_dwordx4 v31, %4, s47 offen offset:3072 lds\n"
"ds_read_b64_tr_b16 v[252:253], v3 offset:12288\n"
"ds_read_b64_tr_b16 v[254:255], v3 offset:14336\n"
"v_cvt_pk_bf16_f32 v39, v50, v51\n"
"v_mfma_f32_16x16x32_bf16 v[116:119], v[236:239], v[152:155], v[116:119]\n"
"buffer_store_dwordx4 v[36:39], v6, %5, s46 offen offset:0 sc0 sc1\n"
"v_mul_f32 v56, v56, v8\n"
"v_mfma_f32_16x16x32_bf16 v[120:123], v[236:239], v[156:159], v[120:123]\n"
"ds_read_b64_tr_b16 v[224:225], v0 offset:16384\n"
"ds_read_b64_tr_b16 v[226:227], v0 offset:18432\n"
"v_mul_f32 v57, v57, v8\n"
"v_mul_f32 v58, v58, v8\n"
"v_mfma_f32_16x16x32_bf16 v[80:83], v[240:243], v[164:167], v[80:83]\n"
"v_mul_f32 v59, v59, v8\n"
"v_mfma_f32_16x16x32_bf16 v[84:87], v[240:243], v[168:171], v[84:87]\n"
"ds_read_b64_tr_b16 v[228:229], v1 offset:16384\n"
"ds_read_b64_tr_b16 v[230:231], v1 offset:18432\n"
"v_mul_f32 v68, v68, v8\n"
"v_mul_f32 v69, v69, v8\n"
"s_waitcnt lgkmcnt(4)\n"
"v_mfma_f32_16x16x32_bf16 v[92:95], v[244:247], v[164:167], v[92:95]\n"
"v_mul_f32 v70, v70, v8\n"
"v_mfma_f32_16x16x32_bf16 v[96:99], v[244:247], v[168:171], v[96:99]\n"
"s_mov_b32 s47, 29312\n"
"s_add_u32 m0, s48, 135168\n"
"buffer_load_dwordx4 v4, %4, s47 offen offset:0 lds\n"
"ds_read_b64_tr_b16 v[232:233], v2 offset:16384\n"
"v_mfma_f32_16x16x32_bf16 v[104:107], v[248:251], v[164:167], v[104:107]\n"
"ds_read_b64_tr_b16 v[234:235], v2 offset:18432\n"
"v_mul_f32 v71, v71, v8\n"
"v_mfma_f32_16x16x32_bf16 v[108:111], v[248:251], v[168:171], v[108:111]\n"
"ds_read_b64_tr_b16 v[236:237], v3 offset:16384\n"
"ds_read_b64_tr_b16 v[238:239], v3 offset:18432\n"
"v_cvt_pk_bf16_f32 v56, v56, v57\n"
"v_cvt_pk_bf16_f32 v57, v58, v59\n"
"v_mfma_f32_16x16x32_bf16 v[116:119], v[252:255], v[164:167], v[116:119]\n"
"v_cvt_pk_bf16_f32 v58, v68, v69\n"
"v_cvt_pk_bf16_f32 v59, v70, v71\n"
"v_mfma_f32_16x16x32_bf16 v[120:123], v[252:255], v[168:171], v[120:123]\n"
"ds_read_b64_tr_b16 v[240:241], v0 offset:20480\n"
"ds_read_b64_tr_b16 v[242:243], v0 offset:22528\n"
"buffer_store_dwordx4 v[56:59], v5, %5, s46 offen offset:1024 sc0 sc1\n"
"v_mul_f32 v60, v60, v9\n"
"s_waitcnt lgkmcnt(4)\n"
"v_mfma_f32_16x16x32_bf16 v[80:83], v[224:227], v[176:179], v[80:83]\n"
"v_mul_f32 v61, v61, v9\n"
"v_mul_f32 v62, v62, v9\n"
"v_mfma_f32_16x16x32_bf16 v[84:87], v[224:227], v[180:183], v[84:87]\n"
"ds_read_b64_tr_b16 v[244:245], v1 offset:20480\n"
"ds_read_b64_tr_b16 v[246:247], v1 offset:22528\n"
"buffer_load_dwordx4 v11, %4, s47 offen offset:1024 lds\n"
"v_mul_f32 v63, v63, v9\n"
"v_mfma_f32_16x16x32_bf16 v[92:95], v[228:231], v[176:179], v[92:95]\n"
"v_mul_f32 v72, v72, v9\n"
"v_mul_f32 v73, v73, v9\n"
"v_mfma_f32_16x16x32_bf16 v[96:99], v[228:231], v[180:183], v[96:99]\n"
"ds_read_b64_tr_b16 v[248:249], v2 offset:20480\n"
"ds_read_b64_tr_b16 v[250:251], v2 offset:22528\n"
"v_mul_f32 v74, v74, v9\n"
"v_mul_f32 v75, v75, v9\n"
"v_mfma_f32_16x16x32_bf16 v[104:107], v[232:235], v[176:179], v[104:107]\n"
"v_cvt_pk_bf16_f32 v60, v60, v61\n"
"v_mfma_f32_16x16x32_bf16 v[108:111], v[232:235], v[180:183], v[108:111]\n"
"ds_read_b64_tr_b16 v[252:253], v3 offset:20480\n"
"ds_read_b64_tr_b16 v[254:255], v3 offset:22528\n"
"v_cvt_pk_bf16_f32 v61, v62, v63\n"
"v_cvt_pk_bf16_f32 v62, v72, v73\n"
"s_waitcnt lgkmcnt(4)\n"
"v_mfma_f32_16x16x32_bf16 v[116:119], v[236:239], v[176:179], v[116:119]\n"
"v_cvt_pk_bf16_f32 v63, v74, v75\n"
"v_mfma_f32_16x16x32_bf16 v[120:123], v[236:239], v[180:183], v[120:123]\n"
"ds_read_b64_tr_b16 v[224:225], v0 offset:24576\n"
"ds_read_b64_tr_b16 v[226:227], v0 offset:26624\n"
"buffer_store_dwordx4 v[60:63], v6, %5, s46 offen offset:1024 sc0 sc1\n"
"v_mfma_f32_16x16x32_bf16 v[80:83], v[240:243], v[188:191], v[80:83]\n"
"buffer_load_dwordx4 v30, %4, s47 offen offset:2048 lds\n"
"v_mfma_f32_16x16x32_bf16 v[84:87], v[240:243], v[192:195], v[84:87]\n"
"ds_read_b64_tr_b16 v[228:229], v1 offset:24576\n"
"ds_read_b64_tr_b16 v[230:231], v1 offset:26624\n"
"v_mfma_f32_16x16x32_bf16 v[92:95], v[244:247], v[188:191], v[92:95]\n"
"v_mfma_f32_16x16x32_bf16 v[96:99], v[244:247], v[192:195], v[96:99]\n"
"ds_read_b64_tr_b16 v[232:233], v2 offset:24576\n"
"ds_read_b64_tr_b16 v[234:235], v2 offset:26624\n"
"s_waitcnt lgkmcnt(6)\n"
"v_mfma_f32_16x16x32_bf16 v[104:107], v[248:251], v[188:191], v[104:107]\n"
"v_mfma_f32_16x16x32_bf16 v[108:111], v[248:251], v[192:195], v[108:111]\n"
"ds_read_b64_tr_b16 v[236:237], v3 offset:24576\n"
"ds_read_b64_tr_b16 v[238:239], v3 offset:26624\n"
"v_mfma_f32_16x16x32_bf16 v[116:119], v[252:255], v[188:191], v[116:119]\n"
"v_mfma_f32_16x16x32_bf16 v[120:123], v[252:255], v[192:195], v[120:123]\n"
"buffer_load_dwordx4 v31, %4, s47 offen offset:3072 lds\n"
"ds_read_b64_tr_b16 v[240:241], v0 offset:28672\n"
"ds_read_b64_tr_b16 v[242:243], v0 offset:30720\n"
"s_waitcnt lgkmcnt(6)\n"
"v_mfma_f32_16x16x32_bf16 v[80:83], v[224:227], v[200:203], v[80:83]\n"
"v_mfma_f32_16x16x32_bf16 v[84:87], v[224:227], v[204:207], v[84:87]\n"
"ds_read_b64_tr_b16 v[244:245], v1 offset:28672\n"
"ds_read_b64_tr_b16 v[246:247], v1 offset:30720\n"
"v_mfma_f32_16x16x32_bf16 v[92:95], v[228:231], v[200:203], v[92:95]\n"
"v_mfma_f32_16x16x32_bf16 v[96:99], v[228:231], v[204:207], v[96:99]\n"
"ds_read_b64_tr_b16 v[248:249], v2 offset:28672\n"
"ds_read_b64_tr_b16 v[250:251], v2 offset:30720\n"
"ds_read_b64_tr_b16 v[252:253], v3 offset:28672\n"
"ds_read_b64_tr_b16 v[254:255], v3 offset:30720\n"
"s_waitcnt lgkmcnt(7)\n"
"v_mfma_f32_16x16x32_bf16 v[104:107], v[232:235], v[200:203], v[104:107]\n"
"v_mfma_f32_16x16x32_bf16 v[108:111], v[232:235], v[204:207], v[108:111]\n"
"v_mfma_f32_16x16x32_bf16 v[116:119], v[236:239], v[200:203], v[116:119]\n"
"v_mfma_f32_16x16x32_bf16 v[120:123], v[236:239], v[204:207], v[120:123]\n"
"s_waitcnt lgkmcnt(4)\n"
"v_mfma_f32_16x16x32_bf16 v[80:83], v[240:243], v[212:215], v[80:83]\n"
"v_mfma_f32_16x16x32_bf16 v[84:87], v[240:243], v[216:219], v[84:87]\n"
"s_waitcnt vmcnt(28) lgkmcnt(0)\n"
"s_barrier\n"
"v_mfma_f32_16x16x32_bf16 v[92:95], v[244:247], v[212:215], v[92:95]\n"
"v_add_u32 v0, 98304, v0\n"
"v_add_u32 v1, 98304, v1\n"
"v_mfma_f32_16x16x32_bf16 v[96:99], v[244:247], v[216:219], v[96:99]\n"
"v_add_u32 v2, 98304, v2\n"
"v_add_u32 v3, 98304, v3\n"
"ds_read_b64_tr_b16 v[224:225], v0 offset:0\n"
"ds_read_b64_tr_b16 v[226:227], v0 offset:2048\n"
"v_mfma_f32_16x16x32_bf16 v[104:107], v[248:251], v[212:215], v[104:107]\n"
"ds_read_b64_tr_b16 v[228:229], v1 offset:0\n"
"ds_read_b64_tr_b16 v[230:231], v1 offset:2048\n"
"v_mfma_f32_16x16x32_bf16 v[108:111], v[248:251], v[216:219], v[108:111]\n"
"ds_read_b64_tr_b16 v[232:233], v2 offset:0\n"
"ds_read_b64_tr_b16 v[234:235], v2 offset:2048\n"
"ds_read_b64_tr_b16 v[236:237], v3 offset:0\n"
"ds_read_b64_tr_b16 v[238:239], v3 offset:2048\n"
"v_mfma_f32_16x16x32_bf16 v[116:119], v[252:255], v[212:215], v[116:119]\n"
"v_mfma_f32_16x16x32_bf16 v[120:123], v[252:255], v[216:219], v[120:123]\n"
"s_waitcnt lgkmcnt(6)\n"
"v_mfma_f32_16x16x32_bf16 v[32:35], v[224:227], v[128:131], 0\n"
"ds_read_b64_tr_b16 v[240:241], v0 offset:4096\n"
"ds_read_b64_tr_b16 v[242:243], v0 offset:6144\n"
"v_mfma_f32_16x16x32_bf16 v[36:39], v[224:227], v[132:135], 0\n"
"s_mov_b32 s47, 768\n"
"s_add_u32 m0, s48, 0\n"
"buffer_load_dwordx4 v4, %4, s47 offen offset:0 lds\n"
"ds_read_b64_tr_b16 v[244:245], v1 offset:4096\n"
"s_waitcnt lgkmcnt(5)\n"
"v_mfma_f32_16x16x32_bf16 v[44:47], v[228:231], v[128:131], 0\n"
"ds_read_b64_tr_b16 v[246:247], v1 offset:6144\n"
"v_mfma_f32_16x16x32_bf16 v[48:51], v[228:231], v[132:135], 0\n"
"ds_read_b64_tr_b16 v[248:249], v2 offset:4096\n"
"ds_read_b64_tr_b16 v[250:251], v2 offset:6144\n"
"v_mfma_f32_16x16x32_bf16 v[56:59], v[232:235], v[128:131], 0\n"
"s_mov_b32 s46, 2048\n"
"v_mfma_f32_16x16x32_bf16 v[60:63], v[232:235], v[132:135], 0\n"
"ds_read_b64_tr_b16 v[252:253], v3 offset:4096\n"
"ds_read_b64_tr_b16 v[254:255], v3 offset:6144\n"
"v_mul_f32 v80, v80, v8\n"
"v_mul_f32 v81, v81, v8\n"
"s_waitcnt lgkmcnt(5)\n"
"v_mfma_f32_16x16x32_bf16 v[68:71], v[236:239], v[128:131], 0\n"
"v_mul_f32 v82, v82, v8\n"
"v_mfma_f32_16x16x32_bf16 v[72:75], v[236:239], v[132:135], 0\n"
"ds_read_b64_tr_b16 v[224:225], v0 offset:8192\n"
"ds_read_b64_tr_b16 v[226:227], v0 offset:10240\n"
"buffer_load_dwordx4 v11, %4, s47 offen offset:1024 lds\n"
"v_mul_f32 v83, v83, v8\n"
"v_mfma_f32_16x16x32_bf16 v[32:35], v[240:243], v[140:143], v[32:35]\n"
"v_mul_f32 v92, v92, v8\n"
"v_mul_f32 v93, v93, v8\n"
"v_mfma_f32_16x16x32_bf16 v[36:39], v[240:243], v[144:147], v[36:39]\n"
"ds_read_b64_tr_b16 v[228:229], v1 offset:8192\n"
"ds_read_b64_tr_b16 v[230:231], v1 offset:10240\n"
"v_mul_f32 v94, v94, v8\n"
"v_mul_f32 v95, v95, v8\n"
"s_waitcnt lgkmcnt(4)\n"
"v_mfma_f32_16x16x32_bf16 v[44:47], v[244:247], v[140:143], v[44:47]\n"
"v_cvt_pk_bf16_f32 v80, v80, v81\n"
"v_mfma_f32_16x16x32_bf16 v[48:51], v[244:247], v[144:147], v[48:51]\n"
"ds_read_b64_tr_b16 v[232:233], v2 offset:8192\n"
"ds_read_b64_tr_b16 v[234:235], v2 offset:10240\n"
"v_cvt_pk_bf16_f32 v81, v82, v83\n"
"v_cvt_pk_bf16_f32 v82, v92, v93\n"
"v_mfma_f32_16x16x32_bf16 v[56:59], v[248:251], v[140:143], v[56:59]\n"
"v_cvt_pk_bf16_f32 v83, v94, v95\n"
"v_mfma_f32_16x16x32_bf16 v[60:63], v[248:251], v[144:147], v[60:63]\n"
"ds_read_b64_tr_b16 v[236:237], v3 offset:8192\n"
"ds_read_b64_tr_b16 v[238:239], v3 offset:10240\n"
"buffer_store_dwordx4 v[80:83], v5, %5, s46 offen offset:0 sc0 sc1\n"
"v_mul_f32 v84, v84, v9\n"
"v_mfma_f32_16x16x32_bf16 v[68:71], v[252:255], v[140:143], v[68:71]\n"
"buffer_load_dwordx4 v30, %4, s47 offen offset:2048 lds\n"
"v_mul_f32 v85, v85, v9\n"
"v_mfma_f32_16x16x32_bf16 v[72:75], v[252:255], v[144:147], v[72:75]\n"
"ds_read_b64_tr_b16 v[240:241], v0 offset:12288\n"
"ds_read_b64_tr_b16 v[242:243], v0 offset:14336\n"
"v_mul_f32 v86, v86, v9\n"
"v_mul_f32 v87, v87, v9\n"
"s_waitcnt lgkmcnt(4)\n"
"v_mfma_f32_16x16x32_bf16 v[32:35], v[224:227], v[152:155], v[32:35]\n"
"v_mul_f32 v96, v96, v9\n"
"v_mfma_f32_16x16x32_bf16 v[36:39], v[224:227], v[156:159], v[36:39]\n"
"ds_read_b64_tr_b16 v[244:245], v1 offset:12288\n"
"ds_read_b64_tr_b16 v[246:247], v1 offset:14336\n"
"v_mul_f32 v97, v97, v9\n"
"v_mul_f32 v98, v98, v9\n"
"v_mfma_f32_16x16x32_bf16 v[44:47], v[228:231], v[152:155], v[44:47]\n"
"v_mul_f32 v99, v99, v9\n"
"v_mfma_f32_16x16x32_bf16 v[48:51], v[228:231], v[156:159], v[48:51]\n"
"ds_read_b64_tr_b16 v[248:249], v2 offset:12288\n"
"ds_read_b64_tr_b16 v[250:251], v2 offset:14336\n"
"v_cvt_pk_bf16_f32 v84, v84, v85\n"
"v_cvt_pk_bf16_f32 v85, v86, v87\n"
"v_mfma_f32_16x16x32_bf16 v[56:59], v[232:235], v[152:155], v[56:59]\n"
"v_cvt_pk_bf16_f32 v86, v96, v97\n"
"v_mfma_f32_16x16x32_bf16 v[60:63], v[232:235], v[156:159], v[60:63]\n"
"buffer_load_dwordx4 v31, %4, s47 offen offset:3072 lds\n"
"ds_read_b64_tr_b16 v[252:253], v3 offset:12288\n"
"ds_read_b64_tr_b16 v[254:255], v3 offset:14336\n"
"v_cvt_pk_bf16_f32 v87, v98, v99\n"
"s_waitcnt lgkmcnt(4)\n"
"v_mfma_f32_16x16x32_bf16 v[68:71], v[236:239], v[152:155], v[68:71]\n"
"buffer_store_dwordx4 v[84:87], v6, %5, s46 offen offset:0 sc0 sc1\n"
"v_mul_f32 v104, v104, v8\n"
"v_mfma_f32_16x16x32_bf16 v[72:75], v[236:239], v[156:159], v[72:75]\n"
"ds_read_b64_tr_b16 v[224:225], v0 offset:16384\n"
"ds_read_b64_tr_b16 v[226:227], v0 offset:18432\n"
"v_mul_f32 v105, v105, v8\n"
"v_mul_f32 v106, v106, v8\n"
"v_mfma_f32_16x16x32_bf16 v[32:35], v[240:243], v[164:167], v[32:35]\n"
"v_mul_f32 v107, v107, v8\n"
"v_mfma_f32_16x16x32_bf16 v[36:39], v[240:243], v[168:171], v[36:39]\n"
"ds_read_b64_tr_b16 v[228:229], v1 offset:16384\n"
"ds_read_b64_tr_b16 v[230:231], v1 offset:18432\n"
"v_mul_f32 v116, v116, v8\n"
"v_mul_f32 v117, v117, v8\n"
"v_mfma_f32_16x16x32_bf16 v[44:47], v[244:247], v[164:167], v[44:47]\n"
"v_mul_f32 v118, v118, v8\n"
"v_mfma_f32_16x16x32_bf16 v[48:51], v[244:247], v[168:171], v[48:51]\n"
"s_mov_b32 s47, 29440\n"
"s_add_u32 m0, s48, 4096\n"
"buffer_load_dwordx4 v4, %4, s47 offen offset:0 lds\n"
"ds_read_b64_tr_b16 v[232:233], v2 offset:16384\n"
"s_waitcnt lgkmcnt(3)\n"
"v_mfma_f32_16x16x32_bf16 v[56:59], v[248:251], v[164:167], v[56:59]\n"
"ds_read_b64_tr_b16 v[234:235], v2 offset:18432\n"
"v_mul_f32 v119, v119, v8\n"
"v_mfma_f32_16x16x32_bf16 v[60:63], v[248:251], v[168:171], v[60:63]\n"
"ds_read_b64_tr_b16 v[236:237], v3 offset:16384\n"
"ds_read_b64_tr_b16 v[238:239], v3 offset:18432\n"
"v_cvt_pk_bf16_f32 v104, v104, v105\n"
"v_cvt_pk_bf16_f32 v105, v106, v107\n"
"v_mfma_f32_16x16x32_bf16 v[68:71], v[252:255], v[164:167], v[68:71]\n"
"v_cvt_pk_bf16_f32 v106, v116, v117\n"
"v_cvt_pk_bf16_f32 v107, v118, v119\n"
"v_mfma_f32_16x16x32_bf16 v[72:75], v[252:255], v[168:171], v[72:75]\n"
"ds_read_b64_tr_b16 v[240:241], v0 offset:20480\n"
"ds_read_b64_tr_b16 v[242:243], v0 offset:22528\n"
"buffer_store_dwordx4 v[104:107], v5, %5, s46 offen offset:1024 sc0 sc1\n"
"v_mul_f32 v108, v108, v9\n"
"v_mfma_f32_16x16x32_bf16 v[32:35], v[224:227], v[176:179], v[32:35]\n"
"v_mul_f32 v109, v109, v9\n"
"v_mul_f32 v110, v110, v9\n"
"v_mfma_f32_16x16x32_bf16 v[36:39], v[224:227], v[180:183], v[36:39]\n"
"ds_read_b64_tr_b16 v[244:245], v1 offset:20480\n"
"ds_read_b64_tr_b16 v[246:247], v1 offset:22528\n"
"buffer_load_dwordx4 v11, %4, s47 offen offset:1024 lds\n"
"v_mul_f32 v111, v111, v9\n"
"s_waitcnt lgkmcnt(4)\n"
"v_mfma_f32_16x16x32_bf16 v[44:47], v[228:231], v[176:179], v[44:47]\n"
"v_mul_f32 v120, v120, v9\n"
"v_mul_f32 v121, v121, v9\n"
"v_mfma_f32_16x16x32_bf16 v[48:51], v[228:231], v[180:183], v[48:51]\n"
"ds_read_b64_tr_b16 v[248:249], v2 offset:20480\n"
"ds_read_b64_tr_b16 v[250:251], v2 offset:22528\n"
"v_mul_f32 v122, v122, v9\n"
"v_mul_f32 v123, v123, v9\n"
"v_mfma_f32_16x16x32_bf16 v[56:59], v[232:235], v[176:179], v[56:59]\n"
"v_cvt_pk_bf16_f32 v108, v108, v109\n"
"v_mfma_f32_16x16x32_bf16 v[60:63], v[232:235], v[180:183], v[60:63]\n"
"ds_read_b64_tr_b16 v[252:253], v3 offset:20480\n"
"ds_read_b64_tr_b16 v[254:255], v3 offset:22528\n"
"v_cvt_pk_bf16_f32 v109, v110, v111\n"
"v_cvt_pk_bf16_f32 v110, v120, v121\n"
"v_mfma_f32_16x16x32_bf16 v[68:71], v[236:239], v[176:179], v[68:71]\n"
"v_cvt_pk_bf16_f32 v111, v122, v123\n"
"v_mfma_f32_16x16x32_bf16 v[72:75], v[236:239], v[180:183], v[72:75]\n"
"ds_read_b64_tr_b16 v[224:225], v0 offset:24576\n"
"ds_read_b64_tr_b16 v[226:227], v0 offset:26624\n"
"buffer_store_dwordx4 v[108:111], v6, %5, s46 offen offset:1024 sc0 sc1\n"
"s_waitcnt lgkmcnt(4)\n"
"v_mfma_f32_16x16x32_bf16 v[32:35], v[240:243], v[188:191], v[32:35]\n"
"buffer_load_dwordx4 v30, %4, s47 offen offset:2048 lds\n"
"v_mfma_f32_16x16x32_bf16 v[36:39], v[240:243], v[192:195], v[36:39]\n"
"ds_read_b64_tr_b16 v[228:229], v1 offset:24576\n"
"ds_read_b64_tr_b16 v[230:231], v1 offset:26624\n"
"v_mfma_f32_16x16x32_bf16 v[44:47], v[244:247], v[188:191], v[44:47]\n"
"v_mfma_f32_16x16x32_bf16 v[48:51], v[244:247], v[192:195], v[48:51]\n"
"ds_read_b64_tr_b16 v[232:233], v2 offset:24576\n"
"ds_read_b64_tr_b16 v[234:235], v2 offset:26624\n"
"v_mfma_f32_16x16x32_bf16 v[56:59], v[248:251], v[188:191], v[56:59]\n"
"v_mfma_f32_16x16x32_bf16 v[60:63], v[248:251], v[192:195], v[60:63]\n"
"ds_read_b64_tr_b16 v[236:237], v3 offset:24576\n"
"ds_read_b64_tr_b16 v[238:239], v3 offset:26624\n"
"s_waitcnt lgkmcnt(6)\n"
"v_mfma_f32_16x16x32_bf16 v[68:71], v[252:255], v[188:191], v[68:71]\n"
"v_mfma_f32_16x16x32_bf16 v[72:75], v[252:255], v[192:195], v[72:75]\n"
"buffer_load_dwordx4 v31, %4, s47 offen offset:3072 lds\n"
"ds_read_b64_tr_b16 v[240:241], v0 offset:28672\n"
"ds_read_b64_tr_b16 v[242:243], v0 offset:30720\n"
"v_mfma_f32_16x16x32_bf16 v[32:35], v[224:227], v[200:203], v[32:35]\n"
"v_mfma_f32_16x16x32_bf16 v[36:39], v[224:227], v[204:207], v[36:39]\n"
"ds_read_b64_tr_b16 v[244:245], v1 offset:28672\n"
"ds_read_b64_tr_b16 v[246:247], v1 offset:30720\n"
"s_waitcnt lgkmcnt(6)\n"
"v_mfma_f32_16x16x32_bf16 v[44:47], v[228:231], v[200:203], v[44:47]\n"
"v_mfma_f32_16x16x32_bf16 v[48:51], v[228:231], v[204:207], v[48:51]\n"
"ds_read_b64_tr_b16 v[248:249], v2 offset:28672\n"
"ds_read_b64_tr_b16 v[250:251], v2 offset:30720\n"
"ds_read_b64_tr_b16 v[252:253], v3 offset:28672\n"
"ds_read_b64_tr_b16 v[254:255], v3 offset:30720\n"
"v_mfma_f32_16x16x32_bf16 v[56:59], v[232:235], v[200:203], v[56:59]\n"
"v_mfma_f32_16x16x32_bf16 v[60:63], v[232:235], v[204:207], v[60:63]\n"
"s_waitcnt lgkmcnt(6)\n"
"v_mfma_f32_16x16x32_bf16 v[68:71], v[236:239], v[200:203], v[68:71]\n"
"v_mfma_f32_16x16x32_bf16 v[72:75], v[236:239], v[204:207], v[72:75]\n"
"v_mfma_f32_16x16x32_bf16 v[32:35], v[240:243], v[212:215], v[32:35]\n"
"v_mfma_f32_16x16x32_bf16 v[36:39], v[240:243], v[216:219], v[36:39]\n"
"s_waitcnt vmcnt(14) lgkmcnt(0)\n"
"s_barrier\n"
"v_mfma_f32_16x16x32_bf16 v[44:47], v[244:247], v[212:215], v[44:47]\n"
"v_subrev_u32 v0, 65536, v0\n"
"v_subrev_u32 v1, 65536, v1\n"
"v_mfma_f32_16x16x32_bf16 v[48:51], v[244:247], v[216:219], v[48:51]\n"
"v_subrev_u32 v2, 65536, v2\n"
"v_subrev_u32 v3, 65536, v3\n"
"ds_read_b64_tr_b16 v[224:225], v0 offset:0\n"
"ds_read_b64_tr_b16 v[226:227], v0 offset:2048\n"
"v_mfma_f32_16x16x32_bf16 v[56:59], v[248:251], v[212:215], v[56:59]\n"
"ds_read_b64_tr_b16 v[228:229], v1 offset:0\n"
"ds_read_b64_tr_b16 v[230:231], v1 offset:2048\n"
"v_mfma_f32_16x16x32_bf16 v[60:63], v[248:251], v[216:219], v[60:63]\n"
"ds_read_b64_tr_b16 v[232:233], v2 offset:0\n"
"ds_read_b64_tr_b16 v[234:235], v2 offset:2048\n"
"ds_read_b64_tr_b16 v[236:237], v3 offset:0\n"
"ds_read_b64_tr_b16 v[238:239], v3 offset:2048\n"
"v_mfma_f32_16x16x32_bf16 v[68:71], v[252:255], v[212:215], v[68:71]\n"
"v_mfma_f32_16x16x32_bf16 v[72:75], v[252:255], v[216:219], v[72:75]\n"
"s_waitcnt lgkmcnt(6)\n"
"v_mfma_f32_16x16x32_bf16 v[80:83], v[224:227], v[128:131], 0\n"
"ds_read_b64_tr_b16 v[240:241], v0 offset:4096\n"
"ds_read_b64_tr_b16 v[242:243], v0 offset:6144\n"
"v_mfma_f32_16x16x32_bf16 v[84:87], v[224:227], v[132:135], 0\n"
"ds_read_b64_tr_b16 v[244:245], v1 offset:4096\n"
"ds_read_b64_tr_b16 v[246:247], v1 offset:6144\n"
"s_waitcnt lgkmcnt(7)\n"
"v_mfma_f32_16x16x32_bf16 v[92:95], v[228:231], v[128:131], 0\n"
"v_mfma_f32_16x16x32_bf16 v[96:99], v[228:231], v[132:135], 0\n"
"ds_read_b64_tr_b16 v[248:249], v2 offset:4096\n"
"ds_read_b64_tr_b16 v[250:251], v2 offset:6144\n"
"s_waitcnt lgkmcnt(6)\n"
"v_mfma_f32_16x16x32_bf16 v[104:107], v[232:235], v[128:131], 0\n"
"s_mov_b32 s46, 4096\n"
"v_mfma_f32_16x16x32_bf16 v[108:111], v[232:235], v[132:135], 0\n"
"ds_read_b64_tr_b16 v[252:253], v3 offset:4096\n"
"ds_read_b64_tr_b16 v[254:255], v3 offset:6144\n"
"v_mul_f32 v32, v32, v8\n"
"v_mul_f32 v33, v33, v8\n"
"v_mfma_f32_16x16x32_bf16 v[116:119], v[236:239], v[128:131], 0\n"
"v_mul_f32 v34, v34, v8\n"
"v_mfma_f32_16x16x32_bf16 v[120:123], v[236:239], v[132:135], 0\n"
"ds_read_b64_tr_b16 v[224:225], v0 offset:8192\n"
"ds_read_b64_tr_b16 v[226:227], v0 offset:10240\n"
"v_mul_f32 v35, v35, v8\n"
"v_mul_f32 v44, v44, v8\n"
"s_waitcnt lgkmcnt(4)\n"
"v_mfma_f32_16x16x32_bf16 v[80:83], v[240:243], v[140:143], v[80:83]\n"
"v_mul_f32 v45, v45, v8\n"
"v_mfma_f32_16x16x32_bf16 v[84:87], v[240:243], v[144:147], v[84:87]\n"
"ds_read_b64_tr_b16 v[228:229], v1 offset:8192\n"
"ds_read_b64_tr_b16 v[230:231], v1 offset:10240\n"
"v_mul_f32 v46, v46, v8\n"
"v_mul_f32 v47, v47, v8\n"
"v_mfma_f32_16x16x32_bf16 v[92:95], v[244:247], v[140:143], v[92:95]\n"
"v_cvt_pk_bf16_f32 v32, v32, v33\n"
"v_mfma_f32_16x16x32_bf16 v[96:99], v[244:247], v[144:147], v[96:99]\n"
"ds_read_b64_tr_b16 v[232:233], v2 offset:8192\n"
"ds_read_b64_tr_b16 v[234:235], v2 offset:10240\n"
"v_cvt_pk_bf16_f32 v33, v34, v35\n"
"v_cvt_pk_bf16_f32 v34, v44, v45\n"
"v_mfma_f32_16x16x32_bf16 v[104:107], v[248:251], v[140:143], v[104:107]\n"
"v_cvt_pk_bf16_f32 v35, v46, v47\n"
"v_mfma_f32_16x16x32_bf16 v[108:111], v[248:251], v[144:147], v[108:111]\n"
"ds_read_b64_tr_b16 v[236:237], v3 offset:8192\n"
"ds_read_b64_tr_b16 v[238:239], v3 offset:10240\n"
"buffer_store_dwordx4 v[32:35], v5, %5, s46 offen offset:0 sc0 sc1\n"
"v_mul_f32 v36, v36, v9\n"
"s_waitcnt lgkmcnt(4)\n"
"v_mfma_f32_16x16x32_bf16 v[116:119], v[252:255], v[140:143], v[116:119]\n"
"v_mul_f32 v37, v37, v9\n"
"v_mfma_f32_16x16x32_bf16 v[120:123], v[252:255], v[144:147], v[120:123]\n"
"ds_read_b64_tr_b16 v[240:241], v0 offset:12288\n"
"ds_read_b64_tr_b16 v[242:243], v0 offset:14336\n"
"v_mul_f32 v38, v38, v9\n"
"v_mul_f32 v39, v39, v9\n"
"v_mfma_f32_16x16x32_bf16 v[80:83], v[224:227], v[152:155], v[80:83]\n"
"v_mul_f32 v48, v48, v9\n"
"v_mfma_f32_16x16x32_bf16 v[84:87], v[224:227], v[156:159], v[84:87]\n"
"ds_read_b64_tr_b16 v[244:245], v1 offset:12288\n"
"ds_read_b64_tr_b16 v[246:247], v1 offset:14336\n"
"v_mul_f32 v49, v49, v9\n"
"v_mul_f32 v50, v50, v9\n"
"v_mfma_f32_16x16x32_bf16 v[92:95], v[228:231], v[152:155], v[92:95]\n"
"v_mul_f32 v51, v51, v9\n"
"v_mfma_f32_16x16x32_bf16 v[96:99], v[228:231], v[156:159], v[96:99]\n"
"ds_read_b64_tr_b16 v[248:249], v2 offset:12288\n"
"ds_read_b64_tr_b16 v[250:251], v2 offset:14336\n"
"v_cvt_pk_bf16_f32 v36, v36, v37\n"
"v_cvt_pk_bf16_f32 v37, v38, v39\n"
"s_waitcnt lgkmcnt(4)\n"
"v_mfma_f32_16x16x32_bf16 v[104:107], v[232:235], v[152:155], v[104:107]\n"
"v_cvt_pk_bf16_f32 v38, v48, v49\n"
"v_mfma_f32_16x16x32_bf16 v[108:111], v[232:235], v[156:159], v[108:111]\n"
"ds_read_b64_tr_b16 v[252:253], v3 offset:12288\n"
"ds_read_b64_tr_b16 v[254:255], v3 offset:14336\n"
"v_cvt_pk_bf16_f32 v39, v50, v51\n"
"buffer_store_dwordx4 v[36:39], v6, %5, s46 offen offset:0 sc0 sc1\n"
"v_mfma_f32_16x16x32_bf16 v[116:119], v[236:239], v[152:155], v[116:119]\n"
"v_mul_f32 v56, v56, v8\n"
"v_mfma_f32_16x16x32_bf16 v[120:123], v[236:239], v[156:159], v[120:123]\n"
"ds_read_b64_tr_b16 v[224:225], v0 offset:16384\n"
"ds_read_b64_tr_b16 v[226:227], v0 offset:18432\n"
"v_mul_f32 v57, v57, v8\n"
"v_mul_f32 v58, v58, v8\n"
"v_mfma_f32_16x16x32_bf16 v[80:83], v[240:243], v[164:167], v[80:83]\n"
"v_mul_f32 v59, v59, v8\n"
"v_mfma_f32_16x16x32_bf16 v[84:87], v[240:243], v[168:171], v[84:87]\n"
"ds_read_b64_tr_b16 v[228:229], v1 offset:16384\n"
"ds_read_b64_tr_b16 v[230:231], v1 offset:18432\n"
"v_mul_f32 v68, v68, v8\n"
"v_mul_f32 v69, v69, v8\n"
"s_waitcnt lgkmcnt(4)\n"
"v_mfma_f32_16x16x32_bf16 v[92:95], v[244:247], v[164:167], v[92:95]\n"
"v_mul_f32 v70, v70, v8\n"
"v_mfma_f32_16x16x32_bf16 v[96:99], v[244:247], v[168:171], v[96:99]\n"
"ds_read_b64_tr_b16 v[232:233], v2 offset:16384\n"
"ds_read_b64_tr_b16 v[234:235], v2 offset:18432\n"
"v_mul_f32 v71, v71, v8\n"
"v_cvt_pk_bf16_f32 v56, v56, v57\n"
"v_mfma_f32_16x16x32_bf16 v[104:107], v[248:251], v[164:167], v[104:107]\n"
"v_cvt_pk_bf16_f32 v57, v58, v59\n"
"v_mfma_f32_16x16x32_bf16 v[108:111], v[248:251], v[168:171], v[108:111]\n"
"ds_read_b64_tr_b16 v[236:237], v3 offset:16384\n"
"ds_read_b64_tr_b16 v[238:239], v3 offset:18432\n"
"v_cvt_pk_bf16_f32 v58, v68, v69\n"
"v_cvt_pk_bf16_f32 v59, v70, v71\n"
"v_mfma_f32_16x16x32_bf16 v[116:119], v[252:255], v[164:167], v[116:119]\n"
"buffer_store_dwordx4 v[56:59], v5, %5, s46 offen offset:1024 sc0 sc1\n"
"v_mfma_f32_16x16x32_bf16 v[120:123], v[252:255], v[168:171], v[120:123]\n"
"ds_read_b64_tr_b16 v[240:241], v0 offset:20480\n"
"ds_read_b64_tr_b16 v[242:243], v0 offset:22528\n"
"v_mul_f32 v60, v60, v9\n"
"v_mul_f32 v61, v61, v9\n"
"s_waitcnt lgkmcnt(4)\n"
"v_mfma_f32_16x16x32_bf16 v[80:83], v[224:227], v[176:179], v[80:83]\n"
"v_mul_f32 v62, v62, v9\n"
"v_mfma_f32_16x16x32_bf16 v[84:87], v[224:227], v[180:183], v[84:87]\n"
"ds_read_b64_tr_b16 v[244:245], v1 offset:20480\n"
"ds_read_b64_tr_b16 v[246:247], v1 offset:22528\n"
"v_mul_f32 v63, v63, v9\n"
"v_mul_f32 v72, v72, v9\n"
"v_mfma_f32_16x16x32_bf16 v[92:95], v[228:231], v[176:179], v[92:95]\n"
"v_mul_f32 v73, v73, v9\n"
"v_mfma_f32_16x16x32_bf16 v[96:99], v[228:231], v[180:183], v[96:99]\n"
"ds_read_b64_tr_b16 v[248:249], v2 offset:20480\n"
"ds_read_b64_tr_b16 v[250:251], v2 offset:22528\n"
"v_mul_f32 v74, v74, v9\n"
"v_mul_f32 v75, v75, v9\n"
"v_mfma_f32_16x16x32_bf16 v[104:107], v[232:235], v[176:179], v[104:107]\n"
"v_cvt_pk_bf16_f32 v60, v60, v61\n"
"v_mfma_f32_16x16x32_bf16 v[108:111], v[232:235], v[180:183], v[108:111]\n"
"ds_read_b64_tr_b16 v[252:253], v3 offset:20480\n"
"ds_read_b64_tr_b16 v[254:255], v3 offset:22528\n"
"v_cvt_pk_bf16_f32 v61, v62, v63\n"
"v_cvt_pk_bf16_f32 v62, v72, v73\n"
"s_waitcnt lgkmcnt(4)\n"
"v_mfma_f32_16x16x32_bf16 v[116:119], v[236:239], v[176:179], v[116:119]\n"
"v_cvt_pk_bf16_f32 v63, v74, v75\n"
"v_mfma_f32_16x16x32_bf16 v[120:123], v[236:239], v[180:183], v[120:123]\n"
"ds_read_b64_tr_b16 v[224:225], v0 offset:24576\n"
"ds_read_b64_tr_b16 v[226:227], v0 offset:26624\n"
"buffer_store_dwordx4 v[60:63], v6, %5, s46 offen offset:1024 sc0 sc1\n"
"v_mfma_f32_16x16x32_bf16 v[80:83], v[240:243], v[188:191], v[80:83]\n"
"v_mfma_f32_16x16x32_bf16 v[84:87], v[240:243], v[192:195], v[84:87]\n"
"ds_read_b64_tr_b16 v[228:229], v1 offset:24576\n"
"ds_read_b64_tr_b16 v[230:231], v1 offset:26624\n"
"v_mfma_f32_16x16x32_bf16 v[92:95], v[244:247], v[188:191], v[92:95]\n"
"v_mfma_f32_16x16x32_bf16 v[96:99], v[244:247], v[192:195], v[96:99]\n"
"ds_read_b64_tr_b16 v[232:233], v2 offset:24576\n"
"ds_read_b64_tr_b16 v[234:235], v2 offset:26624\n"
"s_waitcnt lgkmcnt(6)\n"
"v_mfma_f32_16x16x32_bf16 v[104:107], v[248:251], v[188:191], v[104:107]\n"
"v_mfma_f32_16x16x32_bf16 v[108:111], v[248:251], v[192:195], v[108:111]\n"
"ds_read_b64_tr_b16 v[236:237], v3 offset:24576\n"
"ds_read_b64_tr_b16 v[238:239], v3 offset:26624\n"
"v_mfma_f32_16x16x32_bf16 v[116:119], v[252:255], v[188:191], v[116:119]\n"
"v_mfma_f32_16x16x32_bf16 v[120:123], v[252:255], v[192:195], v[120:123]\n"
"ds_read_b64_tr_b16 v[240:241], v0 offset:28672\n"
"ds_read_b64_tr_b16 v[242:243], v0 offset:30720\n"
"s_waitcnt lgkmcnt(6)\n"
"v_mfma_f32_16x16x32_bf16 v[80:83], v[224:227], v[200:203], v[80:83]\n"
"v_mfma_f32_16x16x32_bf16 v[84:87], v[224:227], v[204:207], v[84:87]\n"
"ds_read_b64_tr_b16 v[244:245], v1 offset:28672\n"
"ds_read_b64_tr_b16 v[246:247], v1 offset:30720\n"
"v_mfma_f32_16x16x32_bf16 v[92:95], v[228:231], v[200:203], v[92:95]\n"
"v_mfma_f32_16x16x32_bf16 v[96:99], v[228:231], v[204:207], v[96:99]\n"
"ds_read_b64_tr_b16 v[248:249], v2 offset:28672\n"
"ds_read_b64_tr_b16 v[250:251], v2 offset:30720\n"
"ds_read_b64_tr_b16 v[252:253], v3 offset:28672\n"
"ds_read_b64_tr_b16 v[254:255], v3 offset:30720\n"
"s_waitcnt lgkmcnt(7)\n"
"v_mfma_f32_16x16x32_bf16 v[104:107], v[232:235], v[200:203], v[104:107]\n"
"v_mfma_f32_16x16x32_bf16 v[108:111], v[232:235], v[204:207], v[108:111]\n"
"v_mfma_f32_16x16x32_bf16 v[116:119], v[236:239], v[200:203], v[116:119]\n"
"v_mfma_f32_16x16x32_bf16 v[120:123], v[236:239], v[204:207], v[120:123]\n"
"s_waitcnt lgkmcnt(4)\n"
"v_mfma_f32_16x16x32_bf16 v[80:83], v[240:243], v[212:215], v[80:83]\n"
"v_mfma_f32_16x16x32_bf16 v[84:87], v[240:243], v[216:219], v[84:87]\n"
"s_waitcnt vmcnt(6) lgkmcnt(0)\n"
"s_barrier\n"
"v_mfma_f32_16x16x32_bf16 v[92:95], v[244:247], v[212:215], v[92:95]\n"
"v_add_u32 v0, 32768, v0\n"
"v_add_u32 v1, 32768, v1\n"
"v_mfma_f32_16x16x32_bf16 v[96:99], v[244:247], v[216:219], v[96:99]\n"
"v_add_u32 v2, 32768, v2\n"
"v_add_u32 v3, 32768, v3\n"
"ds_read_b64_tr_b16 v[224:225], v0 offset:0\n"
"ds_read_b64_tr_b16 v[226:227], v0 offset:2048\n"
"v_mfma_f32_16x16x32_bf16 v[104:107], v[248:251], v[212:215], v[104:107]\n"
"ds_read_b64_tr_b16 v[228:229], v1 offset:0\n"
"ds_read_b64_tr_b16 v[230:231], v1 offset:2048\n"
"v_mfma_f32_16x16x32_bf16 v[108:111], v[248:251], v[216:219], v[108:111]\n"
"ds_read_b64_tr_b16 v[232:233], v2 offset:0\n"
"ds_read_b64_tr_b16 v[234:235], v2 offset:2048\n"
"ds_read_b64_tr_b16 v[236:237], v3 offset:0\n"
"ds_read_b64_tr_b16 v[238:239], v3 offset:2048\n"
"v_mfma_f32_16x16x32_bf16 v[116:119], v[252:255], v[212:215], v[116:119]\n"
"v_mfma_f32_16x16x32_bf16 v[120:123], v[252:255], v[216:219], v[120:123]\n"
"s_waitcnt lgkmcnt(6)\n"
"v_mfma_f32_16x16x32_bf16 v[32:35], v[224:227], v[128:131], 0\n"
"ds_read_b64_tr_b16 v[240:241], v0 offset:4096\n"
"ds_read_b64_tr_b16 v[242:243], v0 offset:6144\n"
"v_mfma_f32_16x16x32_bf16 v[36:39], v[224:227], v[132:135], 0\n"
"ds_read_b64_tr_b16 v[244:245], v1 offset:4096\n"
"ds_read_b64_tr_b16 v[246:247], v1 offset:6144\n"
"s_waitcnt lgkmcnt(7)\n"
"v_mfma_f32_16x16x32_bf16 v[44:47], v[228:231], v[128:131], 0\n"
"v_mfma_f32_16x16x32_bf16 v[48:51], v[228:231], v[132:135], 0\n"
"ds_read_b64_tr_b16 v[248:249], v2 offset:4096\n"
"ds_read_b64_tr_b16 v[250:251], v2 offset:6144\n"
"s_waitcnt lgkmcnt(6)\n"
"v_mfma_f32_16x16x32_bf16 v[56:59], v[232:235], v[128:131], 0\n"
"s_mov_b32 s46, 6144\n"
"v_mfma_f32_16x16x32_bf16 v[60:63], v[232:235], v[132:135], 0\n"
"ds_read_b64_tr_b16 v[252:253], v3 offset:4096\n"
"ds_read_b64_tr_b16 v[254:255], v3 offset:6144\n"
"v_mul_f32 v80, v80, v8\n"
"v_mul_f32 v81, v81, v8\n"
"v_mfma_f32_16x16x32_bf16 v[68:71], v[236:239], v[128:131], 0\n"
"v_mul_f32 v82, v82, v8\n"
"v_mfma_f32_16x16x32_bf16 v[72:75], v[236:239], v[132:135], 0\n"
"ds_read_b64_tr_b16 v[224:225], v0 offset:8192\n"
"ds_read_b64_tr_b16 v[226:227], v0 offset:10240\n"
"v_mul_f32 v83, v83, v8\n"
"v_mul_f32 v92, v92, v8\n"
"s_waitcnt lgkmcnt(4)\n"
"v_mfma_f32_16x16x32_bf16 v[32:35], v[240:243], v[140:143], v[32:35]\n"
"v_mul_f32 v93, v93, v8\n"
"v_mfma_f32_16x16x32_bf16 v[36:39], v[240:243], v[144:147], v[36:39]\n"
"ds_read_b64_tr_b16 v[228:229], v1 offset:8192\n"
"ds_read_b64_tr_b16 v[230:231], v1 offset:10240\n"
"v_mul_f32 v94, v94, v8\n"
"v_mul_f32 v95, v95, v8\n"
"v_mfma_f32_16x16x32_bf16 v[44:47], v[244:247], v[140:143], v[44:47]\n"
"v_cvt_pk_bf16_f32 v80, v80, v81\n"
"v_mfma_f32_16x16x32_bf16 v[48:51], v[244:247], v[144:147], v[48:51]\n"
"ds_read_b64_tr_b16 v[232:233], v2 offset:8192\n"
"ds_read_b64_tr_b16 v[234:235], v2 offset:10240\n"
"v_cvt_pk_bf16_f32 v81, v82, v83\n"
"v_cvt_pk_bf16_f32 v82, v92, v93\n"
"v_mfma_f32_16x16x32_bf16 v[56:59], v[248:251], v[140:143], v[56:59]\n"
"v_cvt_pk_bf16_f32 v83, v94, v95\n"
"v_mfma_f32_16x16x32_bf16 v[60:63], v[248:251], v[144:147], v[60:63]\n"
"ds_read_b64_tr_b16 v[236:237], v3 offset:8192\n"
"ds_read_b64_tr_b16 v[238:239], v3 offset:10240\n"
"buffer_store_dwordx4 v[80:83], v5, %5, s46 offen offset:0 sc0 sc1\n"
"v_mul_f32 v84, v84, v9\n"
"s_waitcnt lgkmcnt(4)\n"
"v_mfma_f32_16x16x32_bf16 v[68:71], v[252:255], v[140:143], v[68:71]\n"
"v_mul_f32 v85, v85, v9\n"
"v_mfma_f32_16x16x32_bf16 v[72:75], v[252:255], v[144:147], v[72:75]\n"
"ds_read_b64_tr_b16 v[240:241], v0 offset:12288\n"
"ds_read_b64_tr_b16 v[242:243], v0 offset:14336\n"
"v_mul_f32 v86, v86, v9\n"
"v_mul_f32 v87, v87, v9\n"
"v_mfma_f32_16x16x32_bf16 v[32:35], v[224:227], v[152:155], v[32:35]\n"
"v_mul_f32 v96, v96, v9\n"
"v_mfma_f32_16x16x32_bf16 v[36:39], v[224:227], v[156:159], v[36:39]\n"
"ds_read_b64_tr_b16 v[244:245], v1 offset:12288\n"
"ds_read_b64_tr_b16 v[246:247], v1 offset:14336\n"
"v_mul_f32 v97, v97, v9\n"
"v_mul_f32 v98, v98, v9\n"
"v_mfma_f32_16x16x32_bf16 v[44:47], v[228:231], v[152:155], v[44:47]\n"
"v_mul_f32 v99, v99, v9\n"
"v_mfma_f32_16x16x32_bf16 v[48:51], v[228:231], v[156:159], v[48:51]\n"
"ds_read_b64_tr_b16 v[248:249], v2 offset:12288\n"
"ds_read_b64_tr_b16 v[250:251], v2 offset:14336\n"
"v_cvt_pk_bf16_f32 v84, v84, v85\n"
"v_cvt_pk_bf16_f32 v85, v86, v87\n"
"s_waitcnt lgkmcnt(4)\n"
"v_mfma_f32_16x16x32_bf16 v[56:59], v[232:235], v[152:155], v[56:59]\n"
"v_cvt_pk_bf16_f32 v86, v96, v97\n"
"v_mfma_f32_16x16x32_bf16 v[60:63], v[232:235], v[156:159], v[60:63]\n"
"ds_read_b64_tr_b16 v[252:253], v3 offset:12288\n"
"ds_read_b64_tr_b16 v[254:255], v3 offset:14336\n"
"v_cvt_pk_bf16_f32 v87, v98, v99\n"
"buffer_store_dwordx4 v[84:87], v6, %5, s46 offen offset:0 sc0 sc1\n"
"v_mfma_f32_16x16x32_bf16 v[68:71], v[236:239], v[152:155], v[68:71]\n"
"v_mul_f32 v104, v104, v8\n"
"v_mfma_f32_16x16x32_bf16 v[72:75], v[236:239], v[156:159], v[72:75]\n"
"ds_read_b64_tr_b16 v[224:225], v0 offset:16384\n"
"ds_read_b64_tr_b16 v[226:227], v0 offset:18432\n"
"v_mul_f32 v105, v105, v8\n"
"v_mul_f32 v106, v106, v8\n"
"v_mfma_f32_16x16x32_bf16 v[32:35], v[240:243], v[164:167], v[32:35]\n"
"v_mul_f32 v107, v107, v8\n"
"v_mfma_f32_16x16x32_bf16 v[36:39], v[240:243], v[168:171], v[36:39]\n"
"ds_read_b64_tr_b16 v[228:229], v1 offset:16384\n"
"ds_read_b64_tr_b16 v[230:231], v1 offset:18432\n"
"v_mul_f32 v116, v116, v8\n"
"v_mul_f32 v117, v117, v8\n"
"s_waitcnt lgkmcnt(4)\n"
"v_mfma_f32_16x16x32_bf16 v[44:47], v[244:247], v[164:167], v[44:47]\n"
"v_mul_f32 v118, v118, v8\n"
"v_mfma_f32_16x16x32_bf16 v[48:51], v[244:247], v[168:171], v[48:51]\n"
"ds_read_b64_tr_b16 v[232:233], v2 offset:16384\n"
"ds_read_b64_tr_b16 v[234:235], v2 offset:18432\n"
"v_mul_f32 v119, v119, v8\n"
"v_cvt_pk_bf16_f32 v104, v104, v105\n"
"v_mfma_f32_16x16x32_bf16 v[56:59], v[248:251], v[164:167], v[56:59]\n"
"v_cvt_pk_bf16_f32 v105, v106, v107\n"
"v_mfma_f32_16x16x32_bf16 v[60:63], v[248:251], v[168:171], v[60:63]\n"
"ds_read_b64_tr_b16 v[236:237], v3 offset:16384\n"
"ds_read_b64_tr_b16 v[238:239], v3 offset:18432\n"
"v_cvt_pk_bf16_f32 v106, v116, v117\n"
"v_cvt_pk_bf16_f32 v107, v118, v119\n"
"v_mfma_f32_16x16x32_bf16 v[68:71], v[252:255], v[164:167], v[68:71]\n"
"buffer_store_dwordx4 v[104:107], v5, %5, s46 offen offset:1024 sc0 sc1\n"
"v_mfma_f32_16x16x32_bf16 v[72:75], v[252:255], v[168:171], v[72:75]\n"
"ds_read_b64_tr_b16 v[240:241], v0 offset:20480\n"
"ds_read_b64_tr_b16 v[242:243], v0 offset:22528\n"
"v_mul_f32 v108, v108, v9\n"
"v_mul_f32 v109, v109, v9\n"
"s_waitcnt lgkmcnt(4)\n"
"v_mfma_f32_16x16x32_bf16 v[32:35], v[224:227], v[176:179], v[32:35]\n"
"v_mul_f32 v110, v110, v9\n"
"v_mfma_f32_16x16x32_bf16 v[36:39], v[224:227], v[180:183], v[36:39]\n"
"ds_read_b64_tr_b16 v[244:245], v1 offset:20480\n"
"ds_read_b64_tr_b16 v[246:247], v1 offset:22528\n"
"v_mul_f32 v111, v111, v9\n"
"v_mul_f32 v120, v120, v9\n"
"v_mfma_f32_16x16x32_bf16 v[44:47], v[228:231], v[176:179], v[44:47]\n"
"v_mul_f32 v121, v121, v9\n"
"v_mfma_f32_16x16x32_bf16 v[48:51], v[228:231], v[180:183], v[48:51]\n"
"ds_read_b64_tr_b16 v[248:249], v2 offset:20480\n"
"ds_read_b64_tr_b16 v[250:251], v2 offset:22528\n"
"v_mul_f32 v122, v122, v9\n"
"v_mul_f32 v123, v123, v9\n"
"v_mfma_f32_16x16x32_bf16 v[56:59], v[232:235], v[176:179], v[56:59]\n"
"v_cvt_pk_bf16_f32 v108, v108, v109\n"
"v_mfma_f32_16x16x32_bf16 v[60:63], v[232:235], v[180:183], v[60:63]\n"
"ds_read_b64_tr_b16 v[252:253], v3 offset:20480\n"
"ds_read_b64_tr_b16 v[254:255], v3 offset:22528\n"
"v_cvt_pk_bf16_f32 v109, v110, v111\n"
"v_cvt_pk_bf16_f32 v110, v120, v121\n"
"s_waitcnt lgkmcnt(4)\n"
"v_mfma_f32_16x16x32_bf16 v[68:71], v[236:239], v[176:179], v[68:71]\n"
"v_cvt_pk_bf16_f32 v111, v122, v123\n"
"v_mfma_f32_16x16x32_bf16 v[72:75], v[236:239], v[180:183], v[72:75]\n"
"ds_read_b64_tr_b16 v[224:225], v0 offset:24576\n"
"ds_read_b64_tr_b16 v[226:227], v0 offset:26624\n"
"buffer_store_dwordx4 v[108:111], v6, %5, s46 offen offset:1024 sc0 sc1\n"
"v_mfma_f32_16x16x32_bf16 v[32:35], v[240:243], v[188:191], v[32:35]\n"
"v_mfma_f32_16x16x32_bf16 v[36:39], v[240:243], v[192:195], v[36:39]\n"
"ds_read_b64_tr_b16 v[228:229], v1 offset:24576\n"
"ds_read_b64_tr_b16 v[230:231], v1 offset:26624\n"
"v_mfma_f32_16x16x32_bf16 v[44:47], v[244:247], v[188:191], v[44:47]\n"
"v_mfma_f32_16x16x32_bf16 v[48:51], v[244:247], v[192:195], v[48:51]\n"
"ds_read_b64_tr_b16 v[232:233], v2 offset:24576\n"
"ds_read_b64_tr_b16 v[234:235], v2 offset:26624\n"
"s_waitcnt lgkmcnt(6)\n"
"v_mfma_f32_16x16x32_bf16 v[56:59], v[248:251], v[188:191], v[56:59]\n"
"v_mfma_f32_16x16x32_bf16 v[60:63], v[248:251], v[192:195], v[60:63]\n"
"ds_read_b64_tr_b16 v[236:237], v3 offset:24576\n"
"ds_read_b64_tr_b16 v[238:239], v3 offset:26624\n"
"v_mfma_f32_16x16x32_bf16 v[68:71], v[252:255], v[188:191], v[68:71]\n"
"v_mfma_f32_16x16x32_bf16 v[72:75], v[252:255], v[192:195], v[72:75]\n"
"ds_read_b64_tr_b16 v[240:241], v0 offset:28672\n"
"ds_read_b64_tr_b16 v[242:243], v0 offset:30720\n"
"s_waitcnt lgkmcnt(6)\n"
"v_mfma_f32_16x16x32_bf16 v[32:35], v[224:227], v[200:203], v[32:35]\n"
"v_mfma_f32_16x16x32_bf16 v[36:39], v[224:227], v[204:207], v[36:39]\n"
"ds_read_b64_tr_b16 v[244:245], v1 offset:28672\n"
"ds_read_b64_tr_b16 v[246:247], v1 offset:30720\n"
"v_mfma_f32_16x16x32_bf16 v[44:47], v[228:231], v[200:203], v[44:47]\n"
"v_mfma_f32_16x16x32_bf16 v[48:51], v[228:231], v[204:207], v[48:51]\n"
"ds_read_b64_tr_b16 v[248:249], v2 offset:28672\n"
"ds_read_b64_tr_b16 v[250:251], v2 offset:30720\n"
"ds_read_b64_tr_b16 v[252:253], v3 offset:28672\n"
"ds_read_b64_tr_b16 v[254:255], v3 offset:30720\n"
"s_waitcnt lgkmcnt(7)\n"
"v_mfma_f32_16x16x32_bf16 v[56:59], v[232:235], v[200:203], v[56:59]\n"
"v_mfma_f32_16x16x32_bf16 v[60:63], v[232:235], v[204:207], v[60:63]\n"
"v_mfma_f32_16x16x32_bf16 v[68:71], v[236:239], v[200:203], v[68:71]\n"
"v_mfma_f32_16x16x32_bf16 v[72:75], v[236:239], v[204:207], v[72:75]\n"
"s_waitcnt lgkmcnt(4)\n"
"v_mfma_f32_16x16x32_bf16 v[32:35], v[240:243], v[212:215], v[32:35]\n"
"v_mfma_f32_16x16x32_bf16 v[36:39], v[240:243], v[216:219], v[36:39]\n"
"s_waitcnt vmcnt(4) lgkmcnt(0)\n"
"s_barrier\n"
"v_mfma_f32_16x16x32_bf16 v[44:47], v[244:247], v[212:215], v[44:47]\n"
"v_add_u32 v0, 65536, v0\n"
"v_add_u32 v1, 65536, v1\n"
"v_mfma_f32_16x16x32_bf16 v[48:51], v[244:247], v[216:219], v[48:51]\n"
"v_add_u32 v2, 65536, v2\n"
"v_add_u32 v3, 65536, v3\n"
"ds_read_b64_tr_b16 v[224:225], v0 offset:0\n"
"ds_read_b64_tr_b16 v[226:227], v0 offset:2048\n"
"v_mfma_f32_16x16x32_bf16 v[56:59], v[248:251], v[212:215], v[56:59]\n"
"ds_read_b64_tr_b16 v[228:229], v1 offset:0\n"
"ds_read_b64_tr_b16 v[230:231], v1 offset:2048\n"
"v_mfma_f32_16x16x32_bf16 v[60:63], v[248:251], v[216:219], v[60:63]\n"
"ds_read_b64_tr_b16 v[232:233], v2 offset:0\n"
"ds_read_b64_tr_b16 v[234:235], v2 offset:2048\n"
"ds_read_b64_tr_b16 v[236:237], v3 offset:0\n"
"ds_read_b64_tr_b16 v[238:239], v3 offset:2048\n"
"v_mfma_f32_16x16x32_bf16 v[68:71], v[252:255], v[212:215], v[68:71]\n"
"v_mfma_f32_16x16x32_bf16 v[72:75], v[252:255], v[216:219], v[72:75]\n"
"s_waitcnt lgkmcnt(6)\n"
"v_mfma_f32_16x16x32_bf16 v[80:83], v[224:227], v[128:131], 0\n"
"ds_read_b64_tr_b16 v[240:241], v0 offset:4096\n"
"ds_read_b64_tr_b16 v[242:243], v0 offset:6144\n"
"v_mfma_f32_16x16x32_bf16 v[84:87], v[224:227], v[132:135], 0\n"
"ds_read_b64_tr_b16 v[244:245], v1 offset:4096\n"
"ds_read_b64_tr_b16 v[246:247], v1 offset:6144\n"
"s_waitcnt lgkmcnt(7)\n"
"v_mfma_f32_16x16x32_bf16 v[92:95], v[228:231], v[128:131], 0\n"
"v_mfma_f32_16x16x32_bf16 v[96:99], v[228:231], v[132:135], 0\n"
"ds_read_b64_tr_b16 v[248:249], v2 offset:4096\n"
"ds_read_b64_tr_b16 v[250:251], v2 offset:6144\n"
"s_waitcnt lgkmcnt(6)\n"
"v_mfma_f32_16x16x32_bf16 v[104:107], v[232:235], v[128:131], 0\n"
"s_mov_b32 s46, 8192\n"
"v_mfma_f32_16x16x32_bf16 v[108:111], v[232:235], v[132:135], 0\n"
"ds_read_b64_tr_b16 v[252:253], v3 offset:4096\n"
"ds_read_b64_tr_b16 v[254:255], v3 offset:6144\n"
"v_mul_f32 v32, v32, v8\n"
"v_mul_f32 v33, v33, v8\n"
"v_mfma_f32_16x16x32_bf16 v[116:119], v[236:239], v[128:131], 0\n"
"v_mul_f32 v34, v34, v8\n"
"v_mfma_f32_16x16x32_bf16 v[120:123], v[236:239], v[132:135], 0\n"
"ds_read_b64_tr_b16 v[224:225], v0 offset:8192\n"
"ds_read_b64_tr_b16 v[226:227], v0 offset:10240\n"
"v_mul_f32 v35, v35, v8\n"
"v_mul_f32 v44, v44, v8\n"
"s_waitcnt lgkmcnt(4)\n"
"v_mfma_f32_16x16x32_bf16 v[80:83], v[240:243], v[140:143], v[80:83]\n"
"v_mul_f32 v45, v45, v8\n"
"v_mfma_f32_16x16x32_bf16 v[84:87], v[240:243], v[144:147], v[84:87]\n"
"ds_read_b64_tr_b16 v[228:229], v1 offset:8192\n"
"ds_read_b64_tr_b16 v[230:231], v1 offset:10240\n"
"v_mul_f32 v46, v46, v8\n"
"v_mul_f32 v47, v47, v8\n"
"v_mfma_f32_16x16x32_bf16 v[92:95], v[244:247], v[140:143], v[92:95]\n"
"v_cvt_pk_bf16_f32 v32, v32, v33\n"
"v_mfma_f32_16x16x32_bf16 v[96:99], v[244:247], v[144:147], v[96:99]\n"
"ds_read_b64_tr_b16 v[232:233], v2 offset:8192\n"
"ds_read_b64_tr_b16 v[234:235], v2 offset:10240\n"
"v_cvt_pk_bf16_f32 v33, v34, v35\n"
"v_cvt_pk_bf16_f32 v34, v44, v45\n"
"v_mfma_f32_16x16x32_bf16 v[104:107], v[248:251], v[140:143], v[104:107]\n"
"v_cvt_pk_bf16_f32 v35, v46, v47\n"
"v_mfma_f32_16x16x32_bf16 v[108:111], v[248:251], v[144:147], v[108:111]\n"
"ds_read_b64_tr_b16 v[236:237], v3 offset:8192\n"
"ds_read_b64_tr_b16 v[238:239], v3 offset:10240\n"
"buffer_store_dwordx4 v[32:35], v5, %5, s46 offen offset:0 sc0 sc1\n"
"v_mul_f32 v36, v36, v9\n"
"s_waitcnt lgkmcnt(4)\n"
"v_mfma_f32_16x16x32_bf16 v[116:119], v[252:255], v[140:143], v[116:119]\n"
"v_mul_f32 v37, v37, v9\n"
"v_mfma_f32_16x16x32_bf16 v[120:123], v[252:255], v[144:147], v[120:123]\n"
"ds_read_b64_tr_b16 v[240:241], v0 offset:12288\n"
"ds_read_b64_tr_b16 v[242:243], v0 offset:14336\n"
"v_mul_f32 v38, v38, v9\n"
"v_mul_f32 v39, v39, v9\n"
"v_mfma_f32_16x16x32_bf16 v[80:83], v[224:227], v[152:155], v[80:83]\n"
"v_mul_f32 v48, v48, v9\n"
"v_mfma_f32_16x16x32_bf16 v[84:87], v[224:227], v[156:159], v[84:87]\n"
"ds_read_b64_tr_b16 v[244:245], v1 offset:12288\n"
"ds_read_b64_tr_b16 v[246:247], v1 offset:14336\n"
"v_mul_f32 v49, v49, v9\n"
"v_mul_f32 v50, v50, v9\n"
"v_mfma_f32_16x16x32_bf16 v[92:95], v[228:231], v[152:155], v[92:95]\n"
"v_mul_f32 v51, v51, v9\n"
"v_mfma_f32_16x16x32_bf16 v[96:99], v[228:231], v[156:159], v[96:99]\n"
"ds_read_b64_tr_b16 v[248:249], v2 offset:12288\n"
"ds_read_b64_tr_b16 v[250:251], v2 offset:14336\n"
"v_cvt_pk_bf16_f32 v36, v36, v37\n"
"v_cvt_pk_bf16_f32 v37, v38, v39\n"
"s_waitcnt lgkmcnt(4)\n"
"v_mfma_f32_16x16x32_bf16 v[104:107], v[232:235], v[152:155], v[104:107]\n"
"v_cvt_pk_bf16_f32 v38, v48, v49\n"
"v_mfma_f32_16x16x32_bf16 v[108:111], v[232:235], v[156:159], v[108:111]\n"
"ds_read_b64_tr_b16 v[252:253], v3 offset:12288\n"
"ds_read_b64_tr_b16 v[254:255], v3 offset:14336\n"
"v_cvt_pk_bf16_f32 v39, v50, v51\n"
"buffer_store_dwordx4 v[36:39], v6, %5, s46 offen offset:0 sc0 sc1\n"
"v_mfma_f32_16x16x32_bf16 v[116:119], v[236:239], v[152:155], v[116:119]\n"
"v_mul_f32 v56, v56, v8\n"
"v_mfma_f32_16x16x32_bf16 v[120:123], v[236:239], v[156:159], v[120:123]\n"
"ds_read_b64_tr_b16 v[224:225], v0 offset:16384\n"
"ds_read_b64_tr_b16 v[226:227], v0 offset:18432\n"
"v_mul_f32 v57, v57, v8\n"
"v_mul_f32 v58, v58, v8\n"
"v_mfma_f32_16x16x32_bf16 v[80:83], v[240:243], v[164:167], v[80:83]\n"
"v_mul_f32 v59, v59, v8\n"
"v_mfma_f32_16x16x32_bf16 v[84:87], v[240:243], v[168:171], v[84:87]\n"
"ds_read_b64_tr_b16 v[228:229], v1 offset:16384\n"
"ds_read_b64_tr_b16 v[230:231], v1 offset:18432\n"
"v_mul_f32 v68, v68, v8\n"
"v_mul_f32 v69, v69, v8\n"
"s_waitcnt lgkmcnt(4)\n"
"v_mfma_f32_16x16x32_bf16 v[92:95], v[244:247], v[164:167], v[92:95]\n"
"v_mul_f32 v70, v70, v8\n"
"v_mfma_f32_16x16x32_bf16 v[96:99], v[244:247], v[168:171], v[96:99]\n"
"ds_read_b64_tr_b16 v[232:233], v2 offset:16384\n"
"ds_read_b64_tr_b16 v[234:235], v2 offset:18432\n"
"v_mul_f32 v71, v71, v8\n"
"v_cvt_pk_bf16_f32 v56, v56, v57\n"
"v_mfma_f32_16x16x32_bf16 v[104:107], v[248:251], v[164:167], v[104:107]\n"
"v_cvt_pk_bf16_f32 v57, v58, v59\n"
"v_mfma_f32_16x16x32_bf16 v[108:111], v[248:251], v[168:171], v[108:111]\n"
"ds_read_b64_tr_b16 v[236:237], v3 offset:16384\n"
"ds_read_b64_tr_b16 v[238:239], v3 offset:18432\n"
"v_cvt_pk_bf16_f32 v58, v68, v69\n"
"v_cvt_pk_bf16_f32 v59, v70, v71\n"
"v_mfma_f32_16x16x32_bf16 v[116:119], v[252:255], v[164:167], v[116:119]\n"
"buffer_store_dwordx4 v[56:59], v5, %5, s46 offen offset:1024 sc0 sc1\n"
"v_mfma_f32_16x16x32_bf16 v[120:123], v[252:255], v[168:171], v[120:123]\n"
"ds_read_b64_tr_b16 v[240:241], v0 offset:20480\n"
"ds_read_b64_tr_b16 v[242:243], v0 offset:22528\n"
"v_mul_f32 v60, v60, v9\n"
"v_mul_f32 v61, v61, v9\n"
"s_waitcnt lgkmcnt(4)\n"
"v_mfma_f32_16x16x32_bf16 v[80:83], v[224:227], v[176:179], v[80:83]\n"
"v_mul_f32 v62, v62, v9\n"
"v_mfma_f32_16x16x32_bf16 v[84:87], v[224:227], v[180:183], v[84:87]\n"
"ds_read_b64_tr_b16 v[244:245], v1 offset:20480\n"
"ds_read_b64_tr_b16 v[246:247], v1 offset:22528\n"
"v_mul_f32 v63, v63, v9\n"
"v_mul_f32 v72, v72, v9\n"
"v_mfma_f32_16x16x32_bf16 v[92:95], v[228:231], v[176:179], v[92:95]\n"
"v_mul_f32 v73, v73, v9\n"
"v_mfma_f32_16x16x32_bf16 v[96:99], v[228:231], v[180:183], v[96:99]\n"
"ds_read_b64_tr_b16 v[248:249], v2 offset:20480\n"
"ds_read_b64_tr_b16 v[250:251], v2 offset:22528\n"
"v_mul_f32 v74, v74, v9\n"
"v_mul_f32 v75, v75, v9\n"
"v_mfma_f32_16x16x32_bf16 v[104:107], v[232:235], v[176:179], v[104:107]\n"
"v_cvt_pk_bf16_f32 v60, v60, v61\n"
"v_mfma_f32_16x16x32_bf16 v[108:111], v[232:235], v[180:183], v[108:111]\n"
"ds_read_b64_tr_b16 v[252:253], v3 offset:20480\n"
"ds_read_b64_tr_b16 v[254:255], v3 offset:22528\n"
"v_cvt_pk_bf16_f32 v61, v62, v63\n"
"v_cvt_pk_bf16_f32 v62, v72, v73\n"
"s_waitcnt lgkmcnt(4)\n"
"v_mfma_f32_16x16x32_bf16 v[116:119], v[236:239], v[176:179], v[116:119]\n"
"v_cvt_pk_bf16_f32 v63, v74, v75\n"
"v_mfma_f32_16x16x32_bf16 v[120:123], v[236:239], v[180:183], v[120:123]\n"
"ds_read_b64_tr_b16 v[224:225], v0 offset:24576\n"
"ds_read_b64_tr_b16 v[226:227], v0 offset:26624\n"
"buffer_store_dwordx4 v[60:63], v6, %5, s46 offen offset:1024 sc0 sc1\n"
"v_mfma_f32_16x16x32_bf16 v[80:83], v[240:243], v[188:191], v[80:83]\n"
"v_mfma_f32_16x16x32_bf16 v[84:87], v[240:243], v[192:195], v[84:87]\n"
"ds_read_b64_tr_b16 v[228:229], v1 offset:24576\n"
"ds_read_b64_tr_b16 v[230:231], v1 offset:26624\n"
"v_mfma_f32_16x16x32_bf16 v[92:95], v[244:247], v[188:191], v[92:95]\n"
"v_mfma_f32_16x16x32_bf16 v[96:99], v[244:247], v[192:195], v[96:99]\n"
"ds_read_b64_tr_b16 v[232:233], v2 offset:24576\n"
"ds_read_b64_tr_b16 v[234:235], v2 offset:26624\n"
"s_waitcnt lgkmcnt(6)\n"
"v_mfma_f32_16x16x32_bf16 v[104:107], v[248:251], v[188:191], v[104:107]\n"
"v_mfma_f32_16x16x32_bf16 v[108:111], v[248:251], v[192:195], v[108:111]\n"
"ds_read_b64_tr_b16 v[236:237], v3 offset:24576\n"
"ds_read_b64_tr_b16 v[238:239], v3 offset:26624\n"
"v_mfma_f32_16x16x32_bf16 v[116:119], v[252:255], v[188:191], v[116:119]\n"
"v_mfma_f32_16x16x32_bf16 v[120:123], v[252:255], v[192:195], v[120:123]\n"
"ds_read_b64_tr_b16 v[240:241], v0 offset:28672\n"
"ds_read_b64_tr_b16 v[242:243], v0 offset:30720\n"
"s_waitcnt lgkmcnt(6)\n"
"v_mfma_f32_16x16x32_bf16 v[80:83], v[224:227], v[200:203], v[80:83]\n"
"v_mfma_f32_16x16x32_bf16 v[84:87], v[224:227], v[204:207], v[84:87]\n"
"ds_read_b64_tr_b16 v[244:245], v1 offset:28672\n"
"ds_read_b64_tr_b16 v[246:247], v1 offset:30720\n"
"v_mfma_f32_16x16x32_bf16 v[92:95], v[228:231], v[200:203], v[92:95]\n"
"v_mfma_f32_16x16x32_bf16 v[96:99], v[228:231], v[204:207], v[96:99]\n"
"ds_read_b64_tr_b16 v[248:249], v2 offset:28672\n"
"ds_read_b64_tr_b16 v[250:251], v2 offset:30720\n"
"ds_read_b64_tr_b16 v[252:253], v3 offset:28672\n"
"ds_read_b64_tr_b16 v[254:255], v3 offset:30720\n"
"s_waitcnt lgkmcnt(7)\n"
"v_mfma_f32_16x16x32_bf16 v[104:107], v[232:235], v[200:203], v[104:107]\n"
"v_mfma_f32_16x16x32_bf16 v[108:111], v[232:235], v[204:207], v[108:111]\n"
"v_mfma_f32_16x16x32_bf16 v[116:119], v[236:239], v[200:203], v[116:119]\n"
"v_mfma_f32_16x16x32_bf16 v[120:123], v[236:239], v[204:207], v[120:123]\n"
"s_waitcnt lgkmcnt(4)\n"
"v_mfma_f32_16x16x32_bf16 v[80:83], v[240:243], v[212:215], v[80:83]\n"
"v_mfma_f32_16x16x32_bf16 v[84:87], v[240:243], v[216:219], v[84:87]\n"
"s_waitcnt vmcnt(4) lgkmcnt(0)\n"
"s_barrier\n"
"v_mfma_f32_16x16x32_bf16 v[92:95], v[244:247], v[212:215], v[92:95]\n"
"v_subrev_u32 v0, 131072, v0\n"
"v_subrev_u32 v1, 131072, v1\n"
"v_mfma_f32_16x16x32_bf16 v[96:99], v[244:247], v[216:219], v[96:99]\n"
"v_subrev_u32 v2, 131072, v2\n"
"v_subrev_u32 v3, 131072, v3\n"
"ds_read_b64_tr_b16 v[224:225], v0 offset:0\n"
"ds_read_b64_tr_b16 v[226:227], v0 offset:2048\n"
"v_mfma_f32_16x16x32_bf16 v[104:107], v[248:251], v[212:215], v[104:107]\n"
"ds_read_b64_tr_b16 v[228:229], v1 offset:0\n"
"ds_read_b64_tr_b16 v[230:231], v1 offset:2048\n"
"v_mfma_f32_16x16x32_bf16 v[108:111], v[248:251], v[216:219], v[108:111]\n"
"ds_read_b64_tr_b16 v[232:233], v2 offset:0\n"
"ds_read_b64_tr_b16 v[234:235], v2 offset:2048\n"
"ds_read_b64_tr_b16 v[236:237], v3 offset:0\n"
"ds_read_b64_tr_b16 v[238:239], v3 offset:2048\n"
"v_mfma_f32_16x16x32_bf16 v[116:119], v[252:255], v[212:215], v[116:119]\n"
"v_mfma_f32_16x16x32_bf16 v[120:123], v[252:255], v[216:219], v[120:123]\n"
"s_waitcnt lgkmcnt(6)\n"
"v_mfma_f32_16x16x32_bf16 v[32:35], v[224:227], v[128:131], 0\n"
"ds_read_b64_tr_b16 v[240:241], v0 offset:4096\n"
"ds_read_b64_tr_b16 v[242:243], v0 offset:6144\n"
"v_mfma_f32_16x16x32_bf16 v[36:39], v[224:227], v[132:135], 0\n"
"ds_read_b64_tr_b16 v[244:245], v1 offset:4096\n"
"ds_read_b64_tr_b16 v[246:247], v1 offset:6144\n"
"s_waitcnt lgkmcnt(7)\n"
"v_mfma_f32_16x16x32_bf16 v[44:47], v[228:231], v[128:131], 0\n"
"v_mfma_f32_16x16x32_bf16 v[48:51], v[228:231], v[132:135], 0\n"
"ds_read_b64_tr_b16 v[248:249], v2 offset:4096\n"
"ds_read_b64_tr_b16 v[250:251], v2 offset:6144\n"
"s_waitcnt lgkmcnt(6)\n"
"v_mfma_f32_16x16x32_bf16 v[56:59], v[232:235], v[128:131], 0\n"
"s_mov_b32 s46, 10240\n"
"v_mfma_f32_16x16x32_bf16 v[60:63], v[232:235], v[132:135], 0\n"
"ds_read_b64_tr_b16 v[252:253], v3 offset:4096\n"
"ds_read_b64_tr_b16 v[254:255], v3 offset:6144\n"
"v_mul_f32 v80, v80, v8\n"
"v_mul_f32 v81, v81, v8\n"
"v_mfma_f32_16x16x32_bf16 v[68:71], v[236:239], v[128:131], 0\n"
"v_mul_f32 v82, v82, v8\n"
"v_mfma_f32_16x16x32_bf16 v[72:75], v[236:239], v[132:135], 0\n"
"ds_read_b64_tr_b16 v[224:225], v0 offset:8192\n"
"ds_read_b64_tr_b16 v[226:227], v0 offset:10240\n"
"v_mul_f32 v83, v83, v8\n"
"v_mul_f32 v92, v92, v8\n"
"s_waitcnt lgkmcnt(4)\n"
"v_mfma_f32_16x16x32_bf16 v[32:35], v[240:243], v[140:143], v[32:35]\n"
"v_mul_f32 v93, v93, v8\n"
"v_mfma_f32_16x16x32_bf16 v[36:39], v[240:243], v[144:147], v[36:39]\n"
"ds_read_b64_tr_b16 v[228:229], v1 offset:8192\n"
"ds_read_b64_tr_b16 v[230:231], v1 offset:10240\n"
"v_mul_f32 v94, v94, v8\n"
"v_mul_f32 v95, v95, v8\n"
"v_mfma_f32_16x16x32_bf16 v[44:47], v[244:247], v[140:143], v[44:47]\n"
"v_cvt_pk_bf16_f32 v80, v80, v81\n"
"v_mfma_f32_16x16x32_bf16 v[48:51], v[244:247], v[144:147], v[48:51]\n"
"ds_read_b64_tr_b16 v[232:233], v2 offset:8192\n"
"ds_read_b64_tr_b16 v[234:235], v2 offset:10240\n"
"v_cvt_pk_bf16_f32 v81, v82, v83\n"
"v_cvt_pk_bf16_f32 v82, v92, v93\n"
"v_mfma_f32_16x16x32_bf16 v[56:59], v[248:251], v[140:143], v[56:59]\n"
"v_cvt_pk_bf16_f32 v83, v94, v95\n"
"v_mfma_f32_16x16x32_bf16 v[60:63], v[248:251], v[144:147], v[60:63]\n"
"ds_read_b64_tr_b16 v[236:237], v3 offset:8192\n"
"ds_read_b64_tr_b16 v[238:239], v3 offset:10240\n"
"buffer_store_dwordx4 v[80:83], v5, %5, s46 offen offset:0 sc0 sc1\n"
"v_mul_f32 v84, v84, v9\n"
"s_waitcnt lgkmcnt(4)\n"
"v_mfma_f32_16x16x32_bf16 v[68:71], v[252:255], v[140:143], v[68:71]\n"
"v_mul_f32 v85, v85, v9\n"
"v_mfma_f32_16x16x32_bf16 v[72:75], v[252:255], v[144:147], v[72:75]\n"
"ds_read_b64_tr_b16 v[240:241], v0 offset:12288\n"
"ds_read_b64_tr_b16 v[242:243], v0 offset:14336\n"
"v_mul_f32 v86, v86, v9\n"
"v_mul_f32 v87, v87, v9\n"
"v_mfma_f32_16x16x32_bf16 v[32:35], v[224:227], v[152:155], v[32:35]\n"
"v_mul_f32 v96, v96, v9\n"
"v_mfma_f32_16x16x32_bf16 v[36:39], v[224:227], v[156:159], v[36:39]\n"
"ds_read_b64_tr_b16 v[244:245], v1 offset:12288\n"
"ds_read_b64_tr_b16 v[246:247], v1 offset:14336\n"
"v_mul_f32 v97, v97, v9\n"
"v_mul_f32 v98, v98, v9\n"
"v_mfma_f32_16x16x32_bf16 v[44:47], v[228:231], v[152:155], v[44:47]\n"
"v_mul_f32 v99, v99, v9\n"
"v_mfma_f32_16x16x32_bf16 v[48:51], v[228:231], v[156:159], v[48:51]\n"
"ds_read_b64_tr_b16 v[248:249], v2 offset:12288\n"
"ds_read_b64_tr_b16 v[250:251], v2 offset:14336\n"
"v_cvt_pk_bf16_f32 v84, v84, v85\n"
"v_cvt_pk_bf16_f32 v85, v86, v87\n"
"s_waitcnt lgkmcnt(4)\n"
"v_mfma_f32_16x16x32_bf16 v[56:59], v[232:235], v[152:155], v[56:59]\n"
"v_cvt_pk_bf16_f32 v86, v96, v97\n"
"v_mfma_f32_16x16x32_bf16 v[60:63], v[232:235], v[156:159], v[60:63]\n"
"ds_read_b64_tr_b16 v[252:253], v3 offset:12288\n"
"ds_read_b64_tr_b16 v[254:255], v3 offset:14336\n"
"v_cvt_pk_bf16_f32 v87, v98, v99\n"
"buffer_store_dwordx4 v[84:87], v6, %5, s46 offen offset:0 sc0 sc1\n"
"v_mfma_f32_16x16x32_bf16 v[68:71], v[236:239], v[152:155], v[68:71]\n"
"v_mul_f32 v104, v104, v8\n"
"v_mfma_f32_16x16x32_bf16 v[72:75], v[236:239], v[156:159], v[72:75]\n"
"ds_read_b64_tr_b16 v[224:225], v0 offset:16384\n"
"ds_read_b64_tr_b16 v[226:227], v0 offset:18432\n"
"v_mul_f32 v105, v105, v8\n"
"v_mul_f32 v106, v106, v8\n"
"v_mfma_f32_16x16x32_bf16 v[32:35], v[240:243], v[164:167], v[32:35]\n"
"v_mul_f32 v107, v107, v8\n"
"v_mfma_f32_16x16x32_bf16 v[36:39], v[240:243], v[168:171], v[36:39]\n"
"ds_read_b64_tr_b16 v[228:229], v1 offset:16384\n"
"ds_read_b64_tr_b16 v[230:231], v1 offset:18432\n"
"v_mul_f32 v116, v116, v8\n"
"v_mul_f32 v117, v117, v8\n"
"s_waitcnt lgkmcnt(4)\n"
"v_mfma_f32_16x16x32_bf16 v[44:47], v[244:247], v[164:167], v[44:47]\n"
"v_mul_f32 v118, v118, v8\n"
"v_mfma_f32_16x16x32_bf16 v[48:51], v[244:247], v[168:171], v[48:51]\n"
"ds_read_b64_tr_b16 v[232:233], v2 offset:16384\n"
"ds_read_b64_tr_b16 v[234:235], v2 offset:18432\n"
"v_mul_f32 v119, v119, v8\n"
"v_cvt_pk_bf16_f32 v104, v104, v105\n"
"v_mfma_f32_16x16x32_bf16 v[56:59], v[248:251], v[164:167], v[56:59]\n"
"v_cvt_pk_bf16_f32 v105, v106, v107\n"
"v_mfma_f32_16x16x32_bf16 v[60:63], v[248:251], v[168:171], v[60:63]\n"
"ds_read_b64_tr_b16 v[236:237], v3 offset:16384\n"
"ds_read_b64_tr_b16 v[238:239], v3 offset:18432\n"
"v_cvt_pk_bf16_f32 v106, v116, v117\n"
"v_cvt_pk_bf16_f32 v107, v118, v119\n"
"v_mfma_f32_16x16x32_bf16 v[68:71], v[252:255], v[164:167], v[68:71]\n"
"buffer_store_dwordx4 v[104:107], v5, %5, s46 offen offset:1024 sc0 sc1\n"
"v_mfma_f32_16x16x32_bf16 v[72:75], v[252:255], v[168:171], v[72:75]\n"
"ds_read_b64_tr_b16 v[240:241], v0 offset:20480\n"
"ds_read_b64_tr_b16 v[242:243], v0 offset:22528\n"
"v_mul_f32 v108, v108, v9\n"
"v_mul_f32 v109, v109, v9\n"
"s_waitcnt lgkmcnt(4)\n"
"v_mfma_f32_16x16x32_bf16 v[32:35], v[224:227], v[176:179], v[32:35]\n"
"v_mul_f32 v110, v110, v9\n"
"v_mfma_f32_16x16x32_bf16 v[36:39], v[224:227], v[180:183], v[36:39]\n"
"ds_read_b64_tr_b16 v[244:245], v1 offset:20480\n"
"ds_read_b64_tr_b16 v[246:247], v1 offset:22528\n"
"v_mul_f32 v111, v111, v9\n"
"v_mul_f32 v120, v120, v9\n"
"v_mfma_f32_16x16x32_bf16 v[44:47], v[228:231], v[176:179], v[44:47]\n"
"v_mul_f32 v121, v121, v9\n"
"v_mfma_f32_16x16x32_bf16 v[48:51], v[228:231], v[180:183], v[48:51]\n"
"ds_read_b64_tr_b16 v[248:249], v2 offset:20480\n"
"ds_read_b64_tr_b16 v[250:251], v2 offset:22528\n"
"v_mul_f32 v122, v122, v9\n"
"v_mul_f32 v123, v123, v9\n"
"v_mfma_f32_16x16x32_bf16 v[56:59], v[232:235], v[176:179], v[56:59]\n"
"v_cvt_pk_bf16_f32 v108, v108, v109\n"
"v_mfma_f32_16x16x32_bf16 v[60:63], v[232:235], v[180:183], v[60:63]\n"
"ds_read_b64_tr_b16 v[252:253], v3 offset:20480\n"
"ds_read_b64_tr_b16 v[254:255], v3 offset:22528\n"
"v_cvt_pk_bf16_f32 v109, v110, v111\n"
"v_cvt_pk_bf16_f32 v110, v120, v121\n"
"s_waitcnt lgkmcnt(4)\n"
"v_mfma_f32_16x16x32_bf16 v[68:71], v[236:239], v[176:179], v[68:71]\n"
"v_cvt_pk_bf16_f32 v111, v122, v123\n"
"v_mfma_f32_16x16x32_bf16 v[72:75], v[236:239], v[180:183], v[72:75]\n"
"ds_read_b64_tr_b16 v[224:225], v0 offset:24576\n"
"ds_read_b64_tr_b16 v[226:227], v0 offset:26624\n"
"buffer_store_dwordx4 v[108:111], v6, %5, s46 offen offset:1024 sc0 sc1\n"
"v_mfma_f32_16x16x32_bf16 v[32:35], v[240:243], v[188:191], v[32:35]\n"
"v_mfma_f32_16x16x32_bf16 v[36:39], v[240:243], v[192:195], v[36:39]\n"
"ds_read_b64_tr_b16 v[228:229], v1 offset:24576\n"
"ds_read_b64_tr_b16 v[230:231], v1 offset:26624\n"
"v_mfma_f32_16x16x32_bf16 v[44:47], v[244:247], v[188:191], v[44:47]\n"
"v_mfma_f32_16x16x32_bf16 v[48:51], v[244:247], v[192:195], v[48:51]\n"
"ds_read_b64_tr_b16 v[232:233], v2 offset:24576\n"
"ds_read_b64_tr_b16 v[234:235], v2 offset:26624\n"
"s_waitcnt lgkmcnt(6)\n"
"v_mfma_f32_16x16x32_bf16 v[56:59], v[248:251], v[188:191], v[56:59]\n"
"v_mfma_f32_16x16x32_bf16 v[60:63], v[248:251], v[192:195], v[60:63]\n"
"ds_read_b64_tr_b16 v[236:237], v3 offset:24576\n"
"ds_read_b64_tr_b16 v[238:239], v3 offset:26624\n"
"v_mfma_f32_16x16x32_bf16 v[68:71], v[252:255], v[188:191], v[68:71]\n"
"v_mfma_f32_16x16x32_bf16 v[72:75], v[252:255], v[192:195], v[72:75]\n"
"ds_read_b64_tr_b16 v[240:241], v0 offset:28672\n"
"ds_read_b64_tr_b16 v[242:243], v0 offset:30720\n"
"s_waitcnt lgkmcnt(6)\n"
"v_mfma_f32_16x16x32_bf16 v[32:35], v[224:227], v[200:203], v[32:35]\n"
"v_mfma_f32_16x16x32_bf16 v[36:39], v[224:227], v[204:207], v[36:39]\n"
"ds_read_b64_tr_b16 v[244:245], v1 offset:28672\n"
"ds_read_b64_tr_b16 v[246:247], v1 offset:30720\n"
"v_mfma_f32_16x16x32_bf16 v[44:47], v[228:231], v[200:203], v[44:47]\n"
"v_mfma_f32_16x16x32_bf16 v[48:51], v[228:231], v[204:207], v[48:51]\n"
"ds_read_b64_tr_b16 v[248:249], v2 offset:28672\n"
"ds_read_b64_tr_b16 v[250:251], v2 offset:30720\n"
"ds_read_b64_tr_b16 v[252:253], v3 offset:28672\n"
"ds_read_b64_tr_b16 v[254:255], v3 offset:30720\n"
"s_waitcnt lgkmcnt(7)\n"
"v_mfma_f32_16x16x32_bf16 v[56:59], v[232:235], v[200:203], v[56:59]\n"
"v_mfma_f32_16x16x32_bf16 v[60:63], v[232:235], v[204:207], v[60:63]\n"
"v_mfma_f32_16x16x32_bf16 v[68:71], v[236:239], v[200:203], v[68:71]\n"
"v_mfma_f32_16x16x32_bf16 v[72:75], v[236:239], v[204:207], v[72:75]\n"
"s_waitcnt lgkmcnt(4)\n"
"v_mfma_f32_16x16x32_bf16 v[32:35], v[240:243], v[212:215], v[32:35]\n"
"v_mfma_f32_16x16x32_bf16 v[36:39], v[240:243], v[216:219], v[36:39]\n"
"v_mfma_f32_16x16x32_bf16 v[44:47], v[244:247], v[212:215], v[44:47]\n"
"v_mfma_f32_16x16x32_bf16 v[48:51], v[244:247], v[216:219], v[48:51]\n"
"s_waitcnt lgkmcnt(1)\n"
"v_mfma_f32_16x16x32_bf16 v[56:59], v[248:251], v[212:215], v[56:59]\n"
"v_mfma_f32_16x16x32_bf16 v[60:63], v[248:251], v[216:219], v[60:63]\n"
"s_waitcnt lgkmcnt(0)\n"
"v_mfma_f32_16x16x32_bf16 v[68:71], v[252:255], v[212:215], v[68:71]\n"
"v_mfma_f32_16x16x32_bf16 v[72:75], v[252:255], v[216:219], v[72:75]\n"
"s_nop 7\n"
"s_mov_b32 s46, 12288\n"
"v_mul_f32 v32, v32, v8\n"
"v_mul_f32 v33, v33, v8\n"
"v_mul_f32 v34, v34, v8\n"
"v_mul_f32 v35, v35, v8\n"
"v_mul_f32 v44, v44, v8\n"
"v_mul_f32 v45, v45, v8\n"
"v_mul_f32 v46, v46, v8\n"
"v_mul_f32 v47, v47, v8\n"
"v_cvt_pk_bf16_f32 v32, v32, v33\n"
"v_cvt_pk_bf16_f32 v33, v34, v35\n"
"v_cvt_pk_bf16_f32 v34, v44, v45\n"
"v_cvt_pk_bf16_f32 v35, v46, v47\n"
"buffer_store_dwordx4 v[32:35], v5, %5, s46 offen offset:0 sc0 sc1\n"
"v_mul_f32 v36, v36, v9\n"
"v_mul_f32 v37, v37, v9\n"
"v_mul_f32 v38, v38, v9\n"
"v_mul_f32 v39, v39, v9\n"
"v_mul_f32 v48, v48, v9\n"
"v_mul_f32 v49, v49, v9\n"
"v_mul_f32 v50, v50, v9\n"
"v_mul_f32 v51, v51, v9\n"
"v_cvt_pk_bf16_f32 v36, v36, v37\n"
"v_cvt_pk_bf16_f32 v37, v38, v39\n"
"v_cvt_pk_bf16_f32 v38, v48, v49\n"
"v_cvt_pk_bf16_f32 v39, v50, v51\n"
"buffer_store_dwordx4 v[36:39], v6, %5, s46 offen offset:0 sc0 sc1\n"
"v_mul_f32 v56, v56, v8\n"
"v_mul_f32 v57, v57, v8\n"
"v_mul_f32 v58, v58, v8\n"
"v_mul_f32 v59, v59, v8\n"
"v_mul_f32 v68, v68, v8\n"
"v_mul_f32 v69, v69, v8\n"
"v_mul_f32 v70, v70, v8\n"
"v_mul_f32 v71, v71, v8\n"
"v_cvt_pk_bf16_f32 v56, v56, v57\n"
"v_cvt_pk_bf16_f32 v57, v58, v59\n"
"v_cvt_pk_bf16_f32 v58, v68, v69\n"
"v_cvt_pk_bf16_f32 v59, v70, v71\n"
"buffer_store_dwordx4 v[56:59], v5, %5, s46 offen offset:1024 sc0 sc1\n"
"v_mul_f32 v60, v60, v9\n"
"v_mul_f32 v61, v61, v9\n"
"v_mul_f32 v62, v62, v9\n"
"v_mul_f32 v63, v63, v9\n"
"v_mul_f32 v72, v72, v9\n"
"v_mul_f32 v73, v73, v9\n"
"v_mul_f32 v74, v74, v9\n"
"v_mul_f32 v75, v75, v9\n"
"v_cvt_pk_bf16_f32 v60, v60, v61\n"
"v_cvt_pk_bf16_f32 v61, v62, v63\n"
"v_cvt_pk_bf16_f32 v62, v72, v73\n"
"v_cvt_pk_bf16_f32 v63, v74, v75\n"
"buffer_store_dwordx4 v[60:63], v6, %5, s46 offen offset:1024 sc0 sc1\n"
"s_branch LEND_%=\n"
"LRARE2_%=:\n"
"s_nop 7\n"
"s_nop 7\n"
"v_mbcnt_lo_u32_b32 v240, -1, 0\n"
"v_mbcnt_hi_u32_b32 v240, -1, v240\n"
"v_xor_b32 v241, 16, v240\n"
"v_lshlrev_b32 v241, 2, v241\n"
"v_xor_b32 v242, 32, v240\n"
"v_lshlrev_b32 v242, 2, v242\n"
"v_max3_f32 v243, v32, v33, v34\n"
"v_max3_f32 v243, v243, v35, v44\n"
"v_max3_f32 v243, v243, v45, v46\n"
"v_max_f32 v243, v243, v47\n"
"v_max3_f32 v244, v36, v37, v38\n"
"v_max3_f32 v244, v244, v39, v48\n"
"v_max3_f32 v244, v244, v49, v50\n"
"v_max_f32 v244, v244, v51\n"
"ds_bpermute_b32 v248, v241, v243\n"
"ds_bpermute_b32 v249, v241, v244\n"
"s_waitcnt lgkmcnt(0)\n"
"v_max_f32 v243, v243, v248\n"
"v_max_f32 v244, v244, v249\n"
"ds_bpermute_b32 v248, v242, v243\n"
"ds_bpermute_b32 v249, v242, v244\n"
"s_waitcnt lgkmcnt(0)\n"
"v_max_f32 v243, v243, v248\n"
"v_max_f32 v244, v244, v249\n"
"v_ceil_f32 v243, v243\n"
"v_max_f32 v243, s44, v243\n"
"v_sub_f32 v32, v32, v243\n"
"v_sub_f32 v33, v33, v243\n"
"v_sub_f32 v34, v34, v243\n"
"v_sub_f32 v35, v35, v243\n"
"v_sub_f32 v44, v44, v243\n"
"v_sub_f32 v45, v45, v243\n"
"v_sub_f32 v46, v46, v243\n"
"v_sub_f32 v47, v47, v243\n"
"v_sub_f32 v20, v20, v243\n"
"v_sub_f32 v21, v21, v243\n"
"v_sub_f32 v22, v22, v243\n"
"v_sub_f32 v23, v23, v243\n"
"v_cvt_i32_f32 v248, v243\n"
"v_sub_u32 v248, 0, v248\n"
"v_ceil_f32 v244, v244\n"
"v_max_f32 v244, s44, v244\n"
"v_sub_f32 v36, v36, v244\n"
"v_sub_f32 v37, v37, v244\n"
"v_sub_f32 v38, v38, v244\n"
"v_sub_f32 v39, v39, v244\n"
"v_sub_f32 v48, v48, v244\n"
"v_sub_f32 v49, v49, v244\n"
"v_sub_f32 v50, v50, v244\n"
"v_sub_f32 v51, v51, v244\n"
"v_sub_f32 v24, v24, v244\n"
"v_sub_f32 v25, v25, v244\n"
"v_sub_f32 v26, v26, v244\n"
"v_sub_f32 v27, v27, v244\n"
"v_cvt_i32_f32 v249, v244\n"
"v_sub_u32 v249, 0, v249\n"
"s_cmp_lg_u32 s44, 0\n"
"s_cbranch_scc1 LRDONE2_%=\n"
"v_accvgpr_read_b32 v246, a0\n"
"v_ldexp_f32 v246, v246, v248\n"
"v_accvgpr_write_b32 a0, v246\n"
"v_accvgpr_read_b32 v246, a1\n"
"v_ldexp_f32 v246, v246, v248\n"
"v_accvgpr_write_b32 a1, v246\n"
"v_accvgpr_read_b32 v246, a2\n"
"v_ldexp_f32 v246, v246, v248\n"
"v_accvgpr_write_b32 a2, v246\n"
"v_accvgpr_read_b32 v246, a3\n"
"v_ldexp_f32 v246, v246, v248\n"
"v_accvgpr_write_b32 a3, v246\n"
"v_accvgpr_read_b32 v246, a12\n"
"v_ldexp_f32 v246, v246, v248\n"
"v_accvgpr_write_b32 a12, v246\n"
"v_accvgpr_read_b32 v246, a13\n"
"v_ldexp_f32 v246, v246, v248\n"
"v_accvgpr_write_b32 a13, v246\n"
"v_accvgpr_read_b32 v246, a14\n"
"v_ldexp_f32 v246, v246, v248\n"
"v_accvgpr_write_b32 a14, v246\n"
"v_accvgpr_read_b32 v246, a15\n"
"v_ldexp_f32 v246, v246, v248\n"
"v_accvgpr_write_b32 a15, v246\n"
"v_accvgpr_read_b32 v246, a24\n"
"v_ldexp_f32 v246, v246, v248\n"
"v_accvgpr_write_b32 a24, v246\n"
"v_accvgpr_read_b32 v246, a25\n"
"v_ldexp_f32 v246, v246, v248\n"
"v_accvgpr_write_b32 a25, v246\n"
"v_accvgpr_read_b32 v246, a26\n"
"v_ldexp_f32 v246, v246, v248\n"
"v_accvgpr_write_b32 a26, v246\n"
"v_accvgpr_read_b32 v246, a27\n"
"v_ldexp_f32 v246, v246, v248\n"
"v_accvgpr_write_b32 a27, v246\n"
"v_accvgpr_read_b32 v246, a36\n"
"v_ldexp_f32 v246, v246, v248\n"
"v_accvgpr_write_b32 a36, v246\n"
"v_accvgpr_read_b32 v246, a37\n"
"v_ldexp_f32 v246, v246, v248\n"
"v_accvgpr_write_b32 a37, v246\n"
"v_accvgpr_read_b32 v246, a38\n"
"v_ldexp_f32 v246, v246, v248\n"
"v_accvgpr_write_b32 a38, v246\n"
"v_accvgpr_read_b32 v246, a39\n"
"v_ldexp_f32 v246, v246, v248\n"
"v_accvgpr_write_b32 a39, v246\n"
"v_accvgpr_read_b32 v246, a48\n"
"v_ldexp_f32 v246, v246, v248\n"
"v_accvgpr_write_b32 a48, v246\n"
"v_accvgpr_read_b32 v246, a49\n"
"v_ldexp_f32 v246, v246, v248\n"
"v_accvgpr_write_b32 a49, v246\n"
"v_accvgpr_read_b32 v246, a50\n"
"v_ldexp_f32 v246, v246, v248\n"
"v_accvgpr_write_b32 a50, v246\n"
"v_accvgpr_read_b32 v246, a51\n"
"v_ldexp_f32 v246, v246, v248\n"
"v_accvgpr_write_b32 a51, v246\n"
"v_accvgpr_read_b32 v246, a60\n"
"v_ldexp_f32 v246, v246, v248\n"
"v_accvgpr_write_b32 a60, v246\n"
"v_accvgpr_read_b32 v246, a61\n"
"v_ldexp_f32 v246, v246, v248\n"
"v_accvgpr_write_b32 a61, v246\n"
"v_accvgpr_read_b32 v246, a62\n"
"v_ldexp_f32 v246, v246, v248\n"
"v_accvgpr_write_b32 a62, v246\n"
"v_accvgpr_read_b32 v246, a63\n"
"v_ldexp_f32 v246, v246, v248\n"
"v_accvgpr_write_b32 a63, v246\n"
"v_accvgpr_read_b32 v246, a72\n"
"v_ldexp_f32 v246, v246, v248\n"
"v_accvgpr_write_b32 a72, v246\n"
"v_accvgpr_read_b32 v246, a73\n"
"v_ldexp_f32 v246, v246, v248\n"
"v_accvgpr_write_b32 a73, v246\n"
"v_accvgpr_read_b32 v246, a74\n"
"v_ldexp_f32 v246, v246, v248\n"
"v_accvgpr_write_b32 a74, v246\n"
"v_accvgpr_read_b32 v246, a75\n"
"v_ldexp_f32 v246, v246, v248\n"
"v_accvgpr_write_b32 a75, v246\n"
"v_accvgpr_read_b32 v246, a84\n"
"v_ldexp_f32 v246, v246, v248\n"
"v_accvgpr_write_b32 a84, v246\n"
"v_accvgpr_read_b32 v246, a85\n"
"v_ldexp_f32 v246, v246, v248\n"
"v_accvgpr_write_b32 a85, v246\n"
"v_accvgpr_read_b32 v246, a86\n"
"v_ldexp_f32 v246, v246, v248\n"
"v_accvgpr_write_b32 a86, v246\n"
"v_accvgpr_read_b32 v246, a87\n"
"v_ldexp_f32 v246, v246, v248\n"
"v_accvgpr_write_b32 a87, v246\n"
"v_accvgpr_read_b32 v246, a96\n"
"v_ldexp_f32 v246, v246, v248\n"
"v_accvgpr_write_b32 a96, v246\n"
"v_accvgpr_read_b32 v246, a97\n"
"v_ldexp_f32 v246, v246, v248\n"
"v_accvgpr_write_b32 a97, v246\n"
"v_accvgpr_read_b32 v246, a98\n"
"v_ldexp_f32 v246, v246, v248\n"
"v_accvgpr_write_b32 a98, v246\n"
"v_accvgpr_read_b32 v246, a99\n"
"v_ldexp_f32 v246, v246, v248\n"
"v_accvgpr_write_b32 a99, v246\n"
"v_accvgpr_read_b32 v246, a108\n"
"v_ldexp_f32 v246, v246, v248\n"
"v_accvgpr_write_b32 a108, v246\n"
"v_accvgpr_read_b32 v246, a109\n"
"v_ldexp_f32 v246, v246, v248\n"
"v_accvgpr_write_b32 a109, v246\n"
"v_accvgpr_read_b32 v246, a110\n"
"v_ldexp_f32 v246, v246, v248\n"
"v_accvgpr_write_b32 a110, v246\n"
"v_accvgpr_read_b32 v246, a111\n"
"v_ldexp_f32 v246, v246, v248\n"
"v_accvgpr_write_b32 a111, v246\n"
"v_accvgpr_read_b32 v246, a120\n"
"v_ldexp_f32 v246, v246, v248\n"
"v_accvgpr_write_b32 a120, v246\n"
"v_accvgpr_read_b32 v246, a121\n"
"v_ldexp_f32 v246, v246, v248\n"
"v_accvgpr_write_b32 a121, v246\n"
"v_accvgpr_read_b32 v246, a122\n"
"v_ldexp_f32 v246, v246, v248\n"
"v_accvgpr_write_b32 a122, v246\n"
"v_accvgpr_read_b32 v246, a123\n"
"v_ldexp_f32 v246, v246, v248\n"
"v_accvgpr_write_b32 a123, v246\n"
"v_accvgpr_read_b32 v246, a132\n"
"v_ldexp_f32 v246, v246, v248\n"
"v_accvgpr_write_b32 a132, v246\n"
"v_accvgpr_read_b32 v246, a133\n"
"v_ldexp_f32 v246, v246, v248\n"
"v_accvgpr_write_b32 a133, v246\n"
"v_accvgpr_read_b32 v246, a134\n"
"v_ldexp_f32 v246, v246, v248\n"
"v_accvgpr_write_b32 a134, v246\n"
"v_accvgpr_read_b32 v246, a135\n"
"v_ldexp_f32 v246, v246, v248\n"
"v_accvgpr_write_b32 a135, v246\n"
"v_accvgpr_read_b32 v246, a144\n"
"v_ldexp_f32 v246, v246, v248\n"
"v_accvgpr_write_b32 a144, v246\n"
"v_accvgpr_read_b32 v246, a145\n"
"v_ldexp_f32 v246, v246, v248\n"
"v_accvgpr_write_b32 a145, v246\n"
"v_accvgpr_read_b32 v246, a146\n"
"v_ldexp_f32 v246, v246, v248\n"
"v_accvgpr_write_b32 a146, v246\n"
"v_accvgpr_read_b32 v246, a147\n"
"v_ldexp_f32 v246, v246, v248\n"
"v_accvgpr_write_b32 a147, v246\n"
"v_accvgpr_read_b32 v246, a156\n"
"v_ldexp_f32 v246, v246, v248\n"
"v_accvgpr_write_b32 a156, v246\n"
"v_accvgpr_read_b32 v246, a157\n"
"v_ldexp_f32 v246, v246, v248\n"
"v_accvgpr_write_b32 a157, v246\n"
"v_accvgpr_read_b32 v246, a158\n"
"v_ldexp_f32 v246, v246, v248\n"
"v_accvgpr_write_b32 a158, v246\n"
"v_accvgpr_read_b32 v246, a159\n"
"v_ldexp_f32 v246, v246, v248\n"
"v_accvgpr_write_b32 a159, v246\n"
"v_accvgpr_read_b32 v246, a168\n"
"v_ldexp_f32 v246, v246, v248\n"
"v_accvgpr_write_b32 a168, v246\n"
"v_accvgpr_read_b32 v246, a169\n"
"v_ldexp_f32 v246, v246, v248\n"
"v_accvgpr_write_b32 a169, v246\n"
"v_accvgpr_read_b32 v246, a170\n"
"v_ldexp_f32 v246, v246, v248\n"
"v_accvgpr_write_b32 a170, v246\n"
"v_accvgpr_read_b32 v246, a171\n"
"v_ldexp_f32 v246, v246, v248\n"
"v_accvgpr_write_b32 a171, v246\n"
"v_accvgpr_read_b32 v246, a180\n"
"v_ldexp_f32 v246, v246, v248\n"
"v_accvgpr_write_b32 a180, v246\n"
"v_accvgpr_read_b32 v246, a181\n"
"v_ldexp_f32 v246, v246, v248\n"
"v_accvgpr_write_b32 a181, v246\n"
"v_accvgpr_read_b32 v246, a182\n"
"v_ldexp_f32 v246, v246, v248\n"
"v_accvgpr_write_b32 a182, v246\n"
"v_accvgpr_read_b32 v246, a183\n"
"v_ldexp_f32 v246, v246, v248\n"
"v_accvgpr_write_b32 a183, v246\n"
"v_ldexp_f32 v205, v205, v248\n"
"v_lshlrev_b32 v246, 16, v56\n"
"v_and_b32 v247, 0xffff0000, v56\n"
"v_ldexp_f32 v246, v246, v248\n"
"v_ldexp_f32 v247, v247, v248\n"
"v_cvt_pk_bf16_f32 v56, v246, v247\n"
"v_lshlrev_b32 v246, 16, v57\n"
"v_and_b32 v247, 0xffff0000, v57\n"
"v_ldexp_f32 v246, v246, v248\n"
"v_ldexp_f32 v247, v247, v248\n"
"v_cvt_pk_bf16_f32 v57, v246, v247\n"
"v_lshlrev_b32 v246, 16, v58\n"
"v_and_b32 v247, 0xffff0000, v58\n"
"v_ldexp_f32 v246, v246, v248\n"
"v_ldexp_f32 v247, v247, v248\n"
"v_cvt_pk_bf16_f32 v58, v246, v247\n"
"v_lshlrev_b32 v246, 16, v59\n"
"v_and_b32 v247, 0xffff0000, v59\n"
"v_ldexp_f32 v246, v246, v248\n"
"v_ldexp_f32 v247, v247, v248\n"
"v_cvt_pk_bf16_f32 v59, v246, v247\n"
"v_lshlrev_b32 v246, 16, v68\n"
"v_and_b32 v247, 0xffff0000, v68\n"
"v_ldexp_f32 v246, v246, v248\n"
"v_ldexp_f32 v247, v247, v248\n"
"v_cvt_pk_bf16_f32 v68, v246, v247\n"
"v_lshlrev_b32 v246, 16, v69\n"
"v_and_b32 v247, 0xffff0000, v69\n"
"v_ldexp_f32 v246, v246, v248\n"
"v_ldexp_f32 v247, v247, v248\n"
"v_cvt_pk_bf16_f32 v69, v246, v247\n"
"v_lshlrev_b32 v246, 16, v70\n"
"v_and_b32 v247, 0xffff0000, v70\n"
"v_ldexp_f32 v246, v246, v248\n"
"v_ldexp_f32 v247, v247, v248\n"
"v_cvt_pk_bf16_f32 v70, v246, v247\n"
"v_lshlrev_b32 v246, 16, v71\n"
"v_and_b32 v247, 0xffff0000, v71\n"
"v_ldexp_f32 v246, v246, v248\n"
"v_ldexp_f32 v247, v247, v248\n"
"v_cvt_pk_bf16_f32 v71, v246, v247\n"
"v_accvgpr_read_b32 v246, a4\n"
"v_ldexp_f32 v246, v246, v249\n"
"v_accvgpr_write_b32 a4, v246\n"
"v_accvgpr_read_b32 v246, a5\n"
"v_ldexp_f32 v246, v246, v249\n"
"v_accvgpr_write_b32 a5, v246\n"
"v_accvgpr_read_b32 v246, a6\n"
"v_ldexp_f32 v246, v246, v249\n"
"v_accvgpr_write_b32 a6, v246\n"
"v_accvgpr_read_b32 v246, a7\n"
"v_ldexp_f32 v246, v246, v249\n"
"v_accvgpr_write_b32 a7, v246\n"
"v_accvgpr_read_b32 v246, a16\n"
"v_ldexp_f32 v246, v246, v249\n"
"v_accvgpr_write_b32 a16, v246\n"
"v_accvgpr_read_b32 v246, a17\n"
"v_ldexp_f32 v246, v246, v249\n"
"v_accvgpr_write_b32 a17, v246\n"
"v_accvgpr_read_b32 v246, a18\n"
"v_ldexp_f32 v246, v246, v249\n"
"v_accvgpr_write_b32 a18, v246\n"
"v_accvgpr_read_b32 v246, a19\n"
"v_ldexp_f32 v246, v246, v249\n"
"v_accvgpr_write_b32 a19, v246\n"
"v_accvgpr_read_b32 v246, a28\n"
"v_ldexp_f32 v246, v246, v249\n"
"v_accvgpr_write_b32 a28, v246\n"
"v_accvgpr_read_b32 v246, a29\n"
"v_ldexp_f32 v246, v246, v249\n"
"v_accvgpr_write_b32 a29, v246\n"
"v_accvgpr_read_b32 v246, a30\n"
"v_ldexp_f32 v246, v246, v249\n"
"v_accvgpr_write_b32 a30, v246\n"
"v_accvgpr_read_b32 v246, a31\n"
"v_ldexp_f32 v246, v246, v249\n"
"v_accvgpr_write_b32 a31, v246\n"
"v_accvgpr_read_b32 v246, a40\n"
"v_ldexp_f32 v246, v246, v249\n"
"v_accvgpr_write_b32 a40, v246\n"
"v_accvgpr_read_b32 v246, a41\n"
"v_ldexp_f32 v246, v246, v249\n"
"v_accvgpr_write_b32 a41, v246\n"
"v_accvgpr_read_b32 v246, a42\n"
"v_ldexp_f32 v246, v246, v249\n"
"v_accvgpr_write_b32 a42, v246\n"
"v_accvgpr_read_b32 v246, a43\n"
"v_ldexp_f32 v246, v246, v249\n"
"v_accvgpr_write_b32 a43, v246\n"
"v_accvgpr_read_b32 v246, a52\n"
"v_ldexp_f32 v246, v246, v249\n"
"v_accvgpr_write_b32 a52, v246\n"
"v_accvgpr_read_b32 v246, a53\n"
"v_ldexp_f32 v246, v246, v249\n"
"v_accvgpr_write_b32 a53, v246\n"
"v_accvgpr_read_b32 v246, a54\n"
"v_ldexp_f32 v246, v246, v249\n"
"v_accvgpr_write_b32 a54, v246\n"
"v_accvgpr_read_b32 v246, a55\n"
"v_ldexp_f32 v246, v246, v249\n"
"v_accvgpr_write_b32 a55, v246\n"
"v_accvgpr_read_b32 v246, a64\n"
"v_ldexp_f32 v246, v246, v249\n"
"v_accvgpr_write_b32 a64, v246\n"
"v_accvgpr_read_b32 v246, a65\n"
"v_ldexp_f32 v246, v246, v249\n"
"v_accvgpr_write_b32 a65, v246\n"
"v_accvgpr_read_b32 v246, a66\n"
"v_ldexp_f32 v246, v246, v249\n"
"v_accvgpr_write_b32 a66, v246\n"
"v_accvgpr_read_b32 v246, a67\n"
"v_ldexp_f32 v246, v246, v249\n"
"v_accvgpr_write_b32 a67, v246\n"
"v_accvgpr_read_b32 v246, a76\n"
"v_ldexp_f32 v246, v246, v249\n"
"v_accvgpr_write_b32 a76, v246\n"
"v_accvgpr_read_b32 v246, a77\n"
"v_ldexp_f32 v246, v246, v249\n"
"v_accvgpr_write_b32 a77, v246\n"
"v_accvgpr_read_b32 v246, a78\n"
"v_ldexp_f32 v246, v246, v249\n"
"v_accvgpr_write_b32 a78, v246\n"
"v_accvgpr_read_b32 v246, a79\n"
"v_ldexp_f32 v246, v246, v249\n"
"v_accvgpr_write_b32 a79, v246\n"
"v_accvgpr_read_b32 v246, a88\n"
"v_ldexp_f32 v246, v246, v249\n"
"v_accvgpr_write_b32 a88, v246\n"
"v_accvgpr_read_b32 v246, a89\n"
"v_ldexp_f32 v246, v246, v249\n"
"v_accvgpr_write_b32 a89, v246\n"
"v_accvgpr_read_b32 v246, a90\n"
"v_ldexp_f32 v246, v246, v249\n"
"v_accvgpr_write_b32 a90, v246\n"
"v_accvgpr_read_b32 v246, a91\n"
"v_ldexp_f32 v246, v246, v249\n"
"v_accvgpr_write_b32 a91, v246\n"
"v_accvgpr_read_b32 v246, a100\n"
"v_ldexp_f32 v246, v246, v249\n"
"v_accvgpr_write_b32 a100, v246\n"
"v_accvgpr_read_b32 v246, a101\n"
"v_ldexp_f32 v246, v246, v249\n"
"v_accvgpr_write_b32 a101, v246\n"
"v_accvgpr_read_b32 v246, a102\n"
"v_ldexp_f32 v246, v246, v249\n"
"v_accvgpr_write_b32 a102, v246\n"
"v_accvgpr_read_b32 v246, a103\n"
"v_ldexp_f32 v246, v246, v249\n"
"v_accvgpr_write_b32 a103, v246\n"
"v_accvgpr_read_b32 v246, a112\n"
"v_ldexp_f32 v246, v246, v249\n"
"v_accvgpr_write_b32 a112, v246\n"
"v_accvgpr_read_b32 v246, a113\n"
"v_ldexp_f32 v246, v246, v249\n"
"v_accvgpr_write_b32 a113, v246\n"
"v_accvgpr_read_b32 v246, a114\n"
"v_ldexp_f32 v246, v246, v249\n"
"v_accvgpr_write_b32 a114, v246\n"
"v_accvgpr_read_b32 v246, a115\n"
"v_ldexp_f32 v246, v246, v249\n"
"v_accvgpr_write_b32 a115, v246\n"
"v_accvgpr_read_b32 v246, a124\n"
"v_ldexp_f32 v246, v246, v249\n"
"v_accvgpr_write_b32 a124, v246\n"
"v_accvgpr_read_b32 v246, a125\n"
"v_ldexp_f32 v246, v246, v249\n"
"v_accvgpr_write_b32 a125, v246\n"
"v_accvgpr_read_b32 v246, a126\n"
"v_ldexp_f32 v246, v246, v249\n"
"v_accvgpr_write_b32 a126, v246\n"
"v_accvgpr_read_b32 v246, a127\n"
"v_ldexp_f32 v246, v246, v249\n"
"v_accvgpr_write_b32 a127, v246\n"
"v_accvgpr_read_b32 v246, a136\n"
"v_ldexp_f32 v246, v246, v249\n"
"v_accvgpr_write_b32 a136, v246\n"
"v_accvgpr_read_b32 v246, a137\n"
"v_ldexp_f32 v246, v246, v249\n"
"v_accvgpr_write_b32 a137, v246\n"
"v_accvgpr_read_b32 v246, a138\n"
"v_ldexp_f32 v246, v246, v249\n"
"v_accvgpr_write_b32 a138, v246\n"
"v_accvgpr_read_b32 v246, a139\n"
"v_ldexp_f32 v246, v246, v249\n"
"v_accvgpr_write_b32 a139, v246\n"
"v_accvgpr_read_b32 v246, a148\n"
"v_ldexp_f32 v246, v246, v249\n"
"v_accvgpr_write_b32 a148, v246\n"
"v_accvgpr_read_b32 v246, a149\n"
"v_ldexp_f32 v246, v246, v249\n"
"v_accvgpr_write_b32 a149, v246\n"
"v_accvgpr_read_b32 v246, a150\n"
"v_ldexp_f32 v246, v246, v249\n"
"v_accvgpr_write_b32 a150, v246\n"
"v_accvgpr_read_b32 v246, a151\n"
"v_ldexp_f32 v246, v246, v249\n"
"v_accvgpr_write_b32 a151, v246\n"
"v_accvgpr_read_b32 v246, a160\n"
"v_ldexp_f32 v246, v246, v249\n"
"v_accvgpr_write_b32 a160, v246\n"
"v_accvgpr_read_b32 v246, a161\n"
"v_ldexp_f32 v246, v246, v249\n"
"v_accvgpr_write_b32 a161, v246\n"
"v_accvgpr_read_b32 v246, a162\n"
"v_ldexp_f32 v246, v246, v249\n"
"v_accvgpr_write_b32 a162, v246\n"
"v_accvgpr_read_b32 v246, a163\n"
"v_ldexp_f32 v246, v246, v249\n"
"v_accvgpr_write_b32 a163, v246\n"
"v_accvgpr_read_b32 v246, a172\n"
"v_ldexp_f32 v246, v246, v249\n"
"v_accvgpr_write_b32 a172, v246\n"
"v_accvgpr_read_b32 v246, a173\n"
"v_ldexp_f32 v246, v246, v249\n"
"v_accvgpr_write_b32 a173, v246\n"
"v_accvgpr_read_b32 v246, a174\n"
"v_ldexp_f32 v246, v246, v249\n"
"v_accvgpr_write_b32 a174, v246\n"
"v_accvgpr_read_b32 v246, a175\n"
"v_ldexp_f32 v246, v246, v249\n"
"v_accvgpr_write_b32 a175, v246\n"
"v_accvgpr_read_b32 v246, a184\n"
"v_ldexp_f32 v246, v246, v249\n"
"v_accvgpr_write_b32 a184, v246\n"
"v_accvgpr_read_b32 v246, a185\n"
"v_ldexp_f32 v246, v246, v249\n"
"v_accvgpr_write_b32 a185, v246\n"
"v_accvgpr_read_b32 v246, a186\n"
"v_ldexp_f32 v246, v246, v249\n"
"v_accvgpr_write_b32 a186, v246\n"
"v_accvgpr_read_b32 v246, a187\n"
"v_ldexp_f32 v246, v246, v249\n"
"v_accvgpr_write_b32 a187, v246\n"
"v_ldexp_f32 v206, v206, v249\n"
"v_lshlrev_b32 v246, 16, v60\n"
"v_and_b32 v247, 0xffff0000, v60\n"
"v_ldexp_f32 v246, v246, v249\n"
"v_ldexp_f32 v247, v247, v249\n"
"v_cvt_pk_bf16_f32 v60, v246, v247\n"
"v_lshlrev_b32 v246, 16, v61\n"
"v_and_b32 v247, 0xffff0000, v61\n"
"v_ldexp_f32 v246, v246, v249\n"
"v_ldexp_f32 v247, v247, v249\n"
"v_cvt_pk_bf16_f32 v61, v246, v247\n"
"v_lshlrev_b32 v246, 16, v62\n"
"v_and_b32 v247, 0xffff0000, v62\n"
"v_ldexp_f32 v246, v246, v249\n"
"v_ldexp_f32 v247, v247, v249\n"
"v_cvt_pk_bf16_f32 v62, v246, v247\n"
"v_lshlrev_b32 v246, 16, v63\n"
"v_and_b32 v247, 0xffff0000, v63\n"
"v_ldexp_f32 v246, v246, v249\n"
"v_ldexp_f32 v247, v247, v249\n"
"v_cvt_pk_bf16_f32 v63, v246, v247\n"
"v_lshlrev_b32 v246, 16, v72\n"
"v_and_b32 v247, 0xffff0000, v72\n"
"v_ldexp_f32 v246, v246, v249\n"
"v_ldexp_f32 v247, v247, v249\n"
"v_cvt_pk_bf16_f32 v72, v246, v247\n"
"v_lshlrev_b32 v246, 16, v73\n"
"v_and_b32 v247, 0xffff0000, v73\n"
"v_ldexp_f32 v246, v246, v249\n"
"v_ldexp_f32 v247, v247, v249\n"
"v_cvt_pk_bf16_f32 v73, v246, v247\n"
"v_lshlrev_b32 v246, 16, v74\n"
"v_and_b32 v247, 0xffff0000, v74\n"
"v_ldexp_f32 v246, v246, v249\n"
"v_ldexp_f32 v247, v247, v249\n"
"v_cvt_pk_bf16_f32 v74, v246, v247\n"
"v_lshlrev_b32 v246, 16, v75\n"
"v_and_b32 v247, 0xffff0000, v75\n"
"v_ldexp_f32 v246, v246, v249\n"
"v_ldexp_f32 v247, v247, v249\n"
"v_cvt_pk_bf16_f32 v75, v246, v247\n"
"LRDONE2_%=:\n"
"s_mov_b32 s44, 0\n"
"s_nop 4\n"
"s_cmp_eq_u32 s45, 12\n"
"s_cbranch_scc1 LRET12_%=\n"
"s_cmp_eq_u32 s45, 13\n"
"s_cbranch_scc1 LRET13_%=\n"
"s_cmp_eq_u32 s45, 14\n"
"s_cbranch_scc1 LRET14_%=\n"
"s_cmp_eq_u32 s45, 15\n"
"s_cbranch_scc1 LRET15_%=\n"
"s_cmp_eq_u32 s45, 16\n"
"s_cbranch_scc1 LRET16_%=\n"
"s_cmp_eq_u32 s45, 17\n"
"s_cbranch_scc1 LRET17_%=\n"
"s_cmp_eq_u32 s45, 18\n"
"s_cbranch_scc1 LRET18_%=\n"
"s_cmp_eq_u32 s45, 19\n"
"s_cbranch_scc1 LRET19_%=\n"
"s_cmp_eq_u32 s45, 20\n"
"s_cbranch_scc1 LRET20_%=\n"
"s_cmp_eq_u32 s45, 21\n"
"s_cbranch_scc1 LRET21_%=\n"
"s_cmp_eq_u32 s45, 22\n"
"s_cbranch_scc1 LRET22_%=\n"
"s_cmp_eq_u32 s45, 23\n"
"s_cbranch_scc1 LRET23_%=\n"
"s_endpgm\n"
"LSTUB12_%=:\n"
"s_mov_b32 s45, 12\n"
"s_branch LRARE2_%=\n"
"LSTUB13_%=:\n"
"s_mov_b32 s45, 13\n"
"s_branch LRARE2_%=\n"
"LSTUB14_%=:\n"
"s_mov_b32 s45, 14\n"
"s_branch LRARE2_%=\n"
"LSTUB15_%=:\n"
"s_mov_b32 s45, 15\n"
"s_branch LRARE2_%=\n"
"LSTUB16_%=:\n"
"s_mov_b32 s45, 16\n"
"s_branch LRARE2_%=\n"
"LSTUB17_%=:\n"
"s_mov_b32 s45, 17\n"
"s_branch LRARE2_%=\n"
"LSTUB18_%=:\n"
"s_mov_b32 s45, 18\n"
"s_branch LRARE2_%=\n"
"LSTUB19_%=:\n"
"s_mov_b32 s45, 19\n"
"s_branch LRARE2_%=\n"
"LSTUB20_%=:\n"
"s_mov_b32 s45, 20\n"
"s_branch LRARE2_%=\n"
"LSTUB21_%=:\n"
"s_mov_b32 s45, 21\n"
"s_branch LRARE2_%=\n"
"LSTUB22_%=:\n"
"s_mov_b32 s45, 22\n"
"s_branch LRARE2_%=\n"
"LSTUB23_%=:\n"
"s_mov_b32 s45, 23\n"
"s_branch LRARE2_%=\n"
"LEND_%=:\n"
    :
    : "s"(rk), "s"(rv), "s"(rx), "s"(rwq), "s"(rwo), "s"(rp), "s"(wid), "s"(ldsb), "s"(dbg), "s"(toff), "s"(prob), "s"(pfo), "s"(pfw)
    : "memory", "vcc", "scc", "v0", "v1", "v2", "v3", "v4", "v5", "v6", "v7", "v8", "v9", "v10", "v11", "v12", "v13", "v14", "v15", "v16", "v17", "v18", "v19", "v20", "v21", "v22", "v23", "v24", "v25", "v26", "v27", "v28", "v29", "v30", "v31", "v32", "v33", "v34", "v35", "v36", "v37", "v38", "v39", "v40", "v41", "v42", "v43", "v44", "v45", "v46", "v47", "v48", "v49", "v50", "v51", "v52", "v53", "v54", "v55", "v56", "v57", "v58", "v59", "v60", "v61", "v62", "v63", "v64", "v65", "v66", "v67", "v68", "v69", "v70", "v71", "v72", "v73", "v74", "v75", "v76", "v77", "v78", "v79", "v80", "v81", "v82", "v83", "v84", "v85", "v86", "v87", "v88", "v89", "v90", "v91", "v92", "v93", "v94", "v95", "v96", "v97", "v98", "v99", "v100", "v101", "v102", "v103", "v104", "v105", "v106", "v107", "v108", "v109", "v110", "v111", "v112", "v113", "v114", "v115", "v116", "v117", "v118", "v119", "v120", "v121", "v122", "v123", "v124", "v125", "v126", "v127", "v128", "v129", "v130", "v131", "v132", "v133", "v134", "v135", "v136", "v137", "v138", "v139", "v140", "v141", "v142", "v143", "v144", "v145", "v146", "v147", "v148", "v149", "v150", "v151", "v152", "v153", "v154", "v155", "v156", "v157", "v158", "v159", "v160", "v161", "v162", "v163", "v164", "v165", "v166", "v167", "v168", "v169", "v170", "v171", "v172", "v173", "v174", "v175", "v176", "v177", "v178", "v179", "v180", "v181", "v182", "v183", "v184", "v185", "v186", "v187", "v188", "v189", "v190", "v191", "v192", "v193", "v194", "v195", "v196", "v197", "v198", "v199", "v200", "v201", "v202", "v203", "v204", "v205", "v206", "v207", "v208", "v209", "v210", "v211", "v212", "v213", "v214", "v215", "v216", "v217", "v218", "v219", "v220", "v221", "v222", "v223", "v224", "v225", "v226", "v227", "v228", "v229", "v230", "v231", "v232", "v233", "v234", "v235", "v236", "v237", "v238", "v239", "v240", "v241", "v242", "v243", "v244", "v245", "v246", "v247", "v248", "v249", "v250", "v251", "v252", "v253", "v254", "v255", "a0", "a1", "a2", "a3", "a4", "a5", "a6", "a7", "a8", "a9", "a10", "a11", "a12", "a13", "a14", "a15", "a16", "a17", "a18", "a19", "a20", "a21", "a22", "a23", "a24", "a25", "a26", "a27", "a28", "a29", "a30", "a31", "a32", "a33", "a34", "a35", "a36", "a37", "a38", "a39", "a40", "a41", "a42", "a43", "a44", "a45", "a46", "a47", "a48", "a49", "a50", "a51", "a52", "a53", "a54", "a55", "a56", "a57", "a58", "a59", "a60", "a61", "a62", "a63", "a64", "a65", "a66", "a67", "a68", "a69", "a70", "a71", "a72", "a73", "a74", "a75", "a76", "a77", "a78", "a79", "a80", "a81", "a82", "a83", "a84", "a85", "a86", "a87", "a88", "a89", "a90", "a91", "a92", "a93", "a94", "a95", "a96", "a97", "a98", "a99", "a100", "a101", "a102", "a103", "a104", "a105", "a106", "a107", "a108", "a109", "a110", "a111", "a112", "a113", "a114", "a115", "a116", "a117", "a118", "a119", "a120", "a121", "a122", "a123", "a124", "a125", "a126", "a127", "a128", "a129", "a130", "a131", "a132", "a133", "a134", "a135", "a136", "a137", "a138", "a139", "a140", "a141", "a142", "a143", "a144", "a145", "a146", "a147", "a148", "a149", "a150", "a151", "a152", "a153", "a154", "a155", "a156", "a157", "a158", "a159", "a160", "a161", "a162", "a163", "a164", "a165", "a166", "a167", "a168", "a169", "a170", "a171", "a172", "a173", "a174", "a175", "a176", "a177", "a178", "a179", "a180", "a181", "a182", "a183", "a184", "a185", "a186", "a187", "a188", "a189", "a190", "a191", "a192", "a193", "a194", "a195", "a196", "a197", "a198", "a199", "a200", "a201", "a202", "a203", "a204", "a205", "a206", "a207", "a208", "a209", "a210", "a211", "a212", "a213", "a214", "a215", "a216", "a217", "a218", "a219", "a220", "a221", "a222", "a223", "a224", "a225", "a226", "a227", "a228", "a229", "a230", "a231", "a232", "a233", "a234", "a235", "a236", "a237", "a238", "a239", "a240", "a241", "a242", "a243", "a244", "a245", "a246", "a247", "a248", "a249", "a250", "a251", "a252", "a253", "a254", "a255", "s40", "s41", "s42", "s43", "s44", "s45", "s46", "s47", "s48", "s49", "s50", "s51", "s52", "s53", "s54", "s55", "s56", "s57", "s58", "s59", "s60", "s61", "s62", "s63", "s64", "s65", "s66", "s67", "s68", "s69", "s70", "s71", "s72", "s73", "s74", "s75", "s76", "s77", "s78", "s79", "s80", "s81", "s82", "s83", "s84", "s85", "s86", "s87", "s88", "s89", "s90", "s91", "s92", "s93", "s94", "s95", "s96", "s97", "s98", "s99");
}

__global__ __launch_bounds__(256) void reduce_kernel(const uint4* __restrict__ part, __hip_bfloat16* __restrict__ out) {
  const unsigned t = blockIdx.x * 256u + threadIdx.x;
  const unsigned per = PART_BYTES / 16u;
  const unsigned pb = t / per, r = t - pb * per;
  const uint4* p = part + (size_t)pb * NH * per + r;
  uint4 q[NH];
#pragma unroll
  for (int h = 0; h < NH; ++h) q[h] = p[(size_t)h * per];
  float s[8] = {0.f, 0.f, 0.f, 0.f, 0.f, 0.f, 0.f, 0.f};
#pragma unroll
  for (int h = 0; h < NH; ++h) {
    const unsigned w[4] = {q[h].x, q[h].y, q[h].z, q[h].w};
#pragma unroll
    for (int k = 0; k < 4; ++k) { s[2 * k] += __uint_as_float(w[k] << 16); s[2 * k + 1] += __uint_as_float(w[k] & 0xffff0000u); }
  }
  const unsigned lane = r & 63u, hh = (r >> 6) & 1u, qc = r >> 7, qb = qc / 7u, c = qc - qb * 7u;
  const unsigned prob = pb >> 3, b = pb & 7u;
  const unsigned row = b * SQ + 16u * qb + (lane & 15u), col = prob * DM + 64u * c + 32u * hh + 4u * (lane >> 4);
  typedef __bf16 bf16x4 __attribute__((ext_vector_type(4)));
  bf16x4 o0, o1;
#pragma unroll
  for (int k = 0; k < 4; ++k) { o0[k] = (__bf16)s[k]; o1[k] = (__bf16)s[4 + k]; }
  char* dst = (char*)out + ((size_t)row * (2 * DM) + col) * 2;
  *(bf16x4*)dst = o0; *(bf16x4*)(dst + 32) = o1;
}

extern "C" void kernel_launch(void* const* d_in, const int* in_sizes, int n_in, void* d_out, int out_size, void* d_ws, size_t ws_size,
                              hipStream_t stream) {
  Args a;
  for (int p = 0; p < 2; ++p) {
    a.x[p] = (const char*)d_in[5 * p + 0]; a.k[p] = (const char*)d_in[5 * p + 1]; a.v[p] = (const char*)d_in[5 * p + 2];
    a.wq[p] = (const char*)d_in[5 * p + 3]; a.wo[p] = (const char*)d_in[5 * p + 4];
  }
  a.ws = (char*)d_ws;
  attn_fused_kernel<<<256, 256, 0, stream>>>(a);
  reduce_kernel<<<(16u * (PART_BYTES / 16u)) / 256u, 256, 0, stream>>>((const uint4*)d_ws, (__hip_bfloat16*)d_out);
}
```
